```python
import torch

def _softmax(x, axis=-1):
    return torch.softmax(x, dim=axis)

def _gelu(x):
    return torch.nn.functional.gelu(x, approximate='tanh')

def _silu(x):
    return torch.nn.functional.silu(x)

def _sigmoid(x):
    return torch.sigmoid(x)

def _layernorm(x, axis=-1, eps=1e-05):
    m = x.mean(dim=axis, keepdim=True)
    v = x.var(dim=axis, keepdim=True, unbiased=False)
    return (x - m) / torch.sqrt(v + eps)

def _scatter_add(tgt, idx, src, axis=0):
    return tgt.clone().index_add_(axis, idx, src)

def _segment_sum(src, seg, n):
    out = torch.zeros((n,) + src.shape[1:], dtype=src.dtype)
    return out.index_add_(0, seg, src)

def _pad(x, pw):
    flat = tuple((v for pair in reversed(pw) for v in pair))
    return torch.nn.functional.pad(x, flat)
kernel_name = 'adaLN_cond__gqa_block_cached__glu_gated_swiglu__prompt'

def setup_inputs():

    _w = lambda a: (lambda t: t.to(torch.bfloat16) if t.is_floating_point() else t)(torch.as_tensor(a))

    def _A_setup():
        B, Sq, D = (1, 2048, 576)
        Hq, Hkv, Sk, Dh = (32, 16, 2048, 128)
        assert Hq % Hkv == 0, (Hq, Hkv)
        return {'x': _w((lambda *s: torch.randn(*s, dtype=torch.bfloat16) * 0.05)(B, Sq, D)), 'k_cache': _w((lambda *s: torch.randn(*s, dtype=torch.bfloat16) * 3.2)(B, Hkv, Sk, Dh)), 'v_cache': _w((lambda *s: torch.randn(*s, dtype=torch.bfloat16) * 1.6)(B, Hkv, Sk, Dh)), 'wq': _w((lambda *s: torch.randn(*s, dtype=torch.bfloat16) * 3.2)(D, Hq * Dh) * D ** (-0.5)), 'wo': _w((lambda *s: torch.randn(*s, dtype=torch.bfloat16) * 1.6)(Hq * Dh, D) * (Hq * Dh) ** (-0.5))}
    _A_in = _A_setup()

    def _B_setup():
        B, S, D, F = (1, 2048, 576, 4608)
        return {'x': _w((lambda *s: torch.randn(*s, dtype=torch.bfloat16) * 0.05)(B, S, D)), 'w1': _w((lambda *s: torch.randn(*s, dtype=torch.bfloat16) * 0.8)(D, F) * (2.0 / D) ** 0.5), 'w3': _w((lambda *s: torch.randn(*s, dtype=torch.bfloat16) * 0.8)(D, F) * (2.0 / D) ** 0.5), 'w2': _w((lambda *s: torch.randn(*s, dtype=torch.bfloat16) * 0.8)(F, D) * (2.0 / F) ** 0.5)}
    _B_in = _B_setup()
    return {'A_x': _A_in['x'], 'A_k_cache': _A_in['k_cache'], 'A_v_cache': _A_in['v_cache'], 'A_wq': _A_in['wq'], 'A_wo': _A_in['wo'], 'B_x': _B_in['x'], 'B_w1': _B_in['w1'], 'B_w3': _B_in['w3'], 'B_w2': _B_in['w2'], 'c': _w((lambda *s: torch.randn(*s, dtype=torch.bfloat16) * 0.2)(1, 576) * 8.0), 'w_mod': _w((lambda *s: torch.randn(*s, dtype=torch.bfloat16) * 0.2)(576, 6 * 576) * 576 ** (-0.5) * 8.0)}

def reference(A_x, A_k_cache, A_v_cache, A_wq, A_wo, B_x, B_w1, B_w3, B_w2, c, w_mod):

    def _A_ref(x, k_cache, v_cache, wq, wo):
        B, Sq, D = x.shape
        Hq, Dh = (32, 128)
        Hkv = k_cache.shape[1]
        rep = Hq // Hkv
        q = (lambda x, *d: x.permute(*d))((x @ wq).reshape(B, Sq, Hq, Dh), 0, 2, 1, 3)
        k = (lambda x, n, axis: torch.repeat_interleave(x, n, dim=axis))(k_cache, rep, axis=1)
        v = (lambda x, n, axis: torch.repeat_interleave(x, n, dim=axis))(v_cache, rep, axis=1)
        scores = torch.einsum('bhqd,bhkd->bhqk', q, k) * Dh ** (-0.5)
        p = _softmax(scores, axis=-1)
        o = torch.einsum('bhqk,bhkd->bhqd', p, v)
        return (lambda x, *d: x.permute(*d))(o, 0, 2, 1, 3).reshape(B, Sq, Hq * Dh) @ wo

    def _B_ref(x, w1, w3, w2):
        gate = torch.einsum('bsd,df->bsf', x, w1)
        up = torch.einsum('bsd,df->bsf', x, w3)
        h = _silu(gate) * up
        out = torch.einsum('bsf,fd->bsd', h, w2)
        return out
    x = A_x
    mod = (_silu(c) @ w_mod).reshape(c.shape[0], 6, 1, -1)
    sa, ca, ga, sb, cb, gb = (mod[:, 0].clone(), mod[:, 1].clone(), mod[:, 2].clone(), mod[:, 3].clone(), mod[:, 4].clone(), mod[:, 5].clone())
    h = _layernorm(x, axis=-1) * (1 + ca) + sa
    x = x + ga * _A_ref(h, A_k_cache, A_v_cache, A_wq, A_wo)
    h = _layernorm(x, axis=-1) * (1 + cb) + sb
    x = x + gb * _B_ref(h, B_w1, B_w3, B_w2)
    return x
```

```cpp
#include <hip/hip_runtime.h>
#include <hip/hip_bf16.h>
#include <cstdio>
using bf16 = __hip_bfloat16;
typedef unsigned short u16;
using bf16x8 = __attribute__((ext_vector_type(8))) short;
using s16x4  = __attribute__((ext_vector_type(4))) short;
using f32x16 = __attribute__((ext_vector_type(16))) float;
using f32x8  = __attribute__((ext_vector_type(8))) float;
using u32x4  = __attribute__((ext_vector_type(4))) unsigned;
constexpr int S = 2048, DM = 576, HQ = 32, HKV = 16, DH = 128, FF = 4608;
#define DI __device__ __forceinline__
DI float bf2f(u16 x) { return __uint_as_float(((unsigned)x) << 16); }
DI u16 f2bf(float x) { __bf16 b = (__bf16)x; return __builtin_bit_cast(u16, b); }
DI float rbf(float x) { return bf2f(f2bf(x)); }

__global__ void k_mod(const u16* __restrict__ c, const u16* __restrict__ wmod, float* __restrict__ mod) {
  __shared__ float red[4][64];
  __shared__ float sc[DM];
  int tid = threadIdx.x;
  for (int k = tid; k < DM; k += 256) { float v = bf2f(c[k]); sc[k] = rbf(v / (1.f + __expf(-v))); }
  __syncthreads();
  int col = blockIdx.x * 64 + (tid & 63), kq = tid >> 6;
  float acc = 0.f;
  for (int k = kq; k < DM; k += 4) acc += sc[k] * bf2f(wmod[(long)k * 3456 + col]);
  red[kq][tid & 63] = acc;
  __syncthreads();
  if (tid < 64) mod[col] = rbf(red[0][tid] + red[1][tid] + red[2][tid] + red[3][tid]);
}
__global__ void k_ln(const u16* __restrict__ x, const float* __restrict__ sh, const float* __restrict__ sc, u16* __restrict__ h) {
  int row = blockIdx.x * 4 + (threadIdx.x >> 6), lane = threadIdx.x & 63;
  float v[9]; float s = 0.f;
  for (int i = 0; i < 9; ++i) { v[i] = bf2f(x[(long)row * DM + lane + 64 * i]); s += v[i]; }
  for (int o = 32; o; o >>= 1) s += __shfl_xor(s, o);
  float m = s / DM; float q = 0.f;
  for (int i = 0; i < 9; ++i) { float d = v[i] - m; q += d * d; }
  for (int o = 32; o; o >>= 1) q += __shfl_xor(q, o);
  float rs = rsqrtf(q / DM + 1e-5f);
  for (int i = 0; i < 9; ++i) { int cidx = lane + 64 * i; h[(long)row * DM + cidx] = f2bf((v[i] - m) * rs * (1.f + sc[cidx]) + sh[cidx]); }
}
template <int EPI>
__global__ __launch_bounds__(256) void k_gemm(const u16* __restrict__ A, const u16* __restrict__ B, const u16* __restrict__ B2, u16* __restrict__ C,
                                              int M, int N, int K, const u16* __restrict__ res, const float* __restrict__ g) {
  __shared__ __attribute__((aligned(16))) u16 As[64][40];
  __shared__ __attribute__((aligned(16))) u16 Bs[64][40];
  __shared__ __attribute__((aligned(16))) u16 Bs2[64][40];
  int tid = threadIdx.x, wid = tid >> 6, lane = tid & 63, r32 = lane & 31, hi = lane >> 5;
  int m0 = blockIdx.y * 64, n0 = blockIdx.x * 64, wm = (wid >> 1) * 32, wn = (wid & 1) * 32;
  f32x16 acc = {}, acc2 = {};
  for (int k0 = 0; k0 < K; k0 += 32) {
    { int r = tid >> 2, cc = (tid & 3) * 8; *(bf16x8*)&As[r][cc] = *(const bf16x8*)&A[(long)(m0 + r) * K + k0 + cc]; }
    { int kr = tid >> 3, cc = (tid & 7) * 8; bf16x8 v = *(const bf16x8*)&B[(long)(k0 + kr) * N + n0 + cc];
      for (int j = 0; j < 8; ++j) Bs[cc + j][kr] = v[j];
      if (EPI == 2) { bf16x8 v2 = *(const bf16x8*)&B2[(long)(k0 + kr) * N + n0 + cc]; for (int j = 0; j < 8; ++j) Bs2[cc + j][kr] = v2[j]; } }
    __syncthreads();
    for (int ks = 0; ks < 2; ++ks) {
      bf16x8 a = *(const bf16x8*)&As[wm + r32][ks * 16 + hi * 8];
      bf16x8 b = *(const bf16x8*)&Bs[wn + r32][ks * 16 + hi * 8];
      acc = __builtin_amdgcn_mfma_f32_32x32x16_bf16(a, b, acc, 0, 0, 0);
      if (EPI == 2) { bf16x8 b2 = *(const bf16x8*)&Bs2[wn + r32][ks * 16 + hi * 8]; acc2 = __builtin_amdgcn_mfma_f32_32x32x16_bf16(a, b2, acc2, 0, 0, 0); }
    }
    __syncthreads();
  }
  for (int r = 0; r < 16; ++r) {
    int row = m0 + wm + (r & 3) + 8 * (r >> 2) + 4 * hi, col = n0 + wn + r32;
    float v = acc[r];
    if (EPI == 1) v = bf2f(res[(long)row * N + col]) + rbf(g[col] * rbf(v));
    if (EPI == 2) { float gt = rbf(v), up = rbf(acc2[r]); v = rbf(gt / (1.f + __expf(-gt))) * up; }
    C[(long)row * N + col] = f2bf(v);
  }
}

constexpr int D = 128, NW = 8, QBLK = 32, KVBLK = 64;
constexpr float SCALE = 0.088388347648318440f;
constexpr float THR = 8.f;
constexpr int LDQ = HQ * D, LDK = D, LDO = HQ * D;
constexpr size_t SHM_V = KVBLK * D * 2, SHM_K = KVBLK * D * 2, SHM_ATTN = 2 * SHM_V + 2 * SHM_K + NW * 64 * 4;
#define KSWZ(row, colB) ((row) * 256 + ((colB) ^ (((row) & 7) << 4)))
#define SBAR() __builtin_amdgcn_sched_barrier(0)
DI int crow(int r, int hi) { return (r & 3) + 8 * (r >> 2) + 4 * hi; }
DI unsigned cvtpk(float lo, float hi) { unsigned r; asm volatile("v_cvt_pk_bf16_f32 %0, %1, %2" : "=v"(r) : "v"(lo), "v"(hi)); return r; }
DI void partialSM(f32x16& p0, f32x16& p1, float& m_reg, float& mn, float& alpha) {
  constexpr float C = SCALE * 1.4426950408889634f;
  float pmax = p0[0]; for (int r = 1; r < 16; ++r) pmax = fmaxf(pmax, p0[r]); for (int r = 0; r < 16; ++r) pmax = fmaxf(pmax, p1[r]);
  { auto rr = __builtin_amdgcn_permlane32_swap(__float_as_uint(pmax), __float_as_uint(pmax), false, false);
    pmax = fmaxf(__uint_as_float(rr[0]), __uint_as_float(rr[1])); }
  if (__builtin_expect(__all(pmax - m_reg <= THR / SCALE), 1)) { mn = m_reg; alpha = 1.f; }
  else { mn = fmaxf(m_reg, pmax); alpha = __builtin_amdgcn_exp2f((m_reg - mn) * C); m_reg = mn; }
  float mnC = -mn * C;
  for (int r = 0; r < 16; ++r) p0[r] = fmaf(p0[r], C, mnC); for (int r = 0; r < 16; ++r) p1[r] = fmaf(p1[r], C, mnC);
  for (int r = 0; r < 16; ++r) p0[r] = __builtin_amdgcn_exp2f(p0[r]);
}
DI void finishSM(f32x16& p0, f32x16& p1, float alpha, float& l_reg, bf16x8& pa0, bf16x8& pa1, bf16x8& pa2, bf16x8& pa3) {
  for (int r = 0; r < 16; ++r) p1[r] = __builtin_amdgcn_exp2f(p1[r]);
  float ps = 0; for (int r = 0; r < 16; ++r) ps += p0[r]; for (int r = 0; r < 16; ++r) ps += p1[r];
  { auto rr = __builtin_amdgcn_permlane32_swap(__float_as_uint(ps), __float_as_uint(ps), false, false);
    ps = __uint_as_float(rr[0]) + __uint_as_float(rr[1]); }
  l_reg = l_reg * alpha + ps;
#define PK4(P, BASE, OUT) do { unsigned a0 = cvtpk(P[BASE + 0], P[BASE + 1]), a1 = cvtpk(P[BASE + 2], P[BASE + 3]);   \
    unsigned b0 = cvtpk(P[BASE + 4], P[BASE + 5]), b1 = cvtpk(P[BASE + 6], P[BASE + 7]);                              \
    auto r0 = __builtin_amdgcn_permlane32_swap(a0, b0, false, false); auto r1 = __builtin_amdgcn_permlane32_swap(a1, b1, false, false); \
    u32x4 w = {r0[0], r1[0], r0[1], r1[1]}; OUT = *reinterpret_cast<bf16x8*>(&w); } while (0)
  PK4(p0, 0, pa0); PK4(p0, 8, pa1); PK4(p1, 0, pa2); PK4(p1, 8, pa3);
#undef PK4
}
DI void qkt(f32x16& p0, f32x16& p1, const u16* Ks, const bf16x8* qr, int r32, int hi) {
  p0 = f32x16{}; p1 = f32x16{};
  for (int d0 = 0; d0 < 8; ++d0) { int cb = (d0 * 16 + hi * 8) * 2;
    bf16x8 b0 = *reinterpret_cast<const bf16x8*>((const char*)Ks + KSWZ(r32, cb));
    bf16x8 b1 = *reinterpret_cast<const bf16x8*>((const char*)Ks + KSWZ(32 + r32, cb));
    p0 = __builtin_amdgcn_mfma_f32_32x32x16_bf16(b0, qr[d0], p0, 0, 0, 0);
    p1 = __builtin_amdgcn_mfma_f32_32x32x16_bf16(b1, qr[d0], p1, 0, 0, 0); }
}
DI int v_st(int k, int c) { const int kk = (k & ~0xC) | ((k & 4) << 1) | ((k & 8) >> 1); return ((kk >> 3) * 4 + (c >> 5)) * 512 + ((kk & 7) * 32 + (c & 31)) * 2; }
DI int v_rd_base(int lane) { return ((lane & 3) << 3) | (((lane >> 2) & 3) << 6) | (((lane >> 4) & 1) << 5) | (((lane >> 5) & 1) << 8); }
constexpr int v_rd_off(int d0, int ks, int half) { return d0 * 512 + ks * 4096 + half * 2048; }
template <int OFF> DI s16x4 tr_read(int vb) {
  s16x4 r; asm volatile("ds_read_b64_tr_b16 %0, %1 offset:%2" : "=&v"(r) : "v"(vb), "i"(OFF) : "memory"); return r;
}
template <int D0> DI void pv_one(f32x16& od, int vb, bf16x8 pa0, bf16x8 pa1, bf16x8 pa2, bf16x8 pa3) {
  const s16x4 l0 = tr_read<v_rd_off(D0, 0, 0)>(vb), h0 = tr_read<v_rd_off(D0, 0, 1)>(vb), l1 = tr_read<v_rd_off(D0, 1, 0)>(vb), h1 = tr_read<v_rd_off(D0, 1, 1)>(vb);
  const s16x4 l2 = tr_read<v_rd_off(D0, 2, 0)>(vb), h2 = tr_read<v_rd_off(D0, 2, 1)>(vb), l3 = tr_read<v_rd_off(D0, 3, 0)>(vb), h3 = tr_read<v_rd_off(D0, 3, 1)>(vb);
  asm volatile("s_waitcnt lgkmcnt(0)" ::: "memory"); SBAR();
#define PK(L, H) (bf16x8){L[0], L[1], L[2], L[3], H[0], H[1], H[2], H[3]}
  od = __builtin_amdgcn_mfma_f32_32x32x16_bf16(pa0, PK(l0, h0), od, 0, 0, 0);
  od = __builtin_amdgcn_mfma_f32_32x32x16_bf16(pa1, PK(l1, h1), od, 0, 0, 0);
  od = __builtin_amdgcn_mfma_f32_32x32x16_bf16(pa2, PK(l2, h2), od, 0, 0, 0);
  od = __builtin_amdgcn_mfma_f32_32x32x16_bf16(pa3, PK(l3, h3), od, 0, 0, 0);
#undef PK
}
DI void pv_d0(f32x16* o, int vb, bf16x8 pa0, bf16x8 pa1, bf16x8 pa2, bf16x8 pa3) {
  pv_one<0>(o[0], vb, pa0, pa1, pa2, pa3); pv_one<1>(o[1], vb, pa0, pa1, pa2, pa3); pv_one<2>(o[2], vb, pa0, pa1, pa2, pa3); pv_one<3>(o[3], vb, pa0, pa1, pa2, pa3);
}
__global__ __launch_bounds__(64 * NW, 1)
void k_attn(const u16* __restrict__ Q, const u16* __restrict__ K, const u16* __restrict__ V, u16* __restrict__ O) {
  extern __shared__ __attribute__((aligned(16))) char lds[];
  const long h = blockIdx.y, kvh = h / (HQ / HKV), i0 = (long)blockIdx.x * (QBLK * NW);
  const u16* Qb = Q + i0 * LDQ + h * D; const u16* Kh = K + kvh * S * D; const u16* Vh = V + kvh * S * D; u16* Ob = O + i0 * LDO + h * D;
  const int seq = S;
  const int tid = threadIdx.x, wid = tid >> 6, lane = tid & 63, r32 = lane & 31, hi = lane >> 5;
  u16* V_lds = (u16*)lds; u16* K_lds = (u16*)(lds + 2 * SHM_V);
  float* ws = (float*)(lds + 2 * SHM_V + 2 * SHM_K) + wid * 64; float* li_l = ws; float* al_l = ws + 32;
  float m_reg = -1e30f, l_reg = 0; f32x16 o[4] = {}; bf16x8 qr[8];
  const u16* Qw = Qb + (long)(wid * QBLK + r32) * LDQ + hi * 8;
#pragma unroll
  for (int d0 = 0; d0 < 8; ++d0) qr[d0] = *(const bf16x8*)(Qw + d0 * 16);
  const int sr = tid >> 4, sc = (tid & 15) * 8, vst0 = v_st(sr, sc), vst1 = v_st(32 + sr, sc);
  const int vb0 = (int)(uintptr_t)V_lds + v_rd_base(lane);
  struct { bf16x8 vs0, vs1, ks0, ks1; } sr_[2];
#define LD8(p) (*(const bf16x8*)(p))
#define SLOAD(i, k0) do { sr_[i].vs0 = LD8(&Vh[(long)((k0) + sr) * LDK + sc]); sr_[i].vs1 = LD8(&Vh[(long)((k0) + 32 + sr) * LDK + sc]); \
    sr_[i].ks0 = LD8(&Kh[(long)((k0) + sr) * LDK + sc]); sr_[i].ks1 = LD8(&Kh[(long)((k0) + 32 + sr) * LDK + sc]); } while (0)
#define SWRITE(b, i) do { *(bf16x8*)((char*)V_lds + (b) * SHM_V + vst0) = sr_[i].vs0;          \
    *(bf16x8*)((char*)V_lds + (b) * SHM_V + vst1) = sr_[i].vs1; int kc = sc * 2;               \
    *(bf16x8*)((char*)K_lds + (b) * SHM_K + KSWZ(sr, kc)) = sr_[i].ks0;                       \
    *(bf16x8*)((char*)K_lds + (b) * SHM_K + KSWZ(32 + sr, kc)) = sr_[i].ks1; } while (0)
#define SWAIT() asm volatile("s_waitcnt vmcnt(4)" ::: "memory")
#define RESC(a) do { if (__any((a) < 1.f)) { if (hi == 0) al_l[r32] = (a); asm volatile("s_waitcnt lgkmcnt(0)" ::: "memory"); \
    for (int d = 0; d < 4; ++d) for (int r = 0; r < 16; ++r) o[d][r] *= al_l[crow(r, hi)]; } } while (0)
  f32x16 pA0, pA1, pB0, pB1; float mnA, mnB, alA, alB; bf16x8 pa0, pa1, pa2, pa3; const int NT = seq / KVBLK;
  constexpr int SE = 0, SO = 1, SDEPTH = 2;
  SLOAD(SE, 0); asm volatile("s_waitcnt vmcnt(0)" ::: "memory"); SWRITE(0, SE); __syncthreads();
  qkt(pA0, pA1, K_lds, qr, r32, hi); partialSM(pA0, pA1, m_reg, mnA, alA);
  SLOAD(SO, KVBLK); SLOAD(SE, 2 * KVBLK);
  SWAIT(); SWRITE(1, SO); __syncthreads();
  for (int j = 1; j + 1 < NT; j += 2) {
    SBAR(); qkt(pB0, pB1, (u16*)((char*)K_lds + SHM_K), qr, r32, hi);
    finishSM(pA0, pA1, alA, l_reg, pa0, pa1, pa2, pa3); SBAR();
    SLOAD(SO, (j + SDEPTH) * KVBLK); SBAR();
    pv_d0(o, vb0, pa0, pa1, pa2, pa3); partialSM(pB0, pB1, m_reg, mnB, alB);
    __syncthreads(); SWAIT(); SWRITE(0, SE);
    RESC(alB); __syncthreads();
    SBAR(); qkt(pA0, pA1, K_lds, qr, r32, hi);
    finishSM(pB0, pB1, alB, l_reg, pa0, pa1, pa2, pa3); SBAR();
    if (j + 3 < NT) SLOAD(SE, (j + 1 + SDEPTH) * KVBLK); SBAR();
    pv_d0(o, vb0 + (int)SHM_V, pa0, pa1, pa2, pa3); partialSM(pA0, pA1, m_reg, mnA, alA);
    __syncthreads(); SWAIT(); SWRITE(1, SO);
    RESC(alA); __syncthreads();
  }
  SBAR(); qkt(pB0, pB1, (u16*)((char*)K_lds + SHM_K), qr, r32, hi);
  finishSM(pA0, pA1, alA, l_reg, pa0, pa1, pa2, pa3); SBAR();
  pv_d0(o, vb0, pa0, pa1, pa2, pa3); partialSM(pB0, pB1, m_reg, mnB, alB);
  __syncthreads(); RESC(alB);
  finishSM(pB0, pB1, alB, l_reg, pa0, pa1, pa2, pa3); SBAR();
  pv_d0(o, vb0 + (int)SHM_V, pa0, pa1, pa2, pa3);
  if (hi == 0) li_l[r32] = l_reg; asm volatile("s_waitcnt lgkmcnt(0)" ::: "memory");
  float rli[16];
#pragma unroll
  for (int r = 0; r < 16; ++r) rli[r] = __builtin_amdgcn_rcpf(li_l[crow(r, hi)]);
  u16* Ow = Ob + (long)(wid * QBLK) * LDO;
#pragma unroll
  for (int r = 0; r < 16; ++r) { int orow = crow(r, hi);
    for (int d0 = 0; d0 < 4; ++d0) Ow[(long)orow * LDO + d0 * 32 + r32] = f2bf(o[d0][r] * rli[r]); }
}

extern "C" void kernel_launch(void* const* d_in, const int* in_sizes, int n_in, void* d_out, int out_size, void* d_ws, size_t ws_size, hipStream_t stream) {
  const u16* x = (const u16*)d_in[0]; const u16* kc = (const u16*)d_in[1]; const u16* vc = (const u16*)d_in[2];
  const u16* wq = (const u16*)d_in[3]; const u16* wo = (const u16*)d_in[4];
  const u16* w1 = (const u16*)d_in[6]; const u16* w3 = (const u16*)d_in[7]; const u16* w2 = (const u16*)d_in[8];
  const u16* c = (const u16*)d_in[9]; const u16* wmod = (const u16*)d_in[10];
  char* ws = (char*)d_ws;
  float* mod = (float*)ws;
  u16* h1 = (u16*)(ws + (1 << 20));
  u16* q  = (u16*)(ws + (4 << 20));
  u16* o  = (u16*)(ws + (24 << 20));
  u16* x1 = (u16*)(ws + (44 << 20));
  u16* h2 = (u16*)(ws + (48 << 20));
  u16* hh = (u16*)(ws + (52 << 20));
  hipFuncSetAttribute((const void*)k_attn, hipFuncAttributeMaxDynamicSharedMemorySize, (int)SHM_ATTN);
  k_mod<<<54, 256, 0, stream>>>(c, wmod, mod);
  k_ln<<<S / 4, 256, 0, stream>>>(x, mod + 0 * DM, mod + 1 * DM, h1);
  k_gemm<0><<<dim3(4096 / 64, S / 64), 256, 0, stream>>>(h1, wq, nullptr, q, S, 4096, DM, nullptr, nullptr);
  k_attn<<<dim3(S / 256, HQ), 512, SHM_ATTN, stream>>>(q, kc, vc, o);
  k_gemm<1><<<dim3(DM / 64, S / 64), 256, 0, stream>>>(o, wo, nullptr, x1, S, DM, 4096, x, mod + 2 * DM);
  k_ln<<<S / 4, 256, 0, stream>>>(x1, mod + 3 * DM, mod + 4 * DM, h2);
  k_gemm<2><<<dim3(FF / 64, S / 64), 256, 0, stream>>>(h2, w1, w3, hh, S, FF, DM, nullptr, nullptr);
  k_gemm<1><<<dim3(DM / 64, S / 64), 256, 0, stream>>>(hh, w2, nullptr, (u16*)d_out, S, DM, FF, x1, mod + 5 * DM);
}
```

```cpp
#include <hip/hip_runtime.h>
typedef unsigned short u16;
__global__ __launch_bounds__(256, 1) void k_block(char* ws, const u16* x, const u16* K, const u16* V, const u16* wq, const u16* wo, const u16* w1, const u16* w3,
                                                  const u16* w2, const u16* c, const u16* wmod, u16* out) {
  unsigned wid = __builtin_amdgcn_readfirstlane(threadIdx.x >> 6);
  asm volatile(
    "s_load_dwordx16 s[64:79], %0, 0x0\n"
    "s_load_dwordx8 s[80:87], %0, 0x40\n"
    "s_getpc_b64 s[60:61]\n"
    "v_mbcnt_lo_u32_b32 v234, -1, 0\n"
    "v_mbcnt_hi_u32_b32 v234, -1, v234\n"
    "v_lshlrev_b32_e32 v190, 7, v234\n"
    "global_load_dword v190, v190, s[60:61]\n"
    "s_mov_b32 s88, %1\n"
    "s_mov_b32 s89, %2\n"
    "v_lshlrev_b32_e32 v232, 4, v234\n"
    "s_and_b32 s40, s89, 7\n"
    "s_bfe_u32 s41, s89, 0x20003\n"
    "s_lshr_b32 s58, s89, 5\n"
    "s_lshl_b32 s57, s40, 2\n"
    "s_add_u32 s57, s57, s41\n"
    "s_lshl_b32 s36, s58, 5\n"
    "s_add_u32 s36, s36, s57\n"
    "s_lshl_b32 s43, s41, 2\n"
    "s_and_b32 s43, s43, 15\n"
    "s_add_u32 s43, s43, s88\n"
    "v_lshrrev_b32_e32 v184, 1, v234\n"
    "v_mul_u32_u24_e32 v184, 6912, v184\n"
    "v_and_b32_e32 v185, 16, v232\n"
    "v_add_u32_e32 v184, v184, v185\n"
    "s_lshl_b32 s42, s88, 9\n"
    "v_lshl_add_u32 v185, v234, 3, s42\n"
    "s_cmp_lt_u32 s36, 216\n"
    "s_cselect_b32 s22, 3974432, 0\n"
    "s_mov_b32 s23, 0x20000\n"
    "s_mov_b32 s26, 1152\n"
    "s_mov_b32 s27, 0x20000\n"
    "v_mov_b32_e32 v136, v232\n"
    "v_add_u32_e32 v137, 1024, v232\n"
    "v_add_u32_e32 v138, 2048, v232\n"
    "v_mov_b32_e32 v120, 0x7f000000\n"
    "v_cmp_gt_u32_e32 vcc, 256, v232\n"
    "v_lshrrev_b32_e32 v139, 1, v136\n"
    "v_lshrrev_b32_e32 v140, 1, v137\n"
    "v_cndmask_b32_e32 v138, v120, v138, vcc\n"
    "v_lshrrev_b32_e32 v141, 1, v138\n"
    "s_waitcnt lgkmcnt(0)\n"
    "s_load_dwordx2 s[90:91], s[64:65], 233472\n"
    "s_mov_b32 s24, s82\n"
    "s_and_b32 s25, s83, 0xffff\n"
    "buffer_load_dwordx2 v[186:187], v185, s[24:27], 0 offen\n"
    "s_lshl_b32 s42, s89, 2\n"
    "s_add_u32 s42, s42, s88\n"
    "s_and_b32 s40, s42, 15\n"
    "s_lshr_b32 s41, s42, 4\n"
    "s_lshl_b32 s41, s41, 5\n"
    "s_add_u32 s40, s40, s41\n"
    "s_mul_i32 s40, s40, 1152\n"
    "s_add_u32 s28, s66, s40\n"
    "s_addc_u32 s29, s67, 0\n"
    "s_and_b32 s29, s29, 0xffff\n"
    "s_mov_b32 s30, 19584\n"
    "s_mov_b32 s31, 0x20000\n"
    "s_mov_b32 s37, 18432\n"
    "buffer_load_dwordx2 v[84:85], v139, s[28:31], 0 offen\n"
    "buffer_load_dwordx2 v[86:87], v139, s[28:31], s37 offen\n"
    "buffer_load_dwordx2 v[88:89], v140, s[28:31], 0 offen\n"
    "buffer_load_dwordx2 v[90:91], v140, s[28:31], s37 offen\n"
    "buffer_load_dwordx2 v[92:93], v141, s[28:31], 0 offen\n"
    "buffer_load_dwordx2 v[94:95], v141, s[28:31], s37 offen\n"
    "s_lshl_b32 s40, s36, 5\n"
    "s_add_u32 s20, s84, s40\n"
    "s_addc_u32 s21, s85, 0\n"
    "s_and_b32 s21, s21, 0xffff\n"
    "s_lshl_b32 s40, s43, 10\n"
    "s_add_u32 m0, s40, 123904\n"
    "s_mul_i32 s40, s43, 221184\n"
    "buffer_load_dwordx4 v184, s[20:23], s40 offen lds\n"
    "s_add_u32 s43, s43, 4\n"
    "s_cmp_ge_u32 s43, 20\n"
    "s_cselect_b32 s40, 20, 0\n"
    "s_sub_u32 s43, s43, s40\n"
    "s_lshl_b32 s40, s43, 10\n"
    "s_add_u32 m0, s40, 123904\n"
    "s_mul_i32 s40, s43, 221184\n"
    "buffer_load_dwordx4 v184, s[20:23], s40 offen lds\n"
    "s_add_u32 s43, s43, 4\n"
    "s_cmp_ge_u32 s43, 20\n"
    "s_cselect_b32 s40, 20, 0\n"
    "s_sub_u32 s43, s43, s40\n"
    "s_lshl_b32 s40, s43, 10\n"
    "s_add_u32 m0, s40, 123904\n"
    "s_mul_i32 s40, s43, 221184\n"
    "buffer_load_dwordx4 v184, s[20:23], s40 offen lds\n"
    "s_add_u32 s43, s43, 4\n"
    "s_cmp_ge_u32 s43, 20\n"
    "s_cselect_b32 s40, 20, 0\n"
    "s_sub_u32 s43, s43, s40\n"
    "s_lshl_b32 s40, s43, 10\n"
    "s_add_u32 m0, s40, 123904\n"
    "s_mul_i32 s40, s43, 221184\n"
    "buffer_load_dwordx4 v184, s[20:23], s40 offen lds\n"
    "s_add_u32 s43, s43, 4\n"
    "s_cmp_ge_u32 s43, 20\n"
    "s_cselect_b32 s40, 20, 0\n"
    "s_sub_u32 s43, s43, s40\n"
    "s_lshl_b32 s40, s43, 10\n"
    "s_add_u32 m0, s40, 123904\n"
    "s_mul_i32 s40, s43, 221184\n"
    "buffer_load_dwordx4 v184, s[20:23], s40 offen lds\n"
    "v_bfe_u32 v235, v234, 2, 2\n"
    "v_lshlrev_b32_e32 v236, 8, v235\n"
    "v_lshrrev_b32_e32 v237, 4, v234\n"
    "v_lshl_add_u32 v236, v237, 6, v236\n"
    "v_and_b32_e32 v224, 3, v234\n"
    "v_lshl_add_u32 v231, v224, 4, v236\n"
    "v_xor_b32_e32 v233, 32, v231\n"
    "v_mul_u32_u24_e32 v245, 7936, v235\n"
    "v_add_u32_e32 v245, v245, v231\n"
    "s_lshl_b32 s39, s88, 12\n"
    "s_add_u32 s96, s64, 0\n"
    "s_addc_u32 s97, s65, 0\n"
    "s_and_b32 s97, s97, 0xffff\n"
    "s_mov_b32 s98, 1048576\n"
    "s_mov_b32 s99, 0x20000\n"
    "s_lshr_b32 s40, s89, 2\n"
    "s_mul_i32 s40, s40, 36864\n"
    "s_and_b32 s41, s89, 3\n"
    "s_lshl_b32 s41, s41, 6\n"
    "s_add_u32 s40, s40, s41\n"
    "s_add_u32 s40, s40, 1048576\n"
    "s_add_u32 s32, s64, s40\n"
    "s_addc_u32 s33, s65, 0\n"
    "s_and_b32 s33, s33, 0xffff\n"
    "s_mov_b32 s34, 36672\n"
    "s_mov_b32 s35, 0x20000\n"
    "v_lshrrev_b32_e32 v142, 7, v232\n"
    "v_lshlrev_b32_e32 v142, 8, v142\n"
    "v_bfe_u32 v128, v232, 4, 2\n"
    "v_lshl_add_u32 v142, v128, 6, v142\n"
    "v_bfe_u32 v128, v232, 6, 1\n"
    "v_lshl_add_u32 v142, v128, 3, v142\n"
    "s_lshl_b32 s40, s88, 4\n"
    "s_add_u32 s40, s40, 99328\n"
    "v_add_u32_e32 v142, s40, v142\n"
    "s_lshl_b32 s40, s88, 10\n"
    "s_add_u32 s40, s40, 99328\n"
    "v_add_u32_e32 v143, s40, v232\n"
    "v_lshrrev_b32_e32 v144, 6, v232\n"
    "v_lshlrev_b32_e32 v144, 8, v144\n"
    "v_and_b32_e32 v128, 48, v232\n"
    "v_add_u32_e32 v144, v144, v128\n"
    "s_lshl_b32 s40, s88, 12\n"
    "v_add_u32_e32 v144, s40, v144\n"
    "s_lshl_b32 s40, s57, 8\n"
    "s_add_u32 s48, s72, s40\n"
    "s_addc_u32 s49, s73, 0\n"
    "s_and_b32 s49, s49, 0xffff\n"
    "s_mov_b32 s50, 4710656\n"
    "s_mov_b32 s51, 0x20000\n"
    "s_sub_u32 s40, s88, 1\n"
    "s_mul_i32 s93, s40, 8256\n"
    "s_mul_i32 s94, s40, 262144\n"
    "v_lshrrev_b32_e32 v188, 6, v185\n"
    "v_lshlrev_b32_e32 v188, 6, v188\n"
    "v_and_b32_e32 v189, 48, v232\n"
    "v_add_u32_e32 v188, v188, v189\n"
    "v_bfe_u32 v189, v232, 6, 1\n"
    "v_lshl_add_u32 v188, v189, 3, v188\n"
    "v_add_u32_e32 v188, 144384, v188\n"
    "v_add_u32_e32 v202, 123904, v139\n"
    "v_lshlrev_b32_e32 v203, 4, v237\n"
    "v_add_u32_e32 v203, 144384, v203\n"
    "v_mul_u32_u24_e32 v236, 1032, v237\n"
    "v_lshl_add_u32 v236, v235, 6, v236\n"
    "v_lshl_add_u32 v246, v224, 4, v236\n"
    "v_add_u32_e32 v247, 0x10000, v246\n"
    "v_add_u32_e32 v248, 0x20000, v246\n"
    "v_lshlrev_b32_e32 v236, 10, v235\n"
    "v_lshl_add_u32 v236, v224, 6, v236\n"
    "v_and_b32_e32 v228, 2, v235\n"
    "v_xor_b32_e32 v228, v237, v228\n"
    "v_lshl_add_u32 v228, v228, 4, v236\n"
    "v_lshlrev_b32_e32 v236, 10, v237\n"
    "v_lshl_add_u32 v236, v235, 6, v236\n"
    "v_lshl_add_u32 v236, v224, 3, v236\n"
    "v_and_b32_e32 v229, 1, v237\n"
    "v_lshl_add_u32 v229, v229, 5, v236\n"
    "v_xor_b32_e32 v230, 32, v229\n"
    "v_lshrrev_b32_e32 v236, 2, v234\n"
    "v_lshlrev_b32_e32 v236, 7, v236\n"
    "v_lshl_add_u32 v236, v224, 3, v236\n"
    "v_bfe_u32 v253, v234, 3, 2\n"
    "v_lshl_add_u32 v236, v253, 5, v236\n"
    "s_mul_i32 s40, s88, 6144\n"
    "s_add_u32 s40, s40, 131072\n"
    "v_add_u32_e32 v253, s40, v236\n"
    "s_mov_b64 exec, 0\n"
    "s_cmp_eq_u32 s88, 3\n"
    "s_cbranch_scc1 L_chain0x3_mk\n"
    "s_cmp_eq_u32 s88, 2\n"
    "s_cbranch_scc1 L_chain0x2_mk\n"
    "s_cmp_eq_u32 s88, 1\n"
    "s_cbranch_scc1 L_chain0x1_mk\n"
    "s_cmp_eq_u32 s88, 0\n"
    "s_cbranch_scc1 L_chain0x0_mk\n"
    "L_ret0_mk:\n"
    "s_mov_b64 exec, -1\n"
    "L_chain0x3_mk:\n"
    "s_cbranch_execz L_pad0_4_mk\n"
    "s_waitcnt vmcnt(11)\n"
    "v_lshlrev_b32_e32 v192, 16, v186\n"
    "v_and_b32_e32 v193, 0xffff0000, v186\n"
    "v_lshlrev_b32_e32 v194, 16, v187\n"
    "v_and_b32_e32 v195, 0xffff0000, v187\n"
    "v_mul_f32_e32 v196, 0xbfb8aa3b, v192\n"
    "v_mul_f32_e32 v197, 0xbfb8aa3b, v193\n"
    "v_mul_f32_e32 v198, 0xbfb8aa3b, v194\n"
    "v_mul_f32_e32 v199, 0xbfb8aa3b, v195\n"
    "v_exp_f32_e32 v196, v196\n"
    "v_exp_f32_e32 v197, v197\n"
    "v_exp_f32_e32 v198, v198\n"
    "v_exp_f32_e32 v199, v199\n"
    "v_add_f32_e32 v196, 1.0, v196\n"
    "v_add_f32_e32 v197, 1.0, v197\n"
    "v_add_f32_e32 v198, 1.0, v198\n"
    "v_add_f32_e32 v199, 1.0, v199\n"
    "v_rcp_f32_e32 v196, v196\n"
    "v_rcp_f32_e32 v197, v197\n"
    "v_rcp_f32_e32 v198, v198\n"
    "v_rcp_f32_e32 v199, v199\n"
    "v_mul_f32_e32 v192, v192, v196\n"
    "v_mul_f32_e32 v193, v193, v197\n"
    "v_mul_f32_e32 v194, v194, v198\n"
    "v_mul_f32_e32 v195, v195, v199\n"
    "v_cvt_pk_bf16_f32 v192, v192, v193\n"
    "v_cvt_pk_bf16_f32 v193, v194, v195\n"
    "ds_write_b64 v188, v[192:193]\n"
    "s_waitcnt vmcnt(5)\n"
    "s_and_b32 s40, s89, 7\n"
    "s_lshl_b32 s40, s40, 10\n"
    "s_add_u32 s24, s72, s40\n"
    "s_addc_u32 s25, s73, 0\n"
    "s_and_b32 s25, s25, 0xffff\n"
    "s_mov_b32 s26, 4711424\n"
    "s_mov_b32 s27, 0x20000\n"
    "s_xor_b32 s40, s88, 3\n"
    "s_lshl_b32 s40, s40, 5\n"
    "s_lshr_b32 s41, s89, 3\n"
    "s_add_u32 s40, s40, s41\n"
    "s_lshl_b32 s40, s40, 16\n"
    "v_lshrrev_b32_e32 v201, 3, v234\n"
    "v_lshlrev_b32_e32 v201, 13, v201\n"
    "v_and_b32_e32 v204, 7, v234\n"
    "v_lshl_add_u32 v201, v204, 7, v201\n"
    "buffer_load_dword v201, v201, s[24:27], s40 offen\n"
    "v_lshlrev_b32_e32 v96, 16, v84\n"
    "v_and_b32_e32 v97, 0xffff0000, v84\n"
    "v_lshlrev_b32_e32 v98, 16, v85\n"
    "v_and_b32_e32 v99, 0xffff0000, v85\n"
    "v_lshlrev_b32_e32 v100, 16, v86\n"
    "v_and_b32_e32 v101, 0xffff0000, v86\n"
    "v_lshlrev_b32_e32 v102, 16, v87\n"
    "v_and_b32_e32 v103, 0xffff0000, v87\n"
    "v_lshlrev_b32_e32 v104, 16, v88\n"
    "v_and_b32_e32 v105, 0xffff0000, v88\n"
    "v_lshlrev_b32_e32 v106, 16, v89\n"
    "v_and_b32_e32 v107, 0xffff0000, v89\n"
    "v_lshlrev_b32_e32 v108, 16, v90\n"
    "v_and_b32_e32 v109, 0xffff0000, v90\n"
    "v_lshlrev_b32_e32 v110, 16, v91\n"
    "v_and_b32_e32 v111, 0xffff0000, v91\n"
    "v_lshlrev_b32_e32 v112, 16, v92\n"
    "v_and_b32_e32 v113, 0xffff0000, v92\n"
    "v_lshlrev_b32_e32 v114, 16, v93\n"
    "v_and_b32_e32 v115, 0xffff0000, v93\n"
    "v_lshlrev_b32_e32 v116, 16, v94\n"
    "v_and_b32_e32 v117, 0xffff0000, v94\n"
    "v_lshlrev_b32_e32 v118, 16, v95\n"
    "v_and_b32_e32 v119, 0xffff0000, v95\n"
    "v_pk_add_f32 v[128:129], v[96:97], v[98:99]\n"
    "v_pk_add_f32 v[130:131], v[100:101], v[102:103]\n"
    "v_pk_add_f32 v[128:129], v[128:129], v[104:105]\n"
    "v_pk_add_f32 v[130:131], v[130:131], v[108:109]\n"
    "v_pk_add_f32 v[128:129], v[128:129], v[106:107]\n"
    "v_pk_add_f32 v[130:131], v[130:131], v[110:111]\n"
    "v_pk_add_f32 v[128:129], v[128:129], v[112:113]\n"
    "v_pk_add_f32 v[130:131], v[130:131], v[116:117]\n"
    "v_pk_add_f32 v[128:129], v[128:129], v[114:115]\n"
    "v_pk_add_f32 v[130:131], v[130:131], v[118:119]\n"
    "v_add_f32_e32 v120, v128, v129\n"
    "v_add_f32_e32 v122, v130, v131\n"
    "s_nop 0\n"
    "s_nop 0\n"
    "v_add_f32_dpp v120, v120, v120 quad_perm:[1,0,3,2] row_mask:0xf bank_mask:0xf\n"
    "v_add_f32_dpp v122, v122, v122 quad_perm:[1,0,3,2] row_mask:0xf bank_mask:0xf\n"
    "s_nop 0\n"
    "v_add_f32_dpp v120, v120, v120 quad_perm:[2,3,0,1] row_mask:0xf bank_mask:0xf\n"
    "L_chain0x2_mk:\n"
    "s_cbranch_execz L_pad0_5_mk\n"
    "v_add_f32_dpp v122, v122, v122 quad_perm:[2,3,0,1] row_mask:0xf bank_mask:0xf\n"
    "s_nop 0\n"
    "v_add_f32_dpp v120, v120, v120 row_half_mirror row_mask:0xf bank_mask:0xf\n"
    "v_add_f32_dpp v122, v122, v122 row_half_mirror row_mask:0xf bank_mask:0xf\n"
    "s_nop 0\n"
    "v_add_f32_dpp v120, v120, v120 row_mirror row_mask:0xf bank_mask:0xf\n"
    "v_add_f32_dpp v122, v122, v122 row_mirror row_mask:0xf bank_mask:0xf\n"
    "v_mov_b32_e32 v124, v120\n"
    "v_mov_b32_e32 v125, v122\n"
    "s_nop 0\n"
    "v_permlane16_swap_b32_e32 v120, v124\n"
    "v_permlane16_swap_b32_e32 v122, v125\n"
    "v_add_f32_e32 v120, v120, v124\n"
    "v_add_f32_e32 v122, v122, v125\n"
    "v_mov_b32_e32 v124, v120\n"
    "v_mov_b32_e32 v125, v122\n"
    "s_nop 0\n"
    "v_permlane32_swap_b32_e32 v120, v124\n"
    "v_permlane32_swap_b32_e32 v122, v125\n"
    "v_add_f32_e32 v120, v120, v124\n"
    "v_add_f32_e32 v122, v122, v125\n"
    "v_mul_f32_e32 v120, 0xbae38e39, v120\n"
    "v_mul_f32_e32 v122, 0xbae38e39, v122\n"
    "v_pk_add_f32 v[96:97], v[96:97], v[120:121] op_sel_hi:[1,0]\n"
    "v_pk_add_f32 v[100:101], v[100:101], v[122:123] op_sel_hi:[1,0]\n"
    "v_pk_add_f32 v[98:99], v[98:99], v[120:121] op_sel_hi:[1,0]\n"
    "v_pk_add_f32 v[102:103], v[102:103], v[122:123] op_sel_hi:[1,0]\n"
    "v_pk_add_f32 v[104:105], v[104:105], v[120:121] op_sel_hi:[1,0]\n"
    "v_pk_add_f32 v[108:109], v[108:109], v[122:123] op_sel_hi:[1,0]\n"
    "v_pk_add_f32 v[106:107], v[106:107], v[120:121] op_sel_hi:[1,0]\n"
    "v_pk_add_f32 v[110:111], v[110:111], v[122:123] op_sel_hi:[1,0]\n"
    "s_mov_b64 exec, 0xffff\n"
    "v_pk_add_f32 v[112:113], v[112:113], v[120:121] op_sel_hi:[1,0]\n"
    "v_pk_add_f32 v[116:117], v[116:117], v[122:123] op_sel_hi:[1,0]\n"
    "v_pk_add_f32 v[114:115], v[114:115], v[120:121] op_sel_hi:[1,0]\n"
    "v_pk_add_f32 v[118:119], v[118:119], v[122:123] op_sel_hi:[1,0]\n"
    "s_mov_b64 exec, -1\n"
    "v_pk_mul_f32 v[128:129], v[96:97], v[96:97]\n"
    "v_pk_mul_f32 v[130:131], v[100:101], v[100:101]\n"
    "v_pk_fma_f32 v[128:129], v[98:99], v[98:99], v[128:129]\n"
    "v_pk_fma_f32 v[130:131], v[102:103], v[102:103], v[130:131]\n"
    "v_pk_fma_f32 v[128:129], v[104:105], v[104:105], v[128:129]\n"
    "v_pk_fma_f32 v[130:131], v[108:109], v[108:109], v[130:131]\n"
    "v_pk_fma_f32 v[128:129], v[106:107], v[106:107], v[128:129]\n"
    "v_pk_fma_f32 v[130:131], v[110:111], v[110:111], v[130:131]\n"
    "v_pk_fma_f32 v[128:129], v[112:113], v[112:113], v[128:129]\n"
    "v_pk_fma_f32 v[130:131], v[116:117], v[116:117], v[130:131]\n"
    "v_pk_fma_f32 v[128:129], v[114:115], v[114:115], v[128:129]\n"
    "v_pk_fma_f32 v[130:131], v[118:119], v[118:119], v[130:131]\n"
    "v_add_f32_e32 v120, v128, v129\n"
    "v_add_f32_e32 v122, v130, v131\n"
    "v_mov_b32_e32 v128, 0x3ae38e39\n"
    "s_nop 0\n"
    "v_add_f32_dpp v120, v120, v120 quad_perm:[1,0,3,2] row_mask:0xf bank_mask:0xf\n"
    "v_add_f32_dpp v122, v122, v122 quad_perm:[1,0,3,2] row_mask:0xf bank_mask:0xf\n"
    "s_nop 0\n"
    "v_add_f32_dpp v120, v120, v120 quad_perm:[2,3,0,1] row_mask:0xf bank_mask:0xf\n"
    "v_add_f32_dpp v122, v122, v122 quad_perm:[2,3,0,1] row_mask:0xf bank_mask:0xf\n"
    "s_nop 0\n"
    "v_add_f32_dpp v120, v120, v120 row_half_mirror row_mask:0xf bank_mask:0xf\n"
    "v_add_f32_dpp v122, v122, v122 row_half_mirror row_mask:0xf bank_mask:0xf\n"
    "s_nop 0\n"
    "v_add_f32_dpp v120, v120, v120 row_mirror row_mask:0xf bank_mask:0xf\n"
    "v_add_f32_dpp v122, v122, v122 row_mirror row_mask:0xf bank_mask:0xf\n"
    "v_mov_b32_e32 v124, v120\n"
    "v_mov_b32_e32 v125, v122\n"
    "s_nop 0\n"
    "v_permlane16_swap_b32_e32 v120, v124\n"
    "v_permlane16_swap_b32_e32 v122, v125\n"
    "v_add_f32_e32 v120, v120, v124\n"
    "v_add_f32_e32 v122, v122, v125\n"
    "v_mov_b32_e32 v124, v120\n"
    "v_mov_b32_e32 v125, v122\n"
    "s_nop 0\n"
    "v_permlane32_swap_b32_e32 v120, v124\n"
    "v_permlane32_swap_b32_e32 v122, v125\n"
    "v_add_f32_e32 v120, v120, v124\n"
    "v_add_f32_e32 v122, v122, v125\n"
    "v_fmaak_f32 v120, v120, v128, 0x3727c5ac\n"
    "v_fmaak_f32 v122, v122, v128, 0x3727c5ac\n"
    "v_rsq_f32_e32 v120, v120\n"
    "v_rsq_f32_e32 v122, v122\n"
    "s_nop 0\n"
    "v_pk_mul_f32 v[96:97], v[96:97], v[120:121] op_sel_hi:[1,0]\n"
    "L_chain0x1_mk:\n"
    "s_cbranch_execz L_pad0_6_mk\n"
    "v_pk_mul_f32 v[100:101], v[100:101], v[122:123] op_sel_hi:[1,0]\n"
    "v_pk_mul_f32 v[98:99], v[98:99], v[120:121] op_sel_hi:[1,0]\n"
    "v_pk_mul_f32 v[102:103], v[102:103], v[122:123] op_sel_hi:[1,0]\n"
    "v_pk_mul_f32 v[104:105], v[104:105], v[120:121] op_sel_hi:[1,0]\n"
    "v_pk_mul_f32 v[108:109], v[108:109], v[122:123] op_sel_hi:[1,0]\n"
    "v_pk_mul_f32 v[106:107], v[106:107], v[120:121] op_sel_hi:[1,0]\n"
    "v_pk_mul_f32 v[110:111], v[110:111], v[122:123] op_sel_hi:[1,0]\n"
    "v_pk_mul_f32 v[112:113], v[112:113], v[120:121] op_sel_hi:[1,0]\n"
    "v_pk_mul_f32 v[116:117], v[116:117], v[122:123] op_sel_hi:[1,0]\n"
    "v_pk_mul_f32 v[114:115], v[114:115], v[120:121] op_sel_hi:[1,0]\n"
    "v_pk_mul_f32 v[118:119], v[118:119], v[122:123] op_sel_hi:[1,0]\n"
    "s_waitcnt vmcnt(1) lgkmcnt(0)\n"
    "s_barrier\n"
    "s_add_u32 s90, s90, 1\n"
    "s_addc_u32 s91, s91, 0\n"
    "s_cmp_eq_u32 s22, 0\n"
    "s_cselect_b32 s40, 7, s88\n"
    "s_cmp_lg_u32 s40, 0\n"
    "s_cbranch_scc1 L_sig3_mk\n"
    "ds_read_b64_tr_b16 a[0:1], v202 offset:0\n"
    "ds_read_b64_tr_b16 a[2:3], v202 offset:512\n"
    "ds_read_b128 a[4:7], v203 offset:0\n"
    "ds_read_b64_tr_b16 a[8:9], v202 offset:1024\n"
    "ds_read_b64_tr_b16 a[10:11], v202 offset:1536\n"
    "ds_read_b128 a[12:15], v203 offset:64\n"
    "ds_read_b64_tr_b16 a[16:17], v202 offset:2048\n"
    "ds_read_b64_tr_b16 a[18:19], v202 offset:2560\n"
    "ds_read_b128 a[20:23], v203 offset:128\n"
    "ds_read_b64_tr_b16 a[24:25], v202 offset:3072\n"
    "ds_read_b64_tr_b16 a[26:27], v202 offset:3584\n"
    "ds_read_b128 a[28:31], v203 offset:192\n"
    "s_waitcnt lgkmcnt(9)\n"
    "v_mfma_f32_16x16x32_bf16 v[192:195], a[0:3], a[4:7], 0\n"
    "ds_read_b64_tr_b16 a[32:33], v202 offset:4096\n"
    "ds_read_b64_tr_b16 a[34:35], v202 offset:4608\n"
    "ds_read_b128 a[36:39], v203 offset:256\n"
    "s_waitcnt lgkmcnt(9)\n"
    "v_mfma_f32_16x16x32_bf16 v[192:195], a[8:11], a[12:15], v[192:195]\n"
    "ds_read_b64_tr_b16 a[40:41], v202 offset:5120\n"
    "ds_read_b64_tr_b16 a[42:43], v202 offset:5632\n"
    "ds_read_b128 a[44:47], v203 offset:320\n"
    "s_waitcnt lgkmcnt(9)\n"
    "v_mfma_f32_16x16x32_bf16 v[192:195], a[16:19], a[20:23], v[192:195]\n"
    "ds_read_b64_tr_b16 a[48:49], v202 offset:6144\n"
    "ds_read_b64_tr_b16 a[50:51], v202 offset:6656\n"
    "ds_read_b128 a[52:55], v203 offset:384\n"
    "s_waitcnt lgkmcnt(9)\n"
    "v_mfma_f32_16x16x32_bf16 v[192:195], a[24:27], a[28:31], v[192:195]\n"
    "ds_read_b64_tr_b16 a[56:57], v202 offset:7168\n"
    "ds_read_b64_tr_b16 a[58:59], v202 offset:7680\n"
    "ds_read_b128 a[60:63], v203 offset:448\n"
    "s_waitcnt lgkmcnt(9)\n"
    "v_mfma_f32_16x16x32_bf16 v[192:195], a[32:35], a[36:39], v[192:195]\n"
    "ds_read_b64_tr_b16 a[64:65], v202 offset:8192\n"
    "ds_read_b64_tr_b16 a[66:67], v202 offset:8704\n"
    "ds_read_b128 a[68:71], v203 offset:512\n"
    "s_waitcnt lgkmcnt(9)\n"
    "v_mfma_f32_16x16x32_bf16 v[192:195], a[40:43], a[44:47], v[192:195]\n"
    "ds_read_b64_tr_b16 a[72:73], v202 offset:9216\n"
    "ds_read_b64_tr_b16 a[74:75], v202 offset:9728\n"
    "ds_read_b128 a[76:79], v203 offset:576\n"
    "s_waitcnt lgkmcnt(9)\n"
    "v_mfma_f32_16x16x32_bf16 v[192:195], a[48:51], a[52:55], v[192:195]\n"
    "ds_read_b64_tr_b16 a[80:81], v202 offset:10240\n"
    "ds_read_b64_tr_b16 a[82:83], v202 offset:10752\n"
    "ds_read_b128 a[84:87], v203 offset:640\n"
    "s_waitcnt lgkmcnt(9)\n"
    "v_mfma_f32_16x16x32_bf16 v[192:195], a[56:59], a[60:63], v[192:195]\n"
    "ds_read_b64_tr_b16 a[88:89], v202 offset:11264\n"
    "ds_read_b64_tr_b16 a[90:91], v202 offset:11776\n"
    "ds_read_b128 a[92:95], v203 offset:704\n"
    "s_waitcnt lgkmcnt(9)\n"
    "L_chain0x0_mk:\n"
    "s_cbranch_execz L_pad0_7_mk\n"
    "v_mfma_f32_16x16x32_bf16 v[192:195], a[64:67], a[68:71], v[192:195]\n"
    "ds_read_b64_tr_b16 a[96:97], v202 offset:12288\n"
    "ds_read_b64_tr_b16 a[98:99], v202 offset:12800\n"
    "ds_read_b128 a[100:103], v203 offset:768\n"
    "s_waitcnt lgkmcnt(9)\n"
    "v_mfma_f32_16x16x32_bf16 v[192:195], a[72:75], a[76:79], v[192:195]\n"
    "ds_read_b64_tr_b16 a[104:105], v202 offset:13312\n"
    "ds_read_b64_tr_b16 a[106:107], v202 offset:13824\n"
    "ds_read_b128 a[108:111], v203 offset:832\n"
    "s_waitcnt lgkmcnt(9)\n"
    "v_mfma_f32_16x16x32_bf16 v[192:195], a[80:83], a[84:87], v[192:195]\n"
    "ds_read_b64_tr_b16 a[112:113], v202 offset:14336\n"
    "ds_read_b64_tr_b16 a[114:115], v202 offset:14848\n"
    "ds_read_b128 a[116:119], v203 offset:896\n"
    "s_waitcnt lgkmcnt(9)\n"
    "v_mfma_f32_16x16x32_bf16 v[192:195], a[88:91], a[92:95], v[192:195]\n"
    "ds_read_b64_tr_b16 a[120:121], v202 offset:15360\n"
    "ds_read_b64_tr_b16 a[122:123], v202 offset:15872\n"
    "ds_read_b128 a[124:127], v203 offset:960\n"
    "s_waitcnt lgkmcnt(9)\n"
    "v_mfma_f32_16x16x32_bf16 v[192:195], a[96:99], a[100:103], v[192:195]\n"
    "ds_read_b64_tr_b16 a[128:129], v202 offset:16384\n"
    "ds_read_b64_tr_b16 a[130:131], v202 offset:16896\n"
    "ds_read_b128 a[132:135], v203 offset:1024\n"
    "s_waitcnt lgkmcnt(9)\n"
    "v_mfma_f32_16x16x32_bf16 v[192:195], a[104:107], a[108:111], v[192:195]\n"
    "ds_read_b64_tr_b16 a[136:137], v202 offset:17408\n"
    "ds_read_b64_tr_b16 a[138:139], v202 offset:17920\n"
    "ds_read_b128 a[140:143], v203 offset:1088\n"
    "s_waitcnt lgkmcnt(9)\n"
    "v_mfma_f32_16x16x32_bf16 v[192:195], a[112:115], a[116:119], v[192:195]\n"
    "s_waitcnt lgkmcnt(6)\n"
    "v_mfma_f32_16x16x32_bf16 v[192:195], a[120:123], a[124:127], v[192:195]\n"
    "s_waitcnt lgkmcnt(3)\n"
    "v_mfma_f32_16x16x32_bf16 v[192:195], a[128:131], a[132:135], v[192:195]\n"
    "s_waitcnt lgkmcnt(0)\n"
    "v_mfma_f32_16x16x32_bf16 v[192:195], a[136:139], a[140:143], v[192:195]\n"
    "s_lshl_b32 s40, s36, 6\n"
    "s_add_u32 s40, s40, 262144\n"
    "v_and_b32_e32 v200, 0x70, v232\n"
    "v_lshlrev_b32_e32 v200, 10, v200\n"
    "v_lshl_add_u32 v200, v237, 4, v200\n"
    "v_mov_b32_e32 v198, s90\n"
    "v_mov_b32_e32 v199, s91\n"
    "s_mov_b32 exec_lo, 0xff00ff\n"
    "s_mov_b32 exec_hi, 0xff00ff\n"
    "v_cvt_pk_bf16_f32 v196, v192, v193\n"
    "v_cvt_pk_bf16_f32 v197, v194, v195\n"
    "buffer_store_dwordx4 v[196:199], v200, s[96:99], s40 offen sc1\n"
    "s_mov_b64 exec, -1\n"
    "L_sig3_mk:\n"
    "s_getpc_b64 s[42:43]\n"
    "L_pc4_mk:\n"
    "s_add_u32 s42, s42, L_code0_mk-L_pc4_mk\n"
    "s_addc_u32 s43, s43, 0\n"
    "s_lshr_b32 s40, s89, 3\n"
    "s_lshl_b32 s40, s40, 2\n"
    "s_add_u32 s40, s40, s88\n"
    "s_lshl_b32 s40, s40, 10\n"
    "v_add_u32_e32 v191, s40, v232\n"
    "v_min_u32_e32 v191, L_end_mk-L_code0_mk-4, v191\n"
    "global_load_dword v191, v191, s[42:43]\n"
    "s_mul_i32 s40, s89, 256\n"
    "s_add_u32 s40, s40, 163840\n"
    "v_mul_u32_u24_e32 v145, 4, v234\n"
    "buffer_load_dword v145, v145, s[96:99], s40 offen sc1\n"
    "s_lshr_b32 s42, s57, 1\n"
    "s_lshl_b32 s42, s42, 19\n"
    "s_add_u32 s24, s70, s42\n"
    "s_addc_u32 s25, s71, 0\n"
    "s_and_b32 s25, s25, 0xffff\n"
    "s_mov_b32 s26, 524288\n"
    "s_mov_b32 s27, 0x20000\n"
    "s_add_u32 s70, s68, s42\n"
    "s_addc_u32 s71, s69, 0\n"
    "s_and_b32 s68, s89, 7\n"
    "s_lshl_b32 s68, s68, 14\n"
    "s_add_u32 s68, s68, 262144\n"
    "s_lshl_b32 s42, s58, 2\n"
    "s_add_u32 s42, s42, s88\n"
    "s_mul_i32 s40, s42, 73728\n"
    "s_add_u32 s40, s40, 1048576\n"
    "L_pad0_4_mk:\n"
    "s_cbranch_execz L_pad0_8_mk\n"
    "s_add_u32 s28, s64, s40\n"
    "s_addc_u32 s29, s65, 0\n"
    "s_and_b32 s29, s29, 0xffff\n"
    "s_mov_b32 s30, 73728\n"
    "s_mov_b32 s31, 0x20000\n"
    "s_lshl_b32 s42, s89, 2\n"
    "s_add_u32 s42, s42, s88\n"
    "s_and_b32 s42, s42, 511\n"
    "v_lshrrev_b32_e32 v208, 6, v232\n"
    "v_bfe_u32 v213, v232, 4, 2\n"
    "s_cmp_ge_u32 s42, 245\n"
    "s_cbranch_scc1 L_clsB5_mk\n"
    "s_mul_i32 s40, s42, 10\n"
    "v_add_u32_e32 v208, s40, v208\n"
    "v_cmp_le_u32_e32 vcc, 1152, v208\n"
    "v_cmp_gt_u32_e64 s[40:41], v208, v208\n"
    "v_subrev_u32_e32 v209, 1152, v208\n"
    "v_cndmask_b32_e32 v209, v208, v209, vcc\n"
    "v_min_u32_e32 v209, 1295, v209\n"
    "v_mul_u32_u24_e32 v210, 7282, v209\n"
    "v_lshrrev_b32_e32 v210, 16, v210\n"
    "v_mul_u32_u24_e32 v211, 9, v210\n"
    "v_sub_u32_e32 v211, v209, v211\n"
    "v_mul_u32_u24_e32 v212, 36864, v210\n"
    "v_lshl_add_u32 v212, v211, 7, v212\n"
    "v_mov_b32_e32 v249, s74\n"
    "v_mov_b32_e32 v209, s80\n"
    "v_cndmask_b32_e32 v249, v249, v209, vcc\n"
    "v_mov_b32_e32 v250, s75\n"
    "v_mov_b32_e32 v209, s81\n"
    "v_cndmask_b32_e32 v250, v250, v209, vcc\n"
    "v_lshl_add_u32 v211, v211, 2, v213\n"
    "v_mul_u32_u24_e32 v209, 7282, v211\n"
    "v_lshrrev_b32_e32 v209, 16, v209\n"
    "v_mul_u32_u24_e32 v213, 9, v209\n"
    "v_sub_u32_e32 v211, v211, v213\n"
    "v_mul_u32_u24_e32 v213, 7282, v210\n"
    "v_lshrrev_b32_e32 v213, 16, v213\n"
    "v_mul_u32_u24_e32 v215, 9, v213\n"
    "v_sub_u32_e32 v215, v210, v215\n"
    "v_lshl_add_u32 v215, v215, 1, 1\n"
    "v_subrev_u32_e32 v218, 9, v215\n"
    "v_min_u32_e32 v215, v215, v218\n"
    "v_lshrrev_b32_e32 v218, 2, v210\n"
    "v_and_b32_e32 v218, 7, v218\n"
    "v_cndmask_b32_e32 v215, v218, v215, vcc\n"
    "v_cndmask_b32_e32 v216, v210, v213, vcc\n"
    "v_and_b32_e32 v216, 3, v216\n"
    "v_lshrrev_b32_e32 v214, 3, v210\n"
    "v_cndmask_b32_e32 v214, v214, v213, vcc\n"
    "v_lshrrev_b32_e32 v214, 2, v214\n"
    "v_cndmask_b32_e64 v217, 8, 9, vcc\n"
    "v_lshl_add_u32 v209, v209, 2, v214\n"
    "v_mul_u32_u24_e32 v209, 9, v209\n"
    "v_cmp_le_u32_e64 s[40:41], 7, v211\n"
    "v_mov_b32_e32 v213, 0\n"
    "v_mov_b32_e32 v218, 7\n"
    "v_cndmask_b32_e64 v213, v213, 7, s[40:41]\n"
    "v_cndmask_b32_e64 v218, v218, 1, s[40:41]\n"
    "v_cmp_le_u32_e64 s[40:41], 8, v211\n"
    "s_nop 1\n"
    "v_cndmask_b32_e64 v213, v213, 8, s[40:41]\n"
    "v_cndmask_b32_e64 v218, v218, 1, s[40:41]\n"
    "v_sub_u32_e32 v211, v211, v213\n"
    "v_add_u32_e32 v209, v209, v213\n"
    "v_mad_u32_u24 v209, v209, v217, v211\n"
    "v_mad_u32_u24 v209, v215, v218, v209\n"
    "v_lshl_add_u32 v209, v209, 2, v216\n"
    "v_mov_b32_e32 v213, 58720256\n"
    "v_mov_b32_e32 v251, 75497472\n"
    "v_cndmask_b32_e32 v213, v213, v251, vcc\n"
    "v_lshl_add_u32 v251, v209, 10, v213\n"
    "v_cmp_gt_u32_e32 vcc, 2448, v208\n"
    "s_mov_b32 s43, 1152\n"
    "s_branch L_clsE6_mk\n"
    "L_clsB5_mk:\n"
    "s_sub_u32 s40, s42, 245\n"
    "s_mul_i32 s40, s40, 10\n"
    "v_add_u32_e32 v208, s40, v208\n"
    "v_cmp_le_u32_e32 vcc, 1296, v208\n"
    "v_subrev_u32_e32 v209, 1296, v208\n"
    "s_nop 0\n"
    "v_cndmask_b32_e32 v209, v208, v209, vcc\n"
    "v_min_u32_e32 v209, 1295, v209\n"
    "v_mul_u32_u24_e32 v210, 1821, v209\n"
    "v_lshrrev_b32_e32 v210, 17, v210\n"
    "v_mul_u32_u24_e32 v211, 72, v210\n"
    "v_sub_u32_e32 v211, v209, v211\n"
    "v_mul_u32_u24_e32 v212, 294912, v210\n"
    "L_pad0_5_mk:\n"
    "s_cbranch_execz L_pad0_9_mk\n"
    "v_lshl_add_u32 v212, v211, 7, v212\n"
    "v_mov_b32_e32 v249, s76\n"
    "v_mov_b32_e32 v209, s78\n"
    "v_cndmask_b32_e32 v249, v249, v209, vcc\n"
    "v_mov_b32_e32 v250, s77\n"
    "v_mov_b32_e32 v209, s79\n"
    "v_cndmask_b32_e32 v250, v250, v209, vcc\n"
    "v_cndmask_b32_e64 v214, 0, 1, vcc\n"
    "v_lshl_add_u32 v211, v211, 2, v213\n"
    "v_mul_u32_u24_e32 v215, 7282, v211\n"
    "v_lshrrev_b32_e32 v215, 16, v215\n"
    "v_mul_u32_u24_e32 v213, 9, v215\n"
    "v_sub_u32_e32 v211, v211, v213\n"
    "v_and_b32_e32 v213, 1, v215\n"
    "v_xor_b32_e32 v213, 1, v213\n"
    "v_add_u32_e32 v211, v211, v213\n"
    "v_cmp_ne_u32_e32 vcc, 9, v211\n"
    "s_nop 1\n"
    "v_cndmask_b32_e32 v211, 0, v211, vcc\n"
    "v_mul_u32_u24_e32 v209, 10923, v210\n"
    "v_lshrrev_b32_e32 v209, 16, v209\n"
    "v_mul_u32_u24_e32 v216, 6, v210\n"
    "v_lshl_add_u32 v216, v211, 1, v216\n"
    "v_mul_u32_u24_e32 v217, 36, v211\n"
    "v_lshl_add_u32 v217, v210, 1, v217\n"
    "v_cmp_gt_u32_e32 vcc, 3, v211\n"
    "s_nop 1\n"
    "v_cndmask_b32_e32 v217, v217, v216, vcc\n"
    "v_cmp_le_u32_e32 vcc, 7, v211\n"
    "v_lshlrev_b32_e32 v216, 2, v210\n"
    "v_lshl_add_u32 v216, v211, 1, v216\n"
    "v_add_u32_e32 v216, 238, v216\n"
    "v_cndmask_b32_e32 v217, v217, v216, vcc\n"
    "v_mul_u32_u24_e32 v215, 324, v215\n"
    "v_add3_u32 v209, v215, v217, v214\n"
    "v_mov_b32_e32 v213, 63963136\n"
    "v_lshl_add_u32 v251, v209, 10, v213\n"
    "v_cmp_gt_u32_e32 vcc, 2592, v208\n"
    "s_mov_b32 s43, 9216\n"
    "L_clsE6_mk:\n"
    "v_mov_b32_e32 v213, 0x7f000000\n"
    "s_nop 0\n"
    "v_cndmask_b32_e32 v251, v213, v251, vcc\n"
    "v_add_co_u32_e32 v249, vcc, v249, v212\n"
    "s_nop 1\n"
    "v_addc_co_u32_e32 v250, vcc, 0, v250, vcc\n"
    "v_and_b32_e32 v250, 0xffff, v250\n"
    "v_lshrrev_b32_e32 v209, 3, v232\n"
    "v_and_b32_e32 v209, 0x60, v209\n"
    "v_xor_b32_e32 v210, v209, v232\n"
    "v_and_b32_e32 v210, 0x70, v210\n"
    "v_lshrrev_b32_e32 v209, 7, v232\n"
    "v_mad_u32_u24 v252, v209, s43, v210\n"
    "s_mul_i32 s69, s88, 6144\n"
    "s_add_u32 s69, s69, 131072\n"
    "s_lshl_b32 s40, s43, 3\n"
    "s_sub_u32 s80, s40, 1024\n"
    "s_lshl_b32 s81, s80, 1\n"
    "s_add_u32 s82, s81, s80\n"
    "s_mul_i32 s74, s43, 31\n"
    "s_add_u32 s74, s74, 128\n"
    "s_mov_b32 s75, 0x20000\n"
    "s_mov_b32 s76, s64\n"
    "L_pad0_6_mk:\n"
    "s_cbranch_execz L_ret0_mk\n"
    "s_and_b32 s77, s65, 0xffff\n"
    "s_mov_b32 s78, 81788928\n"
    "s_mov_b32 s79, 0x20000\n"
    "v_add_u32_e32 v204, 111616, v139\n"
    "s_cmp_eq_u32 s88, 0\n"
    "s_cbranch_scc1 L_w0a10_mk\n"
    "s_add_u32 m0, s93, 0\n"
    "s_add_u32 s95, s94, 0\n"
    "buffer_load_dwordx4 v245, s[48:51], s95 offen offset:0 lds\n"
    "s_add_u32 s95, s94, 31736\n"
    "buffer_load_dwordx4 v245, s[48:51], s95 offen offset:1032 lds\n"
    "s_add_u32 s95, s94, 63472\n"
    "buffer_load_dwordx4 v245, s[48:51], s95 offen offset:2064 lds\n"
    "s_add_u32 s95, s94, 95208\n"
    "buffer_load_dwordx4 v245, s[48:51], s95 offen offset:3096 lds\n"
    "s_add_u32 m0, s93, 4128\n"
    "s_add_u32 s95, s94, 131072\n"
    "buffer_load_dwordx4 v245, s[48:51], s95 offen offset:0 lds\n"
    "s_add_u32 s95, s94, 162808\n"
    "buffer_load_dwordx4 v245, s[48:51], s95 offen offset:1032 lds\n"
    "s_add_u32 s95, s94, 194544\n"
    "buffer_load_dwordx4 v245, s[48:51], s95 offen offset:2064 lds\n"
    "s_add_u32 s95, s94, 226280\n"
    "buffer_load_dwordx4 v245, s[48:51], s95 offen offset:3096 lds\n"
    "s_add_u32 m0, s93, 24768\n"
    "s_add_u32 s95, s94, 786432\n"
    "buffer_load_dwordx4 v245, s[48:51], s95 offen offset:0 lds\n"
    "s_add_u32 s95, s94, 818168\n"
    "buffer_load_dwordx4 v245, s[48:51], s95 offen offset:1032 lds\n"
    "s_add_u32 s95, s94, 849904\n"
    "buffer_load_dwordx4 v245, s[48:51], s95 offen offset:2064 lds\n"
    "s_add_u32 s95, s94, 881640\n"
    "buffer_load_dwordx4 v245, s[48:51], s95 offen offset:3096 lds\n"
    "s_add_u32 m0, s93, 28896\n"
    "s_add_u32 s95, s94, 917504\n"
    "buffer_load_dwordx4 v245, s[48:51], s95 offen offset:0 lds\n"
    "s_add_u32 s95, s94, 949240\n"
    "buffer_load_dwordx4 v245, s[48:51], s95 offen offset:1032 lds\n"
    "s_add_u32 s95, s94, 980976\n"
    "buffer_load_dwordx4 v245, s[48:51], s95 offen offset:2064 lds\n"
    "s_add_u32 s95, s94, 1012712\n"
    "buffer_load_dwordx4 v245, s[48:51], s95 offen offset:3096 lds\n"
    "s_add_u32 m0, s93, 49536\n"
    "s_add_u32 s95, s94, 1572864\n"
    "buffer_load_dwordx4 v245, s[48:51], s95 offen offset:0 lds\n"
    "s_add_u32 s95, s94, 1604600\n"
    "buffer_load_dwordx4 v245, s[48:51], s95 offen offset:1032 lds\n"
    "s_add_u32 s95, s94, 1636336\n"
    "buffer_load_dwordx4 v245, s[48:51], s95 offen offset:2064 lds\n"
    "s_add_u32 s95, s94, 1668072\n"
    "buffer_load_dwordx4 v245, s[48:51], s95 offen offset:3096 lds\n"
    "s_add_u32 m0, s93, 53664\n"
    "s_add_u32 s95, s94, 1703936\n"
    "buffer_load_dwordx4 v245, s[48:51], s95 offen offset:0 lds\n"
    "s_add_u32 s95, s94, 1735672\n"
    "buffer_load_dwordx4 v245, s[48:51], s95 offen offset:1032 lds\n"
    "s_add_u32 s95, s94, 1767408\n"
    "buffer_load_dwordx4 v245, s[48:51], s95 offen offset:2064 lds\n"
    "s_add_u32 s95, s94, 1799144\n"
    "buffer_load_dwordx4 v245, s[48:51], s95 offen offset:3096 lds\n"
    "s_add_u32 m0, s93, 74304\n"
    "s_add_u32 s95, s94, 2359296\n"
    "buffer_load_dwordx4 v245, s[48:51], s95 offen offset:0 lds\n"
    "s_add_u32 s95, s94, 2391032\n"
    "buffer_load_dwordx4 v245, s[48:51], s95 offen offset:1032 lds\n"
    "s_add_u32 s95, s94, 2422768\n"
    "L_pad0_7_mk:\n"
    "s_cbranch_execz L_ret0_mk\n"
    "buffer_load_dwordx4 v245, s[48:51], s95 offen offset:2064 lds\n"
    "s_add_u32 s95, s94, 2454504\n"
    "buffer_load_dwordx4 v245, s[48:51], s95 offen offset:3096 lds\n"
    "s_add_u32 m0, s93, 78432\n"
    "s_add_u32 s95, s94, 2490368\n"
    "buffer_load_dwordx4 v245, s[48:51], s95 offen offset:0 lds\n"
    "s_add_u32 s95, s94, 2522104\n"
    "buffer_load_dwordx4 v245, s[48:51], s95 offen offset:1032 lds\n"
    "s_add_u32 s95, s94, 2553840\n"
    "buffer_load_dwordx4 v245, s[48:51], s95 offen offset:2064 lds\n"
    "s_add_u32 s95, s94, 2585576\n"
    "buffer_load_dwordx4 v245, s[48:51], s95 offen offset:3096 lds\n"
    "s_branch L_w0b11_mk\n"
    "L_w0a10_mk:\n"
    "s_sleep 8\n"
    "s_mov_b32 s92, 0x40000\n"
    "L_gpoll8_mk:\n"
    "s_add_u32 s40, s68, 0\n"
    "buffer_load_dwordx4 v[148:151], v136, s[96:99], s40 offen sc1\n"
    "buffer_load_dwordx4 v[152:155], v137, s[96:99], s40 offen sc1\n"
    "buffer_load_dwordx4 v[156:159], v138, s[96:99], s40 offen sc1\n"
    "s_add_u32 s40, s68, 2304\n"
    "buffer_load_dwordx4 v[160:163], v136, s[96:99], s40 offen sc1\n"
    "buffer_load_dwordx4 v[164:167], v137, s[96:99], s40 offen sc1\n"
    "buffer_load_dwordx4 v[168:171], v138, s[96:99], s40 offen sc1\n"
    "s_waitcnt vmcnt(0)\n"
    "v_cmp_ne_u64_e64 s[42:43], s[90:91], v[150:151]\n"
    "v_cmp_ne_u64_e32 vcc, s[90:91], v[162:163]\n"
    "s_or_b64 s[42:43], s[42:43], vcc\n"
    "v_cmp_ne_u64_e32 vcc, s[90:91], v[154:155]\n"
    "s_or_b64 s[42:43], s[42:43], vcc\n"
    "v_cmp_ne_u64_e32 vcc, s[90:91], v[166:167]\n"
    "s_or_b64 s[42:43], s[42:43], vcc\n"
    "s_mov_b64 exec, 0xffff\n"
    "v_cmp_ne_u64_e32 vcc, s[90:91], v[158:159]\n"
    "s_or_b64 s[42:43], s[42:43], vcc\n"
    "v_cmp_ne_u64_e32 vcc, s[90:91], v[170:171]\n"
    "s_or_b64 s[42:43], s[42:43], vcc\n"
    "s_mov_b64 exec, -1\n"
    "s_cbranch_scc0 L_gdone9_mk\n"
    "s_sub_u32 s92, s92, 1\n"
    "s_cbranch_scc0 L_gpoll8_mk\n"
    "L_gdone9_mk:\n"
    "ds_write_b64 v204, v[148:149] offset:0\n"
    "ds_write_b64 v204, v[152:153] offset:512\n"
    "ds_write_b64 v204, v[156:157] offset:1024\n"
    "ds_write_b64 v204, v[160:161] offset:1536\n"
    "ds_write_b64 v204, v[164:165] offset:2048\n"
    "ds_write_b64 v204, v[168:169] offset:2560\n"
    "s_waitcnt lgkmcnt(0)\n"
    "L_w0b11_mk:\n"
    "s_barrier\n"
    "ds_read_b64 v[148:149], v204 offset:0\n"
    "ds_read_b64 v[152:153], v204 offset:512\n"
    "ds_read_b64 v[156:157], v204 offset:1024\n"
    "ds_read_b64 v[160:161], v204 offset:1536\n"
    "ds_read_b64 v[164:165], v204 offset:2048\n"
    "ds_read_b64 v[168:169], v204 offset:2560\n"
    "s_waitcnt lgkmcnt(0)\n"
    "v_lshlrev_b32_e32 v60, 16, v148\n"
    "v_and_b32_e32 v61, 0xffff0000, v148\n"
    "v_lshlrev_b32_e32 v62, 16, v149\n"
    "v_and_b32_e32 v63, 0xffff0000, v149\n"
    "v_lshlrev_b32_e32 v64, 16, v152\n"
    "v_and_b32_e32 v65, 0xffff0000, v152\n"
    "v_lshlrev_b32_e32 v66, 16, v153\n"
    "v_and_b32_e32 v67, 0xffff0000, v153\n"
    "v_lshlrev_b32_e32 v68, 16, v156\n"
    "v_and_b32_e32 v69, 0xffff0000, v156\n"
    "v_lshlrev_b32_e32 v70, 16, v157\n"
    "v_and_b32_e32 v71, 0xffff0000, v157\n"
    "v_lshlrev_b32_e32 v72, 16, v160\n"
    "v_and_b32_e32 v73, 0xffff0000, v160\n"
    "v_lshlrev_b32_e32 v74, 16, v161\n"
    "v_and_b32_e32 v75, 0xffff0000, v161\n"
    "v_lshlrev_b32_e32 v76, 16, v164\n"
    "v_and_b32_e32 v77, 0xffff0000, v164\n"
    "v_lshlrev_b32_e32 v78, 16, v165\n"
    "v_and_b32_e32 v79, 0xffff0000, v165\n"
    "v_lshlrev_b32_e32 v80, 16, v168\n"
    "v_and_b32_e32 v81, 0xffff0000, v168\n"
    "v_lshlrev_b32_e32 v82, 16, v169\n"
    "v_and_b32_e32 v83, 0xffff0000, v169\n"
    "v_mov_b32_e32 v120, 0x3e0293ee\n"
    "L_pad0_8_mk:\n"
    "s_cbranch_execz L_ret0_mk\n"
    "v_mov_b32_e32 v121, 0x3e0293ee\n"
    "v_pk_fma_f32 v[72:73], v[72:73], v[120:121], v[120:121]\n"
    "v_pk_mul_f32 v[60:61], v[60:61], v[120:121]\n"
    "v_pk_fma_f32 v[74:75], v[74:75], v[120:121], v[120:121]\n"
    "v_pk_mul_f32 v[62:63], v[62:63], v[120:121]\n"
    "v_pk_fma_f32 v[76:77], v[76:77], v[120:121], v[120:121]\n"
    "v_pk_mul_f32 v[64:65], v[64:65], v[120:121]\n"
    "v_pk_fma_f32 v[78:79], v[78:79], v[120:121], v[120:121]\n"
    "v_pk_mul_f32 v[66:67], v[66:67], v[120:121]\n"
    "v_pk_fma_f32 v[80:81], v[80:81], v[120:121], v[120:121]\n"
    "v_pk_mul_f32 v[68:69], v[68:69], v[120:121]\n"
    "v_pk_fma_f32 v[82:83], v[82:83], v[120:121], v[120:121]\n"
    "v_pk_mul_f32 v[70:71], v[70:71], v[120:121]\n"
    "v_pk_fma_f32 v[96:97], v[96:97], v[72:73], v[60:61]\n"
    "v_pk_fma_f32 v[98:99], v[98:99], v[74:75], v[62:63]\n"
    "v_cvt_pk_bf16_f32 v96, v96, v97\n"
    "v_cvt_pk_bf16_f32 v97, v98, v99\n"
    "ds_write_b64 v142, v[96:97] offset:0\n"
    "v_pk_fma_f32 v[100:101], v[100:101], v[72:73], v[60:61]\n"
    "v_pk_fma_f32 v[102:103], v[102:103], v[74:75], v[62:63]\n"
    "v_cvt_pk_bf16_f32 v100, v100, v101\n"
    "v_cvt_pk_bf16_f32 v101, v102, v103\n"
    "ds_write_b64 v142, v[100:101] offset:4608\n"
    "v_pk_fma_f32 v[104:105], v[104:105], v[76:77], v[64:65]\n"
    "v_pk_fma_f32 v[106:107], v[106:107], v[78:79], v[66:67]\n"
    "v_cvt_pk_bf16_f32 v104, v104, v105\n"
    "v_cvt_pk_bf16_f32 v105, v106, v107\n"
    "ds_write_b64 v142, v[104:105] offset:2048\n"
    "v_pk_fma_f32 v[108:109], v[108:109], v[76:77], v[64:65]\n"
    "v_pk_fma_f32 v[110:111], v[110:111], v[78:79], v[66:67]\n"
    "v_cvt_pk_bf16_f32 v108, v108, v109\n"
    "v_cvt_pk_bf16_f32 v109, v110, v111\n"
    "ds_write_b64 v142, v[108:109] offset:6656\n"
    "s_mov_b64 exec, 0xffff\n"
    "v_pk_fma_f32 v[112:113], v[112:113], v[80:81], v[68:69]\n"
    "v_pk_fma_f32 v[114:115], v[114:115], v[82:83], v[70:71]\n"
    "v_cvt_pk_bf16_f32 v112, v112, v113\n"
    "v_cvt_pk_bf16_f32 v113, v114, v115\n"
    "ds_write_b64 v142, v[112:113] offset:4096\n"
    "v_pk_fma_f32 v[116:117], v[116:117], v[80:81], v[68:69]\n"
    "v_pk_fma_f32 v[118:119], v[118:119], v[82:83], v[70:71]\n"
    "v_cvt_pk_bf16_f32 v116, v116, v117\n"
    "v_cvt_pk_bf16_f32 v117, v118, v119\n"
    "ds_write_b64 v142, v[116:117] offset:8704\n"
    "s_mov_b64 exec, -1\n"
    "s_waitcnt lgkmcnt(0)\n"
    "s_barrier\n"
    "ds_read_b128 v[0:3], v143 offset:0\n"
    "ds_read_b128 v[4:7], v143 offset:4096\n"
    "ds_read_b128 v[8:11], v143 offset:8192\n"
    "s_mov_b32 s40, 0\n"
    "s_waitcnt lgkmcnt(2)\n"
    "buffer_store_dwordx4 v[0:3], v144, s[32:35], s40 offen sc1\n"
    "s_mov_b32 s40, 16384\n"
    "s_waitcnt lgkmcnt(1)\n"
    "buffer_store_dwordx4 v[4:7], v144, s[32:35], s40 offen sc1\n"
    "s_mov_b32 s40, 32768\n"
    "s_waitcnt lgkmcnt(0)\n"
    "buffer_store_dwordx4 v[8:11], v144, s[32:35], s40 offen sc1\n"
    "v_lshrrev_b32_e32 v240, 4, v232\n"
    "s_and_b32 s43, s89, 0xe0\n"
    "s_and_b32 s40, s89, 31\n"
    "v_mul_u32_u24_e32 v240, 256, v240\n"
    "s_mul_i32 s43, s43, 256\n"
    "s_mul_i32 s40, s40, 8\n"
    "s_add_u32 s43, s43, s40\n"
    "s_add_u32 s43, s43, 163840\n"
    "v_mov_b32_e32 v238, s90\n"
    "v_mov_b32_e32 v239, s91\n"
    "s_cmp_eq_u32 s88, 0\n"
    "s_cbranch_scc1 L_w0a12_mk\n"
    "L_pad0_9_mk:\n"
    "s_cbranch_execz L_ret0_mk\n"
    "s_waitcnt vmcnt(0)\n"
    "s_branch L_w0b13_mk\n"
    "L_w0a12_mk:\n"
    "s_waitcnt vmcnt(0)\n"
    "L_w0b13_mk:\n"
    "s_barrier\n"
    "s_cmp_lg_u32 s88, 1\n"
    "s_cbranch_scc1 L_psh14_mk\n"
    "s_mov_b32 exec_hi, 0\n"
    "buffer_store_dwordx2 v[238:239], v240, s[96:99], s43 offen sc1\n"
    "s_mov_b64 exec, -1\n"
    "L_psh14_mk:\n"
    "s_lshl_b32 s42, s58, 2\n"
    "s_add_u32 s42, s42, s88\n"
    "s_mul_i32 s40, s42, 589824\n"
    "s_lshl_b32 s41, s57, 12\n"
    "s_add_u32 s40, s40, s41\n"
    "s_add_u32 s40, s40, 38797312\n"
    "s_add_u32 s32, s64, s40\n"
    "s_addc_u32 s33, s65, 0\n"
    "s_and_b32 s33, s33, 0xffff\n"
    "s_mov_b32 s34, 446464\n"
    "s_mov_b32 s35, 0x20000\n"
    "s_mov_b32 s20, s70\n"
    "s_and_b32 s21, s71, 0xffff\n"
    "s_mov_b32 s22, 524288\n"
    "s_cmp_eq_u32 s88, 0\n"
    "s_cbranch_scc1 L_w0a15_mk\n"
    "s_add_u32 m0, s93, 99072\n"
    "s_add_u32 s95, s94, 3145728\n"
    "buffer_load_dwordx4 v245, s[48:51], s95 offen offset:0 lds\n"
    "s_add_u32 s95, s94, 3177464\n"
    "buffer_load_dwordx4 v245, s[48:51], s95 offen offset:1032 lds\n"
    "s_add_u32 s95, s94, 3209200\n"
    "buffer_load_dwordx4 v245, s[48:51], s95 offen offset:2064 lds\n"
    "s_add_u32 s95, s94, 3240936\n"
    "buffer_load_dwordx4 v245, s[48:51], s95 offen offset:3096 lds\n"
    "s_add_u32 m0, s93, 103200\n"
    "s_add_u32 s95, s94, 3276800\n"
    "buffer_load_dwordx4 v245, s[48:51], s95 offen offset:0 lds\n"
    "s_add_u32 s95, s94, 3308536\n"
    "buffer_load_dwordx4 v245, s[48:51], s95 offen offset:1032 lds\n"
    "s_add_u32 s95, s94, 3340272\n"
    "buffer_load_dwordx4 v245, s[48:51], s95 offen offset:2064 lds\n"
    "s_add_u32 s95, s94, 3372008\n"
    "buffer_load_dwordx4 v245, s[48:51], s95 offen offset:3096 lds\n"
    "L_w0a15_mk:\n"
    "s_mov_b64 exec, 0\n"
    "s_cmp_eq_u32 s88, 3\n"
    "s_cbranch_scc1 L_chain1x3_mk\n"
    "s_cmp_eq_u32 s88, 2\n"
    "s_cbranch_scc1 L_chain1x2_mk\n"
    "s_cmp_eq_u32 s88, 1\n"
    "s_cbranch_scc1 L_chain1x1_mk\n"
    "L_ret1_mk:\n"
    "s_mov_b64 exec, -1\n"
    "s_cmp_lg_u32 s88, 0\n"
    "s_cbranch_scc1 L_polled18_mk\n"
    "s_mul_i32 s41, s89, 256\n"
    "s_add_u32 s41, s41, 163840\n"
    "v_and_b32_e32 v198, 496, v232\n"
    "v_lshrrev_b32_e32 v198, 1, v198\n"
    "s_mov_b32 s92, 0x40000\n"
    "L_poll17_mk:\n"
    "buffer_load_dwordx2 v[196:197], v198, s[96:99], s41 offen sc1\n"
    "s_sub_u32 s92, s92, 1\n"
    "s_waitcnt vmcnt(0)\n"
    "v_cmp_ne_u64_e32 vcc, s[90:91], v[196:197]\n"
    "s_cbranch_scc1 L_polled18_mk\n"
    "s_cbranch_vccnz L_poll17_mk\n"
    "L_polled18_mk:\n"
    "s_barrier\n"
    "L_code0_mk:\n"
    "ds_read_b64_tr_b16 v[128:129], v246 offset:0\n"
    "ds_read_b64_tr_b16 v[130:131], v246 offset:4128\n"
    "ds_read_b64_tr_b16 v[132:133], v246 offset:8\n"
    "ds_read_b64_tr_b16 v[134:135], v246 offset:4136\n"
    "ds_read_b64_tr_b16 v[136:137], v246 offset:256\n"
    "ds_read_b64_tr_b16 v[138:139], v246 offset:4384\n"
    "ds_read_b64_tr_b16 v[140:141], v246 offset:264\n"
    "ds_read_b64_tr_b16 v[142:143], v246 offset:4392\n"
    "ds_read_b64_tr_b16 v[144:145], v246 offset:512\n"
    "ds_read_b64_tr_b16 v[146:147], v246 offset:4640\n"
    "ds_read_b64_tr_b16 v[148:149], v246 offset:520\n"
    "ds_read_b64_tr_b16 v[150:151], v246 offset:4648\n"
    "s_mov_b32 s37, 0\n"
    "buffer_load_dwordx4 a[0:3], v232, s[28:31], s37 offen offset:0 sc1\n"
    "s_mov_b32 s37, 18432\n"
    "buffer_load_dwordx4 a[4:7], v232, s[28:31], s37 offen offset:0 sc1\n"
    "s_mov_b32 s37, 36864\n"
    "buffer_load_dwordx4 a[8:11], v232, s[28:31], s37 offen offset:0 sc1\n"
    "s_mov_b32 s37, 55296\n"
    "buffer_load_dwordx4 a[12:15], v232, s[28:31], s37 offen offset:0 sc1\n"
    "s_mov_b32 s37, 0\n"
    "buffer_load_dwordx4 a[16:19], v232, s[28:31], s37 offen offset:1024 sc1\n"
    "s_mov_b32 s37, 18432\n"
    "buffer_load_dwordx4 a[20:23], v232, s[28:31], s37 offen offset:1024 sc1\n"
    "s_mov_b32 s37, 36864\n"
    "buffer_load_dwordx4 a[24:27], v232, s[28:31], s37 offen offset:1024 sc1\n"
    "s_mov_b32 s37, 55296\n"
    "buffer_load_dwordx4 a[28:31], v232, s[28:31], s37 offen offset:1024 sc1\n"
    "s_mov_b32 s37, 0\n"
    "buffer_load_dwordx4 a[32:35], v232, s[28:31], s37 offen offset:2048 sc1\n"
    "s_mov_b32 s37, 18432\n"
    "buffer_load_dwordx4 a[36:39], v232, s[28:31], s37 offen offset:2048 sc1\n"
    "s_mov_b32 s37, 36864\n"
    "buffer_load_dwordx4 a[40:43], v232, s[28:31], s37 offen offset:2048 sc1\n"
    "s_mov_b32 s37, 55296\n"
    "buffer_load_dwordx4 a[44:47], v232, s[28:31], s37 offen offset:2048 sc1\n"
    "s_and_b32 s41, s88, 1\n"
    "s_lshl_b32 s41, s41, 5\n"
    "v_xor_b32_e32 v234, s41, v231\n"
    "v_mov_b64_e32 v[220:221], 0\n"
    "v_mov_b64_e32 v[222:223], 0\n"
    "v_mov_b32_e32 v235, 0x7f000000\n"
    "v_cmp_gt_u32_e32 vcc, 1024, v232\n"
    "v_add_u32_e32 v231, s39, v231\n"
    "v_add_u32_e32 v233, s39, v233\n"
    "v_cndmask_b32_e32 v235, v235, v232, vcc\n"
    "s_add_u32 s70, s20, 8192\n"
    "s_addc_u32 s71, s21, 0\n"
    "s_waitcnt vmcnt(11) lgkmcnt(6)\n"
    "v_mfma_f32_16x16x32_bf16 v[0:3], v[128:131], a[0:3], 0\n"
    "ds_read_b64_tr_b16 v[152:153], v246 offset:768\n"
    "ds_read_b64_tr_b16 v[154:155], v246 offset:4896\n"
    "v_mfma_f32_16x16x32_bf16 v[4:7], v[132:135], a[0:3], 0\n"
    "s_mov_b32 s37, 3072\n"
    "buffer_load_dwordx4 a[48:51], v232, s[28:31], s37 offen offset:0 sc1\n"
    "v_mfma_f32_16x16x32_bf16 v[8:11], v[136:139], a[0:3], 0\n"
    "ds_read_b64_tr_b16 v[156:157], v246 offset:776\n"
    "ds_read_b64_tr_b16 v[158:159], v246 offset:4904\n"
    "s_waitcnt vmcnt(11)\n"
    "v_mfma_f32_16x16x32_bf16 v[40:43], v[136:139], a[4:7], 0\n"
    "ds_read_b64_tr_b16 v[160:161], v246 offset:8256\n"
    "ds_read_b64_tr_b16 v[162:163], v246 offset:12384\n"
    "v_mfma_f32_16x16x32_bf16 v[36:39], v[132:135], a[4:7], 0\n"
    "buffer_load_dwordx4 a[64:67], v232, s[28:31], s37 offen offset:1024 sc1\n"
    "v_mfma_f32_16x16x32_bf16 v[32:35], v[128:131], a[4:7], 0\n"
    "s_waitcnt vmcnt(11)\n"
    "v_mfma_f32_16x16x32_bf16 v[64:67], v[128:131], a[8:11], 0\n"
    "v_mfma_f32_16x16x32_bf16 v[68:71], v[132:135], a[8:11], 0\n"
    "v_mfma_f32_16x16x32_bf16 v[72:75], v[136:139], a[8:11], 0\n"
    "buffer_load_dwordx4 a[80:83], v232, s[28:31], s37 offen offset:2048 sc1\n"
    "s_waitcnt vmcnt(11)\n"
    "v_mfma_f32_16x16x32_bf16 v[104:107], v[136:139], a[12:15], 0\n"
    "v_mfma_f32_16x16x32_bf16 v[100:103], v[132:135], a[12:15], 0\n"
    "v_mfma_f32_16x16x32_bf16 v[96:99], v[128:131], a[12:15], 0\n"
    "s_waitcnt lgkmcnt(6)\n"
    "v_mfma_f32_16x16x32_bf16 v[12:15], v[140:143], a[0:3], 0\n"
    "ds_read_b64_tr_b16 v[164:165], v246 offset:8264\n"
    "ds_read_b64_tr_b16 v[166:167], v246 offset:12392\n"
    "v_mfma_f32_16x16x32_bf16 v[16:19], v[144:147], a[0:3], 0\n"
    "s_mov_b32 s37, 21504\n"
    "buffer_load_dwordx4 a[52:55], v232, s[28:31], s37 offen offset:0 sc1\n"
    "v_mfma_f32_16x16x32_bf16 v[20:23], v[148:151], a[0:3], 0\n"
    "ds_read_b64_tr_b16 v[168:169], v246 offset:8512\n"
    "ds_read_b64_tr_b16 v[170:171], v246 offset:12640\n"
    "v_mfma_f32_16x16x32_bf16 v[52:55], v[148:151], a[4:7], 0\n"
    "ds_read_b64_tr_b16 v[172:173], v246 offset:8520\n"
    "ds_read_b64_tr_b16 v[174:175], v246 offset:12648\n"
    "v_mfma_f32_16x16x32_bf16 v[48:51], v[144:147], a[4:7], 0\n"
    "buffer_load_dwordx4 a[68:71], v232, s[28:31], s37 offen offset:1024 sc1\n"
    "v_mfma_f32_16x16x32_bf16 v[44:47], v[140:143], a[4:7], 0\n"
    "v_mfma_f32_16x16x32_bf16 v[76:79], v[140:143], a[8:11], 0\n"
    "v_mfma_f32_16x16x32_bf16 v[80:83], v[144:147], a[8:11], 0\n"
    "v_mfma_f32_16x16x32_bf16 v[84:87], v[148:151], a[8:11], 0\n"
    "buffer_load_dwordx4 a[84:87], v232, s[28:31], s37 offen offset:2048 sc1\n"
    "v_mfma_f32_16x16x32_bf16 v[116:119], v[148:151], a[12:15], 0\n"
    "v_mfma_f32_16x16x32_bf16 v[112:115], v[144:147], a[12:15], 0\n"
    "v_mfma_f32_16x16x32_bf16 v[108:111], v[140:143], a[12:15], 0\n"
    "s_waitcnt lgkmcnt(6)\n"
    "v_mfma_f32_16x16x32_bf16 v[24:27], v[152:155], a[0:3], 0\n"
    "ds_read_b64_tr_b16 v[128:129], v246 offset:8768\n"
    "ds_read_b64_tr_b16 v[130:131], v246 offset:12896\n"
    "v_mfma_f32_16x16x32_bf16 v[28:31], v[156:159], a[0:3], 0\n"
    "s_mov_b32 s37, 39936\n"
    "buffer_load_dwordx4 a[56:59], v232, s[28:31], s37 offen offset:0 sc1\n"
    "s_waitcnt vmcnt(14)\n"
    "v_mfma_f32_16x16x32_bf16 v[0:3], v[160:163], a[16:19], v[0:3]\n"
    "ds_read_b64_tr_b16 v[132:133], v246 offset:8776\n"
    "ds_read_b64_tr_b16 v[134:135], v246 offset:12904\n"
    "s_waitcnt vmcnt(13)\n"
    "v_mfma_f32_16x16x32_bf16 v[32:35], v[160:163], a[20:23], v[32:35]\n"
    "ds_read_b64_tr_b16 v[136:137], v246 offset:9024\n"
    "ds_read_b64_tr_b16 v[138:139], v246 offset:13152\n"
    "v_mfma_f32_16x16x32_bf16 v[60:63], v[156:159], a[4:7], 0\n"
    "buffer_load_dwordx4 a[72:75], v232, s[28:31], s37 offen offset:1024 sc1\n"
    "v_mfma_f32_16x16x32_bf16 v[56:59], v[152:155], a[4:7], 0\n"
    "v_mfma_f32_16x16x32_bf16 v[88:91], v[152:155], a[8:11], 0\n"
    "v_mfma_f32_16x16x32_bf16 v[92:95], v[156:159], a[8:11], 0\n"
    "s_waitcnt vmcnt(13)\n"
    "v_mfma_f32_16x16x32_bf16 v[64:67], v[160:163], a[24:27], v[64:67]\n"
    "buffer_load_dwordx4 a[88:91], v232, s[28:31], s37 offen offset:2048 sc1\n"
    "s_waitcnt vmcnt(13)\n"
    "v_mfma_f32_16x16x32_bf16 v[96:99], v[160:163], a[28:31], v[96:99]\n"
    "v_mfma_f32_16x16x32_bf16 v[124:127], v[156:159], a[12:15], 0\n"
    "v_mfma_f32_16x16x32_bf16 v[120:123], v[152:155], a[12:15], 0\n"
    "s_waitcnt lgkmcnt(6)\n"
    "v_mfma_f32_16x16x32_bf16 v[4:7], v[164:167], a[16:19], v[4:7]\n"
    "ds_read_b64_tr_b16 v[140:141], v246 offset:9032\n"
    "ds_read_b64_tr_b16 v[142:143], v246 offset:13160\n"
    "v_mfma_f32_16x16x32_bf16 v[8:11], v[168:171], a[16:19], v[8:11]\n"
    "s_mov_b32 s37, 58368\n"
    "buffer_load_dwordx4 a[60:63], v232, s[28:31], s37 offen offset:0 sc1\n"
    "v_mfma_f32_16x16x32_bf16 v[12:15], v[172:175], a[16:19], v[12:15]\n"
    "ds_read_b64_tr_b16 v[144:145], v246 offset:16512\n"
    "ds_read_b64_tr_b16 v[146:147], v246 offset:20640\n"
    "v_mfma_f32_16x16x32_bf16 v[44:47], v[172:175], a[20:23], v[44:47]\n"
    "ds_read_b64_tr_b16 v[148:149], v246 offset:16520\n"
    "ds_read_b64_tr_b16 v[150:151], v246 offset:20648\n"
    "v_mfma_f32_16x16x32_bf16 v[40:43], v[168:171], a[20:23], v[40:43]\n"
    "buffer_load_dwordx4 a[76:79], v232, s[28:31], s37 offen offset:1024 sc1\n"
    "v_mfma_f32_16x16x32_bf16 v[36:39], v[164:167], a[20:23], v[36:39]\n"
    "v_mfma_f32_16x16x32_bf16 v[68:71], v[164:167], a[24:27], v[68:71]\n"
    "v_mfma_f32_16x16x32_bf16 v[72:75], v[168:171], a[24:27], v[72:75]\n"
    "v_mfma_f32_16x16x32_bf16 v[76:79], v[172:175], a[24:27], v[76:79]\n"
    "buffer_load_dwordx4 a[92:95], v232, s[28:31], s37 offen offset:2048 sc1\n"
    "v_mfma_f32_16x16x32_bf16 v[108:111], v[172:175], a[28:31], v[108:111]\n"
    "v_mfma_f32_16x16x32_bf16 v[104:107], v[168:171], a[28:31], v[104:107]\n"
    "v_mfma_f32_16x16x32_bf16 v[100:103], v[164:167], a[28:31], v[100:103]\n"
    "s_waitcnt lgkmcnt(6)\n"
    "v_mfma_f32_16x16x32_bf16 v[16:19], v[128:131], a[16:19], v[16:19]\n"
    "ds_read_b64_tr_b16 v[152:153], v246 offset:16768\n"
    "ds_read_b64_tr_b16 v[154:155], v246 offset:20896\n"
    "v_mfma_f32_16x16x32_bf16 v[20:23], v[132:135], a[16:19], v[20:23]\n"
    "s_mov_b32 s37, 6144\n"
    "buffer_load_dwordx4 a[96:99], v232, s[28:31], s37 offen offset:0 sc1\n"
    "v_mfma_f32_16x16x32_bf16 v[24:27], v[136:139], a[16:19], v[24:27]\n"
    "ds_read_b64_tr_b16 v[156:157], v246 offset:16776\n"
    "ds_read_b64_tr_b16 v[158:159], v246 offset:20904\n"
    "v_mfma_f32_16x16x32_bf16 v[56:59], v[136:139], a[20:23], v[56:59]\n"
    "ds_read_b64_tr_b16 v[160:161], v246 offset:17024\n"
    "ds_read_b64_tr_b16 v[162:163], v246 offset:21152\n"
    "v_mfma_f32_16x16x32_bf16 v[52:55], v[132:135], a[20:23], v[52:55]\n"
    "buffer_load_dwordx4 a[112:115], v232, s[28:31], s37 offen offset:1024 sc1\n"
    "v_mfma_f32_16x16x32_bf16 v[48:51], v[128:131], a[20:23], v[48:51]\n"
    "v_mfma_f32_16x16x32_bf16 v[80:83], v[128:131], a[24:27], v[80:83]\n"
    "v_mfma_f32_16x16x32_bf16 v[84:87], v[132:135], a[24:27], v[84:87]\n"
    "v_mfma_f32_16x16x32_bf16 v[88:91], v[136:139], a[24:27], v[88:91]\n"
    "buffer_load_dwordx4 a[128:131], v232, s[28:31], s37 offen offset:2048 sc1\n"
    "v_mfma_f32_16x16x32_bf16 v[120:123], v[136:139], a[28:31], v[120:123]\n"
    "v_mfma_f32_16x16x32_bf16 v[116:119], v[132:135], a[28:31], v[116:119]\n"
    "v_mfma_f32_16x16x32_bf16 v[112:115], v[128:131], a[28:31], v[112:115]\n"
    "s_waitcnt lgkmcnt(6)\n"
    "v_mfma_f32_16x16x32_bf16 v[28:31], v[140:143], a[16:19], v[28:31]\n"
    "ds_read_b64_tr_b16 v[164:165], v246 offset:17032\n"
    "ds_read_b64_tr_b16 v[166:167], v246 offset:21160\n"
    "s_waitcnt vmcnt(18)\n"
    "v_mfma_f32_16x16x32_bf16 v[0:3], v[144:147], a[32:35], v[0:3]\n"
    "s_mov_b32 s37, 24576\n"
    "buffer_load_dwordx4 a[100:103], v232, s[28:31], s37 offen offset:0 sc1\n"
    "v_mfma_f32_16x16x32_bf16 v[4:7], v[148:151], a[32:35], v[4:7]\n"
    "ds_read_b64_tr_b16 v[168:169], v246 offset:17280\n"
    "ds_read_b64_tr_b16 v[170:171], v246 offset:21408\n"
    "s_waitcnt vmcnt(18)\n"
    "v_mfma_f32_16x16x32_bf16 v[36:39], v[148:151], a[36:39], v[36:39]\n"
    "ds_read_b64_tr_b16 v[172:173], v246 offset:17288\n"
    "ds_read_b64_tr_b16 v[174:175], v246 offset:21416\n"
    "v_mfma_f32_16x16x32_bf16 v[32:35], v[144:147], a[36:39], v[32:35]\n"
    "buffer_load_dwordx4 a[116:119], v232, s[28:31], s37 offen offset:1024 sc1\n"
    "v_mfma_f32_16x16x32_bf16 v[60:63], v[140:143], a[20:23], v[60:63]\n"
    "v_mfma_f32_16x16x32_bf16 v[92:95], v[140:143], a[24:27], v[92:95]\n"
    "s_waitcnt vmcnt(18)\n"
    "v_mfma_f32_16x16x32_bf16 v[64:67], v[144:147], a[40:43], v[64:67]\n"
    "v_mfma_f32_16x16x32_bf16 v[68:71], v[148:151], a[40:43], v[68:71]\n"
    "buffer_load_dwordx4 a[132:135], v232, s[28:31], s37 offen offset:2048 sc1\n"
    "s_waitcnt vmcnt(18)\n"
    "v_mfma_f32_16x16x32_bf16 v[100:103], v[148:151], a[44:47], v[100:103]\n"
    "v_mfma_f32_16x16x32_bf16 v[96:99], v[144:147], a[44:47], v[96:99]\n"
    "v_mfma_f32_16x16x32_bf16 v[124:127], v[140:143], a[28:31], v[124:127]\n"
    "s_waitcnt lgkmcnt(6)\n"
    "v_mfma_f32_16x16x32_bf16 v[8:11], v[152:155], a[32:35], v[8:11]\n"
    "ds_read_b64_tr_b16 v[128:129], v246 offset:24768\n"
    "ds_read_b64_tr_b16 v[130:131], v246 offset:28896\n"
    "v_mfma_f32_16x16x32_bf16 v[12:15], v[156:159], a[32:35], v[12:15]\n"
    "s_mov_b32 s37, 43008\n"
    "buffer_load_dwordx4 a[104:107], v232, s[28:31], s37 offen offset:0 sc1\n"
    "v_mfma_f32_16x16x32_bf16 v[16:19], v[160:163], a[32:35], v[16:19]\n"
    "ds_read_b64_tr_b16 v[132:133], v246 offset:33024\n"
    "ds_read_b64_tr_b16 v[134:135], v246 offset:37152\n"
    "v_mfma_f32_16x16x32_bf16 v[48:51], v[160:163], a[36:39], v[48:51]\n"
    "ds_read_b64_tr_b16 v[136:137], v246 offset:41280\n"
    "ds_read_b64_tr_b16 v[138:139], v246 offset:45408\n"
    "v_mfma_f32_16x16x32_bf16 v[44:47], v[156:159], a[36:39], v[44:47]\n"
    "buffer_load_dwordx4 a[120:123], v232, s[28:31], s37 offen offset:1024 sc1\n"
    "v_mfma_f32_16x16x32_bf16 v[40:43], v[152:155], a[36:39], v[40:43]\n"
    "v_mfma_f32_16x16x32_bf16 v[72:75], v[152:155], a[40:43], v[72:75]\n"
    "v_mfma_f32_16x16x32_bf16 v[76:79], v[156:159], a[40:43], v[76:79]\n"
    "v_mfma_f32_16x16x32_bf16 v[80:83], v[160:163], a[40:43], v[80:83]\n"
    "buffer_load_dwordx4 a[136:139], v232, s[28:31], s37 offen offset:2048 sc1\n"
    "v_mfma_f32_16x16x32_bf16 v[112:115], v[160:163], a[44:47], v[112:115]\n"
    "v_mfma_f32_16x16x32_bf16 v[108:111], v[156:159], a[44:47], v[108:111]\n"
    "v_mfma_f32_16x16x32_bf16 v[104:107], v[152:155], a[44:47], v[104:107]\n"
    "s_waitcnt lgkmcnt(6)\n"
    "v_mfma_f32_16x16x32_bf16 v[20:23], v[164:167], a[32:35], v[20:23]\n"
    "ds_read_b64_tr_b16 v[140:141], v246 offset:24776\n"
    "ds_read_b64_tr_b16 v[142:143], v246 offset:28904\n"
    "v_mfma_f32_16x16x32_bf16 v[24:27], v[168:171], a[32:35], v[24:27]\n"
    "s_mov_b32 s37, 61440\n"
    "buffer_load_dwordx4 a[108:111], v232, s[28:31], s37 offen offset:0 sc1\n"
    "v_mfma_f32_16x16x32_bf16 v[28:31], v[172:175], a[32:35], v[28:31]\n"
    "ds_read_b64_tr_b16 v[144:145], v246 offset:33032\n"
    "ds_read_b64_tr_b16 v[146:147], v246 offset:37160\n"
    "v_mfma_f32_16x16x32_bf16 v[60:63], v[172:175], a[36:39], v[60:63]\n"
    "ds_read_b64_tr_b16 v[148:149], v246 offset:41288\n"
    "ds_read_b64_tr_b16 v[150:151], v246 offset:45416\n"
    "v_mfma_f32_16x16x32_bf16 v[56:59], v[168:171], a[36:39], v[56:59]\n"
    "buffer_load_dwordx4 a[124:127], v232, s[28:31], s37 offen offset:1024 sc1\n"
    "v_mfma_f32_16x16x32_bf16 v[52:55], v[164:167], a[36:39], v[52:55]\n"
    "v_mfma_f32_16x16x32_bf16 v[84:87], v[164:167], a[40:43], v[84:87]\n"
    "v_mfma_f32_16x16x32_bf16 v[88:91], v[168:171], a[40:43], v[88:91]\n"
    "v_mfma_f32_16x16x32_bf16 v[92:95], v[172:175], a[40:43], v[92:95]\n"
    "buffer_load_dwordx4 a[140:143], v232, s[28:31], s37 offen offset:2048 sc1\n"
    "v_mfma_f32_16x16x32_bf16 v[124:127], v[172:175], a[44:47], v[124:127]\n"
    "v_mfma_f32_16x16x32_bf16 v[120:123], v[168:171], a[44:47], v[120:123]\n"
    "v_mfma_f32_16x16x32_bf16 v[116:119], v[164:167], a[44:47], v[116:119]\n"
    "s_waitcnt vmcnt(12) lgkmcnt(6)\n"
    "v_mfma_f32_16x16x32_bf16 v[0:3], v[128:131], a[48:51], v[0:3]\n"
    "ds_read_b64_tr_b16 v[152:153], v246 offset:25024\n"
    "ds_read_b64_tr_b16 v[154:155], v246 offset:29152\n"
    "v_mfma_f32_16x16x32_bf16 v[0:3], v[132:135], a[64:67], v[0:3]\n"
    "s_mov_b32 s37, 9216\n"
    "buffer_load_dwordx4 a[144:147], v232, s[28:31], s37 offen offset:0 sc1\n"
    "v_mfma_f32_16x16x32_bf16 v[0:3], v[136:139], a[80:83], v[0:3]\n"
    "ds_read_b64_tr_b16 v[156:157], v246 offset:33280\n"
    "ds_read_b64_tr_b16 v[158:159], v246 offset:37408\n"
    "v_mfma_f32_16x16x32_bf16 v[32:35], v[136:139], a[84:87], v[32:35]\n"
    "ds_read_b64_tr_b16 v[160:161], v246 offset:41536\n"
    "ds_read_b64_tr_b16 v[162:163], v246 offset:45664\n"
    "v_mfma_f32_16x16x32_bf16 v[32:35], v[132:135], a[68:71], v[32:35]\n"
    "buffer_load_dwordx4 a[160:163], v232, s[28:31], s37 offen offset:1024 sc1\n"
    "v_mfma_f32_16x16x32_bf16 v[32:35], v[128:131], a[52:55], v[32:35]\n"
    "v_mfma_f32_16x16x32_bf16 v[64:67], v[128:131], a[56:59], v[64:67]\n"
    "v_mfma_f32_16x16x32_bf16 v[64:67], v[132:135], a[72:75], v[64:67]\n"
    "v_mfma_f32_16x16x32_bf16 v[64:67], v[136:139], a[88:91], v[64:67]\n"
    "buffer_load_dwordx4 a[176:179], v232, s[28:31], s37 offen offset:2048 sc1\n"
    "v_mfma_f32_16x16x32_bf16 v[96:99], v[136:139], a[92:95], v[96:99]\n"
    "v_mfma_f32_16x16x32_bf16 v[96:99], v[132:135], a[76:79], v[96:99]\n"
    "v_mfma_f32_16x16x32_bf16 v[96:99], v[128:131], a[60:63], v[96:99]\n"
    "s_waitcnt lgkmcnt(6)\n"
    "v_mfma_f32_16x16x32_bf16 v[100:103], v[140:143], a[60:63], v[100:103]\n"
    "ds_read_b64_tr_b16 v[164:165], v246 offset:25032\n"
    "ds_read_b64_tr_b16 v[166:167], v246 offset:29160\n"
    "v_mfma_f32_16x16x32_bf16 v[100:103], v[144:147], a[76:79], v[100:103]\n"
    "s_mov_b32 s37, 27648\n"
    "buffer_load_dwordx4 a[148:151], v232, s[28:31], s37 offen offset:0 sc1\n"
    "v_mfma_f32_16x16x32_bf16 v[100:103], v[148:151], a[92:95], v[100:103]\n"
    "ds_read_b64_tr_b16 v[168:169], v246 offset:33288\n"
    "ds_read_b64_tr_b16 v[170:171], v246 offset:37416\n"
    "v_mfma_f32_16x16x32_bf16 v[68:71], v[148:151], a[88:91], v[68:71]\n"
    "ds_read_b64_tr_b16 v[172:173], v246 offset:41544\n"
    "ds_read_b64_tr_b16 v[174:175], v246 offset:45672\n"
    "v_mfma_f32_16x16x32_bf16 v[68:71], v[144:147], a[72:75], v[68:71]\n"
    "v_mfma_f32_16x16x32_bf16 v[68:71], v[140:143], a[56:59], v[68:71]\n"
    "v_mfma_f32_16x16x32_bf16 v[36:39], v[140:143], a[52:55], v[36:39]\n"
    "buffer_load_dwordx4 a[164:167], v232, s[28:31], s37 offen offset:1024 sc1\n"
    "v_mfma_f32_16x16x32_bf16 v[36:39], v[144:147], a[68:71], v[36:39]\n"
    "v_mfma_f32_16x16x32_bf16 v[36:39], v[148:151], a[84:87], v[36:39]\n"
    "v_mfma_f32_16x16x32_bf16 v[4:7], v[148:151], a[80:83], v[4:7]\n"
    "v_mfma_f32_16x16x32_bf16 v[4:7], v[144:147], a[64:67], v[4:7]\n"
    "v_mfma_f32_16x16x32_bf16 v[4:7], v[140:143], a[48:51], v[4:7]\n"
    "L_chain1x3_mk:\n"
    "s_cbranch_execz L_pad1_3_mk\n"
    "s_waitcnt lgkmcnt(6)\n"
    "v_mfma_f32_16x16x32_bf16 v[8:11], v[152:155], a[48:51], v[8:11]\n"
    "ds_read_b64_tr_b16 v[128:129], v246 offset:25280\n"
    "ds_read_b64_tr_b16 v[130:131], v246 offset:29408\n"
    "v_mfma_f32_16x16x32_bf16 v[8:11], v[156:159], a[64:67], v[8:11]\n"
    "buffer_load_dwordx4 a[180:183], v232, s[28:31], s37 offen offset:2048 sc1\n"
    "v_mfma_f32_16x16x32_bf16 v[8:11], v[160:163], a[80:83], v[8:11]\n"
    "ds_read_b64_tr_b16 v[132:133], v246 offset:33536\n"
    "ds_read_b64_tr_b16 v[134:135], v246 offset:37664\n"
    "v_mfma_f32_16x16x32_bf16 v[40:43], v[160:163], a[84:87], v[40:43]\n"
    "ds_read_b64_tr_b16 v[136:137], v246 offset:41792\n"
    "ds_read_b64_tr_b16 v[138:139], v246 offset:45920\n"
    "v_mfma_f32_16x16x32_bf16 v[40:43], v[156:159], a[68:71], v[40:43]\n"
    "v_mfma_f32_16x16x32_bf16 v[40:43], v[152:155], a[52:55], v[40:43]\n"
    "v_mfma_f32_16x16x32_bf16 v[72:75], v[152:155], a[56:59], v[72:75]\n"
    "s_mov_b32 s37, 46080\n"
    "buffer_load_dwordx4 a[152:155], v232, s[28:31], s37 offen offset:0 sc1\n"
    "v_mfma_f32_16x16x32_bf16 v[72:75], v[156:159], a[72:75], v[72:75]\n"
    "v_mfma_f32_16x16x32_bf16 v[72:75], v[160:163], a[88:91], v[72:75]\n"
    "v_mfma_f32_16x16x32_bf16 v[104:107], v[160:163], a[92:95], v[104:107]\n"
    "v_mfma_f32_16x16x32_bf16 v[104:107], v[156:159], a[76:79], v[104:107]\n"
    "v_mfma_f32_16x16x32_bf16 v[104:107], v[152:155], a[60:63], v[104:107]\n"
    "s_waitcnt lgkmcnt(6)\n"
    "v_mfma_f32_16x16x32_bf16 v[108:111], v[164:167], a[60:63], v[108:111]\n"
    "ds_read_b64_tr_b16 v[140:141], v246 offset:25288\n"
    "ds_read_b64_tr_b16 v[142:143], v246 offset:29416\n"
    "v_mfma_f32_16x16x32_bf16 v[108:111], v[168:171], a[76:79], v[108:111]\n"
    "buffer_load_dwordx4 a[168:171], v232, s[28:31], s37 offen offset:1024 sc1\n"
    "v_mfma_f32_16x16x32_bf16 v[108:111], v[172:175], a[92:95], v[108:111]\n"
    "ds_read_b64_tr_b16 v[144:145], v246 offset:33544\n"
    "ds_read_b64_tr_b16 v[146:147], v246 offset:37672\n"
    "v_mfma_f32_16x16x32_bf16 v[76:79], v[172:175], a[88:91], v[76:79]\n"
    "ds_read_b64_tr_b16 v[148:149], v246 offset:41800\n"
    "ds_read_b64_tr_b16 v[150:151], v246 offset:45928\n"
    "v_mfma_f32_16x16x32_bf16 v[76:79], v[168:171], a[72:75], v[76:79]\n"
    "v_mfma_f32_16x16x32_bf16 v[76:79], v[164:167], a[56:59], v[76:79]\n"
    "v_mfma_f32_16x16x32_bf16 v[44:47], v[164:167], a[52:55], v[44:47]\n"
    "buffer_load_dwordx4 a[184:187], v232, s[28:31], s37 offen offset:2048 sc1\n"
    "v_mfma_f32_16x16x32_bf16 v[44:47], v[168:171], a[68:71], v[44:47]\n"
    "v_mfma_f32_16x16x32_bf16 v[44:47], v[172:175], a[84:87], v[44:47]\n"
    "v_mfma_f32_16x16x32_bf16 v[12:15], v[172:175], a[80:83], v[12:15]\n"
    "v_mfma_f32_16x16x32_bf16 v[12:15], v[168:171], a[64:67], v[12:15]\n"
    "v_mfma_f32_16x16x32_bf16 v[12:15], v[164:167], a[48:51], v[12:15]\n"
    "s_waitcnt lgkmcnt(6)\n"
    "v_mfma_f32_16x16x32_bf16 v[16:19], v[128:131], a[48:51], v[16:19]\n"
    "ds_read_b64_tr_b16 v[152:153], v246 offset:25536\n"
    "ds_read_b64_tr_b16 v[154:155], v246 offset:29664\n"
    "v_mfma_f32_16x16x32_bf16 v[16:19], v[132:135], a[64:67], v[16:19]\n"
    "s_mov_b32 s37, 64512\n"
    "buffer_load_dwordx4 a[156:159], v232, s[28:31], s37 offen offset:0 sc1\n"
    "v_mfma_f32_16x16x32_bf16 v[16:19], v[136:139], a[80:83], v[16:19]\n"
    "ds_read_b64_tr_b16 v[156:157], v246 offset:33792\n"
    "ds_read_b64_tr_b16 v[158:159], v246 offset:37920\n"
    "v_mfma_f32_16x16x32_bf16 v[48:51], v[136:139], a[84:87], v[48:51]\n"
    "ds_read_b64_tr_b16 v[160:161], v246 offset:42048\n"
    "ds_read_b64_tr_b16 v[162:163], v246 offset:46176\n"
    "v_mfma_f32_16x16x32_bf16 v[48:51], v[132:135], a[68:71], v[48:51]\n"
    "buffer_load_dwordx4 a[172:175], v232, s[28:31], s37 offen offset:1024 sc1\n"
    "v_mfma_f32_16x16x32_bf16 v[48:51], v[128:131], a[52:55], v[48:51]\n"
    "v_mfma_f32_16x16x32_bf16 v[80:83], v[128:131], a[56:59], v[80:83]\n"
    "v_mfma_f32_16x16x32_bf16 v[80:83], v[132:135], a[72:75], v[80:83]\n"
    "v_mfma_f32_16x16x32_bf16 v[80:83], v[136:139], a[88:91], v[80:83]\n"
    "buffer_load_dwordx4 a[188:191], v232, s[28:31], s37 offen offset:2048 sc1\n"
    "v_mfma_f32_16x16x32_bf16 v[112:115], v[136:139], a[92:95], v[112:115]\n"
    "v_mfma_f32_16x16x32_bf16 v[112:115], v[132:135], a[76:79], v[112:115]\n"
    "v_mfma_f32_16x16x32_bf16 v[112:115], v[128:131], a[60:63], v[112:115]\n"
    "s_waitcnt lgkmcnt(6)\n"
    "v_mfma_f32_16x16x32_bf16 v[116:119], v[140:143], a[60:63], v[116:119]\n"
    "s_mul_i32 s93, s88, 6192\n"
    "s_mul_i32 s94, s88, 196608\n"
    "s_add_u32 m0, s93, 123840\n"
    "v_mfma_f32_16x16x32_bf16 v[116:119], v[144:147], a[76:79], v[116:119]\n"
    "s_add_u32 s95, s94, 3932160\n"
    "buffer_load_dwordx4 v245, s[48:51], s95 offen offset:0 lds\n"
    "v_mfma_f32_16x16x32_bf16 v[116:119], v[148:151], a[92:95], v[116:119]\n"
    "ds_read_b64_tr_b16 v[164:165], v246 offset:25544\n"
    "ds_read_b64_tr_b16 v[166:167], v246 offset:29672\n"
    "v_mfma_f32_16x16x32_bf16 v[84:87], v[148:151], a[88:91], v[84:87]\n"
    "ds_read_b64_tr_b16 v[168:169], v246 offset:33800\n"
    "ds_read_b64_tr_b16 v[170:171], v246 offset:37928\n"
    "v_mfma_f32_16x16x32_bf16 v[84:87], v[144:147], a[72:75], v[84:87]\n"
    "ds_read_b64_tr_b16 v[172:173], v246 offset:42056\n"
    "ds_read_b64_tr_b16 v[174:175], v246 offset:46184\n"
    "v_mfma_f32_16x16x32_bf16 v[84:87], v[140:143], a[56:59], v[84:87]\n"
    "v_mfma_f32_16x16x32_bf16 v[52:55], v[140:143], a[52:55], v[52:55]\n"
    "s_add_u32 s95, s94, 3963896\n"
    "buffer_load_dwordx4 v245, s[48:51], s95 offen offset:1032 lds\n"
    "v_mfma_f32_16x16x32_bf16 v[52:55], v[144:147], a[68:71], v[52:55]\n"
    "v_mfma_f32_16x16x32_bf16 v[52:55], v[148:151], a[84:87], v[52:55]\n"
    "v_mfma_f32_16x16x32_bf16 v[20:23], v[148:151], a[80:83], v[20:23]\n"
    "v_mfma_f32_16x16x32_bf16 v[20:23], v[144:147], a[64:67], v[20:23]\n"
    "v_mfma_f32_16x16x32_bf16 v[20:23], v[140:143], a[48:51], v[20:23]\n"
    "s_waitcnt lgkmcnt(6)\n"
    "v_mfma_f32_16x16x32_bf16 v[24:27], v[152:155], a[48:51], v[24:27]\n"
    "ds_read_b64_tr_b16 v[128:129], v246 offset:49536\n"
    "ds_read_b64_tr_b16 v[130:131], v246 offset:53664\n"
    "v_mfma_f32_16x16x32_bf16 v[24:27], v[156:159], a[64:67], v[24:27]\n"
    "s_add_u32 s95, s94, 3995632\n"
    "buffer_load_dwordx4 v245, s[48:51], s95 offen offset:2064 lds\n"
    "v_mfma_f32_16x16x32_bf16 v[24:27], v[160:163], a[80:83], v[24:27]\n"
    "ds_read_b64_tr_b16 v[132:133], v246 offset:57792\n"
    "ds_read_b64_tr_b16 v[134:135], v246 offset:61920\n"
    "v_mfma_f32_16x16x32_bf16 v[56:59], v[160:163], a[84:87], v[56:59]\n"
    "ds_read_b64_tr_b16 v[136:137], v247 offset:512\n"
    "ds_read_b64_tr_b16 v[138:139], v247 offset:4640\n"
    "v_mfma_f32_16x16x32_bf16 v[56:59], v[156:159], a[68:71], v[56:59]\n"
    "v_mfma_f32_16x16x32_bf16 v[56:59], v[152:155], a[52:55], v[56:59]\n"
    "s_add_u32 m0, s93, 126936\n"
    "v_mfma_f32_16x16x32_bf16 v[88:91], v[152:155], a[56:59], v[88:91]\n"
    "s_add_u32 s95, s94, 4030464\n"
    "buffer_load_dwordx4 v245, s[48:51], s95 offen offset:0 lds\n"
    "v_mfma_f32_16x16x32_bf16 v[88:91], v[156:159], a[72:75], v[88:91]\n"
    "v_mfma_f32_16x16x32_bf16 v[88:91], v[160:163], a[88:91], v[88:91]\n"
    "v_mfma_f32_16x16x32_bf16 v[120:123], v[160:163], a[92:95], v[120:123]\n"
    "v_mfma_f32_16x16x32_bf16 v[120:123], v[156:159], a[76:79], v[120:123]\n"
    "v_mfma_f32_16x16x32_bf16 v[120:123], v[152:155], a[60:63], v[120:123]\n"
    "s_waitcnt lgkmcnt(6)\n"
    "v_mfma_f32_16x16x32_bf16 v[124:127], v[164:167], a[60:63], v[124:127]\n"
    "ds_read_b64_tr_b16 v[140:141], v246 offset:49544\n"
    "ds_read_b64_tr_b16 v[142:143], v246 offset:53672\n"
    "v_mfma_f32_16x16x32_bf16 v[124:127], v[168:171], a[76:79], v[124:127]\n"
    "s_add_u32 s95, s94, 4062200\n"
    "buffer_load_dwordx4 v245, s[48:51], s95 offen offset:1032 lds\n"
    "v_mfma_f32_16x16x32_bf16 v[124:127], v[172:175], a[92:95], v[124:127]\n"
    "ds_read_b64_tr_b16 v[144:145], v246 offset:57800\n"
    "ds_read_b64_tr_b16 v[146:147], v246 offset:61928\n"
    "v_mfma_f32_16x16x32_bf16 v[92:95], v[172:175], a[88:91], v[92:95]\n"
    "ds_read_b64_tr_b16 v[148:149], v247 offset:520\n"
    "ds_read_b64_tr_b16 v[150:151], v247 offset:4648\n"
    "v_mfma_f32_16x16x32_bf16 v[92:95], v[168:171], a[72:75], v[92:95]\n"
    "v_mfma_f32_16x16x32_bf16 v[92:95], v[164:167], a[56:59], v[92:95]\n"
    "v_mfma_f32_16x16x32_bf16 v[60:63], v[164:167], a[52:55], v[60:63]\n"
    "s_add_u32 s95, s94, 4093936\n"
    "buffer_load_dwordx4 v245, s[48:51], s95 offen offset:2064 lds\n"
    "v_mfma_f32_16x16x32_bf16 v[60:63], v[168:171], a[68:71], v[60:63]\n"
    "v_mfma_f32_16x16x32_bf16 v[60:63], v[172:175], a[84:87], v[60:63]\n"
    "v_mfma_f32_16x16x32_bf16 v[28:31], v[172:175], a[80:83], v[28:31]\n"
    "v_mfma_f32_16x16x32_bf16 v[28:31], v[168:171], a[64:67], v[28:31]\n"
    "v_mfma_f32_16x16x32_bf16 v[28:31], v[164:167], a[48:51], v[28:31]\n"
    "s_waitcnt vmcnt(18) lgkmcnt(6)\n"
    "v_mfma_f32_16x16x32_bf16 v[0:3], v[128:131], a[96:99], v[0:3]\n"
    "ds_read_b64_tr_b16 v[152:153], v246 offset:49792\n"
    "ds_read_b64_tr_b16 v[154:155], v246 offset:53920\n"
    "v_mfma_f32_16x16x32_bf16 v[0:3], v[132:135], a[112:115], v[0:3]\n"
    "s_mov_b32 s37, 12288\n"
    "buffer_load_dwordx4 a[192:195], v232, s[28:31], s37 offen offset:0 sc1\n"
    "v_mfma_f32_16x16x32_bf16 v[0:3], v[136:139], a[128:131], v[0:3]\n"
    "ds_read_b64_tr_b16 v[156:157], v246 offset:58048\n"
    "ds_read_b64_tr_b16 v[158:159], v246 offset:62176\n"
    "v_mfma_f32_16x16x32_bf16 v[32:35], v[136:139], a[132:135], v[32:35]\n"
    "ds_read_b64_tr_b16 v[160:161], v247 offset:768\n"
    "ds_read_b64_tr_b16 v[162:163], v247 offset:4896\n"
    "v_mfma_f32_16x16x32_bf16 v[32:35], v[132:135], a[116:119], v[32:35]\n"
    "v_mfma_f32_16x16x32_bf16 v[32:35], v[128:131], a[100:103], v[32:35]\n"
    "v_mfma_f32_16x16x32_bf16 v[64:67], v[128:131], a[104:107], v[64:67]\n"
    "buffer_load_dwordx4 a[208:211], v232, s[28:31], s37 offen offset:1024 sc1\n"
    "v_mfma_f32_16x16x32_bf16 v[64:67], v[132:135], a[120:123], v[64:67]\n"
    "v_mfma_f32_16x16x32_bf16 v[64:67], v[136:139], a[136:139], v[64:67]\n"
    "v_mfma_f32_16x16x32_bf16 v[96:99], v[136:139], a[140:143], v[96:99]\n"
    "v_mfma_f32_16x16x32_bf16 v[96:99], v[132:135], a[124:127], v[96:99]\n"
    "v_mfma_f32_16x16x32_bf16 v[96:99], v[128:131], a[108:111], v[96:99]\n"
    "s_waitcnt lgkmcnt(6)\n"
    "v_mfma_f32_16x16x32_bf16 v[100:103], v[140:143], a[108:111], v[100:103]\n"
    "ds_read_b64_tr_b16 v[164:165], v246 offset:49800\n"
    "ds_read_b64_tr_b16 v[166:167], v246 offset:53928\n"
    "v_mfma_f32_16x16x32_bf16 v[100:103], v[144:147], a[124:127], v[100:103]\n"
    "buffer_load_dwordx4 a[224:227], v232, s[28:31], s37 offen offset:2048 sc1\n"
    "v_mfma_f32_16x16x32_bf16 v[100:103], v[148:151], a[140:143], v[100:103]\n"
    "ds_read_b64_tr_b16 v[168:169], v246 offset:58056\n"
    "ds_read_b64_tr_b16 v[170:171], v246 offset:62184\n"
    "v_mfma_f32_16x16x32_bf16 v[68:71], v[148:151], a[136:139], v[68:71]\n"
    "ds_read_b64_tr_b16 v[172:173], v247 offset:776\n"
    "ds_read_b64_tr_b16 v[174:175], v247 offset:4904\n"
    "v_mfma_f32_16x16x32_bf16 v[68:71], v[144:147], a[120:123], v[68:71]\n"
    "v_mfma_f32_16x16x32_bf16 v[68:71], v[140:143], a[104:107], v[68:71]\n"
    "v_mfma_f32_16x16x32_bf16 v[36:39], v[140:143], a[100:103], v[36:39]\n"
    "s_mov_b32 s37, 30720\n"
    "buffer_load_dwordx4 a[196:199], v232, s[28:31], s37 offen offset:0 sc1\n"
    "v_mfma_f32_16x16x32_bf16 v[36:39], v[144:147], a[116:119], v[36:39]\n"
    "v_mfma_f32_16x16x32_bf16 v[36:39], v[148:151], a[132:135], v[36:39]\n"
    "v_mfma_f32_16x16x32_bf16 v[4:7], v[148:151], a[128:131], v[4:7]\n"
    "v_mfma_f32_16x16x32_bf16 v[4:7], v[144:147], a[112:115], v[4:7]\n"
    "v_mfma_f32_16x16x32_bf16 v[4:7], v[140:143], a[96:99], v[4:7]\n"
    "s_waitcnt lgkmcnt(6)\n"
    "v_mfma_f32_16x16x32_bf16 v[8:11], v[152:155], a[96:99], v[8:11]\n"
    "ds_read_b64_tr_b16 v[128:129], v246 offset:50048\n"
    "ds_read_b64_tr_b16 v[130:131], v246 offset:54176\n"
    "v_mfma_f32_16x16x32_bf16 v[8:11], v[156:159], a[112:115], v[8:11]\n"
    "buffer_load_dwordx4 a[212:215], v232, s[28:31], s37 offen offset:1024 sc1\n"
    "v_mfma_f32_16x16x32_bf16 v[8:11], v[160:163], a[128:131], v[8:11]\n"
    "ds_read_b64_tr_b16 v[132:133], v246 offset:58304\n"
    "ds_read_b64_tr_b16 v[134:135], v246 offset:62432\n"
    "v_mfma_f32_16x16x32_bf16 v[40:43], v[160:163], a[132:135], v[40:43]\n"
    "ds_read_b64_tr_b16 v[136:137], v247 offset:1024\n"
    "ds_read_b64_tr_b16 v[138:139], v247 offset:5152\n"
    "v_mfma_f32_16x16x32_bf16 v[40:43], v[156:159], a[116:119], v[40:43]\n"
    "v_mfma_f32_16x16x32_bf16 v[40:43], v[152:155], a[100:103], v[40:43]\n"
    "v_mfma_f32_16x16x32_bf16 v[72:75], v[152:155], a[104:107], v[72:75]\n"
    "buffer_load_dwordx4 a[228:231], v232, s[28:31], s37 offen offset:2048 sc1\n"
    "v_mfma_f32_16x16x32_bf16 v[72:75], v[156:159], a[120:123], v[72:75]\n"
    "v_mfma_f32_16x16x32_bf16 v[72:75], v[160:163], a[136:139], v[72:75]\n"
    "v_mfma_f32_16x16x32_bf16 v[104:107], v[160:163], a[140:143], v[104:107]\n"
    "v_mfma_f32_16x16x32_bf16 v[104:107], v[156:159], a[124:127], v[104:107]\n"
    "v_mfma_f32_16x16x32_bf16 v[104:107], v[152:155], a[108:111], v[104:107]\n"
    "s_waitcnt lgkmcnt(6)\n"
    "v_mfma_f32_16x16x32_bf16 v[108:111], v[164:167], a[108:111], v[108:111]\n"
    "ds_read_b64_tr_b16 v[140:141], v246 offset:50056\n"
    "ds_read_b64_tr_b16 v[142:143], v246 offset:54184\n"
    "v_mfma_f32_16x16x32_bf16 v[108:111], v[168:171], a[124:127], v[108:111]\n"
    "s_mov_b32 s37, 49152\n"
    "buffer_load_dwordx4 a[200:203], v232, s[28:31], s37 offen offset:0 sc1\n"
    "v_mfma_f32_16x16x32_bf16 v[108:111], v[172:175], a[140:143], v[108:111]\n"
    "ds_read_b64_tr_b16 v[144:145], v246 offset:58312\n"
    "ds_read_b64_tr_b16 v[146:147], v246 offset:62440\n"
    "v_mfma_f32_16x16x32_bf16 v[76:79], v[172:175], a[136:139], v[76:79]\n"
    "ds_read_b64_tr_b16 v[148:149], v247 offset:1032\n"
    "ds_read_b64_tr_b16 v[150:151], v247 offset:5160\n"
    "v_mfma_f32_16x16x32_bf16 v[76:79], v[168:171], a[120:123], v[76:79]\n"
    "v_mfma_f32_16x16x32_bf16 v[76:79], v[164:167], a[104:107], v[76:79]\n"
    "v_mfma_f32_16x16x32_bf16 v[44:47], v[164:167], a[100:103], v[44:47]\n"
    "buffer_load_dwordx4 a[216:219], v232, s[28:31], s37 offen offset:1024 sc1\n"
    "v_mfma_f32_16x16x32_bf16 v[44:47], v[168:171], a[116:119], v[44:47]\n"
    "v_mfma_f32_16x16x32_bf16 v[44:47], v[172:175], a[132:135], v[44:47]\n"
    "v_mfma_f32_16x16x32_bf16 v[12:15], v[172:175], a[128:131], v[12:15]\n"
    "v_mfma_f32_16x16x32_bf16 v[12:15], v[168:171], a[112:115], v[12:15]\n"
    "v_mfma_f32_16x16x32_bf16 v[12:15], v[164:167], a[96:99], v[12:15]\n"
    "s_waitcnt lgkmcnt(6)\n"
    "v_mfma_f32_16x16x32_bf16 v[16:19], v[128:131], a[96:99], v[16:19]\n"
    "ds_read_b64_tr_b16 v[152:153], v246 offset:50304\n"
    "ds_read_b64_tr_b16 v[154:155], v246 offset:54432\n"
    "v_mfma_f32_16x16x32_bf16 v[16:19], v[132:135], a[112:115], v[16:19]\n"
    "buffer_load_dwordx4 a[232:235], v232, s[28:31], s37 offen offset:2048 sc1\n"
    "v_mfma_f32_16x16x32_bf16 v[16:19], v[136:139], a[128:131], v[16:19]\n"
    "ds_read_b64_tr_b16 v[156:157], v246 offset:58560\n"
    "ds_read_b64_tr_b16 v[158:159], v246 offset:62688\n"
    "v_mfma_f32_16x16x32_bf16 v[48:51], v[136:139], a[132:135], v[48:51]\n"
    "ds_read_b64_tr_b16 v[160:161], v247 offset:1280\n"
    "ds_read_b64_tr_b16 v[162:163], v247 offset:5408\n"
    "v_mfma_f32_16x16x32_bf16 v[48:51], v[132:135], a[116:119], v[48:51]\n"
    "v_mfma_f32_16x16x32_bf16 v[48:51], v[128:131], a[100:103], v[48:51]\n"
    "v_mfma_f32_16x16x32_bf16 v[80:83], v[128:131], a[104:107], v[80:83]\n"
    "s_mov_b32 s37, 67584\n"
    "buffer_load_dwordx4 a[204:207], v232, s[28:31], s37 offen offset:0 sc1\n"
    "v_mfma_f32_16x16x32_bf16 v[80:83], v[132:135], a[120:123], v[80:83]\n"
    "v_mfma_f32_16x16x32_bf16 v[80:83], v[136:139], a[136:139], v[80:83]\n"
    "v_mfma_f32_16x16x32_bf16 v[112:115], v[136:139], a[140:143], v[112:115]\n"
    "v_mfma_f32_16x16x32_bf16 v[112:115], v[132:135], a[124:127], v[112:115]\n"
    "v_mfma_f32_16x16x32_bf16 v[112:115], v[128:131], a[108:111], v[112:115]\n"
    "s_waitcnt lgkmcnt(6)\n"
    "v_mfma_f32_16x16x32_bf16 v[116:119], v[140:143], a[108:111], v[116:119]\n"
    "ds_read_b64_tr_b16 v[164:165], v246 offset:50312\n"
    "ds_read_b64_tr_b16 v[166:167], v246 offset:54440\n"
    "v_mfma_f32_16x16x32_bf16 v[116:119], v[144:147], a[124:127], v[116:119]\n"
    "buffer_load_dwordx4 a[220:223], v232, s[28:31], s37 offen offset:1024 sc1\n"
    "v_mfma_f32_16x16x32_bf16 v[116:119], v[148:151], a[140:143], v[116:119]\n"
    "ds_read_b64_tr_b16 v[168:169], v246 offset:58568\n"
    "ds_read_b64_tr_b16 v[170:171], v246 offset:62696\n"
    "v_mfma_f32_16x16x32_bf16 v[84:87], v[148:151], a[136:139], v[84:87]\n"
    "ds_read_b64_tr_b16 v[172:173], v247 offset:1288\n"
    "ds_read_b64_tr_b16 v[174:175], v247 offset:5416\n"
    "v_mfma_f32_16x16x32_bf16 v[84:87], v[144:147], a[120:123], v[84:87]\n"
    "v_mfma_f32_16x16x32_bf16 v[84:87], v[140:143], a[104:107], v[84:87]\n"
    "v_mfma_f32_16x16x32_bf16 v[52:55], v[140:143], a[100:103], v[52:55]\n"
    "buffer_load_dwordx4 a[236:239], v232, s[28:31], s37 offen offset:2048 sc1\n"
    "v_mfma_f32_16x16x32_bf16 v[52:55], v[144:147], a[116:119], v[52:55]\n"
    "v_mfma_f32_16x16x32_bf16 v[52:55], v[148:151], a[132:135], v[52:55]\n"
    "v_mfma_f32_16x16x32_bf16 v[20:23], v[148:151], a[128:131], v[20:23]\n"
    "v_mfma_f32_16x16x32_bf16 v[20:23], v[144:147], a[112:115], v[20:23]\n"
    "v_mfma_f32_16x16x32_bf16 v[20:23], v[140:143], a[96:99], v[20:23]\n"
    "s_waitcnt vmcnt(63) lgkmcnt(0)\n"
    "v_mfma_f32_16x16x32_bf16 v[24:27], v[152:155], a[96:99], v[24:27]\n"
    "s_barrier\n"
    "v_mfma_f32_16x16x32_bf16 v[24:27], v[156:159], a[112:115], v[24:27]\n"
    "s_lshl_b32 s40, s88, 11\n"
    "s_add_u32 m0, s40, 148736\n"
    "s_mul_i32 s41, s89, 2048\n"
    "v_mfma_f32_16x16x32_bf16 v[24:27], v[160:163], a[128:131], v[24:27]\n"
    "buffer_load_dwordx4 v234, s[20:23], s40 offen lds\n"
    "v_mfma_f32_16x16x32_bf16 v[56:59], v[160:163], a[132:135], v[56:59]\n"
    "ds_read_b64_tr_b16 v[128:129], v247 offset:8768\n"
    "ds_read_b64_tr_b16 v[130:131], v247 offset:12896\n"
    "v_mfma_f32_16x16x32_bf16 v[56:59], v[156:159], a[116:119], v[56:59]\n"
    "ds_read_b64_tr_b16 v[132:133], v247 offset:17024\n"
    "ds_read_b64_tr_b16 v[134:135], v247 offset:21152\n"
    "v_mfma_f32_16x16x32_bf16 v[56:59], v[152:155], a[100:103], v[56:59]\n"
    "ds_read_b64_tr_b16 v[136:137], v247 offset:25280\n"
    "ds_read_b64_tr_b16 v[138:139], v247 offset:29408\n"
    "v_mfma_f32_16x16x32_bf16 v[88:91], v[152:155], a[104:107], v[88:91]\n"
    "buffer_load_dwordx4 v234, s[20:23], s40 offen offset:1024 lds\n"
    "v_mfma_f32_16x16x32_bf16 v[88:91], v[156:159], a[120:123], v[88:91]\n"
    "v_mfma_f32_16x16x32_bf16 v[88:91], v[160:163], a[136:139], v[88:91]\n"
    "v_mfma_f32_16x16x32_bf16 v[120:123], v[160:163], a[140:143], v[120:123]\n"
    "v_mfma_f32_16x16x32_bf16 v[120:123], v[156:159], a[124:127], v[120:123]\n"
    "v_mfma_f32_16x16x32_bf16 v[120:123], v[152:155], a[108:111], v[120:123]\n"
    "s_waitcnt lgkmcnt(6)\n"
    "v_mfma_f32_16x16x32_bf16 v[124:127], v[164:167], a[108:111], v[124:127]\n"
    "s_mov_b64 s[20:21], s[70:71]\n"
    "s_mov_b32 s22, 516096\n"
    "s_lshl_b32 s40, s88, 10\n"
    "v_mfma_f32_16x16x32_bf16 v[124:127], v[168:171], a[124:127], v[124:127]\n"
    "s_and_b32 s40, s40, 2047\n"
    "s_add_u32 s41, s41, s40\n"
    "s_add_u32 s41, s41, 524288\n"
    "v_mfma_f32_16x16x32_bf16 v[124:127], v[172:175], a[140:143], v[124:127]\n"
    "buffer_store_dwordx4 v[220:223], v235, s[96:99], s41 offen sc1\n"
    "v_mfma_f32_16x16x32_bf16 v[92:95], v[172:175], a[136:139], v[92:95]\n"
    "ds_read_b64_tr_b16 v[140:141], v247 offset:8776\n"
    "ds_read_b64_tr_b16 v[142:143], v247 offset:12904\n"
    "v_mfma_f32_16x16x32_bf16 v[92:95], v[168:171], a[120:123], v[92:95]\n"
    "ds_read_b64_tr_b16 v[144:145], v247 offset:17032\n"
    "ds_read_b64_tr_b16 v[146:147], v247 offset:21160\n"
    "v_mfma_f32_16x16x32_bf16 v[92:95], v[164:167], a[104:107], v[92:95]\n"
    "ds_read_b64_tr_b16 v[148:149], v247 offset:25288\n"
    "ds_read_b64_tr_b16 v[150:151], v247 offset:29416\n"
    "v_mfma_f32_16x16x32_bf16 v[60:63], v[164:167], a[100:103], v[60:63]\n"
    "v_mfma_f32_16x16x32_bf16 v[60:63], v[168:171], a[116:119], v[60:63]\n"
    "v_mfma_f32_16x16x32_bf16 v[60:63], v[172:175], a[132:135], v[60:63]\n"
    "v_mfma_f32_16x16x32_bf16 v[28:31], v[172:175], a[128:131], v[28:31]\n"
    "v_mfma_f32_16x16x32_bf16 v[28:31], v[168:171], a[112:115], v[28:31]\n"
    "v_mfma_f32_16x16x32_bf16 v[28:31], v[164:167], a[96:99], v[28:31]\n"
    "s_waitcnt vmcnt(21) lgkmcnt(6)\n"
    "v_mfma_f32_16x16x32_bf16 v[0:3], v[128:131], a[144:147], v[0:3]\n"
    "ds_read_b64_tr_b16 v[152:153], v247 offset:9024\n"
    "ds_read_b64_tr_b16 v[154:155], v247 offset:13152\n"
    "v_mfma_f32_16x16x32_bf16 v[0:3], v[132:135], a[160:163], v[0:3]\n"
    "s_mov_b32 s37, 15360\n"
    "buffer_load_dwordx4 a[240:243], v232, s[28:31], s37 offen offset:0 sc1\n"
    "v_mfma_f32_16x16x32_bf16 v[0:3], v[136:139], a[176:179], v[0:3]\n"
    "ds_read_b64_tr_b16 v[156:157], v247 offset:17280\n"
    "ds_read_b64_tr_b16 v[158:159], v247 offset:21408\n"
    "v_mfma_f32_16x16x32_bf16 v[32:35], v[136:139], a[180:183], v[32:35]\n"
    "ds_read_b64_tr_b16 v[160:161], v247 offset:25536\n"
    "ds_read_b64_tr_b16 v[162:163], v247 offset:29664\n"
    "v_mfma_f32_16x16x32_bf16 v[32:35], v[132:135], a[164:167], v[32:35]\n"
    "buffer_load_dwordx4 v[176:179], v232, s[28:31], s37 offen offset:1024 sc1\n"
    "v_mfma_f32_16x16x32_bf16 v[32:35], v[128:131], a[148:151], v[32:35]\n"
    "v_mfma_f32_16x16x32_bf16 v[64:67], v[128:131], a[152:155], v[64:67]\n"
    "v_mfma_f32_16x16x32_bf16 v[64:67], v[132:135], a[168:171], v[64:67]\n"
    "v_mfma_f32_16x16x32_bf16 v[64:67], v[136:139], a[184:187], v[64:67]\n"
    "buffer_load_dwordx4 v[192:195], v232, s[28:31], s37 offen offset:2048 sc1\n"
    "v_mfma_f32_16x16x32_bf16 v[96:99], v[136:139], a[188:191], v[96:99]\n"
    "v_mfma_f32_16x16x32_bf16 v[96:99], v[132:135], a[172:175], v[96:99]\n"
    "v_mfma_f32_16x16x32_bf16 v[96:99], v[128:131], a[156:159], v[96:99]\n"
    "s_waitcnt lgkmcnt(6)\n"
    "v_mfma_f32_16x16x32_bf16 v[100:103], v[140:143], a[156:159], v[100:103]\n"
    "ds_read_b64_tr_b16 v[164:165], v247 offset:9032\n"
    "ds_read_b64_tr_b16 v[166:167], v247 offset:13160\n"
    "v_mfma_f32_16x16x32_bf16 v[100:103], v[144:147], a[172:175], v[100:103]\n"
    "s_mov_b32 s37, 33792\n"
    "buffer_load_dwordx4 a[244:247], v232, s[28:31], s37 offen offset:0 sc1\n"
    "v_mfma_f32_16x16x32_bf16 v[100:103], v[148:151], a[188:191], v[100:103]\n"
    "ds_read_b64_tr_b16 v[168:169], v247 offset:17288\n"
    "ds_read_b64_tr_b16 v[170:171], v247 offset:21416\n"
    "v_mfma_f32_16x16x32_bf16 v[68:71], v[148:151], a[184:187], v[68:71]\n"
    "ds_read_b64_tr_b16 v[172:173], v247 offset:25544\n"
    "ds_read_b64_tr_b16 v[174:175], v247 offset:29672\n"
    "v_mfma_f32_16x16x32_bf16 v[68:71], v[144:147], a[168:171], v[68:71]\n"
    "v_mfma_f32_16x16x32_bf16 v[68:71], v[140:143], a[152:155], v[68:71]\n"
    "v_mfma_f32_16x16x32_bf16 v[36:39], v[140:143], a[148:151], v[36:39]\n"
    "buffer_load_dwordx4 v[180:183], v232, s[28:31], s37 offen offset:1024 sc1\n"
    "v_mfma_f32_16x16x32_bf16 v[36:39], v[144:147], a[164:167], v[36:39]\n"
    "v_mfma_f32_16x16x32_bf16 v[36:39], v[148:151], a[180:183], v[36:39]\n"
    "v_mfma_f32_16x16x32_bf16 v[4:7], v[148:151], a[176:179], v[4:7]\n"
    "v_mfma_f32_16x16x32_bf16 v[4:7], v[144:147], a[160:163], v[4:7]\n"
    "v_mfma_f32_16x16x32_bf16 v[4:7], v[140:143], a[144:147], v[4:7]\n"
    "s_waitcnt lgkmcnt(6)\n"
    "v_mfma_f32_16x16x32_bf16 v[8:11], v[152:155], a[144:147], v[8:11]\n"
    "ds_read_b64_tr_b16 v[128:129], v247 offset:9280\n"
    "ds_read_b64_tr_b16 v[130:131], v247 offset:13408\n"
    "v_mfma_f32_16x16x32_bf16 v[8:11], v[156:159], a[160:163], v[8:11]\n"
    "buffer_load_dwordx4 v[196:199], v232, s[28:31], s37 offen offset:2048 sc1\n"
    "v_mfma_f32_16x16x32_bf16 v[8:11], v[160:163], a[176:179], v[8:11]\n"
    "ds_read_b64_tr_b16 v[132:133], v247 offset:17536\n"
    "ds_read_b64_tr_b16 v[134:135], v247 offset:21664\n"
    "v_mfma_f32_16x16x32_bf16 v[40:43], v[160:163], a[180:183], v[40:43]\n"
    "ds_read_b64_tr_b16 v[136:137], v247 offset:25792\n"
    "ds_read_b64_tr_b16 v[138:139], v247 offset:29920\n"
    "v_mfma_f32_16x16x32_bf16 v[40:43], v[156:159], a[164:167], v[40:43]\n"
    "s_mov_b32 s37, 52224\n"
    "buffer_load_dwordx4 a[248:251], v232, s[28:31], s37 offen offset:0 sc1\n"
    "v_mfma_f32_16x16x32_bf16 v[40:43], v[152:155], a[148:151], v[40:43]\n"
    "v_mfma_f32_16x16x32_bf16 v[72:75], v[152:155], a[152:155], v[72:75]\n"
    "v_mfma_f32_16x16x32_bf16 v[72:75], v[156:159], a[168:171], v[72:75]\n"
    "v_mfma_f32_16x16x32_bf16 v[72:75], v[160:163], a[184:187], v[72:75]\n"
    "buffer_load_dwordx4 v[184:187], v232, s[28:31], s37 offen offset:1024 sc1\n"
    "v_mfma_f32_16x16x32_bf16 v[104:107], v[160:163], a[188:191], v[104:107]\n"
    "v_mfma_f32_16x16x32_bf16 v[104:107], v[156:159], a[172:175], v[104:107]\n"
    "v_mfma_f32_16x16x32_bf16 v[104:107], v[152:155], a[156:159], v[104:107]\n"
    "s_waitcnt lgkmcnt(6)\n"
    "v_mfma_f32_16x16x32_bf16 v[108:111], v[164:167], a[156:159], v[108:111]\n"
    "ds_read_b64_tr_b16 v[140:141], v247 offset:9288\n"
    "ds_read_b64_tr_b16 v[142:143], v247 offset:13416\n"
    "v_mfma_f32_16x16x32_bf16 v[108:111], v[168:171], a[172:175], v[108:111]\n"
    "buffer_load_dwordx4 v[200:203], v232, s[28:31], s37 offen offset:2048 sc1\n"
    "v_mfma_f32_16x16x32_bf16 v[108:111], v[172:175], a[188:191], v[108:111]\n"
    "ds_read_b64_tr_b16 v[144:145], v247 offset:17544\n"
    "ds_read_b64_tr_b16 v[146:147], v247 offset:21672\n"
    "v_mfma_f32_16x16x32_bf16 v[76:79], v[172:175], a[184:187], v[76:79]\n"
    "ds_read_b64_tr_b16 v[148:149], v247 offset:25800\n"
    "ds_read_b64_tr_b16 v[150:151], v247 offset:29928\n"
    "v_mfma_f32_16x16x32_bf16 v[76:79], v[168:171], a[168:171], v[76:79]\n"
    "v_mfma_f32_16x16x32_bf16 v[76:79], v[164:167], a[152:155], v[76:79]\n"
    "v_mfma_f32_16x16x32_bf16 v[44:47], v[164:167], a[148:151], v[44:47]\n"
    "s_mov_b32 s37, 70656\n"
    "buffer_load_dwordx4 a[252:255], v232, s[28:31], s37 offen offset:0 sc1\n"
    "v_mfma_f32_16x16x32_bf16 v[44:47], v[168:171], a[164:167], v[44:47]\n"
    "v_mfma_f32_16x16x32_bf16 v[44:47], v[172:175], a[180:183], v[44:47]\n"
    "v_mfma_f32_16x16x32_bf16 v[12:15], v[172:175], a[176:179], v[12:15]\n"
    "v_mfma_f32_16x16x32_bf16 v[12:15], v[168:171], a[160:163], v[12:15]\n"
    "v_mfma_f32_16x16x32_bf16 v[12:15], v[164:167], a[144:147], v[12:15]\n"
    "s_waitcnt lgkmcnt(6)\n"
    "v_mfma_f32_16x16x32_bf16 v[16:19], v[128:131], a[144:147], v[16:19]\n"
    "ds_read_b64_tr_b16 v[152:153], v247 offset:9536\n"
    "ds_read_b64_tr_b16 v[154:155], v247 offset:13664\n"
    "v_mfma_f32_16x16x32_bf16 v[16:19], v[132:135], a[160:163], v[16:19]\n"
    "buffer_load_dwordx4 v[188:191], v232, s[28:31], s37 offen offset:1024 sc1\n"
    "v_mfma_f32_16x16x32_bf16 v[16:19], v[136:139], a[176:179], v[16:19]\n"
    "ds_read_b64_tr_b16 v[156:157], v247 offset:17792\n"
    "ds_read_b64_tr_b16 v[158:159], v247 offset:21920\n"
    "v_mfma_f32_16x16x32_bf16 v[48:51], v[136:139], a[180:183], v[48:51]\n"
    "ds_read_b64_tr_b16 v[160:161], v247 offset:26048\n"
    "ds_read_b64_tr_b16 v[162:163], v247 offset:30176\n"
    "v_mfma_f32_16x16x32_bf16 v[48:51], v[132:135], a[164:167], v[48:51]\n"
    "buffer_load_dwordx4 v[204:207], v232, s[28:31], s37 offen offset:2048 sc1\n"
    "v_mfma_f32_16x16x32_bf16 v[48:51], v[128:131], a[148:151], v[48:51]\n"
    "v_mfma_f32_16x16x32_bf16 v[80:83], v[128:131], a[152:155], v[80:83]\n"
    "v_mfma_f32_16x16x32_bf16 v[80:83], v[132:135], a[168:171], v[80:83]\n"
    "s_add_u32 m0, s39, 0\n"
    "v_mfma_f32_16x16x32_bf16 v[80:83], v[136:139], a[184:187], v[80:83]\n"
    "s_mov_b32 s38, 0\n"
    "buffer_load_dwordx4 v231, s[20:23], s38 offen offset:0 sc1 lds\n"
    "v_mfma_f32_16x16x32_bf16 v[112:115], v[136:139], a[188:191], v[112:115]\n"
    "v_mfma_f32_16x16x32_bf16 v[112:115], v[132:135], a[172:175], v[112:115]\n"
    "v_mfma_f32_16x16x32_bf16 v[112:115], v[128:131], a[156:159], v[112:115]\n"
    "s_waitcnt lgkmcnt(6)\n"
    "v_mfma_f32_16x16x32_bf16 v[116:119], v[140:143], a[156:159], v[116:119]\n"
    "ds_read_b64_tr_b16 v[164:165], v247 offset:9544\n"
    "ds_read_b64_tr_b16 v[166:167], v247 offset:13672\n"
    "v_mfma_f32_16x16x32_bf16 v[116:119], v[144:147], a[172:175], v[116:119]\n"
    "buffer_load_dwordx4 v231, s[20:23], s38 offen offset:1024 sc1 lds\n"
    "v_mfma_f32_16x16x32_bf16 v[116:119], v[148:151], a[188:191], v[116:119]\n"
    "ds_read_b64_tr_b16 v[168:169], v247 offset:17800\n"
    "ds_read_b64_tr_b16 v[170:171], v247 offset:21928\n"
    "v_mfma_f32_16x16x32_bf16 v[84:87], v[148:151], a[184:187], v[84:87]\n"
    "ds_read_b64_tr_b16 v[172:173], v247 offset:26056\n"
    "ds_read_b64_tr_b16 v[174:175], v247 offset:30184\n"
    "v_mfma_f32_16x16x32_bf16 v[84:87], v[144:147], a[168:171], v[84:87]\n"
    "v_mfma_f32_16x16x32_bf16 v[84:87], v[140:143], a[152:155], v[84:87]\n"
    "v_mfma_f32_16x16x32_bf16 v[52:55], v[140:143], a[148:151], v[52:55]\n"
    "buffer_load_dwordx4 v233, s[20:23], s38 offen offset:2048 sc1 lds\n"
    "v_mfma_f32_16x16x32_bf16 v[52:55], v[144:147], a[164:167], v[52:55]\n"
    "v_mfma_f32_16x16x32_bf16 v[52:55], v[148:151], a[180:183], v[52:55]\n"
    "v_mfma_f32_16x16x32_bf16 v[20:23], v[148:151], a[176:179], v[20:23]\n"
    "v_mfma_f32_16x16x32_bf16 v[20:23], v[144:147], a[160:163], v[20:23]\n"
    "v_mfma_f32_16x16x32_bf16 v[20:23], v[140:143], a[144:147], v[20:23]\n"
    "s_waitcnt vmcnt(16) lgkmcnt(0)\n"
    "v_mfma_f32_16x16x32_bf16 v[24:27], v[152:155], a[144:147], v[24:27]\n"
    "s_barrier\n"
    "v_mfma_f32_16x16x32_bf16 v[24:27], v[156:159], a[160:163], v[24:27]\n"
    "buffer_load_dwordx4 v233, s[20:23], s38 offen offset:3072 sc1 lds\n"
    "v_mfma_f32_16x16x32_bf16 v[24:27], v[160:163], a[176:179], v[24:27]\n"
    "ds_read_b64_tr_b16 v[128:129], v247 offset:33536\n"
    "ds_read_b64_tr_b16 v[130:131], v247 offset:37664\n"
    "v_mfma_f32_16x16x32_bf16 v[56:59], v[160:163], a[180:183], v[56:59]\n"
    "s_add_u32 m0, s39, 16384\n"
    "ds_read_b64_tr_b16 v[132:133], v247 offset:41792\n"
    "ds_read_b64_tr_b16 v[134:135], v247 offset:45920\n"
    "v_mfma_f32_16x16x32_bf16 v[56:59], v[156:159], a[164:167], v[56:59]\n"
    "s_nop 0\n"
    "buffer_load_dwordx4 v231, s[24:27], s38 offen offset:0 sc1 lds\n"
    "v_mfma_f32_16x16x32_bf16 v[56:59], v[152:155], a[148:151], v[56:59]\n"
    "ds_read_b64_tr_b16 v[136:137], v247 offset:50048\n"
    "ds_read_b64_tr_b16 v[138:139], v247 offset:54176\n"
    "v_mfma_f32_16x16x32_bf16 v[88:91], v[152:155], a[152:155], v[88:91]\n"
    "v_mfma_f32_16x16x32_bf16 v[88:91], v[156:159], a[168:171], v[88:91]\n"
    "v_mfma_f32_16x16x32_bf16 v[88:91], v[160:163], a[184:187], v[88:91]\n"
    "buffer_load_dwordx4 v233, s[24:27], s38 offen offset:1024 sc1 lds\n"
    "v_mfma_f32_16x16x32_bf16 v[120:123], v[160:163], a[188:191], v[120:123]\n"
    "v_mfma_f32_16x16x32_bf16 v[120:123], v[156:159], a[172:175], v[120:123]\n"
    "v_mfma_f32_16x16x32_bf16 v[120:123], v[152:155], a[156:159], v[120:123]\n"
    "s_waitcnt lgkmcnt(6)\n"
    "v_mfma_f32_16x16x32_bf16 v[124:127], v[164:167], a[156:159], v[124:127]\n"
    "ds_read_b64_tr_b16 v[140:141], v247 offset:33544\n"
    "ds_read_b64_tr_b16 v[142:143], v247 offset:37672\n"
    "v_mfma_f32_16x16x32_bf16 v[124:127], v[168:171], a[172:175], v[124:127]\n"
    "buffer_load_dwordx4 v231, s[24:27], s38 offen offset:2048 sc1 lds\n"
    "v_mfma_f32_16x16x32_bf16 v[124:127], v[172:175], a[188:191], v[124:127]\n"
    "ds_read_b64_tr_b16 v[144:145], v247 offset:41800\n"
    "ds_read_b64_tr_b16 v[146:147], v247 offset:45928\n"
    "v_mfma_f32_16x16x32_bf16 v[92:95], v[172:175], a[184:187], v[92:95]\n"
    "ds_read_b64_tr_b16 v[148:149], v247 offset:50056\n"
    "ds_read_b64_tr_b16 v[150:151], v247 offset:54184\n"
    "v_mfma_f32_16x16x32_bf16 v[92:95], v[168:171], a[168:171], v[92:95]\n"
    "v_mfma_f32_16x16x32_bf16 v[92:95], v[164:167], a[152:155], v[92:95]\n"
    "v_mfma_f32_16x16x32_bf16 v[60:63], v[164:167], a[148:151], v[60:63]\n"
    "buffer_load_dwordx4 v233, s[24:27], s38 offen offset:3072 sc1 lds\n"
    "v_mfma_f32_16x16x32_bf16 v[60:63], v[168:171], a[164:167], v[60:63]\n"
    "v_mfma_f32_16x16x32_bf16 v[60:63], v[172:175], a[180:183], v[60:63]\n"
    "v_mfma_f32_16x16x32_bf16 v[28:31], v[172:175], a[176:179], v[28:31]\n"
    "v_mfma_f32_16x16x32_bf16 v[28:31], v[168:171], a[160:163], v[28:31]\n"
    "v_mfma_f32_16x16x32_bf16 v[28:31], v[164:167], a[144:147], v[28:31]\n"
    "s_waitcnt vmcnt(23) lgkmcnt(6)\n"
    "v_mfma_f32_16x16x32_bf16 v[0:3], v[128:131], a[192:195], v[0:3]\n"
    "s_add_u32 m0, s39, 32768\n"
    "ds_read_b64_tr_b16 v[152:153], v247 offset:33792\n"
    "ds_read_b64_tr_b16 v[154:155], v247 offset:37920\n"
    "v_mfma_f32_16x16x32_bf16 v[0:3], v[132:135], a[208:211], v[0:3]\n"
    "s_mov_b32 s38, 16384\n"
    "buffer_load_dwordx4 v231, s[20:23], s38 offen offset:0 sc1 lds\n"
    "v_mfma_f32_16x16x32_bf16 v[0:3], v[136:139], a[224:227], v[0:3]\n"
    "ds_read_b64_tr_b16 v[156:157], v247 offset:42048\n"
    "ds_read_b64_tr_b16 v[158:159], v247 offset:46176\n"
    "v_mfma_f32_16x16x32_bf16 v[32:35], v[136:139], a[228:231], v[32:35]\n"
    "ds_read_b64_tr_b16 v[160:161], v247 offset:50304\n"
    "ds_read_b64_tr_b16 v[162:163], v247 offset:54432\n"
    "v_mfma_f32_16x16x32_bf16 v[32:35], v[132:135], a[212:215], v[32:35]\n"
    "v_mfma_f32_16x16x32_bf16 v[32:35], v[128:131], a[196:199], v[32:35]\n"
    "v_mfma_f32_16x16x32_bf16 v[64:67], v[128:131], a[200:203], v[64:67]\n"
    "buffer_load_dwordx4 v231, s[20:23], s38 offen offset:1024 sc1 lds\n"
    "v_mfma_f32_16x16x32_bf16 v[64:67], v[132:135], a[216:219], v[64:67]\n"
    "v_mfma_f32_16x16x32_bf16 v[64:67], v[136:139], a[232:235], v[64:67]\n"
    "v_mfma_f32_16x16x32_bf16 v[96:99], v[136:139], a[236:239], v[96:99]\n"
    "v_mfma_f32_16x16x32_bf16 v[96:99], v[132:135], a[220:223], v[96:99]\n"
    "v_mfma_f32_16x16x32_bf16 v[96:99], v[128:131], a[204:207], v[96:99]\n"
    "s_waitcnt lgkmcnt(6)\n"
    "v_mfma_f32_16x16x32_bf16 v[100:103], v[140:143], a[204:207], v[100:103]\n"
    "ds_read_b64_tr_b16 v[164:165], v247 offset:33800\n"
    "ds_read_b64_tr_b16 v[166:167], v247 offset:37928\n"
    "L_chain1x2_mk:\n"
    "s_cbranch_execz L_pad1_4_mk\n"
    "v_mfma_f32_16x16x32_bf16 v[100:103], v[144:147], a[220:223], v[100:103]\n"
    "buffer_load_dwordx4 v233, s[20:23], s38 offen offset:2048 sc1 lds\n"
    "v_mfma_f32_16x16x32_bf16 v[100:103], v[148:151], a[236:239], v[100:103]\n"
    "ds_read_b64_tr_b16 v[168:169], v247 offset:42056\n"
    "ds_read_b64_tr_b16 v[170:171], v247 offset:46184\n"
    "v_mfma_f32_16x16x32_bf16 v[68:71], v[148:151], a[232:235], v[68:71]\n"
    "ds_read_b64_tr_b16 v[172:173], v247 offset:50312\n"
    "ds_read_b64_tr_b16 v[174:175], v247 offset:54440\n"
    "v_mfma_f32_16x16x32_bf16 v[68:71], v[144:147], a[216:219], v[68:71]\n"
    "v_mfma_f32_16x16x32_bf16 v[68:71], v[140:143], a[200:203], v[68:71]\n"
    "v_mfma_f32_16x16x32_bf16 v[36:39], v[140:143], a[196:199], v[36:39]\n"
    "buffer_load_dwordx4 v233, s[20:23], s38 offen offset:3072 sc1 lds\n"
    "v_mfma_f32_16x16x32_bf16 v[36:39], v[144:147], a[212:215], v[36:39]\n"
    "v_mfma_f32_16x16x32_bf16 v[36:39], v[148:151], a[228:231], v[36:39]\n"
    "v_mfma_f32_16x16x32_bf16 v[4:7], v[148:151], a[224:227], v[4:7]\n"
    "v_mfma_f32_16x16x32_bf16 v[4:7], v[144:147], a[208:211], v[4:7]\n"
    "v_mfma_f32_16x16x32_bf16 v[4:7], v[140:143], a[192:195], v[4:7]\n"
    "s_waitcnt lgkmcnt(6)\n"
    "v_mfma_f32_16x16x32_bf16 v[8:11], v[152:155], a[192:195], v[8:11]\n"
    "s_add_u32 m0, s39, 49152\n"
    "ds_read_b64_tr_b16 v[128:129], v247 offset:34048\n"
    "ds_read_b64_tr_b16 v[130:131], v247 offset:38176\n"
    "v_mfma_f32_16x16x32_bf16 v[8:11], v[156:159], a[208:211], v[8:11]\n"
    "s_nop 0\n"
    "buffer_load_dwordx4 v231, s[24:27], s38 offen offset:0 sc1 lds\n"
    "v_mfma_f32_16x16x32_bf16 v[8:11], v[160:163], a[224:227], v[8:11]\n"
    "ds_read_b64_tr_b16 v[132:133], v247 offset:42304\n"
    "ds_read_b64_tr_b16 v[134:135], v247 offset:46432\n"
    "v_mfma_f32_16x16x32_bf16 v[40:43], v[160:163], a[228:231], v[40:43]\n"
    "ds_read_b64_tr_b16 v[136:137], v247 offset:50560\n"
    "ds_read_b64_tr_b16 v[138:139], v247 offset:54688\n"
    "v_mfma_f32_16x16x32_bf16 v[40:43], v[156:159], a[212:215], v[40:43]\n"
    "v_mfma_f32_16x16x32_bf16 v[40:43], v[152:155], a[196:199], v[40:43]\n"
    "v_mfma_f32_16x16x32_bf16 v[72:75], v[152:155], a[200:203], v[72:75]\n"
    "buffer_load_dwordx4 v233, s[24:27], s38 offen offset:1024 sc1 lds\n"
    "v_mfma_f32_16x16x32_bf16 v[72:75], v[156:159], a[216:219], v[72:75]\n"
    "v_mfma_f32_16x16x32_bf16 v[72:75], v[160:163], a[232:235], v[72:75]\n"
    "v_mfma_f32_16x16x32_bf16 v[104:107], v[160:163], a[236:239], v[104:107]\n"
    "v_mfma_f32_16x16x32_bf16 v[104:107], v[156:159], a[220:223], v[104:107]\n"
    "v_mfma_f32_16x16x32_bf16 v[104:107], v[152:155], a[204:207], v[104:107]\n"
    "s_waitcnt lgkmcnt(6)\n"
    "v_mfma_f32_16x16x32_bf16 v[108:111], v[164:167], a[204:207], v[108:111]\n"
    "ds_read_b64_tr_b16 v[140:141], v247 offset:34056\n"
    "ds_read_b64_tr_b16 v[142:143], v247 offset:38184\n"
    "v_mfma_f32_16x16x32_bf16 v[108:111], v[168:171], a[220:223], v[108:111]\n"
    "buffer_load_dwordx4 v231, s[24:27], s38 offen offset:2048 sc1 lds\n"
    "v_mfma_f32_16x16x32_bf16 v[108:111], v[172:175], a[236:239], v[108:111]\n"
    "ds_read_b64_tr_b16 v[144:145], v247 offset:42312\n"
    "ds_read_b64_tr_b16 v[146:147], v247 offset:46440\n"
    "v_mfma_f32_16x16x32_bf16 v[76:79], v[172:175], a[232:235], v[76:79]\n"
    "ds_read_b64_tr_b16 v[148:149], v247 offset:50568\n"
    "ds_read_b64_tr_b16 v[150:151], v247 offset:54696\n"
    "v_mfma_f32_16x16x32_bf16 v[76:79], v[168:171], a[216:219], v[76:79]\n"
    "v_mfma_f32_16x16x32_bf16 v[76:79], v[164:167], a[200:203], v[76:79]\n"
    "v_mfma_f32_16x16x32_bf16 v[44:47], v[164:167], a[196:199], v[44:47]\n"
    "buffer_load_dwordx4 v233, s[24:27], s38 offen offset:3072 sc1 lds\n"
    "v_mfma_f32_16x16x32_bf16 v[44:47], v[168:171], a[212:215], v[44:47]\n"
    "v_mfma_f32_16x16x32_bf16 v[44:47], v[172:175], a[228:231], v[44:47]\n"
    "v_mfma_f32_16x16x32_bf16 v[12:15], v[172:175], a[224:227], v[12:15]\n"
    "v_mfma_f32_16x16x32_bf16 v[12:15], v[168:171], a[208:211], v[12:15]\n"
    "v_mfma_f32_16x16x32_bf16 v[12:15], v[164:167], a[192:195], v[12:15]\n"
    "s_waitcnt lgkmcnt(6)\n"
    "v_mfma_f32_16x16x32_bf16 v[16:19], v[128:131], a[192:195], v[16:19]\n"
    "s_add_u32 m0, s39, 65536\n"
    "ds_read_b64_tr_b16 v[152:153], v247 offset:34304\n"
    "ds_read_b64_tr_b16 v[154:155], v247 offset:38432\n"
    "v_mfma_f32_16x16x32_bf16 v[16:19], v[132:135], a[208:211], v[16:19]\n"
    "s_mov_b32 s38, 32768\n"
    "buffer_load_dwordx4 v231, s[20:23], s38 offen offset:0 sc1 lds\n"
    "v_mfma_f32_16x16x32_bf16 v[16:19], v[136:139], a[224:227], v[16:19]\n"
    "ds_read_b64_tr_b16 v[156:157], v247 offset:42560\n"
    "ds_read_b64_tr_b16 v[158:159], v247 offset:46688\n"
    "v_mfma_f32_16x16x32_bf16 v[48:51], v[136:139], a[228:231], v[48:51]\n"
    "ds_read_b64_tr_b16 v[160:161], v247 offset:50816\n"
    "ds_read_b64_tr_b16 v[162:163], v247 offset:54944\n"
    "v_mfma_f32_16x16x32_bf16 v[48:51], v[132:135], a[212:215], v[48:51]\n"
    "v_mfma_f32_16x16x32_bf16 v[48:51], v[128:131], a[196:199], v[48:51]\n"
    "v_mfma_f32_16x16x32_bf16 v[80:83], v[128:131], a[200:203], v[80:83]\n"
    "buffer_load_dwordx4 v231, s[20:23], s38 offen offset:1024 sc1 lds\n"
    "v_mfma_f32_16x16x32_bf16 v[80:83], v[132:135], a[216:219], v[80:83]\n"
    "v_mfma_f32_16x16x32_bf16 v[80:83], v[136:139], a[232:235], v[80:83]\n"
    "v_mfma_f32_16x16x32_bf16 v[112:115], v[136:139], a[236:239], v[112:115]\n"
    "v_mfma_f32_16x16x32_bf16 v[112:115], v[132:135], a[220:223], v[112:115]\n"
    "v_mfma_f32_16x16x32_bf16 v[112:115], v[128:131], a[204:207], v[112:115]\n"
    "s_waitcnt lgkmcnt(6)\n"
    "v_mfma_f32_16x16x32_bf16 v[116:119], v[140:143], a[204:207], v[116:119]\n"
    "ds_read_b64_tr_b16 v[164:165], v247 offset:34312\n"
    "ds_read_b64_tr_b16 v[166:167], v247 offset:38440\n"
    "v_mfma_f32_16x16x32_bf16 v[116:119], v[144:147], a[220:223], v[116:119]\n"
    "buffer_load_dwordx4 v233, s[20:23], s38 offen offset:2048 sc1 lds\n"
    "v_mfma_f32_16x16x32_bf16 v[116:119], v[148:151], a[236:239], v[116:119]\n"
    "ds_read_b64_tr_b16 v[168:169], v247 offset:42568\n"
    "ds_read_b64_tr_b16 v[170:171], v247 offset:46696\n"
    "v_mfma_f32_16x16x32_bf16 v[84:87], v[148:151], a[232:235], v[84:87]\n"
    "ds_read_b64_tr_b16 v[172:173], v247 offset:50824\n"
    "ds_read_b64_tr_b16 v[174:175], v247 offset:54952\n"
    "v_mfma_f32_16x16x32_bf16 v[84:87], v[144:147], a[216:219], v[84:87]\n"
    "v_mfma_f32_16x16x32_bf16 v[84:87], v[140:143], a[200:203], v[84:87]\n"
    "v_mfma_f32_16x16x32_bf16 v[52:55], v[140:143], a[196:199], v[52:55]\n"
    "buffer_load_dwordx4 v233, s[20:23], s38 offen offset:3072 sc1 lds\n"
    "v_mfma_f32_16x16x32_bf16 v[52:55], v[144:147], a[212:215], v[52:55]\n"
    "v_mfma_f32_16x16x32_bf16 v[52:55], v[148:151], a[228:231], v[52:55]\n"
    "v_mfma_f32_16x16x32_bf16 v[20:23], v[148:151], a[224:227], v[20:23]\n"
    "v_mfma_f32_16x16x32_bf16 v[20:23], v[144:147], a[208:211], v[20:23]\n"
    "v_mfma_f32_16x16x32_bf16 v[20:23], v[140:143], a[192:195], v[20:23]\n"
    "s_waitcnt lgkmcnt(6)\n"
    "v_mfma_f32_16x16x32_bf16 v[24:27], v[152:155], a[192:195], v[24:27]\n"
    "s_add_u32 m0, s39, 81920\n"
    "ds_read_b64_tr_b16 v[128:129], v247 offset:58304\n"
    "ds_read_b64_tr_b16 v[130:131], v247 offset:62432\n"
    "v_mfma_f32_16x16x32_bf16 v[24:27], v[156:159], a[208:211], v[24:27]\n"
    "s_nop 0\n"
    "buffer_load_dwordx4 v231, s[24:27], s38 offen offset:0 sc1 lds\n"
    "v_mfma_f32_16x16x32_bf16 v[24:27], v[160:163], a[224:227], v[24:27]\n"
    "ds_read_b64_tr_b16 v[132:133], v248 offset:1024\n"
    "ds_read_b64_tr_b16 v[134:135], v248 offset:5152\n"
    "v_mfma_f32_16x16x32_bf16 v[56:59], v[160:163], a[228:231], v[56:59]\n"
    "ds_read_b64_tr_b16 v[136:137], v248 offset:9280\n"
    "ds_read_b64_tr_b16 v[138:139], v248 offset:13408\n"
    "v_mfma_f32_16x16x32_bf16 v[56:59], v[156:159], a[212:215], v[56:59]\n"
    "v_mfma_f32_16x16x32_bf16 v[56:59], v[152:155], a[196:199], v[56:59]\n"
    "v_mfma_f32_16x16x32_bf16 v[88:91], v[152:155], a[200:203], v[88:91]\n"
    "buffer_load_dwordx4 v233, s[24:27], s38 offen offset:1024 sc1 lds\n"
    "v_mfma_f32_16x16x32_bf16 v[88:91], v[156:159], a[216:219], v[88:91]\n"
    "v_mfma_f32_16x16x32_bf16 v[88:91], v[160:163], a[232:235], v[88:91]\n"
    "v_mfma_f32_16x16x32_bf16 v[120:123], v[160:163], a[236:239], v[120:123]\n"
    "v_mfma_f32_16x16x32_bf16 v[120:123], v[156:159], a[220:223], v[120:123]\n"
    "v_mfma_f32_16x16x32_bf16 v[120:123], v[152:155], a[204:207], v[120:123]\n"
    "s_waitcnt lgkmcnt(6)\n"
    "v_mfma_f32_16x16x32_bf16 v[124:127], v[164:167], a[204:207], v[124:127]\n"
    "ds_read_b64_tr_b16 v[140:141], v247 offset:58312\n"
    "ds_read_b64_tr_b16 v[142:143], v247 offset:62440\n"
    "v_mfma_f32_16x16x32_bf16 v[124:127], v[168:171], a[220:223], v[124:127]\n"
    "buffer_load_dwordx4 v231, s[24:27], s38 offen offset:2048 sc1 lds\n"
    "v_mfma_f32_16x16x32_bf16 v[124:127], v[172:175], a[236:239], v[124:127]\n"
    "ds_read_b64_tr_b16 v[144:145], v248 offset:1032\n"
    "ds_read_b64_tr_b16 v[146:147], v248 offset:5160\n"
    "v_mfma_f32_16x16x32_bf16 v[92:95], v[172:175], a[232:235], v[92:95]\n"
    "ds_read_b64_tr_b16 v[148:149], v248 offset:9288\n"
    "ds_read_b64_tr_b16 v[150:151], v248 offset:13416\n"
    "v_mfma_f32_16x16x32_bf16 v[92:95], v[168:171], a[216:219], v[92:95]\n"
    "v_mfma_f32_16x16x32_bf16 v[92:95], v[164:167], a[200:203], v[92:95]\n"
    "v_mfma_f32_16x16x32_bf16 v[60:63], v[164:167], a[196:199], v[60:63]\n"
    "buffer_load_dwordx4 v233, s[24:27], s38 offen offset:3072 sc1 lds\n"
    "v_mfma_f32_16x16x32_bf16 v[60:63], v[168:171], a[212:215], v[60:63]\n"
    "v_mfma_f32_16x16x32_bf16 v[60:63], v[172:175], a[228:231], v[60:63]\n"
    "v_mfma_f32_16x16x32_bf16 v[28:31], v[172:175], a[224:227], v[28:31]\n"
    "v_mfma_f32_16x16x32_bf16 v[28:31], v[168:171], a[208:211], v[28:31]\n"
    "v_mfma_f32_16x16x32_bf16 v[28:31], v[164:167], a[192:195], v[28:31]\n"
    "s_waitcnt vmcnt(24) lgkmcnt(6)\n"
    "v_mfma_f32_16x16x32_bf16 v[0:3], v[128:131], a[240:243], v[0:3]\n"
    "ds_read_b64_tr_b16 v[152:153], v247 offset:58560\n"
    "ds_read_b64_tr_b16 v[154:155], v247 offset:62688\n"
    "v_mfma_f32_16x16x32_bf16 v[0:3], v[132:135], v[176:179], v[0:3]\n"
    "ds_read_b64_tr_b16 v[156:157], v248 offset:1280\n"
    "ds_read_b64_tr_b16 v[158:159], v248 offset:5408\n"
    "v_mfma_f32_16x16x32_bf16 v[0:3], v[136:139], v[192:195], v[0:3]\n"
    "ds_read_b64_tr_b16 v[160:161], v248 offset:9536\n"
    "ds_read_b64_tr_b16 v[162:163], v248 offset:13664\n"
    "v_mfma_f32_16x16x32_bf16 v[32:35], v[136:139], v[196:199], v[32:35]\n"
    "v_mfma_f32_16x16x32_bf16 v[32:35], v[132:135], v[180:183], v[32:35]\n"
    "v_mfma_f32_16x16x32_bf16 v[32:35], v[128:131], a[244:247], v[32:35]\n"
    "v_mfma_f32_16x16x32_bf16 v[64:67], v[128:131], a[248:251], v[64:67]\n"
    "v_mfma_f32_16x16x32_bf16 v[64:67], v[132:135], v[184:187], v[64:67]\n"
    "v_mfma_f32_16x16x32_bf16 v[64:67], v[136:139], v[200:203], v[64:67]\n"
    "v_mfma_f32_16x16x32_bf16 v[96:99], v[136:139], v[204:207], v[96:99]\n"
    "v_mfma_f32_16x16x32_bf16 v[96:99], v[132:135], v[188:191], v[96:99]\n"
    "v_mfma_f32_16x16x32_bf16 v[96:99], v[128:131], a[252:255], v[96:99]\n"
    "s_waitcnt lgkmcnt(6)\n"
    "v_mfma_f32_16x16x32_bf16 v[100:103], v[140:143], a[252:255], v[100:103]\n"
    "ds_read_b64_tr_b16 v[164:165], v247 offset:58568\n"
    "ds_read_b64_tr_b16 v[166:167], v247 offset:62696\n"
    "v_mfma_f32_16x16x32_bf16 v[100:103], v[144:147], v[188:191], v[100:103]\n"
    "ds_read_b64_tr_b16 v[168:169], v248 offset:1288\n"
    "ds_read_b64_tr_b16 v[170:171], v248 offset:5416\n"
    "v_mfma_f32_16x16x32_bf16 v[100:103], v[148:151], v[204:207], v[100:103]\n"
    "ds_read_b64_tr_b16 v[172:173], v248 offset:9544\n"
    "ds_read_b64_tr_b16 v[174:175], v248 offset:13672\n"
    "v_mfma_f32_16x16x32_bf16 v[68:71], v[148:151], v[200:203], v[68:71]\n"
    "v_mfma_f32_16x16x32_bf16 v[68:71], v[144:147], v[184:187], v[68:71]\n"
    "v_mfma_f32_16x16x32_bf16 v[68:71], v[140:143], a[248:251], v[68:71]\n"
    "v_mfma_f32_16x16x32_bf16 v[36:39], v[140:143], a[244:247], v[36:39]\n"
    "v_mfma_f32_16x16x32_bf16 v[36:39], v[144:147], v[180:183], v[36:39]\n"
    "v_mfma_f32_16x16x32_bf16 v[36:39], v[148:151], v[196:199], v[36:39]\n"
    "v_mfma_f32_16x16x32_bf16 v[4:7], v[148:151], v[192:195], v[4:7]\n"
    "v_mfma_f32_16x16x32_bf16 v[4:7], v[144:147], v[176:179], v[4:7]\n"
    "v_mfma_f32_16x16x32_bf16 v[4:7], v[140:143], a[240:243], v[4:7]\n"
    "s_waitcnt lgkmcnt(6)\n"
    "v_mfma_f32_16x16x32_bf16 v[8:11], v[152:155], a[240:243], v[8:11]\n"
    "ds_read_b64_tr_b16 v[128:129], v247 offset:58816\n"
    "ds_read_b64_tr_b16 v[130:131], v247 offset:62944\n"
    "v_mfma_f32_16x16x32_bf16 v[8:11], v[156:159], v[176:179], v[8:11]\n"
    "ds_read_b64_tr_b16 v[132:133], v248 offset:1536\n"
    "ds_read_b64_tr_b16 v[134:135], v248 offset:5664\n"
    "v_mfma_f32_16x16x32_bf16 v[8:11], v[160:163], v[192:195], v[8:11]\n"
    "ds_read_b64_tr_b16 v[136:137], v248 offset:9792\n"
    "ds_read_b64_tr_b16 v[138:139], v248 offset:13920\n"
    "v_mfma_f32_16x16x32_bf16 v[40:43], v[160:163], v[196:199], v[40:43]\n"
    "v_mfma_f32_16x16x32_bf16 v[40:43], v[156:159], v[180:183], v[40:43]\n"
    "v_mfma_f32_16x16x32_bf16 v[40:43], v[152:155], a[244:247], v[40:43]\n"
    "v_mfma_f32_16x16x32_bf16 v[72:75], v[152:155], a[248:251], v[72:75]\n"
    "v_mfma_f32_16x16x32_bf16 v[72:75], v[156:159], v[184:187], v[72:75]\n"
    "v_mfma_f32_16x16x32_bf16 v[72:75], v[160:163], v[200:203], v[72:75]\n"
    "v_mfma_f32_16x16x32_bf16 v[104:107], v[160:163], v[204:207], v[104:107]\n"
    "v_mfma_f32_16x16x32_bf16 v[104:107], v[156:159], v[188:191], v[104:107]\n"
    "v_mfma_f32_16x16x32_bf16 v[104:107], v[152:155], a[252:255], v[104:107]\n"
    "s_waitcnt lgkmcnt(6)\n"
    "v_mfma_f32_16x16x32_bf16 v[108:111], v[164:167], a[252:255], v[108:111]\n"
    "ds_read_b64_tr_b16 v[140:141], v247 offset:58824\n"
    "ds_read_b64_tr_b16 v[142:143], v247 offset:62952\n"
    "v_mfma_f32_16x16x32_bf16 v[108:111], v[168:171], v[188:191], v[108:111]\n"
    "ds_read_b64_tr_b16 v[144:145], v248 offset:1544\n"
    "ds_read_b64_tr_b16 v[146:147], v248 offset:5672\n"
    "v_mfma_f32_16x16x32_bf16 v[108:111], v[172:175], v[204:207], v[108:111]\n"
    "ds_read_b64_tr_b16 v[148:149], v248 offset:9800\n"
    "ds_read_b64_tr_b16 v[150:151], v248 offset:13928\n"
    "v_mfma_f32_16x16x32_bf16 v[76:79], v[172:175], v[200:203], v[76:79]\n"
    "v_cvt_pk_bf16_f32 v208, v0, v1\n"
    "v_cvt_pk_bf16_f32 v209, v2, v3\n"
    "v_mfma_f32_16x16x32_bf16 v[76:79], v[168:171], v[184:187], v[76:79]\n"
    "v_cvt_pk_bf16_f32 v210, v4, v5\n"
    "v_cvt_pk_bf16_f32 v211, v6, v7\n"
    "v_mfma_f32_16x16x32_bf16 v[76:79], v[164:167], a[248:251], v[76:79]\n"
    "v_cvt_pk_bf16_f32 v212, v32, v33\n"
    "v_cvt_pk_bf16_f32 v213, v34, v35\n"
    "v_mfma_f32_16x16x32_bf16 v[44:47], v[164:167], a[244:247], v[44:47]\n"
    "v_cvt_pk_bf16_f32 v214, v36, v37\n"
    "v_cvt_pk_bf16_f32 v215, v38, v39\n"
    "v_mfma_f32_16x16x32_bf16 v[44:47], v[168:171], v[180:183], v[44:47]\n"
    "v_cvt_pk_bf16_f32 v216, v64, v65\n"
    "v_cvt_pk_bf16_f32 v217, v66, v67\n"
    "v_mfma_f32_16x16x32_bf16 v[44:47], v[172:175], v[196:199], v[44:47]\n"
    "v_cvt_pk_bf16_f32 v218, v68, v69\n"
    "v_cvt_pk_bf16_f32 v219, v70, v71\n"
    "v_mfma_f32_16x16x32_bf16 v[12:15], v[172:175], v[192:195], v[12:15]\n"
    "v_cvt_pk_bf16_f32 v220, v96, v97\n"
    "v_cvt_pk_bf16_f32 v221, v98, v99\n"
    "v_mfma_f32_16x16x32_bf16 v[12:15], v[168:171], v[176:179], v[12:15]\n"
    "v_cvt_pk_bf16_f32 v222, v100, v101\n"
    "v_cvt_pk_bf16_f32 v223, v102, v103\n"
    "v_mfma_f32_16x16x32_bf16 v[12:15], v[164:167], a[240:243], v[12:15]\n"
    "s_waitcnt lgkmcnt(6)\n"
    "v_accvgpr_write_b32 a0, v208\n"
    "v_accvgpr_write_b32 a1, v209\n"
    "v_mfma_f32_16x16x32_bf16 v[16:19], v[128:131], a[240:243], v[16:19]\n"
    "ds_read_b64_tr_b16 v[152:153], v247 offset:59072\n"
    "ds_read_b64_tr_b16 v[154:155], v247 offset:63200\n"
    "v_mfma_f32_16x16x32_bf16 v[16:19], v[132:135], v[176:179], v[16:19]\n"
    "ds_read_b64_tr_b16 v[156:157], v248 offset:1792\n"
    "ds_read_b64_tr_b16 v[158:159], v248 offset:5920\n"
    "v_mfma_f32_16x16x32_bf16 v[16:19], v[136:139], v[192:195], v[16:19]\n"
    "ds_read_b64_tr_b16 v[160:161], v248 offset:10048\n"
    "ds_read_b64_tr_b16 v[162:163], v248 offset:14176\n"
    "v_mfma_f32_16x16x32_bf16 v[48:51], v[136:139], v[196:199], v[48:51]\n"
    "v_accvgpr_write_b32 a2, v210\n"
    "v_accvgpr_write_b32 a3, v211\n"
    "v_mfma_f32_16x16x32_bf16 v[48:51], v[132:135], v[180:183], v[48:51]\n"
    "v_accvgpr_write_b32 a16, v212\n"
    "v_accvgpr_write_b32 a17, v213\n"
    "v_mfma_f32_16x16x32_bf16 v[48:51], v[128:131], a[244:247], v[48:51]\n"
    "v_accvgpr_write_b32 a18, v214\n"
    "v_accvgpr_write_b32 a19, v215\n"
    "v_mfma_f32_16x16x32_bf16 v[80:83], v[128:131], a[248:251], v[80:83]\n"
    "v_accvgpr_write_b32 a32, v216\n"
    "v_accvgpr_write_b32 a33, v217\n"
    "v_mfma_f32_16x16x32_bf16 v[80:83], v[132:135], v[184:187], v[80:83]\n"
    "v_accvgpr_write_b32 a34, v218\n"
    "v_accvgpr_write_b32 a35, v219\n"
    "v_mfma_f32_16x16x32_bf16 v[80:83], v[136:139], v[200:203], v[80:83]\n"
    "v_accvgpr_write_b32 a48, v220\n"
    "v_accvgpr_write_b32 a49, v221\n"
    "v_mfma_f32_16x16x32_bf16 v[112:115], v[136:139], v[204:207], v[112:115]\n"
    "v_accvgpr_write_b32 a50, v222\n"
    "v_accvgpr_write_b32 a51, v223\n"
    "v_mfma_f32_16x16x32_bf16 v[112:115], v[132:135], v[188:191], v[112:115]\n"
    "v_mfma_f32_16x16x32_bf16 v[112:115], v[128:131], a[252:255], v[112:115]\n"
    "s_waitcnt lgkmcnt(6)\n"
    "v_mfma_f32_16x16x32_bf16 v[116:119], v[140:143], a[252:255], v[116:119]\n"
    "ds_read_b64_tr_b16 v[164:165], v247 offset:59080\n"
    "ds_read_b64_tr_b16 v[166:167], v247 offset:63208\n"
    "v_mfma_f32_16x16x32_bf16 v[116:119], v[144:147], v[188:191], v[116:119]\n"
    "ds_read_b64_tr_b16 v[168:169], v248 offset:1800\n"
    "ds_read_b64_tr_b16 v[170:171], v248 offset:5928\n"
    "v_mfma_f32_16x16x32_bf16 v[116:119], v[148:151], v[204:207], v[116:119]\n"
    "ds_read_b64_tr_b16 v[172:173], v248 offset:10056\n"
    "ds_read_b64_tr_b16 v[174:175], v248 offset:14184\n"
    "v_mfma_f32_16x16x32_bf16 v[84:87], v[148:151], v[200:203], v[84:87]\n"
    "v_cvt_pk_bf16_f32 v208, v8, v9\n"
    "v_cvt_pk_bf16_f32 v209, v10, v11\n"
    "v_mfma_f32_16x16x32_bf16 v[84:87], v[144:147], v[184:187], v[84:87]\n"
    "v_cvt_pk_bf16_f32 v210, v12, v13\n"
    "v_cvt_pk_bf16_f32 v211, v14, v15\n"
    "v_mfma_f32_16x16x32_bf16 v[84:87], v[140:143], a[248:251], v[84:87]\n"
    "v_cvt_pk_bf16_f32 v212, v40, v41\n"
    "v_cvt_pk_bf16_f32 v213, v42, v43\n"
    "v_mfma_f32_16x16x32_bf16 v[52:55], v[140:143], a[244:247], v[52:55]\n"
    "v_cvt_pk_bf16_f32 v214, v44, v45\n"
    "v_cvt_pk_bf16_f32 v215, v46, v47\n"
    "v_mfma_f32_16x16x32_bf16 v[52:55], v[144:147], v[180:183], v[52:55]\n"
    "v_cvt_pk_bf16_f32 v216, v72, v73\n"
    "v_cvt_pk_bf16_f32 v217, v74, v75\n"
    "v_mfma_f32_16x16x32_bf16 v[52:55], v[148:151], v[196:199], v[52:55]\n"
    "v_cvt_pk_bf16_f32 v218, v76, v77\n"
    "v_cvt_pk_bf16_f32 v219, v78, v79\n"
    "v_mfma_f32_16x16x32_bf16 v[20:23], v[148:151], v[192:195], v[20:23]\n"
    "v_cvt_pk_bf16_f32 v220, v104, v105\n"
    "v_cvt_pk_bf16_f32 v221, v106, v107\n"
    "v_mfma_f32_16x16x32_bf16 v[20:23], v[144:147], v[176:179], v[20:23]\n"
    "v_cvt_pk_bf16_f32 v222, v108, v109\n"
    "v_cvt_pk_bf16_f32 v223, v110, v111\n"
    "v_mfma_f32_16x16x32_bf16 v[20:23], v[140:143], a[240:243], v[20:23]\n"
    "s_waitcnt lgkmcnt(6)\n"
    "v_accvgpr_write_b32 a4, v208\n"
    "v_accvgpr_write_b32 a5, v209\n"
    "v_mfma_f32_16x16x32_bf16 v[24:27], v[152:155], a[240:243], v[24:27]\n"
    "v_accvgpr_write_b32 a6, v210\n"
    "v_accvgpr_write_b32 a7, v211\n"
    "v_mfma_f32_16x16x32_bf16 v[24:27], v[156:159], v[176:179], v[24:27]\n"
    "v_accvgpr_write_b32 a20, v212\n"
    "v_accvgpr_write_b32 a21, v213\n"
    "v_mfma_f32_16x16x32_bf16 v[24:27], v[160:163], v[192:195], v[24:27]\n"
    "v_accvgpr_write_b32 a22, v214\n"
    "v_accvgpr_write_b32 a23, v215\n"
    "v_mfma_f32_16x16x32_bf16 v[56:59], v[160:163], v[196:199], v[56:59]\n"
    "v_accvgpr_write_b32 a36, v216\n"
    "v_accvgpr_write_b32 a37, v217\n"
    "v_mfma_f32_16x16x32_bf16 v[56:59], v[156:159], v[180:183], v[56:59]\n"
    "v_accvgpr_write_b32 a38, v218\n"
    "v_accvgpr_write_b32 a39, v219\n"
    "v_mfma_f32_16x16x32_bf16 v[56:59], v[152:155], a[244:247], v[56:59]\n"
    "v_accvgpr_write_b32 a52, v220\n"
    "v_accvgpr_write_b32 a53, v221\n"
    "v_mfma_f32_16x16x32_bf16 v[88:91], v[152:155], a[248:251], v[88:91]\n"
    "v_accvgpr_write_b32 a54, v222\n"
    "v_accvgpr_write_b32 a55, v223\n"
    "v_mfma_f32_16x16x32_bf16 v[88:91], v[156:159], v[184:187], v[88:91]\n"
    "v_mfma_f32_16x16x32_bf16 v[88:91], v[160:163], v[200:203], v[88:91]\n"
    "v_mfma_f32_16x16x32_bf16 v[120:123], v[160:163], v[204:207], v[120:123]\n"
    "v_mfma_f32_16x16x32_bf16 v[120:123], v[156:159], v[188:191], v[120:123]\n"
    "v_mfma_f32_16x16x32_bf16 v[120:123], v[152:155], a[252:255], v[120:123]\n"
    "s_waitcnt lgkmcnt(0)\n"
    "v_mfma_f32_16x16x32_bf16 v[124:127], v[164:167], a[252:255], v[124:127]\n"
    "v_cvt_pk_bf16_f32 v208, v16, v17\n"
    "v_cvt_pk_bf16_f32 v209, v18, v19\n"
    "v_mfma_f32_16x16x32_bf16 v[124:127], v[168:171], v[188:191], v[124:127]\n"
    "v_cvt_pk_bf16_f32 v210, v20, v21\n"
    "v_cvt_pk_bf16_f32 v211, v22, v23\n"
    "v_mfma_f32_16x16x32_bf16 v[124:127], v[172:175], v[204:207], v[124:127]\n"
    "v_cvt_pk_bf16_f32 v212, v48, v49\n"
    "v_cvt_pk_bf16_f32 v213, v50, v51\n"
    "v_mfma_f32_16x16x32_bf16 v[92:95], v[172:175], v[200:203], v[92:95]\n"
    "v_cvt_pk_bf16_f32 v214, v52, v53\n"
    "v_cvt_pk_bf16_f32 v215, v54, v55\n"
    "v_mfma_f32_16x16x32_bf16 v[92:95], v[168:171], v[184:187], v[92:95]\n"
    "v_cvt_pk_bf16_f32 v216, v80, v81\n"
    "v_cvt_pk_bf16_f32 v217, v82, v83\n"
    "v_mfma_f32_16x16x32_bf16 v[92:95], v[164:167], a[248:251], v[92:95]\n"
    "v_cvt_pk_bf16_f32 v218, v84, v85\n"
    "v_cvt_pk_bf16_f32 v219, v86, v87\n"
    "v_mfma_f32_16x16x32_bf16 v[60:63], v[164:167], a[244:247], v[60:63]\n"
    "v_cvt_pk_bf16_f32 v220, v112, v113\n"
    "v_cvt_pk_bf16_f32 v221, v114, v115\n"
    "v_mfma_f32_16x16x32_bf16 v[60:63], v[168:171], v[180:183], v[60:63]\n"
    "v_cvt_pk_bf16_f32 v222, v116, v117\n"
    "v_cvt_pk_bf16_f32 v223, v118, v119\n"
    "v_mfma_f32_16x16x32_bf16 v[60:63], v[172:175], v[196:199], v[60:63]\n"
    "v_accvgpr_write_b32 a8, v208\n"
    "v_accvgpr_write_b32 a9, v209\n"
    "v_mfma_f32_16x16x32_bf16 v[28:31], v[172:175], v[192:195], v[28:31]\n"
    "v_accvgpr_write_b32 a10, v210\n"
    "v_accvgpr_write_b32 a11, v211\n"
    "v_mfma_f32_16x16x32_bf16 v[28:31], v[168:171], v[176:179], v[28:31]\n"
    "v_accvgpr_write_b32 a24, v212\n"
    "v_accvgpr_write_b32 a25, v213\n"
    "v_mfma_f32_16x16x32_bf16 v[28:31], v[164:167], a[240:243], v[28:31]\n"
    "v_accvgpr_write_b32 a26, v214\n"
    "v_accvgpr_write_b32 a27, v215\n"
    "v_add_u32_e32 v225, 148736, v228\n"
    "ds_read_b128 a[64:67], v225 offset:0\n"
    "ds_read_b128 a[68:71], v225 offset:256\n"
    "ds_read_b128 a[72:75], v225 offset:512\n"
    "ds_read_b128 a[76:79], v225 offset:768\n"
    "ds_read_b128 a[80:83], v225 offset:4096\n"
    "ds_read_b128 a[84:87], v225 offset:4352\n"
    "ds_read_b128 a[88:91], v225 offset:4608\n"
    "ds_read_b128 a[92:95], v225 offset:4864\n"
    "v_accvgpr_write_b32 a40, v216\n"
    "v_accvgpr_write_b32 a41, v217\n"
    "v_accvgpr_write_b32 a42, v218\n"
    "v_accvgpr_write_b32 a43, v219\n"
    "v_accvgpr_write_b32 a56, v220\n"
    "v_accvgpr_write_b32 a57, v221\n"
    "v_accvgpr_write_b32 a58, v222\n"
    "v_accvgpr_write_b32 a59, v223\n"
    "v_cvt_pk_bf16_f32 v208, v24, v25\n"
    "v_cvt_pk_bf16_f32 v209, v26, v27\n"
    "v_cvt_pk_bf16_f32 v210, v28, v29\n"
    "v_cvt_pk_bf16_f32 v211, v30, v31\n"
    "v_cvt_pk_bf16_f32 v212, v56, v57\n"
    "v_cvt_pk_bf16_f32 v213, v58, v59\n"
    "v_cvt_pk_bf16_f32 v214, v60, v61\n"
    "v_cvt_pk_bf16_f32 v215, v62, v63\n"
    "v_cvt_pk_bf16_f32 v216, v88, v89\n"
    "v_cvt_pk_bf16_f32 v217, v90, v91\n"
    "v_cvt_pk_bf16_f32 v218, v92, v93\n"
    "v_cvt_pk_bf16_f32 v219, v94, v95\n"
    "v_cvt_pk_bf16_f32 v220, v120, v121\n"
    "v_cvt_pk_bf16_f32 v221, v122, v123\n"
    "v_cvt_pk_bf16_f32 v222, v124, v125\n"
    "v_cvt_pk_bf16_f32 v223, v126, v127\n"
    "v_accvgpr_write_b32 a12, v208\n"
    "v_accvgpr_write_b32 a13, v209\n"
    "v_accvgpr_write_b32 a14, v210\n"
    "v_accvgpr_write_b32 a15, v211\n"
    "v_accvgpr_write_b32 a28, v212\n"
    "v_accvgpr_write_b32 a29, v213\n"
    "v_accvgpr_write_b32 a30, v214\n"
    "v_accvgpr_write_b32 a31, v215\n"
    "v_accvgpr_write_b32 a44, v216\n"
    "v_accvgpr_write_b32 a45, v217\n"
    "v_accvgpr_write_b32 a46, v218\n"
    "v_accvgpr_write_b32 a47, v219\n"
    "v_accvgpr_write_b32 a60, v220\n"
    "v_accvgpr_write_b32 a61, v221\n"
    "v_accvgpr_write_b32 a62, v222\n"
    "v_accvgpr_write_b32 a63, v223\n"
    "L_chain1x1_mk:\n"
    "s_cbranch_execz L_pad1_5_mk\n"
    "v_lshrrev_b32_e32 v244, 2, v232\n"
    "v_add_u32_e32 v244, 156928, v244\n"
    "v_mov_b32_e32 v238, 49152\n"
    "v_mov_b32_e32 v225, v228\n"
    "v_mov_b32_e32 v226, v229\n"
    "v_mov_b32_e32 v227, v230\n"
    "ds_write_b32 v244, v238\n"
    "s_waitcnt vmcnt(8) lgkmcnt(0)\n"
    "s_barrier\n"
    "v_mfma_f32_16x16x32_bf16 v[128:131], a[64:67], a[0:3], 0\n"
    "s_mov_b32 s38, 49152\n"
    "ds_read_b128 a[96:99], v225 offset:0\n"
    "v_mov_b32_e32 v220, 0\n"
    "v_mfma_f32_16x16x32_bf16 v[128:131], a[68:71], a[4:7], v[128:131]\n"
    "s_add_u32 s37, s39, 98304\n"
    "ds_read_b128 a[100:103], v225 offset:256\n"
    "v_mov_b32_e32 v221, 0\n"
    "v_mfma_f32_16x16x32_bf16 v[128:131], a[72:75], a[8:11], v[128:131]\n"
    "s_mov_b32 m0, s37\n"
    "ds_read_b128 a[104:107], v225 offset:512\n"
    "v_mov_b32_e32 v222, 0\n"
    "v_mfma_f32_16x16x32_bf16 v[128:131], a[76:79], a[12:15], v[128:131]\n"
    "s_mov_b32 s36, 0\n"
    "ds_read_b128 a[108:111], v225 offset:768\n"
    "v_mov_b32_e32 v223, 0\n"
    "v_mfma_f32_16x16x32_bf16 v[132:135], a[80:83], a[0:3], 0\n"
    "ds_read_b128 a[112:115], v225 offset:4096\n"
    "v_mov_b32_e32 v208, 0\n"
    "v_mfma_f32_16x16x32_bf16 v[132:135], a[84:87], a[4:7], v[132:135]\n"
    "ds_read_b128 a[116:119], v225 offset:4352\n"
    "v_mov_b32_e32 v209, 0\n"
    "v_mfma_f32_16x16x32_bf16 v[132:135], a[88:91], a[8:11], v[132:135]\n"
    "ds_read_b128 a[120:123], v225 offset:4608\n"
    "v_mov_b32_e32 v210, 0\n"
    "v_mfma_f32_16x16x32_bf16 v[132:135], a[92:95], a[12:15], v[132:135]\n"
    "ds_read_b128 a[124:127], v225 offset:4864\n"
    "v_mov_b32_e32 v211, 0\n"
    "v_mfma_f32_16x16x32_bf16 v[136:139], a[64:67], a[16:19], 0\n"
    "ds_read_b64_tr_b16 a[128:129], v226 offset:16384\n"
    "v_mov_b32_e32 v212, 0\n"
    "v_mfma_f32_16x16x32_bf16 v[136:139], a[68:71], a[20:23], v[136:139]\n"
    "ds_read_b64_tr_b16 a[130:131], v226 offset:20480\n"
    "v_mov_b32_e32 v213, 0\n"
    "v_mfma_f32_16x16x32_bf16 v[136:139], a[72:75], a[24:27], v[136:139]\n"
    "ds_read_b64_tr_b16 a[132:133], v227 offset:16384\n"
    "v_mov_b32_e32 v214, 0\n"
    "v_mfma_f32_16x16x32_bf16 v[136:139], a[76:79], a[28:31], v[136:139]\n"
    "ds_read_b64_tr_b16 a[134:135], v227 offset:20480\n"
    "v_mov_b32_e32 v215, 0\n"
    "v_mfma_f32_16x16x32_bf16 v[140:143], a[80:83], a[16:19], 0\n"
    "ds_read_b64_tr_b16 a[136:137], v226 offset:16640\n"
    "v_mov_b32_e32 v240, 0\n"
    "v_mfma_f32_16x16x32_bf16 v[140:143], a[84:87], a[20:23], v[140:143]\n"
    "ds_read_b64_tr_b16 a[138:139], v226 offset:20736\n"
    "v_mov_b32_e32 v241, 0\n"
    "v_mfma_f32_16x16x32_bf16 v[140:143], a[88:91], a[24:27], v[140:143]\n"
    "ds_read_b64_tr_b16 a[140:141], v227 offset:16640\n"
    "v_mov_b32_e32 v242, 0\n"
    "v_mfma_f32_16x16x32_bf16 v[140:143], a[92:95], a[28:31], v[140:143]\n"
    "ds_read_b64_tr_b16 a[142:143], v227 offset:20736\n"
    "v_mov_b32_e32 v243, 0\n"
    "v_mfma_f32_16x16x32_bf16 v[144:147], a[64:67], a[32:35], 0\n"
    "ds_read_b64_tr_b16 a[144:145], v226 offset:16896\n"
    "v_mov_b32_e32 v239, 4096\n"
    "v_mfma_f32_16x16x32_bf16 v[144:147], a[68:71], a[36:39], v[144:147]\n"
    "ds_read_b64_tr_b16 a[146:147], v226 offset:20992\n"
    "v_mov_b32_e32 v234, 0x3f803f80\n"
    "v_mfma_f32_16x16x32_bf16 v[144:147], a[72:75], a[40:43], v[144:147]\n"
    "ds_read_b64_tr_b16 a[148:149], v227 offset:16896\n"
    "v_lshrrev_b32_e32 v235, 5, v232\n"
    "v_mfma_f32_16x16x32_bf16 v[144:147], a[76:79], a[44:47], v[144:147]\n"
    "ds_read_b64_tr_b16 a[150:151], v227 offset:20992\n"
    "v_xor_b32_e32 v235, v235, v232\n"
    "v_mfma_f32_16x16x32_bf16 v[148:151], a[80:83], a[32:35], 0\n"
    "ds_read_b64_tr_b16 a[152:153], v226 offset:17152\n"
    "v_and_b32_e32 v235, 16, v235\n"
    "v_mfma_f32_16x16x32_bf16 v[148:151], a[84:87], a[36:39], v[148:151]\n"
    "L_pad1_3_mk:\n"
    "s_cbranch_execz L_pad1_6_mk\n"
    "ds_read_b64_tr_b16 a[154:155], v226 offset:21248\n"
    "v_cmp_eq_u32_e32 vcc, 0, v235\n"
    "v_mfma_f32_16x16x32_bf16 v[148:151], a[88:91], a[40:43], v[148:151]\n"
    "ds_read_b64_tr_b16 a[156:157], v227 offset:17152\n"
    "v_bfe_u32 v235, v232, 5, 1\n"
    "v_mfma_f32_16x16x32_bf16 v[148:151], a[92:95], a[44:47], v[148:151]\n"
    "ds_read_b64_tr_b16 a[158:159], v227 offset:21248\n"
    "v_mul_u32_u24_e32 v235, 0xaaaa, v235\n"
    "v_mfma_f32_16x16x32_bf16 v[152:155], a[64:67], a[48:51], 0\n"
    "v_cndmask_b32_e32 v234, 0, v234, vcc\n"
    "v_add_u32_e32 v245, 0x4444, v235\n"
    "v_mfma_f32_16x16x32_bf16 v[152:155], a[68:71], a[52:55], v[152:155]\n"
    "v_accvgpr_write_b32 a192, v234\n"
    "v_accvgpr_write_b32 a193, v234\n"
    "v_mfma_f32_16x16x32_bf16 v[152:155], a[72:75], a[56:59], v[152:155]\n"
    "buffer_load_dwordx4 v231, s[20:23], s38 offen offset:0 sc1 lds\n"
    "v_mfma_f32_16x16x32_bf16 v[152:155], a[76:79], a[60:63], v[152:155]\n"
    "v_accvgpr_write_b32 a194, v234\n"
    "v_accvgpr_write_b32 a195, v234\n"
    "v_mfma_f32_16x16x32_bf16 v[156:159], a[80:83], a[48:51], 0\n"
    "buffer_load_dwordx4 v231, s[20:23], s38 offen offset:1024 sc1 lds\n"
    "v_mfma_f32_16x16x32_bf16 v[156:159], a[84:87], a[52:55], v[156:159]\n"
    "v_mfma_f32_16x16x32_bf16 v[156:159], a[88:91], a[56:59], v[156:159]\n"
    "buffer_load_dwordx4 v233, s[20:23], s38 offen offset:2048 sc1 lds\n"
    "v_mfma_f32_16x16x32_bf16 v[156:159], a[92:95], a[60:63], v[156:159]\n"
    "v_mfma_f32_32x32x16_bf16 v[0:15], v[220:223], v[220:223], 0\n"
    "v_max3_f32 v192, v128, v129, v130\n"
    "v_max3_f32 v193, v131, v132, v133\n"
    "v_max3_f32 v192, v192, v134, v135\n"
    "v_max_f32_e32 v192, v192, v193\n"
    "L_pad1_4_mk:\n"
    "s_cbranch_execz L_pad1_7_mk\n"
    "v_max3_f32 v194, v136, v137, v138\n"
    "v_max3_f32 v195, v139, v140, v141\n"
    "v_mfma_f32_32x32x16_bf16 v[16:31], v[220:223], v[220:223], 0\n"
    "v_max3_f32 v194, v194, v142, v143\n"
    "v_max_f32_e32 v194, v194, v195\n"
    "v_max3_f32 v196, v144, v145, v146\n"
    "v_max3_f32 v197, v147, v148, v149\n"
    "v_max3_f32 v196, v196, v150, v151\n"
    "v_max_f32_e32 v196, v196, v197\n"
    "v_mfma_f32_32x32x16_bf16 v[32:47], v[220:223], v[220:223], 0\n"
    "v_max3_f32 v198, v152, v153, v154\n"
    "v_max3_f32 v199, v155, v156, v157\n"
    "v_max3_f32 v198, v198, v158, v159\n"
    "v_max_f32_e32 v198, v198, v199\n"
    "v_mov_b32_e32 v193, v192\n"
    "v_mov_b32_e32 v195, v194\n"
    "v_mfma_f32_32x32x16_bf16 v[48:63], v[220:223], v[220:223], 0\n"
    "v_mov_b32_e32 v197, v196\n"
    "v_mov_b32_e32 v199, v198\n"
    "v_permlane32_swap_b32_e32 v192, v193\n"
    "v_permlane32_swap_b32_e32 v194, v195\n"
    "v_permlane32_swap_b32_e32 v196, v197\n"
    "v_permlane32_swap_b32_e32 v198, v199\n"
    "v_mfma_f32_32x32x16_bf16 v[64:79], v[220:223], v[220:223], 0\n"
    "v_max_f32_e32 v192, v192, v193\n"
    "v_max_f32_e32 v194, v194, v195\n"
    "v_max_f32_e32 v196, v196, v197\n"
    "v_max_f32_e32 v198, v198, v199\n"
    "v_mov_b32_e32 v193, v192\n"
    "v_mov_b32_e32 v195, v194\n"
    "v_mfma_f32_32x32x16_bf16 v[80:95], v[220:223], v[220:223], 0\n"
    "v_mov_b32_e32 v197, v196\n"
    "v_mov_b32_e32 v199, v198\n"
    "v_permlane16_swap_b32_e32 v192, v193\n"
    "v_permlane16_swap_b32_e32 v194, v195\n"
    "v_permlane16_swap_b32_e32 v196, v197\n"
    "v_permlane16_swap_b32_e32 v198, v199\n"
    "v_mfma_f32_32x32x16_bf16 v[96:111], v[220:223], v[220:223], 0\n"
    "v_max_f32_e32 v192, v192, v193\n"
    "v_max_f32_e32 v194, v194, v195\n"
    "v_max_f32_e32 v196, v196, v197\n"
    "v_max_f32_e32 v198, v198, v199\n"
    "v_ceil_f32_e32 v192, v192\n"
    "v_ceil_f32_e32 v194, v194\n"
    "v_mfma_f32_32x32x16_bf16 v[112:127], v[220:223], v[220:223], 0\n"
    "v_ceil_f32_e32 v196, v196\n"
    "v_ceil_f32_e32 v198, v198\n"
    "v_sub_f32_e32 v160, 0xc2c00000, v192\n"
    "v_sub_f32_e32 v161, 0xc2c00000, v192\n"
    "v_sub_f32_e32 v164, 0xc2c00000, v194\n"
    "v_sub_f32_e32 v165, 0xc2c00000, v194\n"
    "v_sub_f32_e32 v168, 0xc2c00000, v196\n"
    "v_sub_f32_e32 v169, 0xc2c00000, v196\n"
    "v_sub_f32_e32 v172, 0xc2c00000, v198\n"
    "v_sub_f32_e32 v173, 0xc2c00000, v198\n"
    "s_getpc_b64 s[60:61]\n"
    "L_lp20_mk:\n"
    "s_add_u32 s60, s60, L_loop_mk-L_lp20_mk\n"
    "s_addc_u32 s61, s61, 0\n"
    "s_add_u32 s70, s60, L_mid_mk-L_loop_mk\n"
    "s_addc_u32 s71, s61, 0\n"
    "v_mov_b64_e32 v[162:163], v[160:161]\n"
    "v_pk_add_f32 v[128:129], v[128:129], v[160:161]\n"
    "v_pk_add_f32 v[130:131], v[130:131], v[160:161]\n"
    "v_pk_add_f32 v[132:133], v[132:133], v[160:161]\n"
    "v_pk_add_f32 v[134:135], v[134:135], v[160:161]\n"
    "v_mov_b64_e32 v[166:167], v[164:165]\n"
    "v_pk_add_f32 v[136:137], v[136:137], v[164:165]\n"
    "v_pk_add_f32 v[138:139], v[138:139], v[164:165]\n"
    "v_pk_add_f32 v[140:141], v[140:141], v[164:165]\n"
    "v_pk_add_f32 v[142:143], v[142:143], v[164:165]\n"
    "v_mov_b64_e32 v[170:171], v[168:169]\n"
    "v_pk_add_f32 v[144:145], v[144:145], v[168:169]\n"
    "v_pk_add_f32 v[146:147], v[146:147], v[168:169]\n"
    "v_pk_add_f32 v[148:149], v[148:149], v[168:169]\n"
    "v_pk_add_f32 v[150:151], v[150:151], v[168:169]\n"
    "v_mov_b64_e32 v[174:175], v[172:173]\n"
    "v_pk_add_f32 v[152:153], v[152:153], v[172:173]\n"
    "v_pk_add_f32 v[154:155], v[154:155], v[172:173]\n"
    "v_pk_add_f32 v[156:157], v[156:157], v[172:173]\n"
    "v_pk_add_f32 v[158:159], v[158:159], v[172:173]\n"
    "v_exp_f32_e32 v220, v128\n"
    "v_exp_f32_e32 v221, v129\n"
    "v_exp_f32_e32 v222, v130\n"
    "v_cvt_pk_bf16_f32 v176, v220, v221\n"
    "L_pad1_5_mk:\n"
    "s_cbranch_execz L_pad1_8_mk\n"
    "v_exp_f32_e32 v223, v131\n"
    "v_exp_f32_e32 v220, v132\n"
    "v_cvt_pk_bf16_f32 v177, v222, v223\n"
    "v_exp_f32_e32 v221, v133\n"
    "v_exp_f32_e32 v222, v134\n"
    "v_cvt_pk_bf16_f32 v178, v220, v221\n"
    "v_exp_f32_e32 v223, v135\n"
    "v_exp_f32_e32 v220, v136\n"
    "v_cvt_pk_bf16_f32 v179, v222, v223\n"
    "v_exp_f32_e32 v221, v137\n"
    "v_exp_f32_e32 v222, v138\n"
    "v_cvt_pk_bf16_f32 v180, v220, v221\n"
    "v_exp_f32_e32 v223, v139\n"
    "v_exp_f32_e32 v220, v140\n"
    "v_cvt_pk_bf16_f32 v181, v222, v223\n"
    "v_exp_f32_e32 v221, v141\n"
    "v_exp_f32_e32 v222, v142\n"
    "v_cvt_pk_bf16_f32 v182, v220, v221\n"
    "v_exp_f32_e32 v223, v143\n"
    "s_nop 0\n"
    "v_cvt_pk_bf16_f32 v183, v222, v223\n"
    "v_exp_f32_e32 v220, v144\n"
    "v_exp_f32_e32 v221, v145\n"
    "v_smfmac_f32_16x16x64_bf16 v[208:211], a[192:195], v[176:183], v245\n"
    "v_exp_f32_e32 v222, v146\n"
    "v_cvt_pk_bf16_f32 v184, v220, v221\n"
    "v_exp_f32_e32 v223, v147\n"
    "v_exp_f32_e32 v220, v148\n"
    "v_cvt_pk_bf16_f32 v185, v222, v223\n"
    "v_exp_f32_e32 v221, v149\n"
    "v_exp_f32_e32 v222, v150\n"
    "v_cvt_pk_bf16_f32 v186, v220, v221\n"
    "v_exp_f32_e32 v223, v151\n"
    "v_exp_f32_e32 v220, v152\n"
    "v_cvt_pk_bf16_f32 v187, v222, v223\n"
    "v_exp_f32_e32 v221, v153\n"
    "v_exp_f32_e32 v222, v154\n"
    "v_cvt_pk_bf16_f32 v188, v220, v221\n"
    "v_add_f32_e32 v216, v208, v210\n"
    "v_add_f32_e32 v217, v209, v211\n"
    "s_waitcnt lgkmcnt(0)\n"
    "s_and_b32 s40, s88, 1\n"
    "s_lshl_b32 s95, s40, 14\n"
    "s_add_u32 s59, s95, 65536\n"
    "s_swappc_b64 s[62:63], s[70:71]\n"
    "s_cmp_lt_u32 s89, 128\n"
    "s_cbranch_scc1 L_novis21_mk\n"
    "v_readlane_b32 s72, v249, 0\n"
    "v_readlane_b32 s73, v250, 0\n"
    "v_xor_b32_e32 v246, 32, v253\n"
    "v_xor_b32_e32 v247, 64, v253\n"
    "v_xor_b32_e32 v248, 96, v253\n"
    "s_add_u32 m0, s69, 0\n"
    "s_nop 3\n"
    "buffer_load_dwordx4 v252, s[72:75], 0 offen offset:0 lds\n"
    "buffer_load_dwordx4 v252, s[72:75], s80 offen offset:1024 lds\n"
    "s_add_u32 m0, s69, 0\n"
    "s_nop 0\n"
    "buffer_load_dwordx4 v252, s[72:75], s81 offen offset:2048 lds\n"
    "buffer_load_dwordx4 v252, s[72:75], s82 offen offset:3072 lds\n"
    "s_add_u32 s59, s95, 98304\n"
    "s_swappc_b64 s[62:63], s[60:61]\n"
    "v_readlane_b32 s72, v249, 4\n"
    "v_readlane_b32 s73, v250, 4\n"
    "ds_read_b64_tr_b16 a[196:197], v253 offset:0\n"
    "ds_read_b64_tr_b16 a[198:199], v253 offset:2048\n"
    "ds_read_b64_tr_b16 a[200:201], v246 offset:0\n"
    "ds_read_b64_tr_b16 a[202:203], v246 offset:2048\n"
    "ds_read_b64_tr_b16 a[204:205], v247 offset:0\n"
    "ds_read_b64_tr_b16 a[206:207], v247 offset:2048\n"
    "ds_read_b64_tr_b16 a[208:209], v248 offset:0\n"
    "ds_read_b64_tr_b16 a[210:211], v248 offset:2048\n"
    "v_readlane_b32 s83, v251, 0\n"
    "v_readlane_b32 s84, v251, 1\n"
    "v_readlane_b32 s85, v251, 2\n"
    "v_readlane_b32 s94, v251, 3\n"
    "s_add_u32 m0, s69, 4096\n"
    "s_nop 0\n"
    "buffer_load_dwordx4 v252, s[72:75], 0 offen offset:0 lds\n"
    "buffer_load_dwordx4 v252, s[72:75], s80 offen offset:1024 lds\n"
    "s_add_u32 m0, s69, -2048\n"
    "s_waitcnt lgkmcnt(0)\n"
    "buffer_load_dwordx4 v252, s[72:75], s81 offen offset:2048 lds\n"
    "buffer_load_dwordx4 v252, s[72:75], s82 offen offset:3072 lds\n"
    "L_pad1_6_mk:\n"
    "s_cbranch_execz L_pad1_9_mk\n"
    "buffer_store_dwordx4 a[196:199], v232, s[76:79], s83 offen sc1\n"
    "buffer_store_dwordx4 a[200:203], v232, s[76:79], s84 offen sc1\n"
    "buffer_store_dwordx4 a[204:207], v232, s[76:79], s85 offen sc1\n"
    "buffer_store_dwordx4 a[208:211], v232, s[76:79], s94 offen sc1\n"
    "s_add_u32 s59, s95, 131072\n"
    "s_swappc_b64 s[62:63], s[60:61]\n"
    "v_readlane_b32 s72, v249, 8\n"
    "v_readlane_b32 s73, v250, 8\n"
    "ds_read_b64_tr_b16 a[196:197], v253 offset:4096\n"
    "ds_read_b64_tr_b16 a[198:199], v253 offset:0\n"
    "ds_read_b64_tr_b16 a[200:201], v246 offset:4096\n"
    "ds_read_b64_tr_b16 a[202:203], v246 offset:0\n"
    "ds_read_b64_tr_b16 a[204:205], v247 offset:4096\n"
    "ds_read_b64_tr_b16 a[206:207], v247 offset:0\n"
    "ds_read_b64_tr_b16 a[208:209], v248 offset:4096\n"
    "ds_read_b64_tr_b16 a[210:211], v248 offset:0\n"
    "v_readlane_b32 s83, v251, 4\n"
    "v_readlane_b32 s84, v251, 5\n"
    "v_readlane_b32 s85, v251, 6\n"
    "v_readlane_b32 s94, v251, 7\n"
    "s_add_u32 m0, s69, 2048\n"
    "s_nop 0\n"
    "buffer_load_dwordx4 v252, s[72:75], 0 offen offset:0 lds\n"
    "buffer_load_dwordx4 v252, s[72:75], s80 offen offset:1024 lds\n"
    "s_add_u32 m0, s69, 2048\n"
    "s_waitcnt lgkmcnt(0)\n"
    "buffer_load_dwordx4 v252, s[72:75], s81 offen offset:2048 lds\n"
    "buffer_load_dwordx4 v252, s[72:75], s82 offen offset:3072 lds\n"
    "buffer_store_dwordx4 a[196:199], v232, s[76:79], s83 offen sc1\n"
    "buffer_store_dwordx4 a[200:203], v232, s[76:79], s84 offen sc1\n"
    "buffer_store_dwordx4 a[204:207], v232, s[76:79], s85 offen sc1\n"
    "buffer_store_dwordx4 a[208:211], v232, s[76:79], s94 offen sc1\n"
    "s_add_u32 s59, s95, 163840\n"
    "s_swappc_b64 s[62:63], s[60:61]\n"
    "v_readlane_b32 s72, v249, 12\n"
    "v_readlane_b32 s73, v250, 12\n"
    "ds_read_b64_tr_b16 a[196:197], v253 offset:2048\n"
    "ds_read_b64_tr_b16 a[198:199], v253 offset:4096\n"
    "ds_read_b64_tr_b16 a[200:201], v246 offset:2048\n"
    "ds_read_b64_tr_b16 a[202:203], v246 offset:4096\n"
    "ds_read_b64_tr_b16 a[204:205], v247 offset:2048\n"
    "ds_read_b64_tr_b16 a[206:207], v247 offset:4096\n"
    "ds_read_b64_tr_b16 a[208:209], v248 offset:2048\n"
    "ds_read_b64_tr_b16 a[210:211], v248 offset:4096\n"
    "v_readlane_b32 s83, v251, 8\n"
    "v_readlane_b32 s84, v251, 9\n"
    "v_readlane_b32 s85, v251, 10\n"
    "v_readlane_b32 s94, v251, 11\n"
    "s_add_u32 m0, s69, 0\n"
    "s_nop 0\n"
    "buffer_load_dwordx4 v252, s[72:75], 0 offen offset:0 lds\n"
    "buffer_load_dwordx4 v252, s[72:75], s80 offen offset:1024 lds\n"
    "s_add_u32 m0, s69, 0\n"
    "s_waitcnt lgkmcnt(0)\n"
    "buffer_load_dwordx4 v252, s[72:75], s81 offen offset:2048 lds\n"
    "buffer_load_dwordx4 v252, s[72:75], s82 offen offset:3072 lds\n"
    "buffer_store_dwordx4 a[196:199], v232, s[76:79], s83 offen sc1\n"
    "buffer_store_dwordx4 a[200:203], v232, s[76:79], s84 offen sc1\n"
    "buffer_store_dwordx4 a[204:207], v232, s[76:79], s85 offen sc1\n"
    "buffer_store_dwordx4 a[208:211], v232, s[76:79], s94 offen sc1\n"
    "s_add_u32 s59, s95, 196608\n"
    "s_swappc_b64 s[62:63], s[60:61]\n"
    "v_readlane_b32 s72, v249, 16\n"
    "v_readlane_b32 s73, v250, 16\n"
    "ds_read_b64_tr_b16 a[196:197], v253 offset:0\n"
    "ds_read_b64_tr_b16 a[198:199], v253 offset:2048\n"
    "ds_read_b64_tr_b16 a[200:201], v246 offset:0\n"
    "ds_read_b64_tr_b16 a[202:203], v246 offset:2048\n"
    "L_pad1_7_mk:\n"
    "s_cbranch_execz L_pad1_10_mk\n"
    "ds_read_b64_tr_b16 a[204:205], v247 offset:0\n"
    "ds_read_b64_tr_b16 a[206:207], v247 offset:2048\n"
    "ds_read_b64_tr_b16 a[208:209], v248 offset:0\n"
    "ds_read_b64_tr_b16 a[210:211], v248 offset:2048\n"
    "v_readlane_b32 s83, v251, 12\n"
    "v_readlane_b32 s84, v251, 13\n"
    "v_readlane_b32 s85, v251, 14\n"
    "v_readlane_b32 s94, v251, 15\n"
    "s_add_u32 m0, s69, 4096\n"
    "s_nop 0\n"
    "buffer_load_dwordx4 v252, s[72:75], 0 offen offset:0 lds\n"
    "buffer_load_dwordx4 v252, s[72:75], s80 offen offset:1024 lds\n"
    "s_add_u32 m0, s69, -2048\n"
    "s_waitcnt lgkmcnt(0)\n"
    "buffer_load_dwordx4 v252, s[72:75], s81 offen offset:2048 lds\n"
    "buffer_load_dwordx4 v252, s[72:75], s82 offen offset:3072 lds\n"
    "buffer_store_dwordx4 a[196:199], v232, s[76:79], s83 offen sc1\n"
    "buffer_store_dwordx4 a[200:203], v232, s[76:79], s84 offen sc1\n"
    "buffer_store_dwordx4 a[204:207], v232, s[76:79], s85 offen sc1\n"
    "buffer_store_dwordx4 a[208:211], v232, s[76:79], s94 offen sc1\n"
    "s_add_u32 s59, s95, 229376\n"
    "s_swappc_b64 s[62:63], s[60:61]\n"
    "v_readlane_b32 s72, v249, 20\n"
    "v_readlane_b32 s73, v250, 20\n"
    "ds_read_b64_tr_b16 a[196:197], v253 offset:4096\n"
    "ds_read_b64_tr_b16 a[198:199], v253 offset:0\n"
    "ds_read_b64_tr_b16 a[200:201], v246 offset:4096\n"
    "ds_read_b64_tr_b16 a[202:203], v246 offset:0\n"
    "ds_read_b64_tr_b16 a[204:205], v247 offset:4096\n"
    "ds_read_b64_tr_b16 a[206:207], v247 offset:0\n"
    "ds_read_b64_tr_b16 a[208:209], v248 offset:4096\n"
    "ds_read_b64_tr_b16 a[210:211], v248 offset:0\n"
    "v_readlane_b32 s83, v251, 16\n"
    "v_readlane_b32 s84, v251, 17\n"
    "v_readlane_b32 s85, v251, 18\n"
    "v_readlane_b32 s94, v251, 19\n"
    "s_add_u32 m0, s69, 2048\n"
    "s_nop 0\n"
    "buffer_load_dwordx4 v252, s[72:75], 0 offen offset:0 lds\n"
    "buffer_load_dwordx4 v252, s[72:75], s80 offen offset:1024 lds\n"
    "s_add_u32 m0, s69, 2048\n"
    "s_waitcnt lgkmcnt(0)\n"
    "buffer_load_dwordx4 v252, s[72:75], s81 offen offset:2048 lds\n"
    "buffer_load_dwordx4 v252, s[72:75], s82 offen offset:3072 lds\n"
    "buffer_store_dwordx4 a[196:199], v232, s[76:79], s83 offen sc1\n"
    "buffer_store_dwordx4 a[200:203], v232, s[76:79], s84 offen sc1\n"
    "buffer_store_dwordx4 a[204:207], v232, s[76:79], s85 offen sc1\n"
    "buffer_store_dwordx4 a[208:211], v232, s[76:79], s94 offen sc1\n"
    "s_add_u32 s59, s95, 262144\n"
    "s_swappc_b64 s[62:63], s[60:61]\n"
    "v_readlane_b32 s72, v249, 24\n"
    "v_readlane_b32 s73, v250, 24\n"
    "ds_read_b64_tr_b16 a[196:197], v253 offset:2048\n"
    "ds_read_b64_tr_b16 a[198:199], v253 offset:4096\n"
    "ds_read_b64_tr_b16 a[200:201], v246 offset:2048\n"
    "ds_read_b64_tr_b16 a[202:203], v246 offset:4096\n"
    "ds_read_b64_tr_b16 a[204:205], v247 offset:2048\n"
    "ds_read_b64_tr_b16 a[206:207], v247 offset:4096\n"
    "ds_read_b64_tr_b16 a[208:209], v248 offset:2048\n"
    "ds_read_b64_tr_b16 a[210:211], v248 offset:4096\n"
    "v_readlane_b32 s83, v251, 20\n"
    "v_readlane_b32 s84, v251, 21\n"
    "v_readlane_b32 s85, v251, 22\n"
    "v_readlane_b32 s94, v251, 23\n"
    "s_add_u32 m0, s69, 0\n"
    "s_nop 0\n"
    "buffer_load_dwordx4 v252, s[72:75], 0 offen offset:0 lds\n"
    "buffer_load_dwordx4 v252, s[72:75], s80 offen offset:1024 lds\n"
    "L_pad1_8_mk:\n"
    "s_cbranch_execz L_pad1_11_mk\n"
    "s_add_u32 m0, s69, 0\n"
    "s_waitcnt lgkmcnt(0)\n"
    "buffer_load_dwordx4 v252, s[72:75], s81 offen offset:2048 lds\n"
    "buffer_load_dwordx4 v252, s[72:75], s82 offen offset:3072 lds\n"
    "buffer_store_dwordx4 a[196:199], v232, s[76:79], s83 offen sc1\n"
    "buffer_store_dwordx4 a[200:203], v232, s[76:79], s84 offen sc1\n"
    "buffer_store_dwordx4 a[204:207], v232, s[76:79], s85 offen sc1\n"
    "buffer_store_dwordx4 a[208:211], v232, s[76:79], s94 offen sc1\n"
    "s_add_u32 s59, s95, 294912\n"
    "s_swappc_b64 s[62:63], s[60:61]\n"
    "v_readlane_b32 s72, v249, 28\n"
    "v_readlane_b32 s73, v250, 28\n"
    "ds_read_b64_tr_b16 a[196:197], v253 offset:0\n"
    "ds_read_b64_tr_b16 a[198:199], v253 offset:2048\n"
    "ds_read_b64_tr_b16 a[200:201], v246 offset:0\n"
    "ds_read_b64_tr_b16 a[202:203], v246 offset:2048\n"
    "ds_read_b64_tr_b16 a[204:205], v247 offset:0\n"
    "ds_read_b64_tr_b16 a[206:207], v247 offset:2048\n"
    "ds_read_b64_tr_b16 a[208:209], v248 offset:0\n"
    "ds_read_b64_tr_b16 a[210:211], v248 offset:2048\n"
    "v_readlane_b32 s83, v251, 24\n"
    "v_readlane_b32 s84, v251, 25\n"
    "v_readlane_b32 s85, v251, 26\n"
    "v_readlane_b32 s94, v251, 27\n"
    "s_add_u32 m0, s69, 4096\n"
    "s_nop 0\n"
    "buffer_load_dwordx4 v252, s[72:75], 0 offen offset:0 lds\n"
    "buffer_load_dwordx4 v252, s[72:75], s80 offen offset:1024 lds\n"
    "s_add_u32 m0, s69, -2048\n"
    "s_waitcnt lgkmcnt(0)\n"
    "buffer_load_dwordx4 v252, s[72:75], s81 offen offset:2048 lds\n"
    "buffer_load_dwordx4 v252, s[72:75], s82 offen offset:3072 lds\n"
    "buffer_store_dwordx4 a[196:199], v232, s[76:79], s83 offen sc1\n"
    "buffer_store_dwordx4 a[200:203], v232, s[76:79], s84 offen sc1\n"
    "buffer_store_dwordx4 a[204:207], v232, s[76:79], s85 offen sc1\n"
    "buffer_store_dwordx4 a[208:211], v232, s[76:79], s94 offen sc1\n"
    "s_add_u32 s59, s95, 327680\n"
    "s_swappc_b64 s[62:63], s[60:61]\n"
    "v_readlane_b32 s72, v249, 32\n"
    "v_readlane_b32 s73, v250, 32\n"
    "ds_read_b64_tr_b16 a[196:197], v253 offset:4096\n"
    "ds_read_b64_tr_b16 a[198:199], v253 offset:0\n"
    "ds_read_b64_tr_b16 a[200:201], v246 offset:4096\n"
    "ds_read_b64_tr_b16 a[202:203], v246 offset:0\n"
    "ds_read_b64_tr_b16 a[204:205], v247 offset:4096\n"
    "ds_read_b64_tr_b16 a[206:207], v247 offset:0\n"
    "ds_read_b64_tr_b16 a[208:209], v248 offset:4096\n"
    "ds_read_b64_tr_b16 a[210:211], v248 offset:0\n"
    "v_readlane_b32 s83, v251, 28\n"
    "v_readlane_b32 s84, v251, 29\n"
    "v_readlane_b32 s85, v251, 30\n"
    "v_readlane_b32 s94, v251, 31\n"
    "s_add_u32 m0, s69, 2048\n"
    "s_nop 0\n"
    "buffer_load_dwordx4 v252, s[72:75], 0 offen offset:0 lds\n"
    "buffer_load_dwordx4 v252, s[72:75], s80 offen offset:1024 lds\n"
    "s_add_u32 m0, s69, 2048\n"
    "s_waitcnt lgkmcnt(0)\n"
    "buffer_load_dwordx4 v252, s[72:75], s81 offen offset:2048 lds\n"
    "buffer_load_dwordx4 v252, s[72:75], s82 offen offset:3072 lds\n"
    "buffer_store_dwordx4 a[196:199], v232, s[76:79], s83 offen sc1\n"
    "buffer_store_dwordx4 a[200:203], v232, s[76:79], s84 offen sc1\n"
    "buffer_store_dwordx4 a[204:207], v232, s[76:79], s85 offen sc1\n"
    "buffer_store_dwordx4 a[208:211], v232, s[76:79], s94 offen sc1\n"
    "s_add_u32 s59, s95, 360448\n"
    "s_swappc_b64 s[62:63], s[60:61]\n"
    "v_readlane_b32 s72, v249, 36\n"
    "v_readlane_b32 s73, v250, 36\n"
    "L_pad1_9_mk:\n"
    "s_cbranch_execz L_pad1_12_mk\n"
    "ds_read_b64_tr_b16 a[196:197], v253 offset:2048\n"
    "ds_read_b64_tr_b16 a[198:199], v253 offset:4096\n"
    "ds_read_b64_tr_b16 a[200:201], v246 offset:2048\n"
    "ds_read_b64_tr_b16 a[202:203], v246 offset:4096\n"
    "ds_read_b64_tr_b16 a[204:205], v247 offset:2048\n"
    "ds_read_b64_tr_b16 a[206:207], v247 offset:4096\n"
    "ds_read_b64_tr_b16 a[208:209], v248 offset:2048\n"
    "ds_read_b64_tr_b16 a[210:211], v248 offset:4096\n"
    "v_readlane_b32 s83, v251, 32\n"
    "v_readlane_b32 s84, v251, 33\n"
    "v_readlane_b32 s85, v251, 34\n"
    "v_readlane_b32 s94, v251, 35\n"
    "s_add_u32 m0, s69, 0\n"
    "s_nop 0\n"
    "buffer_load_dwordx4 v252, s[72:75], 0 offen offset:0 lds\n"
    "buffer_load_dwordx4 v252, s[72:75], s80 offen offset:1024 lds\n"
    "s_add_u32 m0, s69, 0\n"
    "s_waitcnt lgkmcnt(0)\n"
    "buffer_load_dwordx4 v252, s[72:75], s81 offen offset:2048 lds\n"
    "buffer_load_dwordx4 v252, s[72:75], s82 offen offset:3072 lds\n"
    "buffer_store_dwordx4 a[196:199], v232, s[76:79], s83 offen sc1\n"
    "buffer_store_dwordx4 a[200:203], v232, s[76:79], s84 offen sc1\n"
    "buffer_store_dwordx4 a[204:207], v232, s[76:79], s85 offen sc1\n"
    "buffer_store_dwordx4 a[208:211], v232, s[76:79], s94 offen sc1\n"
    "s_add_u32 s59, s95, 393216\n"
    "s_swappc_b64 s[62:63], s[60:61]\n"
    "ds_read_b64_tr_b16 a[196:197], v253 offset:0\n"
    "ds_read_b64_tr_b16 a[198:199], v253 offset:2048\n"
    "ds_read_b64_tr_b16 a[200:201], v246 offset:0\n"
    "ds_read_b64_tr_b16 a[202:203], v246 offset:2048\n"
    "ds_read_b64_tr_b16 a[204:205], v247 offset:0\n"
    "ds_read_b64_tr_b16 a[206:207], v247 offset:2048\n"
    "ds_read_b64_tr_b16 a[208:209], v248 offset:0\n"
    "ds_read_b64_tr_b16 a[210:211], v248 offset:2048\n"
    "v_readlane_b32 s83, v251, 36\n"
    "v_readlane_b32 s84, v251, 37\n"
    "v_readlane_b32 s85, v251, 38\n"
    "v_readlane_b32 s94, v251, 39\n"
    "s_nop 3\n"
    "s_waitcnt lgkmcnt(0)\n"
    "buffer_store_dwordx4 a[196:199], v232, s[76:79], s83 offen sc1\n"
    "buffer_store_dwordx4 a[200:203], v232, s[76:79], s84 offen sc1\n"
    "buffer_store_dwordx4 a[204:207], v232, s[76:79], s85 offen sc1\n"
    "buffer_store_dwordx4 a[208:211], v232, s[76:79], s94 offen sc1\n"
    "L_novis21_mk:\n"
    "s_mov_b32 s59, 442368\n"
    "s_swappc_b64 s[62:63], s[60:61]\n"
    "s_lshl_b32 s43, s89, 3\n"
    "s_add_u32 s43, s43, 229376\n"
    "s_cmp_lg_u32 s88, 0\n"
    "s_cbranch_scc1 L_sig22_mk\n"
    "v_mov_b32_e32 v246, s90\n"
    "v_mov_b32_e32 v247, s91\n"
    "s_mov_b64 exec, 1\n"
    "buffer_store_dwordx2 v[246:247], off, s[96:99], s43 sc1\n"
    "s_mov_b64 exec, -1\n"
    "L_sig22_mk:\n"
    "s_mov_b32 s59, 491520\n"
    "s_swappc_b64 s[62:63], s[60:61]\n"
    "s_mov_b32 s43, 229376\n"
    "buffer_load_dwordx4 v[246:249], v232, s[96:99], s43 offen sc1\n"
    "buffer_load_dwordx4 v[250:253], v232, s[96:99], s43 offen offset:1024 sc1\n"
    "s_mov_b32 s59, 524288\n"
    "s_swappc_b64 s[62:63], s[60:61]\n"
    "s_mov_b32 s92, 0x40000\n"
    "L_frc23_mk:\n"
    "v_cmp_ne_u64_e64 s[40:41], s[90:91], v[246:247]\n"
    "v_cmp_ne_u64_e32 vcc, s[90:91], v[248:249]\n"
    "s_or_b64 s[40:41], s[40:41], vcc\n"
    "v_cmp_ne_u64_e32 vcc, s[90:91], v[250:251]\n"
    "s_or_b64 s[40:41], s[40:41], vcc\n"
    "v_cmp_ne_u64_e32 vcc, s[90:91], v[252:253]\n"
    "s_or_b64 s[40:41], s[40:41], vcc\n"
    "s_cbranch_scc1 L_frl25_mk\n"
    "L_frd24_mk:\n"
    "s_bfe_u32 s54, s89, 0x20001\n"
    "s_bfe_u32 s55, s89, 0x20003\n"
    "s_lshr_b32 s56, s89, 5\n"
    "s_lshl_b32 s56, s56, 1\n"
    "s_and_b32 s40, s89, 1\n"
    "s_add_u32 s56, s56, s40\n"
    "s_mul_i32 s40, s55, 1179648\n"
    "L_pad1_10_mk:\n"
    "s_cbranch_execz L_pad1_13_mk\n"
    "s_mul_i32 s41, s54, 294912\n"
    "s_add_u32 s40, s40, s41\n"
    "s_add_u32 s40, s40, 58720256\n"
    "s_add_u32 s48, s64, s40\n"
    "s_addc_u32 s49, s65, 0\n"
    "s_and_b32 s49, s49, 0xffff\n"
    "s_mov_b32 s50, 294912\n"
    "s_mov_b32 s51, 0x20000\n"
    "s_getpc_b64 s[42:43]\n"
    "L_pc26_mk:\n"
    "s_add_u32 s42, s42, L_code1_mk-L_pc26_mk\n"
    "s_addc_u32 s43, s43, 0\n"
    "s_lshr_b32 s40, s89, 3\n"
    "s_lshl_b32 s40, s40, 2\n"
    "s_add_u32 s40, s40, s88\n"
    "s_lshl_b32 s40, s40, 10\n"
    "v_add_u32_e32 v246, s40, v232\n"
    "v_min_u32_e32 v246, L_loop_mk-L_code1_mk-4, v246\n"
    "global_load_dword a220, v246, s[42:43]\n"
    "s_sub_u32 s20, s48, 524288\n"
    "s_subb_u32 s21, s49, 0\n"
    "s_mov_b32 s22, 589824\n"
    "s_add_u32 s24, s20, 16384\n"
    "s_addc_u32 s25, s21, 0\n"
    "s_mov_b32 s26, 589824\n"
    "v_add_u32_e32 v231, s39, v232\n"
    "v_add_u32_e32 v233, s39, v232\n"
    "s_mov_b32 s59, 540672\n"
    "s_swappc_b64 s[62:63], s[60:61]\n"
    "s_add_u32 s20, s20, 16384\n"
    "s_addc_u32 s21, s21, 0\n"
    "s_add_u32 s24, s24, 16384\n"
    "s_addc_u32 s25, s25, 0\n"
    "s_mov_b32 s59, 557056\n"
    "s_swappc_b64 s[62:63], s[60:61]\n"
    "s_add_u32 s20, s20, 16384\n"
    "s_addc_u32 s21, s21, 0\n"
    "s_add_u32 s24, s24, 16384\n"
    "s_addc_u32 s25, s25, 0\n"
    "s_mov_b32 s59, 573440\n"
    "s_swappc_b64 s[62:63], s[60:61]\n"
    "L_code1_mk:\n"
    "v_mfma_f32_16x16x32_bf16 v[0:3], a[160:163], v[192:195], v[0:3]\n"
    "v_exp_f32_e32 v223, v155\n"
    "v_mfma_f32_16x16x32_bf16 v[4:7], a[164:167], v[192:195], v[4:7]\n"
    "v_cvt_pk_bf16_f32 v205, v222, v223\n"
    "v_mfma_f32_16x16x32_bf16 v[8:11], a[168:171], v[192:195], v[8:11]\n"
    "v_exp_f32_e32 v220, v156\n"
    "v_mfma_f32_16x16x32_bf16 v[12:15], a[172:175], v[192:195], v[12:15]\n"
    "v_exp_f32_e32 v221, v157\n"
    "v_mfma_f32_16x16x32_bf16 v[16:19], a[176:179], v[192:195], v[16:19]\n"
    "v_cvt_pk_bf16_f32 v206, v220, v221\n"
    "v_mfma_f32_16x16x32_bf16 v[20:23], a[180:183], v[192:195], v[20:23]\n"
    "v_exp_f32_e32 v222, v158\n"
    "v_mfma_f32_16x16x32_bf16 v[24:27], a[184:187], v[192:195], v[24:27]\n"
    "v_exp_f32_e32 v223, v159\n"
    "v_mfma_f32_16x16x32_bf16 v[28:31], a[188:191], v[192:195], v[28:31]\n"
    "v_cvt_pk_bf16_f32 v207, v222, v223\n"
    "v_mfma_f32_16x16x32_bf16 v[32:35], a[160:163], v[196:199], v[32:35]\n"
    "v_rcp_f32_e32 v240, v240\n"
    "v_mfma_f32_16x16x32_bf16 v[36:39], a[164:167], v[196:199], v[36:39]\n"
    "v_rcp_f32_e32 v241, v241\n"
    "v_smfmac_f32_16x16x64_bf16 v[212:215], a[192:195], v[200:207], v245\n"
    "s_mov_b32 s40, 0\n"
    "v_mul_f32_e32 v0, v0, v240\n"
    "v_mul_f32_e32 v1, v1, v240\n"
    "v_mfma_f32_16x16x32_bf16 v[40:43], a[168:171], v[196:199], v[40:43]\n"
    "v_mul_f32_e32 v2, v2, v240\n"
    "v_mul_f32_e32 v3, v3, v240\n"
    "v_mfma_f32_16x16x32_bf16 v[44:47], a[172:175], v[196:199], v[44:47]\n"
    "v_mul_f32_e32 v4, v4, v240\n"
    "v_mul_f32_e32 v5, v5, v240\n"
    "v_mfma_f32_16x16x32_bf16 v[48:51], a[176:179], v[196:199], v[48:51]\n"
    "v_mul_f32_e32 v6, v6, v240\n"
    "v_mul_f32_e32 v7, v7, v240\n"
    "v_mfma_f32_16x16x32_bf16 v[52:55], a[180:183], v[196:199], v[52:55]\n"
    "v_cvt_pk_bf16_f32 v128, v0, v1\n"
    "v_cvt_pk_bf16_f32 v129, v2, v3\n"
    "v_mfma_f32_16x16x32_bf16 v[56:59], a[184:187], v[196:199], v[56:59]\n"
    "v_add_f32_e32 v242, v212, v214\n"
    "v_add_f32_e32 v243, v213, v215\n"
    "v_mfma_f32_16x16x32_bf16 v[60:63], a[188:191], v[196:199], v[60:63]\n"
    "v_max_f32_e32 v224, v242, v243\n"
    "v_cmp_nge_f32_e32 vcc, 0x67800000, v224\n"
    "s_cbranch_vccnz L_rareT_mk\n"
    "L_backT_mk:\n"
    "v_cvt_pk_bf16_f32 v130, v4, v5\n"
    "L_pad1_11_mk:\n"
    "s_cbranch_execz L_pad1_14_mk\n"
    "v_cvt_pk_bf16_f32 v131, v6, v7\n"
    "buffer_store_dwordx4 v[128:131], v232, s[32:35], s40 offen offset:0 sc1\n"
    "v_pk_mul_f32 v[8:9], v[8:9], v[240:241] op_sel_hi:[1,0]\n"
    "v_pk_mul_f32 v[10:11], v[10:11], v[240:241] op_sel_hi:[1,0]\n"
    "v_pk_mul_f32 v[12:13], v[12:13], v[240:241] op_sel_hi:[1,0]\n"
    "v_pk_mul_f32 v[14:15], v[14:15], v[240:241] op_sel_hi:[1,0]\n"
    "v_cvt_pk_bf16_f32 v132, v8, v9\n"
    "v_cvt_pk_bf16_f32 v133, v10, v11\n"
    "v_cvt_pk_bf16_f32 v134, v12, v13\n"
    "v_cvt_pk_bf16_f32 v135, v14, v15\n"
    "buffer_store_dwordx4 v[132:135], v232, s[32:35], s40 offen offset:1024 sc1\n"
    "v_pk_mul_f32 v[16:17], v[16:17], v[240:241] op_sel_hi:[1,0]\n"
    "v_pk_mul_f32 v[18:19], v[18:19], v[240:241] op_sel_hi:[1,0]\n"
    "v_pk_mul_f32 v[20:21], v[20:21], v[240:241] op_sel_hi:[1,0]\n"
    "v_pk_mul_f32 v[22:23], v[22:23], v[240:241] op_sel_hi:[1,0]\n"
    "v_cvt_pk_bf16_f32 v136, v16, v17\n"
    "v_cvt_pk_bf16_f32 v137, v18, v19\n"
    "v_cvt_pk_bf16_f32 v138, v20, v21\n"
    "v_cvt_pk_bf16_f32 v139, v22, v23\n"
    "buffer_store_dwordx4 v[136:139], v232, s[32:35], s40 offen offset:2048 sc1\n"
    "v_pk_mul_f32 v[24:25], v[24:25], v[240:241] op_sel_hi:[1,0]\n"
    "v_pk_mul_f32 v[26:27], v[26:27], v[240:241] op_sel_hi:[1,0]\n"
    "v_pk_mul_f32 v[28:29], v[28:29], v[240:241] op_sel_hi:[1,0]\n"
    "v_pk_mul_f32 v[30:31], v[30:31], v[240:241] op_sel_hi:[1,0]\n"
    "v_cvt_pk_bf16_f32 v140, v24, v25\n"
    "v_cvt_pk_bf16_f32 v141, v26, v27\n"
    "v_cvt_pk_bf16_f32 v142, v28, v29\n"
    "v_cvt_pk_bf16_f32 v143, v30, v31\n"
    "buffer_store_dwordx4 v[140:143], v232, s[32:35], s40 offen offset:3072 sc1\n"
    "v_mfma_f32_16x16x32_bf16 v[64:67], a[160:163], v[200:203], v[64:67]\n"
    "v_rcp_f32_e32 v242, v242\n"
    "v_mfma_f32_16x16x32_bf16 v[68:71], a[164:167], v[200:203], v[68:71]\n"
    "v_rcp_f32_e32 v243, v243\n"
    "v_mfma_f32_16x16x32_bf16 v[72:75], a[168:171], v[200:203], v[72:75]\n"
    "s_mov_b32 s40, 147456\n"
    "v_mul_f32_e32 v32, v32, v241\n"
    "v_mul_f32_e32 v33, v33, v241\n"
    "v_mfma_f32_16x16x32_bf16 v[76:79], a[172:175], v[200:203], v[76:79]\n"
    "v_mul_f32_e32 v34, v34, v241\n"
    "v_mul_f32_e32 v35, v35, v241\n"
    "v_mfma_f32_16x16x32_bf16 v[80:83], a[176:179], v[200:203], v[80:83]\n"
    "v_mul_f32_e32 v36, v36, v241\n"
    "v_mul_f32_e32 v37, v37, v241\n"
    "v_mfma_f32_16x16x32_bf16 v[84:87], a[180:183], v[200:203], v[84:87]\n"
    "v_mul_f32_e32 v38, v38, v241\n"
    "v_mul_f32_e32 v39, v39, v241\n"
    "v_mfma_f32_16x16x32_bf16 v[88:91], a[184:187], v[200:203], v[88:91]\n"
    "v_cvt_pk_bf16_f32 v144, v32, v33\n"
    "v_cvt_pk_bf16_f32 v145, v34, v35\n"
    "v_mfma_f32_16x16x32_bf16 v[92:95], a[188:191], v[200:203], v[92:95]\n"
    "v_cvt_pk_bf16_f32 v146, v36, v37\n"
    "v_cvt_pk_bf16_f32 v147, v38, v39\n"
    "v_mfma_f32_16x16x32_bf16 v[96:99], a[160:163], v[204:207], v[96:99]\n"
    "buffer_store_dwordx4 v[144:147], v232, s[32:35], s40 offen offset:0 sc1\n"
    "v_mfma_f32_16x16x32_bf16 v[100:103], a[164:167], v[204:207], v[100:103]\n"
    "v_mul_f32_e32 v40, v40, v241\n"
    "v_mul_f32_e32 v41, v41, v241\n"
    "v_mfma_f32_16x16x32_bf16 v[104:107], a[168:171], v[204:207], v[104:107]\n"
    "v_mul_f32_e32 v42, v42, v241\n"
    "v_mul_f32_e32 v43, v43, v241\n"
    "v_mfma_f32_16x16x32_bf16 v[108:111], a[172:175], v[204:207], v[108:111]\n"
    "v_mul_f32_e32 v44, v44, v241\n"
    "v_mul_f32_e32 v45, v45, v241\n"
    "v_mfma_f32_16x16x32_bf16 v[112:115], a[176:179], v[204:207], v[112:115]\n"
    "v_mul_f32_e32 v46, v46, v241\n"
    "v_mul_f32_e32 v47, v47, v241\n"
    "v_mfma_f32_16x16x32_bf16 v[116:119], a[180:183], v[204:207], v[116:119]\n"
    "v_cvt_pk_bf16_f32 v148, v40, v41\n"
    "v_cvt_pk_bf16_f32 v149, v42, v43\n"
    "v_mfma_f32_16x16x32_bf16 v[120:123], a[184:187], v[204:207], v[120:123]\n"
    "v_cvt_pk_bf16_f32 v150, v44, v45\n"
    "v_cvt_pk_bf16_f32 v151, v46, v47\n"
    "L_pad1_12_mk:\n"
    "s_cbranch_execz L_pad1_15_mk\n"
    "v_mfma_f32_16x16x32_bf16 v[124:127], a[188:191], v[204:207], v[124:127]\n"
    "buffer_store_dwordx4 v[148:151], v232, s[32:35], s40 offen offset:1024 sc1\n"
    "v_pk_mul_f32 v[48:49], v[48:49], v[240:241] op_sel:[0,1]\n"
    "v_pk_mul_f32 v[50:51], v[50:51], v[240:241] op_sel:[0,1]\n"
    "v_pk_mul_f32 v[52:53], v[52:53], v[240:241] op_sel:[0,1]\n"
    "v_pk_mul_f32 v[54:55], v[54:55], v[240:241] op_sel:[0,1]\n"
    "v_cvt_pk_bf16_f32 v152, v48, v49\n"
    "v_cvt_pk_bf16_f32 v153, v50, v51\n"
    "v_cvt_pk_bf16_f32 v154, v52, v53\n"
    "v_cvt_pk_bf16_f32 v155, v54, v55\n"
    "buffer_store_dwordx4 v[152:155], v232, s[32:35], s40 offen offset:2048 sc1\n"
    "v_pk_mul_f32 v[56:57], v[56:57], v[240:241] op_sel:[0,1]\n"
    "v_pk_mul_f32 v[58:59], v[58:59], v[240:241] op_sel:[0,1]\n"
    "v_pk_mul_f32 v[60:61], v[60:61], v[240:241] op_sel:[0,1]\n"
    "v_pk_mul_f32 v[62:63], v[62:63], v[240:241] op_sel:[0,1]\n"
    "v_cvt_pk_bf16_f32 v156, v56, v57\n"
    "v_cvt_pk_bf16_f32 v157, v58, v59\n"
    "v_cvt_pk_bf16_f32 v158, v60, v61\n"
    "v_cvt_pk_bf16_f32 v159, v62, v63\n"
    "buffer_store_dwordx4 v[156:159], v232, s[32:35], s40 offen offset:3072 sc1\n"
    "s_mov_b32 s40, 294912\n"
    "v_pk_mul_f32 v[64:65], v[64:65], v[242:243] op_sel_hi:[1,0]\n"
    "v_pk_mul_f32 v[66:67], v[66:67], v[242:243] op_sel_hi:[1,0]\n"
    "v_pk_mul_f32 v[68:69], v[68:69], v[242:243] op_sel_hi:[1,0]\n"
    "v_pk_mul_f32 v[70:71], v[70:71], v[242:243] op_sel_hi:[1,0]\n"
    "v_cvt_pk_bf16_f32 v160, v64, v65\n"
    "v_cvt_pk_bf16_f32 v161, v66, v67\n"
    "v_cvt_pk_bf16_f32 v162, v68, v69\n"
    "v_cvt_pk_bf16_f32 v163, v70, v71\n"
    "buffer_store_dwordx4 v[160:163], v232, s[32:35], s40 offen offset:0 sc1\n"
    "v_pk_mul_f32 v[72:73], v[72:73], v[242:243] op_sel_hi:[1,0]\n"
    "v_pk_mul_f32 v[74:75], v[74:75], v[242:243] op_sel_hi:[1,0]\n"
    "v_pk_mul_f32 v[76:77], v[76:77], v[242:243] op_sel_hi:[1,0]\n"
    "v_pk_mul_f32 v[78:79], v[78:79], v[242:243] op_sel_hi:[1,0]\n"
    "v_cvt_pk_bf16_f32 v164, v72, v73\n"
    "v_cvt_pk_bf16_f32 v165, v74, v75\n"
    "v_cvt_pk_bf16_f32 v166, v76, v77\n"
    "v_cvt_pk_bf16_f32 v167, v78, v79\n"
    "buffer_store_dwordx4 v[164:167], v232, s[32:35], s40 offen offset:1024 sc1\n"
    "v_pk_mul_f32 v[80:81], v[80:81], v[242:243] op_sel_hi:[1,0]\n"
    "v_pk_mul_f32 v[82:83], v[82:83], v[242:243] op_sel_hi:[1,0]\n"
    "v_pk_mul_f32 v[84:85], v[84:85], v[242:243] op_sel_hi:[1,0]\n"
    "v_pk_mul_f32 v[86:87], v[86:87], v[242:243] op_sel_hi:[1,0]\n"
    "v_cvt_pk_bf16_f32 v168, v80, v81\n"
    "v_cvt_pk_bf16_f32 v169, v82, v83\n"
    "v_cvt_pk_bf16_f32 v170, v84, v85\n"
    "v_cvt_pk_bf16_f32 v171, v86, v87\n"
    "buffer_store_dwordx4 v[168:171], v232, s[32:35], s40 offen offset:2048 sc1\n"
    "v_pk_mul_f32 v[88:89], v[88:89], v[242:243] op_sel_hi:[1,0]\n"
    "v_pk_mul_f32 v[90:91], v[90:91], v[242:243] op_sel_hi:[1,0]\n"
    "v_pk_mul_f32 v[92:93], v[92:93], v[242:243] op_sel_hi:[1,0]\n"
    "v_pk_mul_f32 v[94:95], v[94:95], v[242:243] op_sel_hi:[1,0]\n"
    "v_cvt_pk_bf16_f32 v172, v88, v89\n"
    "v_cvt_pk_bf16_f32 v173, v90, v91\n"
    "v_cvt_pk_bf16_f32 v174, v92, v93\n"
    "v_cvt_pk_bf16_f32 v175, v94, v95\n"
    "buffer_store_dwordx4 v[172:175], v232, s[32:35], s40 offen offset:3072 sc1\n"
    "s_mov_b32 s40, 442368\n"
    "v_pk_mul_f32 v[96:97], v[96:97], v[242:243] op_sel:[0,1]\n"
    "v_pk_mul_f32 v[98:99], v[98:99], v[242:243] op_sel:[0,1]\n"
    "v_pk_mul_f32 v[100:101], v[100:101], v[242:243] op_sel:[0,1]\n"
    "v_pk_mul_f32 v[102:103], v[102:103], v[242:243] op_sel:[0,1]\n"
    "v_cvt_pk_bf16_f32 v176, v96, v97\n"
    "v_cvt_pk_bf16_f32 v177, v98, v99\n"
    "L_pad1_13_mk:\n"
    "s_cbranch_execz L_ret1_mk\n"
    "v_cvt_pk_bf16_f32 v178, v100, v101\n"
    "v_cvt_pk_bf16_f32 v179, v102, v103\n"
    "buffer_store_dwordx4 v[176:179], v232, s[32:35], s40 offen offset:0 sc1\n"
    "v_pk_mul_f32 v[104:105], v[104:105], v[242:243] op_sel:[0,1]\n"
    "v_pk_mul_f32 v[106:107], v[106:107], v[242:243] op_sel:[0,1]\n"
    "v_pk_mul_f32 v[108:109], v[108:109], v[242:243] op_sel:[0,1]\n"
    "v_pk_mul_f32 v[110:111], v[110:111], v[242:243] op_sel:[0,1]\n"
    "v_cvt_pk_bf16_f32 v180, v104, v105\n"
    "v_cvt_pk_bf16_f32 v181, v106, v107\n"
    "v_cvt_pk_bf16_f32 v182, v108, v109\n"
    "v_cvt_pk_bf16_f32 v183, v110, v111\n"
    "buffer_store_dwordx4 v[180:183], v232, s[32:35], s40 offen offset:1024 sc1\n"
    "v_pk_mul_f32 v[112:113], v[112:113], v[242:243] op_sel:[0,1]\n"
    "v_pk_mul_f32 v[114:115], v[114:115], v[242:243] op_sel:[0,1]\n"
    "v_pk_mul_f32 v[116:117], v[116:117], v[242:243] op_sel:[0,1]\n"
    "v_pk_mul_f32 v[118:119], v[118:119], v[242:243] op_sel:[0,1]\n"
    "v_cvt_pk_bf16_f32 v184, v112, v113\n"
    "v_cvt_pk_bf16_f32 v185, v114, v115\n"
    "v_cvt_pk_bf16_f32 v186, v116, v117\n"
    "v_cvt_pk_bf16_f32 v187, v118, v119\n"
    "buffer_store_dwordx4 v[184:187], v232, s[32:35], s40 offen offset:2048 sc1\n"
    "v_pk_mul_f32 v[120:121], v[120:121], v[242:243] op_sel:[0,1]\n"
    "v_pk_mul_f32 v[122:123], v[122:123], v[242:243] op_sel:[0,1]\n"
    "v_pk_mul_f32 v[124:125], v[124:125], v[242:243] op_sel:[0,1]\n"
    "v_pk_mul_f32 v[126:127], v[126:127], v[242:243] op_sel:[0,1]\n"
    "v_cvt_pk_bf16_f32 v188, v120, v121\n"
    "v_cvt_pk_bf16_f32 v189, v122, v123\n"
    "v_cvt_pk_bf16_f32 v190, v124, v125\n"
    "v_cvt_pk_bf16_f32 v191, v126, v127\n"
    "buffer_store_dwordx4 v[188:191], v232, s[32:35], s40 offen offset:3072 sc1\n"
    "s_mul_i32 s52, s88, 9216\n"
    "v_mov_b32_e32 v233, v232\n"
    "v_add_u32_e32 v234, 0x10000, v232\n"
    "v_add_u32_e32 v235, 0x20000, v232\n"
    "s_lshl_b32 s40, s56, 2\n"
    "s_add_u32 s42, s40, s88\n"
    "s_mul_i32 s40, s42, 294912\n"
    "s_mul_i32 s41, s54, 32768\n"
    "s_add_u32 s40, s40, s41\n"
    "s_add_u32 s40, s40, 38797312\n"
    "s_add_u32 s24, s64, s40\n"
    "s_addc_u32 s25, s65, 0\n"
    "s_and_b32 s25, s25, 0xffff\n"
    "s_mov_b32 s26, 180224\n"
    "s_mov_b32 s27, 0x20000\n"
    "s_mul_i32 s40, s54, 2359296\n"
    "s_mul_i32 s41, s42, 36864\n"
    "s_add_u32 s40, s40, s41\n"
    "s_mul_i32 s41, s55, 576\n"
    "s_add_u32 s40, s40, s41\n"
    "s_add_u32 s40, s40, 22020096\n"
    "s_add_u32 s28, s64, s40\n"
    "s_addc_u32 s29, s65, 0\n"
    "s_and_b32 s29, s29, 0xffff\n"
    "s_mov_b32 s30, 35136\n"
    "s_mov_b32 s31, 0x20000\n"
    "v_bfe_u32 v236, v232, 4, 4\n"
    "v_mul_u32_u24_e32 v236, 2304, v236\n"
    "v_lshrrev_b32_e32 v237, 8, v232\n"
    "v_lshl_add_u32 v236, v237, 4, v236\n"
    "s_mul_i32 s40, s57, 331776\n"
    "s_add_u32 s40, s40, 63963136\n"
    "s_add_u32 s20, s64, s40\n"
    "s_addc_u32 s21, s65, 0\n"
    "s_and_b32 s21, s21, 0xffff\n"
    "s_mov_b32 s22, 331776\n"
    "s_mov_b32 s23, 0x20000\n"
    "v_lshrrev_b32_e32 v194, 5, v232\n"
    "v_lshlrev_b32_e32 v194, 3, v194\n"
    "v_bfe_u32 v192, v232, 4, 1\n"
    "v_add_u32_e32 v194, v194, v192\n"
    "s_and_b32 s43, s89, 0xe6\n"
    "L_pad1_14_mk:\n"
    "s_cbranch_execz L_ret1_mk\n"
    "s_bfe_u32 s40, s89, 0x20003\n"
    "s_lshl_b32 s40, s40, 1\n"
    "s_and_b32 s41, s89, 1\n"
    "s_add_u32 s40, s40, s41\n"
    "v_mul_u32_u24_e32 v194, 2048, v194\n"
    "s_mul_i32 s43, s43, 2048\n"
    "s_mul_i32 s40, s40, 4\n"
    "s_add_u32 s43, s43, s40\n"
    "s_add_u32 s43, s43, 524320\n"
    "v_mov_b32_e32 v192, 1\n"
    "s_mov_b64 exec, 0\n"
    "s_cmp_eq_u32 s88, 3\n"
    "s_cbranch_scc1 L_chain2x3_mk\n"
    "s_cmp_eq_u32 s88, 2\n"
    "s_cbranch_scc1 L_chain2x2_mk\n"
    "s_cmp_eq_u32 s88, 1\n"
    "s_cbranch_scc1 L_chain2x1_mk\n"
    "s_cmp_eq_u32 s88, 0\n"
    "s_cbranch_scc1 L_chain2x0_mk\n"
    "L_ret2_mk:\n"
    "s_mov_b64 exec, -1\n"
    "s_waitcnt vmcnt(0)\n"
    "s_barrier\n"
    "s_cmp_lg_u32 s88, 1\n"
    "s_cbranch_scc1 L_psh28_mk\n"
    "s_mov_b64 exec, 0xff\n"
    "buffer_store_dword v192, v194, s[96:99], s43 offen sc1\n"
    "s_mov_b64 exec, -1\n"
    "L_psh28_mk:\n"
    "s_cmp_eq_u32 s88, 0\n"
    "s_cbranch_scc1 L_w0a29_mk\n"
    "s_mul_i32 s40, s88, 16384\n"
    "s_add_u32 m0, s40, 81920\n"
    "s_add_u32 s53, s40, 81920\n"
    "buffer_load_dwordx4 v232, s[48:51], s53 offen offset:0 sc1 lds\n"
    "buffer_load_dwordx4 v232, s[48:51], s53 offen offset:1024 sc1 lds\n"
    "buffer_load_dwordx4 v232, s[48:51], s53 offen offset:2048 sc1 lds\n"
    "buffer_load_dwordx4 v232, s[48:51], s53 offen offset:3072 sc1 lds\n"
    "s_add_u32 m0, s40, 86016\n"
    "s_add_u32 s53, s40, 86016\n"
    "buffer_load_dwordx4 v232, s[48:51], s53 offen offset:0 sc1 lds\n"
    "buffer_load_dwordx4 v232, s[48:51], s53 offen offset:1024 sc1 lds\n"
    "buffer_load_dwordx4 v232, s[48:51], s53 offen offset:2048 sc1 lds\n"
    "buffer_load_dwordx4 v232, s[48:51], s53 offen offset:3072 sc1 lds\n"
    "s_add_u32 m0, s40, 90112\n"
    "s_add_u32 s53, s40, 90112\n"
    "buffer_load_dwordx4 v232, s[48:51], s53 offen offset:0 sc1 lds\n"
    "buffer_load_dwordx4 v232, s[48:51], s53 offen offset:1024 sc1 lds\n"
    "buffer_load_dwordx4 v232, s[48:51], s53 offen offset:2048 sc1 lds\n"
    "buffer_load_dwordx4 v232, s[48:51], s53 offen offset:3072 sc1 lds\n"
    "s_add_u32 m0, s40, 94208\n"
    "s_add_u32 s53, s40, 94208\n"
    "buffer_load_dwordx4 v232, s[48:51], s53 offen offset:0 sc1 lds\n"
    "buffer_load_dwordx4 v232, s[48:51], s53 offen offset:1024 sc1 lds\n"
    "buffer_load_dwordx4 v232, s[48:51], s53 offen offset:2048 sc1 lds\n"
    "buffer_load_dwordx4 v232, s[48:51], s53 offen offset:3072 sc1 lds\n"
    "L_w0a29_mk:\n"
    "s_cmp_lg_u32 s88, 0\n"
    "s_cbranch_scc1 L_polled32_mk\n"
    "s_mul_i32 s41, s89, 2048\n"
    "s_add_u32 s41, s41, 524320\n"
    "v_and_b32_e32 v198, 112, v232\n"
    "v_lshrrev_b32_e32 v198, 2, v198\n"
    "s_mov_b32 s92, 0x40000\n"
    "L_poll31_mk:\n"
    "buffer_load_dword v196, v198, s[96:99], s41 offen sc1\n"
    "s_sub_u32 s92, s92, 1\n"
    "s_waitcnt vmcnt(0)\n"
    "v_cmp_gt_u32_e32 vcc, 1, v196\n"
    "s_cbranch_scc1 L_polled32_mk\n"
    "s_cbranch_vccnz L_poll31_mk\n"
    "L_polled32_mk:\n"
    "s_barrier\n"
    "s_mov_b32 s33, 0\n"
    "buffer_load_dwordx4 a[0:3], v232, s[24:27], s33 offen offset:0 sc1\n"
    "buffer_load_dwordx4 a[8:11], v232, s[24:27], s33 offen offset:1024 sc1\n"
    "buffer_load_dwordx4 a[16:19], v232, s[24:27], s33 offen offset:2048 sc1\n"
    "buffer_load_dwordx4 a[24:27], v232, s[24:27], s33 offen offset:3072 sc1\n"
    "s_mov_b32 s33, 147456\n"
    "buffer_load_dwordx4 a[4:7], v232, s[24:27], s33 offen offset:0 sc1\n"
    "buffer_load_dwordx4 a[12:15], v232, s[24:27], s33 offen offset:1024 sc1\n"
    "buffer_load_dwordx4 a[20:23], v232, s[24:27], s33 offen offset:2048 sc1\n"
    "buffer_load_dwordx4 a[28:31], v232, s[24:27], s33 offen offset:3072 sc1\n"
    "s_mov_b32 s33, 4096\n"
    "buffer_load_dwordx4 a[32:35], v232, s[24:27], s33 offen offset:0 sc1\n"
    "buffer_load_dwordx4 a[40:43], v232, s[24:27], s33 offen offset:1024 sc1\n"
    "buffer_load_dwordx4 a[48:51], v232, s[24:27], s33 offen offset:2048 sc1\n"
    "buffer_load_dwordx4 a[56:59], v232, s[24:27], s33 offen offset:3072 sc1\n"
    "s_mov_b32 s33, 151552\n"
    "buffer_load_dwordx4 a[36:39], v232, s[24:27], s33 offen offset:0 sc1\n"
    "buffer_load_dwordx4 a[44:47], v232, s[24:27], s33 offen offset:1024 sc1\n"
    "buffer_load_dwordx4 a[52:55], v232, s[24:27], s33 offen offset:2048 sc1\n"
    "buffer_load_dwordx4 a[60:63], v232, s[24:27], s33 offen offset:3072 sc1\n"
    "ds_read_b128 v[72:75], v233 offset:0\n"
    "ds_read_b128 v[76:79], v233 offset:1024\n"
    "ds_read_b128 v[80:83], v233 offset:2048\n"
    "ds_read_b128 v[84:87], v233 offset:3072\n"
    "ds_read_b128 v[88:91], v233 offset:4096\n"
    "ds_read_b128 v[92:95], v233 offset:5120\n"
    "ds_read_b128 v[96:99], v233 offset:6144\n"
    "ds_read_b128 v[100:103], v233 offset:7168\n"
    "s_waitcnt vmcnt(8) lgkmcnt(4)\n"
    "v_mfma_f32_16x16x32_bf16 v[0:3], v[72:75], a[0:3], 0\n"
    "ds_read_b128 v[104:107], v233 offset:8192\n"
    "ds_read_b128 v[108:111], v233 offset:9216\n"
    "v_mfma_f32_16x16x32_bf16 v[0:3], v[76:79], a[8:11], v[0:3]\n"
    "s_mov_b32 s33, 8192\n"
    "buffer_load_dwordx4 a[64:67], v232, s[24:27], s33 offen offset:0 sc1\n"
    "v_mfma_f32_16x16x32_bf16 v[0:3], v[80:83], a[16:19], v[0:3]\n"
    "ds_read_b128 v[112:115], v233 offset:10240\n"
    "ds_read_b128 v[116:119], v233 offset:11264\n"
    "v_mfma_f32_16x16x32_bf16 v[0:3], v[84:87], a[24:27], v[0:3]\n"
    "v_mfma_f32_16x16x32_bf16 v[4:7], v[84:87], a[28:31], 0\n"
    "v_mfma_f32_16x16x32_bf16 v[4:7], v[80:83], a[20:23], v[4:7]\n"
    "v_mfma_f32_16x16x32_bf16 v[4:7], v[76:79], a[12:15], v[4:7]\n"
    "v_mfma_f32_16x16x32_bf16 v[4:7], v[72:75], a[4:7], v[4:7]\n"
    "s_waitcnt lgkmcnt(4)\n"
    "v_mfma_f32_16x16x32_bf16 v[12:15], v[88:91], a[4:7], 0\n"
    "ds_read_b128 v[72:75], v233 offset:12288\n"
    "ds_read_b128 v[76:79], v233 offset:13312\n"
    "v_mfma_f32_16x16x32_bf16 v[12:15], v[92:95], a[12:15], v[12:15]\n"
    "buffer_load_dwordx4 a[72:75], v232, s[24:27], s33 offen offset:1024 sc1\n"
    "v_mfma_f32_16x16x32_bf16 v[12:15], v[96:99], a[20:23], v[12:15]\n"
    "ds_read_b128 v[80:83], v233 offset:14336\n"
    "ds_read_b128 v[84:87], v233 offset:15360\n"
    "v_mfma_f32_16x16x32_bf16 v[12:15], v[100:103], a[28:31], v[12:15]\n"
    "v_mfma_f32_16x16x32_bf16 v[8:11], v[100:103], a[24:27], 0\n"
    "v_mfma_f32_16x16x32_bf16 v[8:11], v[96:99], a[16:19], v[8:11]\n"
    "buffer_load_dwordx4 a[80:83], v232, s[24:27], s33 offen offset:2048 sc1\n"
    "v_mfma_f32_16x16x32_bf16 v[8:11], v[92:95], a[8:11], v[8:11]\n"
    "v_mfma_f32_16x16x32_bf16 v[8:11], v[88:91], a[0:3], v[8:11]\n"
    "s_waitcnt lgkmcnt(4)\n"
    "v_mfma_f32_16x16x32_bf16 v[16:19], v[104:107], a[0:3], 0\n"
    "ds_read_b128 v[88:91], v233 offset:16384\n"
    "ds_read_b128 v[92:95], v233 offset:17408\n"
    "v_mfma_f32_16x16x32_bf16 v[16:19], v[108:111], a[8:11], v[16:19]\n"
    "buffer_load_dwordx4 a[88:91], v232, s[24:27], s33 offen offset:3072 sc1\n"
    "v_mfma_f32_16x16x32_bf16 v[16:19], v[112:115], a[16:19], v[16:19]\n"
    "ds_read_b128 v[96:99], v233 offset:18432\n"
    "ds_read_b128 v[100:103], v233 offset:19456\n"
    "v_mfma_f32_16x16x32_bf16 v[16:19], v[116:119], a[24:27], v[16:19]\n"
    "v_mfma_f32_16x16x32_bf16 v[20:23], v[116:119], a[28:31], 0\n"
    "v_mfma_f32_16x16x32_bf16 v[20:23], v[112:115], a[20:23], v[20:23]\n"
    "s_mov_b32 s33, 155648\n"
    "buffer_load_dwordx4 a[68:71], v232, s[24:27], s33 offen offset:0 sc1\n"
    "v_mfma_f32_16x16x32_bf16 v[20:23], v[108:111], a[12:15], v[20:23]\n"
    "v_mfma_f32_16x16x32_bf16 v[20:23], v[104:107], a[4:7], v[20:23]\n"
    "s_waitcnt lgkmcnt(4)\n"
    "v_mfma_f32_16x16x32_bf16 v[28:31], v[72:75], a[4:7], 0\n"
    "ds_read_b128 v[104:107], v233 offset:20480\n"
    "ds_read_b128 v[108:111], v233 offset:21504\n"
    "v_mfma_f32_16x16x32_bf16 v[28:31], v[76:79], a[12:15], v[28:31]\n"
    "buffer_load_dwordx4 a[76:79], v232, s[24:27], s33 offen offset:1024 sc1\n"
    "v_mfma_f32_16x16x32_bf16 v[28:31], v[80:83], a[20:23], v[28:31]\n"
    "ds_read_b128 v[112:115], v233 offset:22528\n"
    "ds_read_b128 v[116:119], v233 offset:23552\n"
    "v_mfma_f32_16x16x32_bf16 v[28:31], v[84:87], a[28:31], v[28:31]\n"
    "v_mfma_f32_16x16x32_bf16 v[24:27], v[84:87], a[24:27], 0\n"
    "v_mfma_f32_16x16x32_bf16 v[24:27], v[80:83], a[16:19], v[24:27]\n"
    "v_mfma_f32_16x16x32_bf16 v[24:27], v[76:79], a[8:11], v[24:27]\n"
    "v_mfma_f32_16x16x32_bf16 v[24:27], v[72:75], a[0:3], v[24:27]\n"
    "s_waitcnt lgkmcnt(4)\n"
    "v_mfma_f32_16x16x32_bf16 v[32:35], v[88:91], a[0:3], 0\n"
    "ds_read_b128 v[72:75], v233 offset:24576\n"
    "ds_read_b128 v[76:79], v233 offset:25600\n"
    "v_mfma_f32_16x16x32_bf16 v[32:35], v[92:95], a[8:11], v[32:35]\n"
    "buffer_load_dwordx4 a[84:87], v232, s[24:27], s33 offen offset:2048 sc1\n"
    "v_mfma_f32_16x16x32_bf16 v[32:35], v[96:99], a[16:19], v[32:35]\n"
    "ds_read_b128 v[80:83], v233 offset:26624\n"
    "ds_read_b128 v[84:87], v233 offset:27648\n"
    "v_mfma_f32_16x16x32_bf16 v[32:35], v[100:103], a[24:27], v[32:35]\n"
    "v_mfma_f32_16x16x32_bf16 v[36:39], v[100:103], a[28:31], 0\n"
    "v_mfma_f32_16x16x32_bf16 v[36:39], v[96:99], a[20:23], v[36:39]\n"
    "buffer_load_dwordx4 a[92:95], v232, s[24:27], s33 offen offset:3072 sc1\n"
    "v_mfma_f32_16x16x32_bf16 v[36:39], v[92:95], a[12:15], v[36:39]\n"
    "v_mfma_f32_16x16x32_bf16 v[36:39], v[88:91], a[4:7], v[36:39]\n"
    "s_waitcnt lgkmcnt(4)\n"
    "v_mfma_f32_16x16x32_bf16 v[44:47], v[104:107], a[4:7], 0\n"
    "ds_read_b128 v[88:91], v233 offset:28672\n"
    "ds_read_b128 v[92:95], v233 offset:29696\n"
    "v_mfma_f32_16x16x32_bf16 v[44:47], v[108:111], a[12:15], v[44:47]\n"
    "s_mov_b32 s33, 12288\n"
    "buffer_load_dwordx4 a[96:99], v232, s[24:27], s33 offen offset:0 sc1\n"
    "v_mfma_f32_16x16x32_bf16 v[44:47], v[112:115], a[20:23], v[44:47]\n"
    "ds_read_b128 v[96:99], v233 offset:30720\n"
    "ds_read_b128 v[100:103], v233 offset:31744\n"
    "v_mfma_f32_16x16x32_bf16 v[44:47], v[116:119], a[28:31], v[44:47]\n"
    "v_mfma_f32_16x16x32_bf16 v[40:43], v[116:119], a[24:27], 0\n"
    "v_mfma_f32_16x16x32_bf16 v[40:43], v[112:115], a[16:19], v[40:43]\n"
    "buffer_load_dwordx4 a[104:107], v232, s[24:27], s33 offen offset:1024 sc1\n"
    "v_mfma_f32_16x16x32_bf16 v[40:43], v[108:111], a[8:11], v[40:43]\n"
    "v_mfma_f32_16x16x32_bf16 v[40:43], v[104:107], a[0:3], v[40:43]\n"
    "s_waitcnt lgkmcnt(4)\n"
    "v_mfma_f32_16x16x32_bf16 v[48:51], v[72:75], a[0:3], 0\n"
    "ds_read_b128 v[104:107], v233 offset:32768\n"
    "ds_read_b128 v[108:111], v233 offset:33792\n"
    "v_mfma_f32_16x16x32_bf16 v[48:51], v[76:79], a[8:11], v[48:51]\n"
    "buffer_load_dwordx4 a[112:115], v232, s[24:27], s33 offen offset:2048 sc1\n"
    "v_mfma_f32_16x16x32_bf16 v[48:51], v[80:83], a[16:19], v[48:51]\n"
    "ds_read_b128 v[112:115], v233 offset:34816\n"
    "ds_read_b128 v[116:119], v233 offset:35840\n"
    "v_mfma_f32_16x16x32_bf16 v[48:51], v[84:87], a[24:27], v[48:51]\n"
    "v_mfma_f32_16x16x32_bf16 v[52:55], v[84:87], a[28:31], 0\n"
    "v_mfma_f32_16x16x32_bf16 v[52:55], v[80:83], a[20:23], v[52:55]\n"
    "v_mfma_f32_16x16x32_bf16 v[52:55], v[76:79], a[12:15], v[52:55]\n"
    "v_mfma_f32_16x16x32_bf16 v[52:55], v[72:75], a[4:7], v[52:55]\n"
    "s_waitcnt vmcnt(11) lgkmcnt(0)\n"
    "v_mfma_f32_16x16x32_bf16 v[4:7], v[88:91], a[36:39], v[4:7]\n"
    "s_barrier\n"
    "v_mfma_f32_16x16x32_bf16 v[4:7], v[92:95], a[44:47], v[4:7]\n"
    "buffer_load_dwordx4 a[120:123], v232, s[24:27], s33 offen offset:3072 sc1\n"
    "v_mfma_f32_16x16x32_bf16 v[4:7], v[96:99], a[52:55], v[4:7]\n"
    "ds_read_b128 v[72:75], v233 offset:36864\n"
    "ds_read_b128 v[76:79], v233 offset:37888\n"
    "v_mfma_f32_16x16x32_bf16 v[4:7], v[100:103], a[60:63], v[4:7]\n"
    "ds_read_b128 v[80:83], v233 offset:38912\n"
    "ds_read_b128 v[84:87], v233 offset:39936\n"
    "v_mfma_f32_16x16x32_bf16 v[0:3], v[100:103], a[56:59], v[0:3]\n"
    "v_mfma_f32_16x16x32_bf16 v[0:3], v[96:99], a[48:51], v[0:3]\n"
    "s_mov_b32 s33, 159744\n"
    "buffer_load_dwordx4 a[100:103], v232, s[24:27], s33 offen offset:0 sc1\n"
    "v_mfma_f32_16x16x32_bf16 v[0:3], v[92:95], a[40:43], v[0:3]\n"
    "v_mfma_f32_16x16x32_bf16 v[0:3], v[88:91], a[32:35], v[0:3]\n"
    "v_mfma_f32_16x16x32_bf16 v[8:11], v[104:107], a[32:35], v[8:11]\n"
    "ds_read_b128 v[88:91], v233 offset:40960\n"
    "ds_read_b128 v[92:95], v233 offset:41984\n"
    "v_mfma_f32_16x16x32_bf16 v[8:11], v[108:111], a[40:43], v[8:11]\n"
    "buffer_load_dwordx4 a[108:111], v232, s[24:27], s33 offen offset:1024 sc1\n"
    "v_mfma_f32_16x16x32_bf16 v[8:11], v[112:115], a[48:51], v[8:11]\n"
    "ds_read_b128 v[96:99], v233 offset:43008\n"
    "ds_read_b128 v[100:103], v233 offset:44032\n"
    "v_mfma_f32_16x16x32_bf16 v[8:11], v[116:119], a[56:59], v[8:11]\n"
    "v_mfma_f32_16x16x32_bf16 v[12:15], v[116:119], a[60:63], v[12:15]\n"
    "v_mfma_f32_16x16x32_bf16 v[12:15], v[112:115], a[52:55], v[12:15]\n"
    "buffer_load_dwordx4 a[116:119], v232, s[24:27], s33 offen offset:2048 sc1\n"
    "v_mfma_f32_16x16x32_bf16 v[12:15], v[108:111], a[44:47], v[12:15]\n"
    "v_mfma_f32_16x16x32_bf16 v[12:15], v[104:107], a[36:39], v[12:15]\n"
    "s_waitcnt lgkmcnt(4)\n"
    "v_mfma_f32_16x16x32_bf16 v[20:23], v[72:75], a[36:39], v[20:23]\n"
    "ds_read_b128 v[104:107], v233 offset:45056\n"
    "ds_read_b128 v[108:111], v233 offset:46080\n"
    "v_mfma_f32_16x16x32_bf16 v[20:23], v[76:79], a[44:47], v[20:23]\n"
    "buffer_load_dwordx4 a[124:127], v232, s[24:27], s33 offen offset:3072 sc1\n"
    "v_mfma_f32_16x16x32_bf16 v[20:23], v[80:83], a[52:55], v[20:23]\n"
    "ds_read_b128 v[112:115], v233 offset:47104\n"
    "ds_read_b128 v[116:119], v233 offset:48128\n"
    "v_mfma_f32_16x16x32_bf16 v[20:23], v[84:87], a[60:63], v[20:23]\n"
    "v_mfma_f32_16x16x32_bf16 v[16:19], v[84:87], a[56:59], v[16:19]\n"
    "v_mfma_f32_16x16x32_bf16 v[16:19], v[80:83], a[48:51], v[16:19]\n"
    "v_mfma_f32_16x16x32_bf16 v[16:19], v[76:79], a[40:43], v[16:19]\n"
    "v_mfma_f32_16x16x32_bf16 v[16:19], v[72:75], a[32:35], v[16:19]\n"
    "s_waitcnt lgkmcnt(4)\n"
    "v_mfma_f32_16x16x32_bf16 v[24:27], v[88:91], a[32:35], v[24:27]\n"
    "ds_read_b128 v[72:75], v233 offset:49152\n"
    "ds_read_b128 v[76:79], v233 offset:50176\n"
    "v_mfma_f32_16x16x32_bf16 v[24:27], v[92:95], a[40:43], v[24:27]\n"
    "s_mov_b32 s33, 16384\n"
    "buffer_load_dwordx4 a[128:131], v232, s[24:27], s33 offen offset:0 sc1\n"
    "v_mfma_f32_16x16x32_bf16 v[24:27], v[96:99], a[48:51], v[24:27]\n"
    "ds_read_b128 v[80:83], v233 offset:51200\n"
    "ds_read_b128 v[84:87], v233 offset:52224\n"
    "v_mfma_f32_16x16x32_bf16 v[24:27], v[100:103], a[56:59], v[24:27]\n"
    "v_mfma_f32_16x16x32_bf16 v[28:31], v[100:103], a[60:63], v[28:31]\n"
    "v_mfma_f32_16x16x32_bf16 v[28:31], v[96:99], a[52:55], v[28:31]\n"
    "buffer_load_dwordx4 a[136:139], v232, s[24:27], s33 offen offset:1024 sc1\n"
    "v_mfma_f32_16x16x32_bf16 v[28:31], v[92:95], a[44:47], v[28:31]\n"
    "v_mfma_f32_16x16x32_bf16 v[28:31], v[88:91], a[36:39], v[28:31]\n"
    "s_waitcnt lgkmcnt(4)\n"
    "v_mfma_f32_16x16x32_bf16 v[36:39], v[104:107], a[36:39], v[36:39]\n"
    "ds_read_b128 v[88:91], v233 offset:53248\n"
    "ds_read_b128 v[92:95], v233 offset:54272\n"
    "v_mfma_f32_16x16x32_bf16 v[36:39], v[108:111], a[44:47], v[36:39]\n"
    "buffer_load_dwordx4 a[144:147], v232, s[24:27], s33 offen offset:2048 sc1\n"
    "v_mfma_f32_16x16x32_bf16 v[36:39], v[112:115], a[52:55], v[36:39]\n"
    "ds_read_b128 v[96:99], v233 offset:55296\n"
    "ds_read_b128 v[100:103], v233 offset:56320\n"
    "v_mfma_f32_16x16x32_bf16 v[36:39], v[116:119], a[60:63], v[36:39]\n"
    "v_mfma_f32_16x16x32_bf16 v[32:35], v[116:119], a[56:59], v[32:35]\n"
    "v_mfma_f32_16x16x32_bf16 v[32:35], v[112:115], a[48:51], v[32:35]\n"
    "buffer_load_dwordx4 a[152:155], v232, s[24:27], s33 offen offset:3072 sc1\n"
    "v_mfma_f32_16x16x32_bf16 v[32:35], v[108:111], a[40:43], v[32:35]\n"
    "v_mfma_f32_16x16x32_bf16 v[32:35], v[104:107], a[32:35], v[32:35]\n"
    "s_waitcnt lgkmcnt(4)\n"
    "v_mfma_f32_16x16x32_bf16 v[40:43], v[72:75], a[32:35], v[40:43]\n"
    "ds_read_b128 v[104:107], v233 offset:57344\n"
    "ds_read_b128 v[108:111], v233 offset:58368\n"
    "v_mfma_f32_16x16x32_bf16 v[40:43], v[76:79], a[40:43], v[40:43]\n"
    "s_mov_b32 s33, 163840\n"
    "buffer_load_dwordx4 a[132:135], v232, s[24:27], s33 offen offset:0 sc1\n"
    "v_mfma_f32_16x16x32_bf16 v[40:43], v[80:83], a[48:51], v[40:43]\n"
    "ds_read_b128 v[112:115], v233 offset:59392\n"
    "ds_read_b128 v[116:119], v233 offset:60416\n"
    "v_mfma_f32_16x16x32_bf16 v[40:43], v[84:87], a[56:59], v[40:43]\n"
    "v_mfma_f32_16x16x32_bf16 v[44:47], v[84:87], a[60:63], v[44:47]\n"
    "v_mfma_f32_16x16x32_bf16 v[44:47], v[80:83], a[52:55], v[44:47]\n"
    "v_mfma_f32_16x16x32_bf16 v[44:47], v[76:79], a[44:47], v[44:47]\n"
    "v_mfma_f32_16x16x32_bf16 v[44:47], v[72:75], a[36:39], v[44:47]\n"
    "s_waitcnt lgkmcnt(4)\n"
    "v_mfma_f32_16x16x32_bf16 v[52:55], v[88:91], a[36:39], v[52:55]\n"
    "ds_read_b128 v[72:75], v233 offset:61440\n"
    "ds_read_b128 v[76:79], v233 offset:62464\n"
    "v_mfma_f32_16x16x32_bf16 v[52:55], v[92:95], a[44:47], v[52:55]\n"
    "buffer_load_dwordx4 a[140:143], v232, s[24:27], s33 offen offset:1024 sc1\n"
    "v_mfma_f32_16x16x32_bf16 v[52:55], v[96:99], a[52:55], v[52:55]\n"
    "ds_read_b128 v[80:83], v233 offset:63488\n"
    "ds_read_b128 v[84:87], v233 offset:64512\n"
    "v_mfma_f32_16x16x32_bf16 v[52:55], v[100:103], a[60:63], v[52:55]\n"
    "v_mfma_f32_16x16x32_bf16 v[48:51], v[100:103], a[56:59], v[48:51]\n"
    "v_mfma_f32_16x16x32_bf16 v[48:51], v[96:99], a[48:51], v[48:51]\n"
    "buffer_load_dwordx4 a[148:151], v232, s[24:27], s33 offen offset:2048 sc1\n"
    "v_mfma_f32_16x16x32_bf16 v[48:51], v[92:95], a[40:43], v[48:51]\n"
    "v_mfma_f32_16x16x32_bf16 v[48:51], v[88:91], a[32:35], v[48:51]\n"
    "s_waitcnt vmcnt(15) lgkmcnt(4)\n"
    "v_mfma_f32_16x16x32_bf16 v[0:3], v[104:107], a[64:67], v[0:3]\n"
    "ds_read_b128 v[88:91], v234 offset:0\n"
    "ds_read_b128 v[92:95], v234 offset:1024\n"
    "v_mfma_f32_16x16x32_bf16 v[0:3], v[108:111], a[72:75], v[0:3]\n"
    "buffer_load_dwordx4 a[156:159], v232, s[24:27], s33 offen offset:3072 sc1\n"
    "v_mfma_f32_16x16x32_bf16 v[0:3], v[112:115], a[80:83], v[0:3]\n"
    "ds_read_b128 v[96:99], v234 offset:2048\n"
    "ds_read_b128 v[100:103], v234 offset:3072\n"
    "v_mfma_f32_16x16x32_bf16 v[0:3], v[116:119], a[88:91], v[0:3]\n"
    "v_mfma_f32_16x16x32_bf16 v[4:7], v[116:119], a[92:95], v[4:7]\n"
    "s_add_u32 m0, s52, 0\n"
    "v_mfma_f32_16x16x32_bf16 v[4:7], v[112:115], a[84:87], v[4:7]\n"
    "s_add_u32 s53, s52, 147456\n"
    "buffer_load_dwordx4 v232, s[48:51], s53 offen offset:0 sc1 lds\n"
    "v_mfma_f32_16x16x32_bf16 v[4:7], v[108:111], a[76:79], v[4:7]\n"
    "v_mfma_f32_16x16x32_bf16 v[4:7], v[104:107], a[68:71], v[4:7]\n"
    "s_waitcnt lgkmcnt(4)\n"
    "v_mfma_f32_16x16x32_bf16 v[12:15], v[72:75], a[68:71], v[12:15]\n"
    "ds_read_b128 v[104:107], v234 offset:4096\n"
    "ds_read_b128 v[108:111], v234 offset:5120\n"
    "v_mfma_f32_16x16x32_bf16 v[12:15], v[76:79], a[76:79], v[12:15]\n"
    "buffer_load_dwordx4 v232, s[48:51], s53 offen offset:1024 sc1 lds\n"
    "v_mfma_f32_16x16x32_bf16 v[12:15], v[80:83], a[84:87], v[12:15]\n"
    "ds_read_b128 v[112:115], v234 offset:6144\n"
    "ds_read_b128 v[116:119], v234 offset:7168\n"
    "v_mfma_f32_16x16x32_bf16 v[12:15], v[84:87], a[92:95], v[12:15]\n"
    "v_mfma_f32_16x16x32_bf16 v[8:11], v[84:87], a[88:91], v[8:11]\n"
    "v_mfma_f32_16x16x32_bf16 v[8:11], v[80:83], a[80:83], v[8:11]\n"
    "v_mfma_f32_16x16x32_bf16 v[8:11], v[76:79], a[72:75], v[8:11]\n"
    "v_mfma_f32_16x16x32_bf16 v[8:11], v[72:75], a[64:67], v[8:11]\n"
    "s_waitcnt vmcnt(42) lgkmcnt(0)\n"
    "v_mfma_f32_16x16x32_bf16 v[16:19], v[88:91], a[64:67], v[16:19]\n"
    "s_barrier\n"
    "v_mfma_f32_16x16x32_bf16 v[16:19], v[92:95], a[72:75], v[16:19]\n"
    "buffer_load_dwordx4 v232, s[48:51], s53 offen offset:2048 sc1 lds\n"
    "v_mfma_f32_16x16x32_bf16 v[16:19], v[96:99], a[80:83], v[16:19]\n"
    "ds_read_b128 v[72:75], v234 offset:8192\n"
    "ds_read_b128 v[76:79], v234 offset:9216\n"
    "v_mfma_f32_16x16x32_bf16 v[16:19], v[100:103], a[88:91], v[16:19]\n"
    "ds_read_b128 v[80:83], v234 offset:10240\n"
    "ds_read_b128 v[84:87], v234 offset:11264\n"
    "v_mfma_f32_16x16x32_bf16 v[20:23], v[100:103], a[92:95], v[20:23]\n"
    "v_mfma_f32_16x16x32_bf16 v[20:23], v[96:99], a[84:87], v[20:23]\n"
    "buffer_load_dwordx4 v232, s[48:51], s53 offen offset:3072 sc1 lds\n"
    "v_mfma_f32_16x16x32_bf16 v[20:23], v[92:95], a[76:79], v[20:23]\n"
    "v_mfma_f32_16x16x32_bf16 v[20:23], v[88:91], a[68:71], v[20:23]\n"
    "v_mfma_f32_16x16x32_bf16 v[28:31], v[104:107], a[68:71], v[28:31]\n"
    "s_add_u32 m0, s52, 4096\n"
    "ds_read_b128 v[88:91], v234 offset:12288\n"
    "ds_read_b128 v[92:95], v234 offset:13312\n"
    "v_mfma_f32_16x16x32_bf16 v[28:31], v[108:111], a[76:79], v[28:31]\n"
    "s_add_u32 s53, s52, 151552\n"
    "buffer_load_dwordx4 v232, s[48:51], s53 offen offset:0 sc1 lds\n"
    "v_mfma_f32_16x16x32_bf16 v[28:31], v[112:115], a[84:87], v[28:31]\n"
    "ds_read_b128 v[96:99], v234 offset:14336\n"
    "ds_read_b128 v[100:103], v234 offset:15360\n"
    "v_mfma_f32_16x16x32_bf16 v[28:31], v[116:119], a[92:95], v[28:31]\n"
    "v_mfma_f32_16x16x32_bf16 v[24:27], v[116:119], a[88:91], v[24:27]\n"
    "v_mfma_f32_16x16x32_bf16 v[24:27], v[112:115], a[80:83], v[24:27]\n"
    "buffer_load_dwordx4 v232, s[48:51], s53 offen offset:1024 sc1 lds\n"
    "v_mfma_f32_16x16x32_bf16 v[24:27], v[108:111], a[72:75], v[24:27]\n"
    "v_mfma_f32_16x16x32_bf16 v[24:27], v[104:107], a[64:67], v[24:27]\n"
    "s_waitcnt lgkmcnt(4)\n"
    "v_mfma_f32_16x16x32_bf16 v[32:35], v[72:75], a[64:67], v[32:35]\n"
    "ds_read_b128 v[104:107], v234 offset:16384\n"
    "ds_read_b128 v[108:111], v234 offset:17408\n"
    "v_mfma_f32_16x16x32_bf16 v[32:35], v[76:79], a[72:75], v[32:35]\n"
    "buffer_load_dwordx4 v232, s[48:51], s53 offen offset:2048 sc1 lds\n"
    "v_mfma_f32_16x16x32_bf16 v[32:35], v[80:83], a[80:83], v[32:35]\n"
    "ds_read_b128 v[112:115], v234 offset:18432\n"
    "ds_read_b128 v[116:119], v234 offset:19456\n"
    "v_mfma_f32_16x16x32_bf16 v[32:35], v[84:87], a[88:91], v[32:35]\n"
    "v_mfma_f32_16x16x32_bf16 v[36:39], v[84:87], a[92:95], v[36:39]\n"
    "v_mfma_f32_16x16x32_bf16 v[36:39], v[80:83], a[84:87], v[36:39]\n"
    "v_mfma_f32_16x16x32_bf16 v[36:39], v[76:79], a[76:79], v[36:39]\n"
    "v_mfma_f32_16x16x32_bf16 v[36:39], v[72:75], a[68:71], v[36:39]\n"
    "s_waitcnt lgkmcnt(4)\n"
    "v_mfma_f32_16x16x32_bf16 v[44:47], v[88:91], a[68:71], v[44:47]\n"
    "ds_read_b128 v[72:75], v234 offset:20480\n"
    "ds_read_b128 v[76:79], v234 offset:21504\n"
    "v_mfma_f32_16x16x32_bf16 v[44:47], v[92:95], a[76:79], v[44:47]\n"
    "buffer_load_dwordx4 v232, s[48:51], s53 offen offset:3072 sc1 lds\n"
    "v_mfma_f32_16x16x32_bf16 v[44:47], v[96:99], a[84:87], v[44:47]\n"
    "ds_read_b128 v[80:83], v234 offset:22528\n"
    "ds_read_b128 v[84:87], v234 offset:23552\n"
    "v_mfma_f32_16x16x32_bf16 v[44:47], v[100:103], a[92:95], v[44:47]\n"
    "v_mfma_f32_16x16x32_bf16 v[40:43], v[100:103], a[88:91], v[40:43]\n"
    "s_add_u32 m0, s52, 8192\n"
    "v_mfma_f32_16x16x32_bf16 v[40:43], v[96:99], a[80:83], v[40:43]\n"
    "s_add_u32 s53, s52, 155648\n"
    "buffer_load_dwordx4 v232, s[48:51], s53 offen offset:0 sc1 lds\n"
    "v_mfma_f32_16x16x32_bf16 v[40:43], v[92:95], a[72:75], v[40:43]\n"
    "v_mfma_f32_16x16x32_bf16 v[40:43], v[88:91], a[64:67], v[40:43]\n"
    "s_waitcnt lgkmcnt(4)\n"
    "v_mfma_f32_16x16x32_bf16 v[48:51], v[104:107], a[64:67], v[48:51]\n"
    "ds_read_b128 v[88:91], v234 offset:24576\n"
    "ds_read_b128 v[92:95], v234 offset:25600\n"
    "v_mfma_f32_16x16x32_bf16 v[48:51], v[108:111], a[72:75], v[48:51]\n"
    "s_mov_b32 s33, 20480\n"
    "buffer_load_dwordx4 a[160:163], v232, s[24:27], s33 offen offset:0 sc1\n"
    "v_mfma_f32_16x16x32_bf16 v[48:51], v[112:115], a[80:83], v[48:51]\n"
    "ds_read_b128 v[96:99], v234 offset:26624\n"
    "L_chain2x3_mk:\n"
    "s_cbranch_execz L_pad2_4_mk\n"
    "ds_read_b128 v[100:103], v234 offset:27648\n"
    "v_mfma_f32_16x16x32_bf16 v[48:51], v[116:119], a[88:91], v[48:51]\n"
    "v_mfma_f32_16x16x32_bf16 v[52:55], v[116:119], a[92:95], v[52:55]\n"
    "v_mfma_f32_16x16x32_bf16 v[52:55], v[112:115], a[84:87], v[52:55]\n"
    "buffer_load_dwordx4 a[168:171], v232, s[24:27], s33 offen offset:1024 sc1\n"
    "v_mfma_f32_16x16x32_bf16 v[52:55], v[108:111], a[76:79], v[52:55]\n"
    "v_mfma_f32_16x16x32_bf16 v[52:55], v[104:107], a[68:71], v[52:55]\n"
    "s_waitcnt vmcnt(19) lgkmcnt(4)\n"
    "v_mfma_f32_16x16x32_bf16 v[4:7], v[72:75], a[100:103], v[4:7]\n"
    "ds_read_b128 v[104:107], v234 offset:28672\n"
    "ds_read_b128 v[108:111], v234 offset:29696\n"
    "v_mfma_f32_16x16x32_bf16 v[4:7], v[76:79], a[108:111], v[4:7]\n"
    "buffer_load_dwordx4 a[176:179], v232, s[24:27], s33 offen offset:2048 sc1\n"
    "v_mfma_f32_16x16x32_bf16 v[4:7], v[80:83], a[116:119], v[4:7]\n"
    "ds_read_b128 v[112:115], v234 offset:30720\n"
    "ds_read_b128 v[116:119], v234 offset:31744\n"
    "v_mfma_f32_16x16x32_bf16 v[4:7], v[84:87], a[124:127], v[4:7]\n"
    "v_mfma_f32_16x16x32_bf16 v[0:3], v[84:87], a[120:123], v[0:3]\n"
    "v_mfma_f32_16x16x32_bf16 v[0:3], v[80:83], a[112:115], v[0:3]\n"
    "v_mfma_f32_16x16x32_bf16 v[0:3], v[76:79], a[104:107], v[0:3]\n"
    "v_mfma_f32_16x16x32_bf16 v[0:3], v[72:75], a[96:99], v[0:3]\n"
    "s_waitcnt lgkmcnt(4)\n"
    "v_mfma_f32_16x16x32_bf16 v[8:11], v[88:91], a[96:99], v[8:11]\n"
    "ds_read_b128 v[72:75], v234 offset:32768\n"
    "ds_read_b128 v[76:79], v234 offset:33792\n"
    "v_mfma_f32_16x16x32_bf16 v[8:11], v[92:95], a[104:107], v[8:11]\n"
    "buffer_load_dwordx4 a[184:187], v232, s[24:27], s33 offen offset:3072 sc1\n"
    "v_mfma_f32_16x16x32_bf16 v[8:11], v[96:99], a[112:115], v[8:11]\n"
    "ds_read_b128 v[80:83], v234 offset:34816\n"
    "ds_read_b128 v[84:87], v234 offset:35840\n"
    "v_mfma_f32_16x16x32_bf16 v[8:11], v[100:103], a[120:123], v[8:11]\n"
    "v_mfma_f32_16x16x32_bf16 v[12:15], v[100:103], a[124:127], v[12:15]\n"
    "v_mfma_f32_16x16x32_bf16 v[12:15], v[96:99], a[116:119], v[12:15]\n"
    "s_mov_b32 s33, 167936\n"
    "buffer_load_dwordx4 a[164:167], v232, s[24:27], s33 offen offset:0 sc1\n"
    "v_mfma_f32_16x16x32_bf16 v[12:15], v[92:95], a[108:111], v[12:15]\n"
    "v_mfma_f32_16x16x32_bf16 v[12:15], v[88:91], a[100:103], v[12:15]\n"
    "s_waitcnt lgkmcnt(4)\n"
    "v_mfma_f32_16x16x32_bf16 v[20:23], v[104:107], a[100:103], v[20:23]\n"
    "ds_read_b128 v[88:91], v234 offset:36864\n"
    "ds_read_b128 v[92:95], v234 offset:37888\n"
    "v_mfma_f32_16x16x32_bf16 v[20:23], v[108:111], a[108:111], v[20:23]\n"
    "buffer_load_dwordx4 a[172:175], v232, s[24:27], s33 offen offset:1024 sc1\n"
    "v_mfma_f32_16x16x32_bf16 v[20:23], v[112:115], a[116:119], v[20:23]\n"
    "ds_read_b128 v[96:99], v234 offset:38912\n"
    "ds_read_b128 v[100:103], v234 offset:39936\n"
    "v_mfma_f32_16x16x32_bf16 v[20:23], v[116:119], a[124:127], v[20:23]\n"
    "v_mfma_f32_16x16x32_bf16 v[16:19], v[116:119], a[120:123], v[16:19]\n"
    "v_mfma_f32_16x16x32_bf16 v[16:19], v[112:115], a[112:115], v[16:19]\n"
    "buffer_load_dwordx4 a[180:183], v232, s[24:27], s33 offen offset:2048 sc1\n"
    "v_mfma_f32_16x16x32_bf16 v[16:19], v[108:111], a[104:107], v[16:19]\n"
    "v_mfma_f32_16x16x32_bf16 v[16:19], v[104:107], a[96:99], v[16:19]\n"
    "s_waitcnt lgkmcnt(4)\n"
    "v_mfma_f32_16x16x32_bf16 v[24:27], v[72:75], a[96:99], v[24:27]\n"
    "ds_read_b128 v[104:107], v234 offset:40960\n"
    "ds_read_b128 v[108:111], v234 offset:41984\n"
    "v_mfma_f32_16x16x32_bf16 v[24:27], v[76:79], a[104:107], v[24:27]\n"
    "buffer_load_dwordx4 a[188:191], v232, s[24:27], s33 offen offset:3072 sc1\n"
    "v_mfma_f32_16x16x32_bf16 v[24:27], v[80:83], a[112:115], v[24:27]\n"
    "ds_read_b128 v[112:115], v234 offset:43008\n"
    "ds_read_b128 v[116:119], v234 offset:44032\n"
    "v_mfma_f32_16x16x32_bf16 v[24:27], v[84:87], a[120:123], v[24:27]\n"
    "v_mfma_f32_16x16x32_bf16 v[28:31], v[84:87], a[124:127], v[28:31]\n"
    "v_mfma_f32_16x16x32_bf16 v[28:31], v[80:83], a[116:119], v[28:31]\n"
    "v_mfma_f32_16x16x32_bf16 v[28:31], v[76:79], a[108:111], v[28:31]\n"
    "v_mfma_f32_16x16x32_bf16 v[28:31], v[72:75], a[100:103], v[28:31]\n"
    "s_waitcnt vmcnt(63) lgkmcnt(0)\n"
    "v_mfma_f32_16x16x32_bf16 v[36:39], v[88:91], a[100:103], v[36:39]\n"
    "s_barrier\n"
    "v_mfma_f32_16x16x32_bf16 v[36:39], v[92:95], a[108:111], v[36:39]\n"
    "s_add_u32 m0, s52, 36864\n"
    "s_add_u32 s53, s52, 184320\n"
    "ds_read_b128 v[72:75], v234 offset:45056\n"
    "v_mfma_f32_16x16x32_bf16 v[36:39], v[96:99], a[116:119], v[36:39]\n"
    "buffer_load_dwordx4 v232, s[48:51], s53 offen offset:0 sc1 lds\n"
    "v_mfma_f32_16x16x32_bf16 v[36:39], v[100:103], a[124:127], v[36:39]\n"
    "ds_read_b128 v[76:79], v234 offset:46080\n"
    "ds_read_b128 v[80:83], v234 offset:47104\n"
    "v_mfma_f32_16x16x32_bf16 v[32:35], v[100:103], a[120:123], v[32:35]\n"
    "ds_read_b128 v[84:87], v234 offset:48128\n"
    "v_mfma_f32_16x16x32_bf16 v[32:35], v[96:99], a[112:115], v[32:35]\n"
    "buffer_load_dwordx4 v232, s[48:51], s53 offen offset:1024 sc1 lds\n"
    "v_mfma_f32_16x16x32_bf16 v[32:35], v[92:95], a[104:107], v[32:35]\n"
    "v_mfma_f32_16x16x32_bf16 v[32:35], v[88:91], a[96:99], v[32:35]\n"
    "v_mfma_f32_16x16x32_bf16 v[40:43], v[104:107], a[96:99], v[40:43]\n"
    "ds_read_b128 v[88:91], v234 offset:49152\n"
    "ds_read_b128 v[92:95], v234 offset:50176\n"
    "v_mfma_f32_16x16x32_bf16 v[40:43], v[108:111], a[104:107], v[40:43]\n"
    "buffer_load_dwordx4 v232, s[48:51], s53 offen offset:2048 sc1 lds\n"
    "v_mfma_f32_16x16x32_bf16 v[40:43], v[112:115], a[112:115], v[40:43]\n"
    "ds_read_b128 v[96:99], v234 offset:51200\n"
    "ds_read_b128 v[100:103], v234 offset:52224\n"
    "v_mfma_f32_16x16x32_bf16 v[40:43], v[116:119], a[120:123], v[40:43]\n"
    "v_mfma_f32_16x16x32_bf16 v[44:47], v[116:119], a[124:127], v[44:47]\n"
    "v_mfma_f32_16x16x32_bf16 v[44:47], v[112:115], a[116:119], v[44:47]\n"
    "buffer_load_dwordx4 v232, s[48:51], s53 offen offset:3072 sc1 lds\n"
    "v_mfma_f32_16x16x32_bf16 v[44:47], v[108:111], a[108:111], v[44:47]\n"
    "v_mfma_f32_16x16x32_bf16 v[44:47], v[104:107], a[100:103], v[44:47]\n"
    "s_waitcnt lgkmcnt(4)\n"
    "v_mfma_f32_16x16x32_bf16 v[52:55], v[72:75], a[100:103], v[52:55]\n"
    "s_add_u32 m0, s52, 40960\n"
    "ds_read_b128 v[104:107], v234 offset:53248\n"
    "ds_read_b128 v[108:111], v234 offset:54272\n"
    "v_mfma_f32_16x16x32_bf16 v[52:55], v[76:79], a[108:111], v[52:55]\n"
    "s_add_u32 s53, s52, 188416\n"
    "buffer_load_dwordx4 v232, s[48:51], s53 offen offset:0 sc1 lds\n"
    "v_mfma_f32_16x16x32_bf16 v[52:55], v[80:83], a[116:119], v[52:55]\n"
    "ds_read_b128 v[112:115], v234 offset:55296\n"
    "ds_read_b128 v[116:119], v234 offset:56320\n"
    "v_mfma_f32_16x16x32_bf16 v[52:55], v[84:87], a[124:127], v[52:55]\n"
    "v_mfma_f32_16x16x32_bf16 v[48:51], v[84:87], a[120:123], v[48:51]\n"
    "v_mfma_f32_16x16x32_bf16 v[48:51], v[80:83], a[112:115], v[48:51]\n"
    "v_mfma_f32_16x16x32_bf16 v[48:51], v[76:79], a[104:107], v[48:51]\n"
    "v_mfma_f32_16x16x32_bf16 v[48:51], v[72:75], a[96:99], v[48:51]\n"
    "s_waitcnt vmcnt(22) lgkmcnt(4)\n"
    "v_mfma_f32_16x16x32_bf16 v[0:3], v[88:91], a[128:131], v[0:3]\n"
    "ds_read_b128 v[72:75], v234 offset:57344\n"
    "ds_read_b128 v[76:79], v234 offset:58368\n"
    "v_mfma_f32_16x16x32_bf16 v[0:3], v[92:95], a[136:139], v[0:3]\n"
    "buffer_load_dwordx4 v232, s[48:51], s53 offen offset:1024 sc1 lds\n"
    "v_mfma_f32_16x16x32_bf16 v[0:3], v[96:99], a[144:147], v[0:3]\n"
    "ds_read_b128 v[80:83], v234 offset:59392\n"
    "ds_read_b128 v[84:87], v234 offset:60416\n"
    "v_mfma_f32_16x16x32_bf16 v[0:3], v[100:103], a[152:155], v[0:3]\n"
    "v_mfma_f32_16x16x32_bf16 v[4:7], v[100:103], a[156:159], v[4:7]\n"
    "v_mfma_f32_16x16x32_bf16 v[4:7], v[96:99], a[148:151], v[4:7]\n"
    "buffer_load_dwordx4 v232, s[48:51], s53 offen offset:2048 sc1 lds\n"
    "v_mfma_f32_16x16x32_bf16 v[4:7], v[92:95], a[140:143], v[4:7]\n"
    "v_mfma_f32_16x16x32_bf16 v[4:7], v[88:91], a[132:135], v[4:7]\n"
    "s_waitcnt lgkmcnt(4)\n"
    "v_mfma_f32_16x16x32_bf16 v[12:15], v[104:107], a[132:135], v[12:15]\n"
    "ds_read_b128 v[88:91], v234 offset:61440\n"
    "ds_read_b128 v[92:95], v234 offset:62464\n"
    "v_mfma_f32_16x16x32_bf16 v[12:15], v[108:111], a[140:143], v[12:15]\n"
    "buffer_load_dwordx4 v232, s[48:51], s53 offen offset:3072 sc1 lds\n"
    "v_mfma_f32_16x16x32_bf16 v[12:15], v[112:115], a[148:151], v[12:15]\n"
    "ds_read_b128 v[96:99], v234 offset:63488\n"
    "ds_read_b128 v[100:103], v234 offset:64512\n"
    "v_mfma_f32_16x16x32_bf16 v[12:15], v[116:119], a[156:159], v[12:15]\n"
    "v_mfma_f32_16x16x32_bf16 v[8:11], v[116:119], a[152:155], v[8:11]\n"
    "s_add_u32 m0, s52, 45056\n"
    "v_mfma_f32_16x16x32_bf16 v[8:11], v[112:115], a[144:147], v[8:11]\n"
    "s_add_u32 s53, s52, 192512\n"
    "buffer_load_dwordx4 v232, s[48:51], s53 offen offset:0 sc1 lds\n"
    "v_mfma_f32_16x16x32_bf16 v[8:11], v[108:111], a[136:139], v[8:11]\n"
    "v_mfma_f32_16x16x32_bf16 v[8:11], v[104:107], a[128:131], v[8:11]\n"
    "s_waitcnt lgkmcnt(4)\n"
    "v_mfma_f32_16x16x32_bf16 v[16:19], v[72:75], a[128:131], v[16:19]\n"
    "ds_read_b128 v[104:107], v235 offset:0\n"
    "ds_read_b128 v[108:111], v235 offset:1024\n"
    "v_mfma_f32_16x16x32_bf16 v[16:19], v[76:79], a[136:139], v[16:19]\n"
    "s_mov_b32 s33, 24576\n"
    "buffer_load_dwordx4 a[192:195], v232, s[24:27], s33 offen offset:0 sc1\n"
    "v_mfma_f32_16x16x32_bf16 v[16:19], v[80:83], a[144:147], v[16:19]\n"
    "ds_read_b128 v[112:115], v235 offset:2048\n"
    "ds_read_b128 v[116:119], v235 offset:3072\n"
    "v_mfma_f32_16x16x32_bf16 v[16:19], v[84:87], a[152:155], v[16:19]\n"
    "v_mfma_f32_16x16x32_bf16 v[20:23], v[84:87], a[156:159], v[20:23]\n"
    "v_mfma_f32_16x16x32_bf16 v[20:23], v[80:83], a[148:151], v[20:23]\n"
    "v_mfma_f32_16x16x32_bf16 v[20:23], v[76:79], a[140:143], v[20:23]\n"
    "v_mfma_f32_16x16x32_bf16 v[20:23], v[72:75], a[132:135], v[20:23]\n"
    "s_waitcnt lgkmcnt(4)\n"
    "v_mfma_f32_16x16x32_bf16 v[28:31], v[88:91], a[132:135], v[28:31]\n"
    "ds_read_b128 v[72:75], v235 offset:4096\n"
    "ds_read_b128 v[76:79], v235 offset:5120\n"
    "v_mfma_f32_16x16x32_bf16 v[28:31], v[92:95], a[140:143], v[28:31]\n"
    "buffer_load_dwordx4 a[200:203], v232, s[24:27], s33 offen offset:1024 sc1\n"
    "v_mfma_f32_16x16x32_bf16 v[28:31], v[96:99], a[148:151], v[28:31]\n"
    "ds_read_b128 v[80:83], v235 offset:6144\n"
    "ds_read_b128 v[84:87], v235 offset:7168\n"
    "v_mfma_f32_16x16x32_bf16 v[28:31], v[100:103], a[156:159], v[28:31]\n"
    "v_mfma_f32_16x16x32_bf16 v[24:27], v[100:103], a[152:155], v[24:27]\n"
    "v_mfma_f32_16x16x32_bf16 v[24:27], v[96:99], a[144:147], v[24:27]\n"
    "buffer_load_dwordx4 a[208:211], v232, s[24:27], s33 offen offset:2048 sc1\n"
    "v_mfma_f32_16x16x32_bf16 v[24:27], v[92:95], a[136:139], v[24:27]\n"
    "v_mfma_f32_16x16x32_bf16 v[24:27], v[88:91], a[128:131], v[24:27]\n"
    "s_waitcnt lgkmcnt(4)\n"
    "v_mfma_f32_16x16x32_bf16 v[32:35], v[104:107], a[128:131], v[32:35]\n"
    "ds_read_b128 v[88:91], v235 offset:8192\n"
    "ds_read_b128 v[92:95], v235 offset:9216\n"
    "v_mfma_f32_16x16x32_bf16 v[32:35], v[108:111], a[136:139], v[32:35]\n"
    "buffer_load_dwordx4 a[216:219], v232, s[24:27], s33 offen offset:3072 sc1\n"
    "v_mfma_f32_16x16x32_bf16 v[32:35], v[112:115], a[144:147], v[32:35]\n"
    "ds_read_b128 v[96:99], v235 offset:10240\n"
    "ds_read_b128 v[100:103], v235 offset:11264\n"
    "v_mfma_f32_16x16x32_bf16 v[32:35], v[116:119], a[152:155], v[32:35]\n"
    "v_mfma_f32_16x16x32_bf16 v[36:39], v[116:119], a[156:159], v[36:39]\n"
    "v_mfma_f32_16x16x32_bf16 v[36:39], v[112:115], a[148:151], v[36:39]\n"
    "s_mov_b32 s33, 172032\n"
    "buffer_load_dwordx4 a[196:199], v232, s[24:27], s33 offen offset:0 sc1\n"
    "v_mfma_f32_16x16x32_bf16 v[36:39], v[108:111], a[140:143], v[36:39]\n"
    "v_mfma_f32_16x16x32_bf16 v[36:39], v[104:107], a[132:135], v[36:39]\n"
    "s_waitcnt lgkmcnt(4)\n"
    "v_mfma_f32_16x16x32_bf16 v[44:47], v[72:75], a[132:135], v[44:47]\n"
    "ds_read_b128 v[104:107], v235 offset:12288\n"
    "ds_read_b128 v[108:111], v235 offset:13312\n"
    "v_mfma_f32_16x16x32_bf16 v[44:47], v[76:79], a[140:143], v[44:47]\n"
    "buffer_load_dwordx4 a[204:207], v232, s[24:27], s33 offen offset:1024 sc1\n"
    "v_mfma_f32_16x16x32_bf16 v[44:47], v[80:83], a[148:151], v[44:47]\n"
    "ds_read_b128 v[112:115], v235 offset:14336\n"
    "ds_read_b128 v[116:119], v235 offset:15360\n"
    "v_mfma_f32_16x16x32_bf16 v[44:47], v[84:87], a[156:159], v[44:47]\n"
    "v_mfma_f32_16x16x32_bf16 v[40:43], v[84:87], a[152:155], v[40:43]\n"
    "v_mfma_f32_16x16x32_bf16 v[40:43], v[80:83], a[144:147], v[40:43]\n"
    "v_mfma_f32_16x16x32_bf16 v[40:43], v[76:79], a[136:139], v[40:43]\n"
    "v_mfma_f32_16x16x32_bf16 v[40:43], v[72:75], a[128:131], v[40:43]\n"
    "s_waitcnt vmcnt(23) lgkmcnt(0)\n"
    "v_mfma_f32_16x16x32_bf16 v[48:51], v[88:91], a[128:131], v[48:51]\n"
    "s_barrier\n"
    "v_mfma_f32_16x16x32_bf16 v[48:51], v[92:95], a[136:139], v[48:51]\n"
    "buffer_load_dwordx4 a[212:215], v232, s[24:27], s33 offen offset:2048 sc1\n"
    "v_mfma_f32_16x16x32_bf16 v[48:51], v[96:99], a[144:147], v[48:51]\n"
    "ds_read_b128 v[72:75], v233 offset:0\n"
    "ds_read_b128 v[76:79], v233 offset:1024\n"
    "v_mfma_f32_16x16x32_bf16 v[48:51], v[100:103], a[152:155], v[48:51]\n"
    "ds_read_b128 v[80:83], v233 offset:2048\n"
    "ds_read_b128 v[84:87], v233 offset:3072\n"
    "v_mfma_f32_16x16x32_bf16 v[52:55], v[100:103], a[156:159], v[52:55]\n"
    "v_mfma_f32_16x16x32_bf16 v[52:55], v[96:99], a[148:151], v[52:55]\n"
    "buffer_load_dwordx4 a[220:223], v232, s[24:27], s33 offen offset:3072 sc1\n"
    "v_mfma_f32_16x16x32_bf16 v[52:55], v[92:95], a[140:143], v[52:55]\n"
    "v_mfma_f32_16x16x32_bf16 v[52:55], v[88:91], a[132:135], v[52:55]\n"
    "s_waitcnt vmcnt(17) lgkmcnt(4)\n"
    "v_mfma_f32_16x16x32_bf16 v[4:7], v[104:107], a[164:167], v[4:7]\n"
    "ds_read_b128 v[88:91], v233 offset:4096\n"
    "ds_read_b128 v[92:95], v233 offset:5120\n"
    "v_mfma_f32_16x16x32_bf16 v[4:7], v[108:111], a[172:175], v[4:7]\n"
    "s_mov_b32 s33, 28672\n"
    "buffer_load_dwordx4 a[224:227], v232, s[24:27], s33 offen offset:0 sc1\n"
    "v_mfma_f32_16x16x32_bf16 v[4:7], v[112:115], a[180:183], v[4:7]\n"
    "ds_read_b128 v[96:99], v233 offset:6144\n"
    "ds_read_b128 v[100:103], v233 offset:7168\n"
    "v_mfma_f32_16x16x32_bf16 v[4:7], v[116:119], a[188:191], v[4:7]\n"
    "v_mfma_f32_16x16x32_bf16 v[0:3], v[116:119], a[184:187], v[0:3]\n"
    "v_mfma_f32_16x16x32_bf16 v[0:3], v[112:115], a[176:179], v[0:3]\n"
    "buffer_load_dwordx4 a[232:235], v232, s[24:27], s33 offen offset:1024 sc1\n"
    "v_mfma_f32_16x16x32_bf16 v[0:3], v[108:111], a[168:171], v[0:3]\n"
    "v_mfma_f32_16x16x32_bf16 v[0:3], v[104:107], a[160:163], v[0:3]\n"
    "s_waitcnt lgkmcnt(4)\n"
    "v_mfma_f32_16x16x32_bf16 v[8:11], v[72:75], a[160:163], v[8:11]\n"
    "ds_read_b128 v[104:107], v233 offset:8192\n"
    "ds_read_b128 v[108:111], v233 offset:9216\n"
    "v_mfma_f32_16x16x32_bf16 v[8:11], v[76:79], a[168:171], v[8:11]\n"
    "buffer_load_dwordx4 a[240:243], v232, s[24:27], s33 offen offset:2048 sc1\n"
    "v_mfma_f32_16x16x32_bf16 v[8:11], v[80:83], a[176:179], v[8:11]\n"
    "ds_read_b128 v[112:115], v233 offset:10240\n"
    "ds_read_b128 v[116:119], v233 offset:11264\n"
    "v_mfma_f32_16x16x32_bf16 v[8:11], v[84:87], a[184:187], v[8:11]\n"
    "v_mfma_f32_16x16x32_bf16 v[12:15], v[84:87], a[188:191], v[12:15]\n"
    "v_mfma_f32_16x16x32_bf16 v[12:15], v[80:83], a[180:183], v[12:15]\n"
    "v_mfma_f32_16x16x32_bf16 v[12:15], v[76:79], a[172:175], v[12:15]\n"
    "v_mfma_f32_16x16x32_bf16 v[12:15], v[72:75], a[164:167], v[12:15]\n"
    "s_waitcnt lgkmcnt(4)\n"
    "v_mfma_f32_16x16x32_bf16 v[20:23], v[88:91], a[164:167], v[20:23]\n"
    "ds_read_b128 v[72:75], v233 offset:12288\n"
    "ds_read_b128 v[76:79], v233 offset:13312\n"
    "v_mfma_f32_16x16x32_bf16 v[20:23], v[92:95], a[172:175], v[20:23]\n"
    "buffer_load_dwordx4 a[248:251], v232, s[24:27], s33 offen offset:3072 sc1\n"
    "v_mfma_f32_16x16x32_bf16 v[20:23], v[96:99], a[180:183], v[20:23]\n"
    "ds_read_b128 v[80:83], v233 offset:14336\n"
    "ds_read_b128 v[84:87], v233 offset:15360\n"
    "v_mfma_f32_16x16x32_bf16 v[20:23], v[100:103], a[188:191], v[20:23]\n"
    "v_mfma_f32_16x16x32_bf16 v[16:19], v[100:103], a[184:187], v[16:19]\n"
    "v_mfma_f32_16x16x32_bf16 v[16:19], v[96:99], a[176:179], v[16:19]\n"
    "s_mov_b32 s33, 176128\n"
    "buffer_load_dwordx4 a[228:231], v232, s[24:27], s33 offen offset:0 sc1\n"
    "v_mfma_f32_16x16x32_bf16 v[16:19], v[92:95], a[168:171], v[16:19]\n"
    "v_mfma_f32_16x16x32_bf16 v[16:19], v[88:91], a[160:163], v[16:19]\n"
    "s_waitcnt lgkmcnt(4)\n"
    "v_mfma_f32_16x16x32_bf16 v[24:27], v[104:107], a[160:163], v[24:27]\n"
    "ds_read_b128 v[88:91], v233 offset:16384\n"
    "ds_read_b128 v[92:95], v233 offset:17408\n"
    "v_mfma_f32_16x16x32_bf16 v[24:27], v[108:111], a[168:171], v[24:27]\n"
    "buffer_load_dwordx4 a[236:239], v232, s[24:27], s33 offen offset:1024 sc1\n"
    "v_mfma_f32_16x16x32_bf16 v[24:27], v[112:115], a[176:179], v[24:27]\n"
    "ds_read_b128 v[96:99], v233 offset:18432\n"
    "ds_read_b128 v[100:103], v233 offset:19456\n"
    "v_mfma_f32_16x16x32_bf16 v[24:27], v[116:119], a[184:187], v[24:27]\n"
    "v_mfma_f32_16x16x32_bf16 v[28:31], v[116:119], a[188:191], v[28:31]\n"
    "v_mfma_f32_16x16x32_bf16 v[28:31], v[112:115], a[180:183], v[28:31]\n"
    "buffer_load_dwordx4 a[244:247], v232, s[24:27], s33 offen offset:2048 sc1\n"
    "v_mfma_f32_16x16x32_bf16 v[28:31], v[108:111], a[172:175], v[28:31]\n"
    "v_mfma_f32_16x16x32_bf16 v[28:31], v[104:107], a[164:167], v[28:31]\n"
    "s_waitcnt lgkmcnt(4)\n"
    "v_mfma_f32_16x16x32_bf16 v[36:39], v[72:75], a[164:167], v[36:39]\n"
    "ds_read_b128 v[104:107], v233 offset:20480\n"
    "ds_read_b128 v[108:111], v233 offset:21504\n"
    "v_mfma_f32_16x16x32_bf16 v[36:39], v[76:79], a[172:175], v[36:39]\n"
    "buffer_load_dwordx4 a[252:255], v232, s[24:27], s33 offen offset:3072 sc1\n"
    "v_mfma_f32_16x16x32_bf16 v[36:39], v[80:83], a[180:183], v[36:39]\n"
    "ds_read_b128 v[112:115], v233 offset:22528\n"
    "ds_read_b128 v[116:119], v233 offset:23552\n"
    "v_mfma_f32_16x16x32_bf16 v[36:39], v[84:87], a[188:191], v[36:39]\n"
    "v_mfma_f32_16x16x32_bf16 v[32:35], v[84:87], a[184:187], v[32:35]\n"
    "v_mfma_f32_16x16x32_bf16 v[32:35], v[80:83], a[176:179], v[32:35]\n"
    "v_mfma_f32_16x16x32_bf16 v[32:35], v[76:79], a[168:171], v[32:35]\n"
    "v_mfma_f32_16x16x32_bf16 v[32:35], v[72:75], a[160:163], v[32:35]\n"
    "s_waitcnt lgkmcnt(4)\n"
    "v_mfma_f32_16x16x32_bf16 v[40:43], v[88:91], a[160:163], v[40:43]\n"
    "s_add_u32 m0, s52, 73728\n"
    "ds_read_b128 v[72:75], v233 offset:24576\n"
    "ds_read_b128 v[76:79], v233 offset:25600\n"
    "v_mfma_f32_16x16x32_bf16 v[40:43], v[92:95], a[168:171], v[40:43]\n"
    "s_add_u32 s53, s52, 221184\n"
    "buffer_load_dwordx4 v232, s[48:51], s53 offen offset:0 sc1 lds\n"
    "v_mfma_f32_16x16x32_bf16 v[40:43], v[96:99], a[176:179], v[40:43]\n"
    "ds_read_b128 v[80:83], v233 offset:26624\n"
    "ds_read_b128 v[84:87], v233 offset:27648\n"
    "v_mfma_f32_16x16x32_bf16 v[40:43], v[100:103], a[184:187], v[40:43]\n"
    "v_mfma_f32_16x16x32_bf16 v[44:47], v[100:103], a[188:191], v[44:47]\n"
    "v_mfma_f32_16x16x32_bf16 v[44:47], v[96:99], a[180:183], v[44:47]\n"
    "buffer_load_dwordx4 v232, s[48:51], s53 offen offset:1024 sc1 lds\n"
    "v_mfma_f32_16x16x32_bf16 v[44:47], v[92:95], a[172:175], v[44:47]\n"
    "v_mfma_f32_16x16x32_bf16 v[44:47], v[88:91], a[164:167], v[44:47]\n"
    "s_waitcnt lgkmcnt(4)\n"
    "v_mfma_f32_16x16x32_bf16 v[52:55], v[104:107], a[164:167], v[52:55]\n"
    "ds_read_b128 v[88:91], v233 offset:28672\n"
    "ds_read_b128 v[92:95], v233 offset:29696\n"
    "v_mfma_f32_16x16x32_bf16 v[52:55], v[108:111], a[172:175], v[52:55]\n"
    "buffer_load_dwordx4 v232, s[48:51], s53 offen offset:2048 sc1 lds\n"
    "v_mfma_f32_16x16x32_bf16 v[52:55], v[112:115], a[180:183], v[52:55]\n"
    "ds_read_b128 v[96:99], v233 offset:30720\n"
    "ds_read_b128 v[100:103], v233 offset:31744\n"
    "v_mfma_f32_16x16x32_bf16 v[52:55], v[116:119], a[188:191], v[52:55]\n"
    "v_mfma_f32_16x16x32_bf16 v[48:51], v[116:119], a[184:187], v[48:51]\n"
    "v_mfma_f32_16x16x32_bf16 v[48:51], v[112:115], a[176:179], v[48:51]\n"
    "buffer_load_dwordx4 v232, s[48:51], s53 offen offset:3072 sc1 lds\n"
    "v_mfma_f32_16x16x32_bf16 v[48:51], v[108:111], a[168:171], v[48:51]\n"
    "v_mfma_f32_16x16x32_bf16 v[48:51], v[104:107], a[160:163], v[48:51]\n"
    "s_waitcnt vmcnt(12) lgkmcnt(4)\n"
    "v_mfma_f32_16x16x32_bf16 v[0:3], v[72:75], a[192:195], v[0:3]\n"
    "s_add_u32 m0, s52, 77824\n"
    "ds_read_b128 v[104:107], v233 offset:32768\n"
    "ds_read_b128 v[108:111], v233 offset:33792\n"
    "v_mfma_f32_16x16x32_bf16 v[0:3], v[76:79], a[200:203], v[0:3]\n"
    "s_add_u32 s53, s52, 225280\n"
    "buffer_load_dwordx4 v232, s[48:51], s53 offen offset:0 sc1 lds\n"
    "v_mfma_f32_16x16x32_bf16 v[0:3], v[80:83], a[208:211], v[0:3]\n"
    "ds_read_b128 v[112:115], v233 offset:34816\n"
    "ds_read_b128 v[116:119], v233 offset:35840\n"
    "v_mfma_f32_16x16x32_bf16 v[0:3], v[84:87], a[216:219], v[0:3]\n"
    "v_mfma_f32_16x16x32_bf16 v[4:7], v[84:87], a[220:223], v[4:7]\n"
    "v_mfma_f32_16x16x32_bf16 v[4:7], v[80:83], a[212:215], v[4:7]\n"
    "v_mfma_f32_16x16x32_bf16 v[4:7], v[76:79], a[204:207], v[4:7]\n"
    "v_mfma_f32_16x16x32_bf16 v[4:7], v[72:75], a[196:199], v[4:7]\n"
    "s_waitcnt vmcnt(21) lgkmcnt(0)\n"
    "v_mfma_f32_16x16x32_bf16 v[12:15], v[88:91], a[196:199], v[12:15]\n"
    "s_barrier\n"
    "v_mfma_f32_16x16x32_bf16 v[12:15], v[92:95], a[204:207], v[12:15]\n"
    "buffer_load_dwordx4 v232, s[48:51], s53 offen offset:1024 sc1 lds\n"
    "v_mfma_f32_16x16x32_bf16 v[12:15], v[96:99], a[212:215], v[12:15]\n"
    "ds_read_b128 v[72:75], v233 offset:36864\n"
    "ds_read_b128 v[76:79], v233 offset:37888\n"
    "v_mfma_f32_16x16x32_bf16 v[12:15], v[100:103], a[220:223], v[12:15]\n"
    "ds_read_b128 v[80:83], v233 offset:38912\n"
    "ds_read_b128 v[84:87], v233 offset:39936\n"
    "v_mfma_f32_16x16x32_bf16 v[8:11], v[100:103], a[216:219], v[8:11]\n"
    "v_mfma_f32_16x16x32_bf16 v[8:11], v[96:99], a[208:211], v[8:11]\n"
    "buffer_load_dwordx4 v232, s[48:51], s53 offen offset:2048 sc1 lds\n"
    "v_mfma_f32_16x16x32_bf16 v[8:11], v[92:95], a[200:203], v[8:11]\n"
    "v_mfma_f32_16x16x32_bf16 v[8:11], v[88:91], a[192:195], v[8:11]\n"
    "v_mfma_f32_16x16x32_bf16 v[16:19], v[104:107], a[192:195], v[16:19]\n"
    "ds_read_b128 v[88:91], v233 offset:40960\n"
    "ds_read_b128 v[92:95], v233 offset:41984\n"
    "v_mfma_f32_16x16x32_bf16 v[16:19], v[108:111], a[200:203], v[16:19]\n"
    "buffer_load_dwordx4 v232, s[48:51], s53 offen offset:3072 sc1 lds\n"
    "v_mfma_f32_16x16x32_bf16 v[16:19], v[112:115], a[208:211], v[16:19]\n"
    "ds_read_b128 v[96:99], v233 offset:43008\n"
    "ds_read_b128 v[100:103], v233 offset:44032\n"
    "v_mfma_f32_16x16x32_bf16 v[16:19], v[116:119], a[216:219], v[16:19]\n"
    "v_mfma_f32_16x16x32_bf16 v[20:23], v[116:119], a[220:223], v[20:23]\n"
    "s_add_u32 m0, s52, 81920\n"
    "v_mfma_f32_16x16x32_bf16 v[20:23], v[112:115], a[212:215], v[20:23]\n"
    "s_add_u32 s53, s52, 229376\n"
    "buffer_load_dwordx4 v232, s[48:51], s53 offen offset:0 sc1 lds\n"
    "v_mfma_f32_16x16x32_bf16 v[20:23], v[108:111], a[204:207], v[20:23]\n"
    "v_mfma_f32_16x16x32_bf16 v[20:23], v[104:107], a[196:199], v[20:23]\n"
    "s_waitcnt lgkmcnt(4)\n"
    "v_mfma_f32_16x16x32_bf16 v[28:31], v[72:75], a[196:199], v[28:31]\n"
    "s_add_u32 m0, s52, 110592\n"
    "ds_read_b128 v[104:107], v233 offset:45056\n"
    "ds_read_b128 v[108:111], v233 offset:46080\n"
    "v_mfma_f32_16x16x32_bf16 v[28:31], v[76:79], a[204:207], v[28:31]\n"
    "s_add_u32 s53, s52, 258048\n"
    "buffer_load_dwordx4 v232, s[48:51], s53 offen offset:0 sc1 lds\n"
    "v_mfma_f32_16x16x32_bf16 v[28:31], v[80:83], a[212:215], v[28:31]\n"
    "ds_read_b128 v[112:115], v233 offset:47104\n"
    "ds_read_b128 v[116:119], v233 offset:48128\n"
    "v_mfma_f32_16x16x32_bf16 v[28:31], v[84:87], a[220:223], v[28:31]\n"
    "v_mfma_f32_16x16x32_bf16 v[24:27], v[84:87], a[216:219], v[24:27]\n"
    "v_mfma_f32_16x16x32_bf16 v[24:27], v[80:83], a[208:211], v[24:27]\n"
    "v_mfma_f32_16x16x32_bf16 v[24:27], v[76:79], a[200:203], v[24:27]\n"
    "v_mfma_f32_16x16x32_bf16 v[24:27], v[72:75], a[192:195], v[24:27]\n"
    "s_waitcnt lgkmcnt(4)\n"
    "v_mfma_f32_16x16x32_bf16 v[32:35], v[88:91], a[192:195], v[32:35]\n"
    "ds_read_b128 v[72:75], v233 offset:49152\n"
    "ds_read_b128 v[76:79], v233 offset:50176\n"
    "v_mfma_f32_16x16x32_bf16 v[32:35], v[92:95], a[200:203], v[32:35]\n"
    "buffer_load_dwordx4 v232, s[48:51], s53 offen offset:1024 sc1 lds\n"
    "v_mfma_f32_16x16x32_bf16 v[32:35], v[96:99], a[208:211], v[32:35]\n"
    "ds_read_b128 v[80:83], v233 offset:51200\n"
    "ds_read_b128 v[84:87], v233 offset:52224\n"
    "v_mfma_f32_16x16x32_bf16 v[32:35], v[100:103], a[216:219], v[32:35]\n"
    "v_mfma_f32_16x16x32_bf16 v[36:39], v[100:103], a[220:223], v[36:39]\n"
    "v_mfma_f32_16x16x32_bf16 v[36:39], v[96:99], a[212:215], v[36:39]\n"
    "buffer_load_dwordx4 v232, s[48:51], s53 offen offset:2048 sc1 lds\n"
    "v_mfma_f32_16x16x32_bf16 v[36:39], v[92:95], a[204:207], v[36:39]\n"
    "v_mfma_f32_16x16x32_bf16 v[36:39], v[88:91], a[196:199], v[36:39]\n"
    "s_waitcnt lgkmcnt(4)\n"
    "v_mfma_f32_16x16x32_bf16 v[44:47], v[104:107], a[196:199], v[44:47]\n"
    "ds_read_b128 v[88:91], v233 offset:53248\n"
    "ds_read_b128 v[92:95], v233 offset:54272\n"
    "v_mfma_f32_16x16x32_bf16 v[44:47], v[108:111], a[204:207], v[44:47]\n"
    "buffer_load_dwordx4 v232, s[48:51], s53 offen offset:3072 sc1 lds\n"
    "v_mfma_f32_16x16x32_bf16 v[44:47], v[112:115], a[212:215], v[44:47]\n"
    "ds_read_b128 v[96:99], v233 offset:55296\n"
    "ds_read_b128 v[100:103], v233 offset:56320\n"
    "v_mfma_f32_16x16x32_bf16 v[44:47], v[116:119], a[220:223], v[44:47]\n"
    "v_mfma_f32_16x16x32_bf16 v[40:43], v[116:119], a[216:219], v[40:43]\n"
    "s_add_u32 m0, s52, 114688\n"
    "v_mfma_f32_16x16x32_bf16 v[40:43], v[112:115], a[208:211], v[40:43]\n"
    "s_add_u32 s53, s52, 262144\n"
    "buffer_load_dwordx4 v232, s[48:51], s53 offen offset:0 sc1 lds\n"
    "v_mfma_f32_16x16x32_bf16 v[40:43], v[108:111], a[200:203], v[40:43]\n"
    "v_mfma_f32_16x16x32_bf16 v[40:43], v[104:107], a[192:195], v[40:43]\n"
    "s_waitcnt lgkmcnt(4)\n"
    "v_mfma_f32_16x16x32_bf16 v[48:51], v[72:75], a[192:195], v[48:51]\n"
    "ds_read_b128 v[104:107], v233 offset:57344\n"
    "ds_read_b128 v[108:111], v233 offset:58368\n"
    "v_mfma_f32_16x16x32_bf16 v[48:51], v[76:79], a[200:203], v[48:51]\n"
    "buffer_load_dwordx4 v232, s[48:51], s53 offen offset:1024 sc1 lds\n"
    "v_mfma_f32_16x16x32_bf16 v[48:51], v[80:83], a[208:211], v[48:51]\n"
    "ds_read_b128 v[112:115], v233 offset:59392\n"
    "ds_read_b128 v[116:119], v233 offset:60416\n"
    "v_mfma_f32_16x16x32_bf16 v[48:51], v[84:87], a[216:219], v[48:51]\n"
    "v_mfma_f32_16x16x32_bf16 v[52:55], v[84:87], a[220:223], v[52:55]\n"
    "v_mfma_f32_16x16x32_bf16 v[52:55], v[80:83], a[212:215], v[52:55]\n"
    "v_mfma_f32_16x16x32_bf16 v[52:55], v[76:79], a[204:207], v[52:55]\n"
    "v_mfma_f32_16x16x32_bf16 v[52:55], v[72:75], a[196:199], v[52:55]\n"
    "s_waitcnt vmcnt(15) lgkmcnt(4)\n"
    "v_mfma_f32_16x16x32_bf16 v[4:7], v[88:91], a[228:231], v[4:7]\n"
    "ds_read_b128 v[72:75], v233 offset:61440\n"
    "ds_read_b128 v[76:79], v233 offset:62464\n"
    "v_mfma_f32_16x16x32_bf16 v[4:7], v[92:95], a[236:239], v[4:7]\n"
    "buffer_load_dwordx4 v232, s[48:51], s53 offen offset:2048 sc1 lds\n"
    "v_mfma_f32_16x16x32_bf16 v[4:7], v[96:99], a[244:247], v[4:7]\n"
    "ds_read_b128 v[80:83], v233 offset:63488\n"
    "ds_read_b128 v[84:87], v233 offset:64512\n"
    "v_mfma_f32_16x16x32_bf16 v[4:7], v[100:103], a[252:255], v[4:7]\n"
    "v_mfma_f32_16x16x32_bf16 v[0:3], v[100:103], a[248:251], v[0:3]\n"
    "v_mfma_f32_16x16x32_bf16 v[0:3], v[96:99], a[240:243], v[0:3]\n"
    "buffer_load_dwordx4 v232, s[48:51], s53 offen offset:3072 sc1 lds\n"
    "v_mfma_f32_16x16x32_bf16 v[0:3], v[92:95], a[232:235], v[0:3]\n"
    "v_mfma_f32_16x16x32_bf16 v[0:3], v[88:91], a[224:227], v[0:3]\n"
    "s_waitcnt lgkmcnt(4)\n"
    "v_mfma_f32_16x16x32_bf16 v[8:11], v[104:107], a[224:227], v[8:11]\n"
    "s_add_u32 m0, s52, 118784\n"
    "ds_read_b128 v[88:91], v234 offset:0\n"
    "ds_read_b128 v[92:95], v234 offset:1024\n"
    "v_mfma_f32_16x16x32_bf16 v[8:11], v[108:111], a[232:235], v[8:11]\n"
    "s_add_u32 s53, s52, 266240\n"
    "buffer_load_dwordx4 v232, s[48:51], s53 offen offset:0 sc1 lds\n"
    "v_mfma_f32_16x16x32_bf16 v[8:11], v[112:115], a[240:243], v[8:11]\n"
    "ds_read_b128 v[96:99], v234 offset:2048\n"
    "ds_read_b128 v[100:103], v234 offset:3072\n"
    "v_mfma_f32_16x16x32_bf16 v[8:11], v[116:119], a[248:251], v[8:11]\n"
    "v_mfma_f32_16x16x32_bf16 v[12:15], v[116:119], a[252:255], v[12:15]\n"
    "v_mfma_f32_16x16x32_bf16 v[12:15], v[112:115], a[244:247], v[12:15]\n"
    "v_mfma_f32_16x16x32_bf16 v[12:15], v[108:111], a[236:239], v[12:15]\n"
    "v_mfma_f32_16x16x32_bf16 v[12:15], v[104:107], a[228:231], v[12:15]\n"
    "s_waitcnt lgkmcnt(4)\n"
    "v_mfma_f32_16x16x32_bf16 v[20:23], v[72:75], a[228:231], v[20:23]\n"
    "v_cvt_pk_bf16_f32 v0, v0, v1\n"
    "v_cvt_pk_bf16_f32 v1, v2, v3\n"
    "v_mfma_f32_16x16x32_bf16 v[20:23], v[76:79], a[236:239], v[20:23]\n"
    "v_cvt_pk_bf16_f32 v2, v4, v5\n"
    "v_cvt_pk_bf16_f32 v3, v6, v7\n"
    "v_mfma_f32_16x16x32_bf16 v[20:23], v[80:83], a[244:247], v[20:23]\n"
    "buffer_store_dwordx4 v[0:3], v236, s[28:31], 0 offen offset:0 sc1\n"
    "v_mfma_f32_16x16x32_bf16 v[20:23], v[84:87], a[252:255], v[20:23]\n"
    "ds_read_b128 v[104:107], v234 offset:4096\n"
    "ds_read_b128 v[108:111], v234 offset:5120\n"
    "v_mfma_f32_16x16x32_bf16 v[16:19], v[84:87], a[248:251], v[16:19]\n"
    "ds_read_b128 v[112:115], v234 offset:6144\n"
    "ds_read_b128 v[116:119], v234 offset:7168\n"
    "v_mfma_f32_16x16x32_bf16 v[16:19], v[80:83], a[240:243], v[16:19]\n"
    "v_mfma_f32_16x16x32_bf16 v[16:19], v[76:79], a[232:235], v[16:19]\n"
    "v_mfma_f32_16x16x32_bf16 v[16:19], v[72:75], a[224:227], v[16:19]\n"
    "s_waitcnt vmcnt(10) lgkmcnt(0)\n"
    "v_mfma_f32_16x16x32_bf16 v[24:27], v[88:91], a[224:227], v[24:27]\n"
    "s_barrier\n"
    "v_mfma_f32_16x16x32_bf16 v[24:27], v[92:95], a[232:235], v[24:27]\n"
    "ds_read_b128 v[72:75], v234 offset:8192\n"
    "ds_read_b128 v[76:79], v234 offset:9216\n"
    "v_mfma_f32_16x16x32_bf16 v[24:27], v[96:99], a[240:243], v[24:27]\n"
    "ds_read_b128 v[80:83], v234 offset:10240\n"
    "ds_read_b128 v[84:87], v234 offset:11264\n"
    "v_mfma_f32_16x16x32_bf16 v[24:27], v[100:103], a[248:251], v[24:27]\n"
    "v_mfma_f32_16x16x32_bf16 v[28:31], v[100:103], a[252:255], v[28:31]\n"
    "v_mfma_f32_16x16x32_bf16 v[28:31], v[96:99], a[244:247], v[28:31]\n"
    "v_mfma_f32_16x16x32_bf16 v[28:31], v[92:95], a[236:239], v[28:31]\n"
    "v_mfma_f32_16x16x32_bf16 v[28:31], v[88:91], a[228:231], v[28:31]\n"
    "v_mfma_f32_16x16x32_bf16 v[36:39], v[104:107], a[228:231], v[36:39]\n"
    "v_cvt_pk_bf16_f32 v8, v8, v9\n"
    "v_cvt_pk_bf16_f32 v9, v10, v11\n"
    "v_mfma_f32_16x16x32_bf16 v[36:39], v[108:111], a[236:239], v[36:39]\n"
    "v_cvt_pk_bf16_f32 v10, v12, v13\n"
    "v_cvt_pk_bf16_f32 v11, v14, v15\n"
    "v_mfma_f32_16x16x32_bf16 v[36:39], v[112:115], a[244:247], v[36:39]\n"
    "buffer_store_dwordx4 v[8:11], v236, s[28:31], 0 offen offset:64 sc1\n"
    "v_mfma_f32_16x16x32_bf16 v[36:39], v[116:119], a[252:255], v[36:39]\n"
    "ds_read_b128 v[88:91], v234 offset:12288\n"
    "ds_read_b128 v[92:95], v234 offset:13312\n"
    "v_mfma_f32_16x16x32_bf16 v[32:35], v[116:119], a[248:251], v[32:35]\n"
    "ds_read_b128 v[96:99], v234 offset:14336\n"
    "ds_read_b128 v[100:103], v234 offset:15360\n"
    "v_mfma_f32_16x16x32_bf16 v[32:35], v[112:115], a[240:243], v[32:35]\n"
    "v_mfma_f32_16x16x32_bf16 v[32:35], v[108:111], a[232:235], v[32:35]\n"
    "v_mfma_f32_16x16x32_bf16 v[32:35], v[104:107], a[224:227], v[32:35]\n"
    "s_waitcnt lgkmcnt(4)\n"
    "v_mfma_f32_16x16x32_bf16 v[40:43], v[72:75], a[224:227], v[40:43]\n"
    "ds_read_b128 v[104:107], v234 offset:16384\n"
    "ds_read_b128 v[108:111], v234 offset:17408\n"
    "v_mfma_f32_16x16x32_bf16 v[40:43], v[76:79], a[232:235], v[40:43]\n"
    "v_mfma_f32_16x16x32_bf16 v[40:43], v[80:83], a[240:243], v[40:43]\n"
    "ds_read_b128 v[112:115], v234 offset:18432\n"
    "ds_read_b128 v[116:119], v234 offset:19456\n"
    "v_mfma_f32_16x16x32_bf16 v[40:43], v[84:87], a[248:251], v[40:43]\n"
    "v_mfma_f32_16x16x32_bf16 v[44:47], v[84:87], a[252:255], v[44:47]\n"
    "L_chain2x2_mk:\n"
    "s_cbranch_execz L_pad2_5_mk\n"
    "v_mfma_f32_16x16x32_bf16 v[44:47], v[80:83], a[244:247], v[44:47]\n"
    "v_mfma_f32_16x16x32_bf16 v[44:47], v[76:79], a[236:239], v[44:47]\n"
    "v_mfma_f32_16x16x32_bf16 v[44:47], v[72:75], a[228:231], v[44:47]\n"
    "s_waitcnt lgkmcnt(4)\n"
    "v_mfma_f32_16x16x32_bf16 v[52:55], v[88:91], a[228:231], v[52:55]\n"
    "v_cvt_pk_bf16_f32 v16, v16, v17\n"
    "v_cvt_pk_bf16_f32 v17, v18, v19\n"
    "v_mfma_f32_16x16x32_bf16 v[52:55], v[92:95], a[236:239], v[52:55]\n"
    "v_cvt_pk_bf16_f32 v18, v20, v21\n"
    "v_cvt_pk_bf16_f32 v19, v22, v23\n"
    "v_mfma_f32_16x16x32_bf16 v[52:55], v[96:99], a[244:247], v[52:55]\n"
    "buffer_store_dwordx4 v[16:19], v236, s[28:31], 0 offen offset:128 sc1\n"
    "v_mfma_f32_16x16x32_bf16 v[52:55], v[100:103], a[252:255], v[52:55]\n"
    "ds_read_b128 v[72:75], v234 offset:20480\n"
    "ds_read_b128 v[76:79], v234 offset:21504\n"
    "v_mfma_f32_16x16x32_bf16 v[48:51], v[100:103], a[248:251], v[48:51]\n"
    "ds_read_b128 v[80:83], v234 offset:22528\n"
    "ds_read_b128 v[84:87], v234 offset:23552\n"
    "v_mfma_f32_16x16x32_bf16 v[48:51], v[96:99], a[240:243], v[48:51]\n"
    "v_mfma_f32_16x16x32_bf16 v[48:51], v[92:95], a[232:235], v[48:51]\n"
    "v_mfma_f32_16x16x32_bf16 v[48:51], v[88:91], a[224:227], v[48:51]\n"
    "s_waitcnt lgkmcnt(4)\n"
    "v_mfma_f32_16x16x32_bf16 v[56:59], v[104:107], a[0:3], 0\n"
    "ds_read_b128 v[88:91], v234 offset:24576\n"
    "ds_read_b128 v[92:95], v234 offset:25600\n"
    "v_mfma_f32_16x16x32_bf16 v[56:59], v[108:111], a[8:11], v[56:59]\n"
    "v_mfma_f32_16x16x32_bf16 v[56:59], v[112:115], a[16:19], v[56:59]\n"
    "ds_read_b128 v[96:99], v234 offset:26624\n"
    "ds_read_b128 v[100:103], v234 offset:27648\n"
    "v_mfma_f32_16x16x32_bf16 v[56:59], v[116:119], a[24:27], v[56:59]\n"
    "v_mfma_f32_16x16x32_bf16 v[60:63], v[116:119], a[28:31], 0\n"
    "v_mfma_f32_16x16x32_bf16 v[60:63], v[112:115], a[20:23], v[60:63]\n"
    "v_mfma_f32_16x16x32_bf16 v[60:63], v[108:111], a[12:15], v[60:63]\n"
    "v_mfma_f32_16x16x32_bf16 v[60:63], v[104:107], a[4:7], v[60:63]\n"
    "s_waitcnt lgkmcnt(4)\n"
    "v_mfma_f32_16x16x32_bf16 v[60:63], v[72:75], a[36:39], v[60:63]\n"
    "v_cvt_pk_bf16_f32 v24, v24, v25\n"
    "v_cvt_pk_bf16_f32 v25, v26, v27\n"
    "v_mfma_f32_16x16x32_bf16 v[60:63], v[76:79], a[44:47], v[60:63]\n"
    "v_cvt_pk_bf16_f32 v26, v28, v29\n"
    "v_cvt_pk_bf16_f32 v27, v30, v31\n"
    "v_mfma_f32_16x16x32_bf16 v[60:63], v[80:83], a[52:55], v[60:63]\n"
    "buffer_store_dwordx4 v[24:27], v236, s[28:31], 0 offen offset:192 sc1\n"
    "v_mfma_f32_16x16x32_bf16 v[60:63], v[84:87], a[60:63], v[60:63]\n"
    "ds_read_b128 v[104:107], v234 offset:28672\n"
    "ds_read_b128 v[108:111], v234 offset:29696\n"
    "v_mfma_f32_16x16x32_bf16 v[56:59], v[84:87], a[56:59], v[56:59]\n"
    "ds_read_b128 v[112:115], v234 offset:30720\n"
    "ds_read_b128 v[116:119], v234 offset:31744\n"
    "v_mfma_f32_16x16x32_bf16 v[56:59], v[80:83], a[48:51], v[56:59]\n"
    "v_mfma_f32_16x16x32_bf16 v[56:59], v[76:79], a[40:43], v[56:59]\n"
    "v_mfma_f32_16x16x32_bf16 v[56:59], v[72:75], a[32:35], v[56:59]\n"
    "s_waitcnt lgkmcnt(4)\n"
    "v_mfma_f32_16x16x32_bf16 v[56:59], v[88:91], a[64:67], v[56:59]\n"
    "ds_read_b128 v[72:75], v234 offset:32768\n"
    "ds_read_b128 v[76:79], v234 offset:33792\n"
    "v_mfma_f32_16x16x32_bf16 v[56:59], v[92:95], a[72:75], v[56:59]\n"
    "v_mfma_f32_16x16x32_bf16 v[56:59], v[96:99], a[80:83], v[56:59]\n"
    "ds_read_b128 v[80:83], v234 offset:34816\n"
    "ds_read_b128 v[84:87], v234 offset:35840\n"
    "v_mfma_f32_16x16x32_bf16 v[56:59], v[100:103], a[88:91], v[56:59]\n"
    "v_mfma_f32_16x16x32_bf16 v[60:63], v[100:103], a[92:95], v[60:63]\n"
    "v_mfma_f32_16x16x32_bf16 v[60:63], v[96:99], a[84:87], v[60:63]\n"
    "v_mfma_f32_16x16x32_bf16 v[60:63], v[92:95], a[76:79], v[60:63]\n"
    "v_mfma_f32_16x16x32_bf16 v[60:63], v[88:91], a[68:71], v[60:63]\n"
    "s_waitcnt lgkmcnt(4)\n"
    "v_mfma_f32_16x16x32_bf16 v[60:63], v[104:107], a[100:103], v[60:63]\n"
    "v_cvt_pk_bf16_f32 v32, v32, v33\n"
    "v_cvt_pk_bf16_f32 v33, v34, v35\n"
    "v_mfma_f32_16x16x32_bf16 v[60:63], v[108:111], a[108:111], v[60:63]\n"
    "v_cvt_pk_bf16_f32 v34, v36, v37\n"
    "v_cvt_pk_bf16_f32 v35, v38, v39\n"
    "v_mfma_f32_16x16x32_bf16 v[60:63], v[112:115], a[116:119], v[60:63]\n"
    "buffer_store_dwordx4 v[32:35], v236, s[28:31], 0 offen offset:256 sc1\n"
    "v_mfma_f32_16x16x32_bf16 v[60:63], v[116:119], a[124:127], v[60:63]\n"
    "ds_read_b128 v[88:91], v234 offset:36864\n"
    "ds_read_b128 v[92:95], v234 offset:37888\n"
    "v_mfma_f32_16x16x32_bf16 v[56:59], v[116:119], a[120:123], v[56:59]\n"
    "ds_read_b128 v[96:99], v234 offset:38912\n"
    "ds_read_b128 v[100:103], v234 offset:39936\n"
    "v_mfma_f32_16x16x32_bf16 v[56:59], v[112:115], a[112:115], v[56:59]\n"
    "v_mfma_f32_16x16x32_bf16 v[56:59], v[108:111], a[104:107], v[56:59]\n"
    "v_mfma_f32_16x16x32_bf16 v[56:59], v[104:107], a[96:99], v[56:59]\n"
    "s_waitcnt lgkmcnt(4)\n"
    "v_mfma_f32_16x16x32_bf16 v[56:59], v[72:75], a[128:131], v[56:59]\n"
    "ds_read_b128 v[104:107], v234 offset:40960\n"
    "ds_read_b128 v[108:111], v234 offset:41984\n"
    "v_mfma_f32_16x16x32_bf16 v[56:59], v[76:79], a[136:139], v[56:59]\n"
    "v_mfma_f32_16x16x32_bf16 v[56:59], v[80:83], a[144:147], v[56:59]\n"
    "ds_read_b128 v[112:115], v234 offset:43008\n"
    "ds_read_b128 v[116:119], v234 offset:44032\n"
    "v_mfma_f32_16x16x32_bf16 v[56:59], v[84:87], a[152:155], v[56:59]\n"
    "v_mfma_f32_16x16x32_bf16 v[60:63], v[84:87], a[156:159], v[60:63]\n"
    "v_mfma_f32_16x16x32_bf16 v[60:63], v[80:83], a[148:151], v[60:63]\n"
    "v_mfma_f32_16x16x32_bf16 v[60:63], v[76:79], a[140:143], v[60:63]\n"
    "v_mfma_f32_16x16x32_bf16 v[60:63], v[72:75], a[132:135], v[60:63]\n"
    "s_waitcnt vmcnt(5) lgkmcnt(0)\n"
    "v_mfma_f32_16x16x32_bf16 v[60:63], v[88:91], a[164:167], v[60:63]\n"
    "s_barrier\n"
    "v_mfma_f32_16x16x32_bf16 v[60:63], v[92:95], a[172:175], v[60:63]\n"
    "v_cvt_pk_bf16_f32 v40, v40, v41\n"
    "v_cvt_pk_bf16_f32 v41, v42, v43\n"
    "v_mfma_f32_16x16x32_bf16 v[60:63], v[96:99], a[180:183], v[60:63]\n"
    "v_cvt_pk_bf16_f32 v42, v44, v45\n"
    "v_cvt_pk_bf16_f32 v43, v46, v47\n"
    "v_mfma_f32_16x16x32_bf16 v[60:63], v[100:103], a[188:191], v[60:63]\n"
    "buffer_store_dwordx4 v[40:43], v236, s[28:31], 0 offen offset:320 sc1\n"
    "v_mfma_f32_16x16x32_bf16 v[56:59], v[100:103], a[184:187], v[56:59]\n"
    "ds_read_b128 v[72:75], v234 offset:45056\n"
    "ds_read_b128 v[76:79], v234 offset:46080\n"
    "v_mfma_f32_16x16x32_bf16 v[56:59], v[96:99], a[176:179], v[56:59]\n"
    "ds_read_b128 v[80:83], v234 offset:47104\n"
    "ds_read_b128 v[84:87], v234 offset:48128\n"
    "v_mfma_f32_16x16x32_bf16 v[56:59], v[92:95], a[168:171], v[56:59]\n"
    "v_mfma_f32_16x16x32_bf16 v[56:59], v[88:91], a[160:163], v[56:59]\n"
    "v_mfma_f32_16x16x32_bf16 v[56:59], v[104:107], a[192:195], v[56:59]\n"
    "ds_read_b128 v[88:91], v234 offset:49152\n"
    "ds_read_b128 v[92:95], v234 offset:50176\n"
    "v_mfma_f32_16x16x32_bf16 v[56:59], v[108:111], a[200:203], v[56:59]\n"
    "v_mfma_f32_16x16x32_bf16 v[56:59], v[112:115], a[208:211], v[56:59]\n"
    "ds_read_b128 v[96:99], v234 offset:51200\n"
    "ds_read_b128 v[100:103], v234 offset:52224\n"
    "v_mfma_f32_16x16x32_bf16 v[56:59], v[116:119], a[216:219], v[56:59]\n"
    "v_mfma_f32_16x16x32_bf16 v[60:63], v[116:119], a[220:223], v[60:63]\n"
    "v_mfma_f32_16x16x32_bf16 v[60:63], v[112:115], a[212:215], v[60:63]\n"
    "v_mfma_f32_16x16x32_bf16 v[60:63], v[108:111], a[204:207], v[60:63]\n"
    "v_mfma_f32_16x16x32_bf16 v[60:63], v[104:107], a[196:199], v[60:63]\n"
    "s_waitcnt lgkmcnt(4)\n"
    "v_mfma_f32_16x16x32_bf16 v[60:63], v[72:75], a[228:231], v[60:63]\n"
    "v_cvt_pk_bf16_f32 v48, v48, v49\n"
    "v_cvt_pk_bf16_f32 v49, v50, v51\n"
    "v_mfma_f32_16x16x32_bf16 v[60:63], v[76:79], a[236:239], v[60:63]\n"
    "v_cvt_pk_bf16_f32 v50, v52, v53\n"
    "v_cvt_pk_bf16_f32 v51, v54, v55\n"
    "v_mfma_f32_16x16x32_bf16 v[60:63], v[80:83], a[244:247], v[60:63]\n"
    "buffer_store_dwordx4 v[48:51], v236, s[28:31], 0 offen offset:384 sc1\n"
    "v_mfma_f32_16x16x32_bf16 v[60:63], v[84:87], a[252:255], v[60:63]\n"
    "ds_read_b128 v[104:107], v234 offset:53248\n"
    "ds_read_b128 v[108:111], v234 offset:54272\n"
    "v_mfma_f32_16x16x32_bf16 v[56:59], v[84:87], a[248:251], v[56:59]\n"
    "ds_read_b128 v[112:115], v234 offset:55296\n"
    "ds_read_b128 v[116:119], v234 offset:56320\n"
    "v_mfma_f32_16x16x32_bf16 v[56:59], v[80:83], a[240:243], v[56:59]\n"
    "v_mfma_f32_16x16x32_bf16 v[56:59], v[76:79], a[232:235], v[56:59]\n"
    "v_mfma_f32_16x16x32_bf16 v[56:59], v[72:75], a[224:227], v[56:59]\n"
    "s_waitcnt lgkmcnt(4)\n"
    "v_mfma_f32_16x16x32_bf16 v[64:67], v[88:91], a[0:3], 0\n"
    "ds_read_b128 v[72:75], v234 offset:57344\n"
    "ds_read_b128 v[76:79], v234 offset:58368\n"
    "v_mfma_f32_16x16x32_bf16 v[64:67], v[92:95], a[8:11], v[64:67]\n"
    "v_mfma_f32_16x16x32_bf16 v[64:67], v[96:99], a[16:19], v[64:67]\n"
    "ds_read_b128 v[80:83], v234 offset:59392\n"
    "ds_read_b128 v[84:87], v234 offset:60416\n"
    "v_mfma_f32_16x16x32_bf16 v[64:67], v[100:103], a[24:27], v[64:67]\n"
    "v_mfma_f32_16x16x32_bf16 v[68:71], v[100:103], a[28:31], 0\n"
    "v_mfma_f32_16x16x32_bf16 v[68:71], v[96:99], a[20:23], v[68:71]\n"
    "v_mfma_f32_16x16x32_bf16 v[68:71], v[92:95], a[12:15], v[68:71]\n"
    "v_mfma_f32_16x16x32_bf16 v[68:71], v[88:91], a[4:7], v[68:71]\n"
    "s_waitcnt lgkmcnt(4)\n"
    "v_mfma_f32_16x16x32_bf16 v[68:71], v[104:107], a[36:39], v[68:71]\n"
    "v_cvt_pk_bf16_f32 v56, v56, v57\n"
    "v_cvt_pk_bf16_f32 v57, v58, v59\n"
    "v_mfma_f32_16x16x32_bf16 v[68:71], v[108:111], a[44:47], v[68:71]\n"
    "v_cvt_pk_bf16_f32 v58, v60, v61\n"
    "v_cvt_pk_bf16_f32 v59, v62, v63\n"
    "v_mfma_f32_16x16x32_bf16 v[68:71], v[112:115], a[52:55], v[68:71]\n"
    "buffer_store_dwordx4 v[56:59], v236, s[28:31], 0 offen offset:448 sc1\n"
    "v_mfma_f32_16x16x32_bf16 v[68:71], v[116:119], a[60:63], v[68:71]\n"
    "ds_read_b128 v[88:91], v234 offset:61440\n"
    "ds_read_b128 v[92:95], v234 offset:62464\n"
    "v_mfma_f32_16x16x32_bf16 v[64:67], v[116:119], a[56:59], v[64:67]\n"
    "ds_read_b128 v[96:99], v234 offset:63488\n"
    "ds_read_b128 v[100:103], v234 offset:64512\n"
    "v_mfma_f32_16x16x32_bf16 v[64:67], v[112:115], a[48:51], v[64:67]\n"
    "v_mfma_f32_16x16x32_bf16 v[64:67], v[108:111], a[40:43], v[64:67]\n"
    "v_mfma_f32_16x16x32_bf16 v[64:67], v[104:107], a[32:35], v[64:67]\n"
    "s_waitcnt lgkmcnt(4)\n"
    "v_mfma_f32_16x16x32_bf16 v[64:67], v[72:75], a[64:67], v[64:67]\n"
    "ds_read_b128 v[104:107], v235 offset:0\n"
    "ds_read_b128 v[108:111], v235 offset:1024\n"
    "v_mfma_f32_16x16x32_bf16 v[64:67], v[76:79], a[72:75], v[64:67]\n"
    "v_mfma_f32_16x16x32_bf16 v[64:67], v[80:83], a[80:83], v[64:67]\n"
    "ds_read_b128 v[112:115], v235 offset:2048\n"
    "ds_read_b128 v[116:119], v235 offset:3072\n"
    "v_mfma_f32_16x16x32_bf16 v[64:67], v[84:87], a[88:91], v[64:67]\n"
    "v_mfma_f32_16x16x32_bf16 v[68:71], v[84:87], a[92:95], v[68:71]\n"
    "v_mfma_f32_16x16x32_bf16 v[68:71], v[80:83], a[84:87], v[68:71]\n"
    "v_mfma_f32_16x16x32_bf16 v[68:71], v[76:79], a[76:79], v[68:71]\n"
    "v_mfma_f32_16x16x32_bf16 v[68:71], v[72:75], a[68:71], v[68:71]\n"
    "s_waitcnt lgkmcnt(4)\n"
    "v_mfma_f32_16x16x32_bf16 v[68:71], v[88:91], a[100:103], v[68:71]\n"
    "ds_read_b128 v[72:75], v235 offset:4096\n"
    "ds_read_b128 v[76:79], v235 offset:5120\n"
    "v_mfma_f32_16x16x32_bf16 v[68:71], v[92:95], a[108:111], v[68:71]\n"
    "v_mfma_f32_16x16x32_bf16 v[68:71], v[96:99], a[116:119], v[68:71]\n"
    "ds_read_b128 v[80:83], v235 offset:6144\n"
    "ds_read_b128 v[84:87], v235 offset:7168\n"
    "v_mfma_f32_16x16x32_bf16 v[68:71], v[100:103], a[124:127], v[68:71]\n"
    "v_mfma_f32_16x16x32_bf16 v[64:67], v[100:103], a[120:123], v[64:67]\n"
    "v_mfma_f32_16x16x32_bf16 v[64:67], v[96:99], a[112:115], v[64:67]\n"
    "v_mfma_f32_16x16x32_bf16 v[64:67], v[92:95], a[104:107], v[64:67]\n"
    "v_mfma_f32_16x16x32_bf16 v[64:67], v[88:91], a[96:99], v[64:67]\n"
    "s_waitcnt lgkmcnt(4)\n"
    "v_mfma_f32_16x16x32_bf16 v[64:67], v[104:107], a[128:131], v[64:67]\n"
    "ds_read_b128 v[88:91], v235 offset:8192\n"
    "ds_read_b128 v[92:95], v235 offset:9216\n"
    "v_mfma_f32_16x16x32_bf16 v[64:67], v[108:111], a[136:139], v[64:67]\n"
    "v_mfma_f32_16x16x32_bf16 v[64:67], v[112:115], a[144:147], v[64:67]\n"
    "ds_read_b128 v[96:99], v235 offset:10240\n"
    "ds_read_b128 v[100:103], v235 offset:11264\n"
    "v_mfma_f32_16x16x32_bf16 v[64:67], v[116:119], a[152:155], v[64:67]\n"
    "v_mfma_f32_16x16x32_bf16 v[68:71], v[116:119], a[156:159], v[68:71]\n"
    "v_mfma_f32_16x16x32_bf16 v[68:71], v[112:115], a[148:151], v[68:71]\n"
    "v_mfma_f32_16x16x32_bf16 v[68:71], v[108:111], a[140:143], v[68:71]\n"
    "v_mfma_f32_16x16x32_bf16 v[68:71], v[104:107], a[132:135], v[68:71]\n"
    "s_waitcnt lgkmcnt(4)\n"
    "v_mfma_f32_16x16x32_bf16 v[68:71], v[72:75], a[164:167], v[68:71]\n"
    "ds_read_b128 v[104:107], v235 offset:12288\n"
    "ds_read_b128 v[108:111], v235 offset:13312\n"
    "v_mfma_f32_16x16x32_bf16 v[68:71], v[76:79], a[172:175], v[68:71]\n"
    "v_mfma_f32_16x16x32_bf16 v[68:71], v[80:83], a[180:183], v[68:71]\n"
    "ds_read_b128 v[112:115], v235 offset:14336\n"
    "ds_read_b128 v[116:119], v235 offset:15360\n"
    "v_mfma_f32_16x16x32_bf16 v[68:71], v[84:87], a[188:191], v[68:71]\n"
    "v_mfma_f32_16x16x32_bf16 v[64:67], v[84:87], a[184:187], v[64:67]\n"
    "v_mfma_f32_16x16x32_bf16 v[64:67], v[80:83], a[176:179], v[64:67]\n"
    "v_mfma_f32_16x16x32_bf16 v[64:67], v[76:79], a[168:171], v[64:67]\n"
    "v_mfma_f32_16x16x32_bf16 v[64:67], v[72:75], a[160:163], v[64:67]\n"
    "s_waitcnt lgkmcnt(0)\n"
    "v_mfma_f32_16x16x32_bf16 v[64:67], v[88:91], a[192:195], v[64:67]\n"
    "s_barrier\n"
    "v_mfma_f32_16x16x32_bf16 v[64:67], v[92:95], a[200:203], v[64:67]\n"
    "v_mfma_f32_16x16x32_bf16 v[64:67], v[96:99], a[208:211], v[64:67]\n"
    "v_mfma_f32_16x16x32_bf16 v[64:67], v[100:103], a[216:219], v[64:67]\n"
    "v_mfma_f32_16x16x32_bf16 v[68:71], v[100:103], a[220:223], v[68:71]\n"
    "v_mfma_f32_16x16x32_bf16 v[68:71], v[96:99], a[212:215], v[68:71]\n"
    "v_mfma_f32_16x16x32_bf16 v[68:71], v[92:95], a[204:207], v[68:71]\n"
    "v_mfma_f32_16x16x32_bf16 v[68:71], v[88:91], a[196:199], v[68:71]\n"
    "v_mfma_f32_16x16x32_bf16 v[68:71], v[104:107], a[228:231], v[68:71]\n"
    "v_mfma_f32_16x16x32_bf16 v[68:71], v[108:111], a[236:239], v[68:71]\n"
    "v_mfma_f32_16x16x32_bf16 v[68:71], v[112:115], a[244:247], v[68:71]\n"
    "v_mfma_f32_16x16x32_bf16 v[68:71], v[116:119], a[252:255], v[68:71]\n"
    "v_mfma_f32_16x16x32_bf16 v[64:67], v[116:119], a[248:251], v[64:67]\n"
    "v_mfma_f32_16x16x32_bf16 v[64:67], v[112:115], a[240:243], v[64:67]\n"
    "v_mfma_f32_16x16x32_bf16 v[64:67], v[108:111], a[232:235], v[64:67]\n"
    "v_mfma_f32_16x16x32_bf16 v[64:67], v[104:107], a[224:227], v[64:67]\n"
    "s_nop 7\n"
    "v_cvt_pk_bf16_f32 v64, v64, v65\n"
    "v_cvt_pk_bf16_f32 v65, v66, v67\n"
    "v_cvt_pk_bf16_f32 v66, v68, v69\n"
    "v_cvt_pk_bf16_f32 v67, v70, v71\n"
    "buffer_store_dwordx4 v[64:67], v236, s[28:31], 0 offen offset:512 sc1\n"
    "L_chain2x1_mk:\n"
    "s_cbranch_execz L_pad2_6_mk\n"
    "s_mov_b64 s[48:49], s[20:21]\n"
    "s_mov_b64 s[50:51], s[22:23]\n"
    "s_nop 1\n"
    "s_lshl_b32 s42, s89, 2\n"
    "s_add_u32 s42, s42, s88\n"
    "s_and_b32 s40, s42, 15\n"
    "s_lshr_b32 s41, s42, 4\n"
    "s_lshl_b32 s41, s41, 5\n"
    "s_add_u32 s40, s40, s41\n"
    "s_mul_i32 s40, s40, 1152\n"
    "s_add_u32 s28, s66, s40\n"
    "s_addc_u32 s29, s67, 0\n"
    "s_and_b32 s29, s29, 0xffff\n"
    "s_mov_b32 s30, 19584\n"
    "s_mov_b32 s31, 0x20000\n"
    "s_mov_b32 s37, 18432\n"
    "s_mul_i32 s40, s42, 2304\n"
    "s_add_u32 s40, s40, 22020096\n"
    "s_add_u32 s20, s64, s40\n"
    "s_addc_u32 s21, s65, 0\n"
    "s_and_b32 s21, s21, 0xffff\n"
    "s_mov_b32 s22, 7080192\n"
    "s_mov_b32 s23, 0x20000\n"
    "v_mov_b32_e32 v136, v232\n"
    "v_add_u32_e32 v137, 1024, v232\n"
    "v_add_u32_e32 v138, 2048, v232\n"
    "v_mov_b32_e32 v120, 0x7f000000\n"
    "v_cmp_gt_u32_e32 vcc, 256, v232\n"
    "v_lshrrev_b32_e32 v139, 1, v136\n"
    "v_lshrrev_b32_e32 v140, 1, v137\n"
    "v_cndmask_b32_e32 v138, v120, v138, vcc\n"
    "v_lshrrev_b32_e32 v141, 1, v138\n"
    "s_add_u32 s40, s68, 4608\n"
    "buffer_load_dwordx2 v[148:149], v136, s[96:99], s40 offen sc1\n"
    "buffer_load_dwordx2 v[152:153], v137, s[96:99], s40 offen sc1\n"
    "buffer_load_dwordx2 v[156:157], v138, s[96:99], s40 offen sc1\n"
    "s_add_u32 s40, s68, 6912\n"
    "buffer_load_dwordx2 v[160:161], v136, s[96:99], s40 offen sc1\n"
    "buffer_load_dwordx2 v[164:165], v137, s[96:99], s40 offen sc1\n"
    "buffer_load_dwordx2 v[168:169], v138, s[96:99], s40 offen sc1\n"
    "s_add_u32 s40, s68, 9216\n"
    "buffer_load_dwordx2 v[172:173], v136, s[96:99], s40 offen sc1\n"
    "buffer_load_dwordx2 v[176:177], v137, s[96:99], s40 offen sc1\n"
    "buffer_load_dwordx2 v[180:181], v138, s[96:99], s40 offen sc1\n"
    "buffer_load_dwordx2 v[84:85], v139, s[28:31], 0 offen\n"
    "buffer_load_dwordx2 v[86:87], v139, s[28:31], s37 offen\n"
    "buffer_load_dwordx2 v[88:89], v140, s[28:31], 0 offen\n"
    "buffer_load_dwordx2 v[90:91], v140, s[28:31], s37 offen\n"
    "buffer_load_dwordx2 v[92:93], v141, s[28:31], 0 offen\n"
    "buffer_load_dwordx2 v[94:95], v141, s[28:31], s37 offen\n"
    "s_lshr_b32 s40, s89, 2\n"
    "s_mul_i32 s40, s40, 36864\n"
    "s_and_b32 s41, s89, 3\n"
    "s_lshl_b32 s41, s41, 6\n"
    "s_add_u32 s40, s40, s41\n"
    "s_add_u32 s40, s40, 1048576\n"
    "s_add_u32 s32, s64, s40\n"
    "s_addc_u32 s33, s65, 0\n"
    "s_and_b32 s33, s33, 0xffff\n"
    "s_mov_b32 s34, 36672\n"
    "s_mov_b32 s35, 0x20000\n"
    "v_lshrrev_b32_e32 v142, 7, v232\n"
    "v_lshlrev_b32_e32 v142, 8, v142\n"
    "v_bfe_u32 v128, v232, 4, 2\n"
    "v_lshl_add_u32 v142, v128, 6, v142\n"
    "v_bfe_u32 v128, v232, 6, 1\n"
    "v_lshl_add_u32 v142, v128, 3, v142\n"
    "s_lshl_b32 s40, s88, 4\n"
    "s_add_u32 s40, s40, 147456\n"
    "v_add_u32_e32 v142, s40, v142\n"
    "s_lshl_b32 s40, s88, 10\n"
    "s_add_u32 s40, s40, 147456\n"
    "v_add_u32_e32 v143, s40, v232\n"
    "v_lshrrev_b32_e32 v144, 6, v232\n"
    "v_lshlrev_b32_e32 v144, 8, v144\n"
    "v_and_b32_e32 v128, 48, v232\n"
    "v_add_u32_e32 v144, v144, v128\n"
    "s_lshl_b32 s40, s88, 12\n"
    "v_add_u32_e32 v144, s40, v144\n"
    "s_getpc_b64 s[42:43]\n"
    "L_pc34_mk:\n"
    "s_add_u32 s42, s42, L_code2_mk-L_pc34_mk\n"
    "s_addc_u32 s43, s43, 0\n"
    "s_lshr_b32 s40, s89, 3\n"
    "L_chain2x0_mk:\n"
    "s_cbranch_execz L_pad2_7_mk\n"
    "s_lshl_b32 s40, s40, 2\n"
    "s_add_u32 s40, s40, s88\n"
    "s_lshl_b32 s40, s40, 10\n"
    "v_add_u32_e32 v190, s40, v232\n"
    "v_min_u32_e32 v190, L_loop_mk-L_code2_mk-4, v190\n"
    "global_load_dword v190, v190, s[42:43]\n"
    "v_lshrrev_b32_e32 v194, 4, v232\n"
    "s_and_b32 s43, s89, 0xe0\n"
    "s_and_b32 s41, s89, 1\n"
    "s_lshl_b32 s41, s41, 4\n"
    "s_add_u32 s43, s43, s41\n"
    "s_bfe_u32 s40, s89, 0x40001\n"
    "v_mul_u32_u24_e32 v194, 2048, v194\n"
    "s_mul_i32 s43, s43, 2048\n"
    "s_mul_i32 s40, s40, 64\n"
    "s_add_u32 s43, s43, s40\n"
    "s_add_u32 s43, s43, 524288\n"
    "v_mov_b32_e32 v192, 1\n"
    "s_waitcnt vmcnt(16)\n"
    "s_barrier\n"
    "s_cmp_lg_u32 s88, 1\n"
    "s_cbranch_scc1 L_psh35_mk\n"
    "s_mov_b64 exec, 0xffff\n"
    "buffer_store_dword v192, v194, s[96:99], s43 offen sc1\n"
    "s_mov_b64 exec, -1\n"
    "L_psh35_mk:\n"
    "s_cmp_eq_u32 s88, 0\n"
    "s_cbranch_scc1 L_w0a36_mk\n"
    "s_mul_i32 s40, s88, 24576\n"
    "s_add_u32 m0, s40, -24576\n"
    "s_add_u32 s53, s40, -24576\n"
    "buffer_load_dwordx4 v232, s[48:51], s53 offen offset:0 sc1 lds\n"
    "buffer_load_dwordx4 v232, s[48:51], s53 offen offset:1024 sc1 lds\n"
    "buffer_load_dwordx4 v232, s[48:51], s53 offen offset:2048 sc1 lds\n"
    "buffer_load_dwordx4 v232, s[48:51], s53 offen offset:3072 sc1 lds\n"
    "s_add_u32 m0, s40, -20480\n"
    "s_add_u32 s53, s40, -20480\n"
    "buffer_load_dwordx4 v232, s[48:51], s53 offen offset:0 sc1 lds\n"
    "buffer_load_dwordx4 v232, s[48:51], s53 offen offset:1024 sc1 lds\n"
    "buffer_load_dwordx4 v232, s[48:51], s53 offen offset:2048 sc1 lds\n"
    "buffer_load_dwordx4 v232, s[48:51], s53 offen offset:3072 sc1 lds\n"
    "s_add_u32 m0, s40, -16384\n"
    "s_add_u32 s53, s40, -16384\n"
    "buffer_load_dwordx4 v232, s[48:51], s53 offen offset:0 sc1 lds\n"
    "buffer_load_dwordx4 v232, s[48:51], s53 offen offset:1024 sc1 lds\n"
    "buffer_load_dwordx4 v232, s[48:51], s53 offen offset:2048 sc1 lds\n"
    "buffer_load_dwordx4 v232, s[48:51], s53 offen offset:3072 sc1 lds\n"
    "s_add_u32 m0, s40, -12288\n"
    "s_add_u32 s53, s40, -12288\n"
    "buffer_load_dwordx4 v232, s[48:51], s53 offen offset:0 sc1 lds\n"
    "buffer_load_dwordx4 v232, s[48:51], s53 offen offset:1024 sc1 lds\n"
    "buffer_load_dwordx4 v232, s[48:51], s53 offen offset:2048 sc1 lds\n"
    "buffer_load_dwordx4 v232, s[48:51], s53 offen offset:3072 sc1 lds\n"
    "s_add_u32 m0, s40, -8192\n"
    "s_add_u32 s53, s40, -8192\n"
    "buffer_load_dwordx4 v232, s[48:51], s53 offen offset:0 sc1 lds\n"
    "buffer_load_dwordx4 v232, s[48:51], s53 offen offset:1024 sc1 lds\n"
    "buffer_load_dwordx4 v232, s[48:51], s53 offen offset:2048 sc1 lds\n"
    "buffer_load_dwordx4 v232, s[48:51], s53 offen offset:3072 sc1 lds\n"
    "s_add_u32 m0, s40, -4096\n"
    "s_add_u32 s53, s40, -4096\n"
    "buffer_load_dwordx4 v232, s[48:51], s53 offen offset:0 sc1 lds\n"
    "buffer_load_dwordx4 v232, s[48:51], s53 offen offset:1024 sc1 lds\n"
    "buffer_load_dwordx4 v232, s[48:51], s53 offen offset:2048 sc1 lds\n"
    "buffer_load_dwordx4 v232, s[48:51], s53 offen offset:3072 sc1 lds\n"
    "L_w0a36_mk:\n"
    "s_mov_b64 exec, 0\n"
    "s_cmp_eq_u32 s88, 3\n"
    "s_cbranch_scc1 L_chain3x3_mk\n"
    "s_cmp_eq_u32 s88, 2\n"
    "s_cbranch_scc1 L_chain3x2_mk\n"
    "s_cmp_eq_u32 s88, 1\n"
    "s_cbranch_scc1 L_chain3x1_mk\n"
    "L_ret3_mk:\n"
    "s_mov_b64 exec, -1\n"
    "s_cmp_lg_u32 s88, 0\n"
    "s_cbranch_scc1 L_polled38_mk\n"
    "s_mul_i32 s41, s89, 2048\n"
    "s_add_u32 s41, s41, 524288\n"
    "v_and_b32_e32 v198, 240, v232\n"
    "v_lshlrev_b32_e32 v198, 2, v198\n"
    "L_pad2_4_mk:\n"
    "s_cbranch_execz L_pad2_8_mk\n"
    "s_mov_b32 s92, 0x40000\n"
    "L_poll37_mk:\n"
    "buffer_load_dword v196, v198, s[96:99], s41 offen sc1\n"
    "s_sub_u32 s92, s92, 1\n"
    "s_waitcnt vmcnt(0)\n"
    "v_cmp_gt_u32_e32 vcc, 1, v196\n"
    "s_cbranch_scc1 L_polled38_mk\n"
    "s_cbranch_vccnz L_poll37_mk\n"
    "L_polled38_mk:\n"
    "s_barrier\n"
    "L_code2_mk:\n"
    "s_mov_b32 s40, 0\n"
    "buffer_load_dwordx4 v[0:3], v136, s[20:23], s40 offen sc1\n"
    "s_mov_b32 s40, 2359296\n"
    "buffer_load_dwordx4 v[4:7], v136, s[20:23], s40 offen sc1\n"
    "s_mov_b32 s40, 4718592\n"
    "buffer_load_dwordx4 v[8:11], v136, s[20:23], s40 offen sc1\n"
    "s_mov_b32 s40, 7077888\n"
    "buffer_load_dwordx4 v[12:15], v136, s[20:23], s40 offen sc1\n"
    "s_mov_b32 s40, 0\n"
    "buffer_load_dwordx4 v[16:19], v137, s[20:23], s40 offen sc1\n"
    "s_mov_b32 s40, 2359296\n"
    "buffer_load_dwordx4 v[20:23], v137, s[20:23], s40 offen sc1\n"
    "s_mov_b32 s40, 4718592\n"
    "buffer_load_dwordx4 v[24:27], v137, s[20:23], s40 offen sc1\n"
    "s_mov_b32 s40, 7077888\n"
    "buffer_load_dwordx4 v[28:31], v137, s[20:23], s40 offen sc1\n"
    "s_mov_b32 s40, 0\n"
    "buffer_load_dwordx4 v[32:35], v138, s[20:23], s40 offen sc1\n"
    "s_mov_b32 s40, 2359296\n"
    "buffer_load_dwordx4 v[36:39], v138, s[20:23], s40 offen sc1\n"
    "s_mov_b32 s40, 4718592\n"
    "buffer_load_dwordx4 v[40:43], v138, s[20:23], s40 offen sc1\n"
    "s_mov_b32 s40, 7077888\n"
    "buffer_load_dwordx4 v[44:47], v138, s[20:23], s40 offen sc1\n"
    "s_waitcnt vmcnt(37)\n"
    "v_lshlrev_b32_e32 v48, 16, v148\n"
    "v_and_b32_e32 v49, 0xffff0000, v148\n"
    "v_lshlrev_b32_e32 v50, 16, v149\n"
    "v_and_b32_e32 v51, 0xffff0000, v149\n"
    "v_lshlrev_b32_e32 v52, 16, v152\n"
    "v_and_b32_e32 v53, 0xffff0000, v152\n"
    "v_lshlrev_b32_e32 v54, 16, v153\n"
    "v_and_b32_e32 v55, 0xffff0000, v153\n"
    "v_lshlrev_b32_e32 v56, 16, v156\n"
    "v_and_b32_e32 v57, 0xffff0000, v156\n"
    "v_lshlrev_b32_e32 v58, 16, v157\n"
    "v_and_b32_e32 v59, 0xffff0000, v157\n"
    "v_lshlrev_b32_e32 v60, 16, v160\n"
    "v_and_b32_e32 v61, 0xffff0000, v160\n"
    "v_lshlrev_b32_e32 v62, 16, v161\n"
    "v_and_b32_e32 v63, 0xffff0000, v161\n"
    "v_lshlrev_b32_e32 v64, 16, v164\n"
    "v_and_b32_e32 v65, 0xffff0000, v164\n"
    "v_lshlrev_b32_e32 v66, 16, v165\n"
    "v_and_b32_e32 v67, 0xffff0000, v165\n"
    "v_lshlrev_b32_e32 v68, 16, v168\n"
    "v_and_b32_e32 v69, 0xffff0000, v168\n"
    "v_lshlrev_b32_e32 v70, 16, v169\n"
    "v_and_b32_e32 v71, 0xffff0000, v169\n"
    "v_lshlrev_b32_e32 v72, 16, v172\n"
    "v_and_b32_e32 v73, 0xffff0000, v172\n"
    "v_lshlrev_b32_e32 v74, 16, v173\n"
    "v_and_b32_e32 v75, 0xffff0000, v173\n"
    "v_lshlrev_b32_e32 v76, 16, v176\n"
    "v_and_b32_e32 v77, 0xffff0000, v176\n"
    "v_lshlrev_b32_e32 v78, 16, v177\n"
    "v_and_b32_e32 v79, 0xffff0000, v177\n"
    "v_lshlrev_b32_e32 v80, 16, v180\n"
    "v_and_b32_e32 v81, 0xffff0000, v180\n"
    "v_lshlrev_b32_e32 v82, 16, v181\n"
    "v_and_b32_e32 v83, 0xffff0000, v181\n"
    "v_mov_b32_e32 v120, 0x3fb8aa3b\n"
    "v_mov_b32_e32 v121, 0x3fb8aa3b\n"
    "v_pk_fma_f32 v[72:73], v[72:73], v[120:121], v[120:121]\n"
    "v_pk_mul_f32 v[60:61], v[60:61], v[120:121]\n"
    "v_pk_fma_f32 v[74:75], v[74:75], v[120:121], v[120:121]\n"
    "v_pk_mul_f32 v[62:63], v[62:63], v[120:121]\n"
    "v_pk_fma_f32 v[76:77], v[76:77], v[120:121], v[120:121]\n"
    "v_pk_mul_f32 v[64:65], v[64:65], v[120:121]\n"
    "v_pk_fma_f32 v[78:79], v[78:79], v[120:121], v[120:121]\n"
    "L_pad2_5_mk:\n"
    "s_cbranch_execz L_pad2_9_mk\n"
    "v_pk_mul_f32 v[66:67], v[66:67], v[120:121]\n"
    "v_pk_fma_f32 v[80:81], v[80:81], v[120:121], v[120:121]\n"
    "v_pk_mul_f32 v[68:69], v[68:69], v[120:121]\n"
    "v_pk_fma_f32 v[82:83], v[82:83], v[120:121], v[120:121]\n"
    "v_pk_mul_f32 v[70:71], v[70:71], v[120:121]\n"
    "v_lshlrev_b32_e32 v96, 16, v84\n"
    "v_and_b32_e32 v97, 0xffff0000, v84\n"
    "v_lshlrev_b32_e32 v98, 16, v85\n"
    "v_and_b32_e32 v99, 0xffff0000, v85\n"
    "v_lshlrev_b32_e32 v100, 16, v86\n"
    "v_and_b32_e32 v101, 0xffff0000, v86\n"
    "v_lshlrev_b32_e32 v102, 16, v87\n"
    "v_and_b32_e32 v103, 0xffff0000, v87\n"
    "v_lshlrev_b32_e32 v104, 16, v88\n"
    "v_and_b32_e32 v105, 0xffff0000, v88\n"
    "v_lshlrev_b32_e32 v106, 16, v89\n"
    "v_and_b32_e32 v107, 0xffff0000, v89\n"
    "v_lshlrev_b32_e32 v108, 16, v90\n"
    "v_and_b32_e32 v109, 0xffff0000, v90\n"
    "v_lshlrev_b32_e32 v110, 16, v91\n"
    "v_and_b32_e32 v111, 0xffff0000, v91\n"
    "v_lshlrev_b32_e32 v112, 16, v92\n"
    "v_and_b32_e32 v113, 0xffff0000, v92\n"
    "v_lshlrev_b32_e32 v114, 16, v93\n"
    "v_and_b32_e32 v115, 0xffff0000, v93\n"
    "v_lshlrev_b32_e32 v116, 16, v94\n"
    "v_and_b32_e32 v117, 0xffff0000, v94\n"
    "v_lshlrev_b32_e32 v118, 16, v95\n"
    "v_and_b32_e32 v119, 0xffff0000, v95\n"
    "s_waitcnt vmcnt(8)\n"
    "v_lshlrev_b32_e32 v120, 16, v0\n"
    "v_and_b32_e32 v121, 0xffff0000, v0\n"
    "v_lshlrev_b32_e32 v122, 16, v4\n"
    "v_and_b32_e32 v123, 0xffff0000, v4\n"
    "v_lshlrev_b32_e32 v124, 16, v8\n"
    "v_and_b32_e32 v125, 0xffff0000, v8\n"
    "v_lshlrev_b32_e32 v126, 16, v12\n"
    "v_and_b32_e32 v127, 0xffff0000, v12\n"
    "v_pk_add_f32 v[120:121], v[120:121], v[122:123]\n"
    "v_pk_add_f32 v[124:125], v[124:125], v[126:127]\n"
    "v_pk_add_f32 v[120:121], v[120:121], v[124:125]\n"
    "v_pk_fma_f32 v[96:97], v[120:121], v[48:49], v[96:97]\n"
    "v_cvt_pk_bf16_f32 v240, v96, v97\n"
    "v_lshlrev_b32_e32 v120, 16, v1\n"
    "v_and_b32_e32 v121, 0xffff0000, v1\n"
    "v_lshlrev_b32_e32 v122, 16, v5\n"
    "v_and_b32_e32 v123, 0xffff0000, v5\n"
    "v_lshlrev_b32_e32 v124, 16, v9\n"
    "v_and_b32_e32 v125, 0xffff0000, v9\n"
    "v_lshlrev_b32_e32 v126, 16, v13\n"
    "v_and_b32_e32 v127, 0xffff0000, v13\n"
    "v_pk_add_f32 v[120:121], v[120:121], v[122:123]\n"
    "v_pk_add_f32 v[124:125], v[124:125], v[126:127]\n"
    "v_pk_add_f32 v[120:121], v[120:121], v[124:125]\n"
    "v_pk_fma_f32 v[98:99], v[120:121], v[50:51], v[98:99]\n"
    "v_cvt_pk_bf16_f32 v241, v98, v99\n"
    "v_lshlrev_b32_e32 v120, 16, v2\n"
    "v_and_b32_e32 v121, 0xffff0000, v2\n"
    "v_lshlrev_b32_e32 v122, 16, v6\n"
    "v_and_b32_e32 v123, 0xffff0000, v6\n"
    "v_lshlrev_b32_e32 v124, 16, v10\n"
    "v_and_b32_e32 v125, 0xffff0000, v10\n"
    "v_lshlrev_b32_e32 v126, 16, v14\n"
    "v_and_b32_e32 v127, 0xffff0000, v14\n"
    "v_pk_add_f32 v[120:121], v[120:121], v[122:123]\n"
    "v_pk_add_f32 v[124:125], v[124:125], v[126:127]\n"
    "v_pk_add_f32 v[120:121], v[120:121], v[124:125]\n"
    "v_pk_fma_f32 v[100:101], v[120:121], v[48:49], v[100:101]\n"
    "v_cvt_pk_bf16_f32 v242, v100, v101\n"
    "v_lshlrev_b32_e32 v120, 16, v3\n"
    "v_and_b32_e32 v121, 0xffff0000, v3\n"
    "v_lshlrev_b32_e32 v122, 16, v7\n"
    "v_and_b32_e32 v123, 0xffff0000, v7\n"
    "v_lshlrev_b32_e32 v124, 16, v11\n"
    "v_and_b32_e32 v125, 0xffff0000, v11\n"
    "v_lshlrev_b32_e32 v126, 16, v15\n"
    "v_and_b32_e32 v127, 0xffff0000, v15\n"
    "v_pk_add_f32 v[120:121], v[120:121], v[122:123]\n"
    "L_pad2_6_mk:\n"
    "s_cbranch_execz L_pad2_10_mk\n"
    "v_pk_add_f32 v[124:125], v[124:125], v[126:127]\n"
    "v_pk_add_f32 v[120:121], v[120:121], v[124:125]\n"
    "v_pk_fma_f32 v[102:103], v[120:121], v[50:51], v[102:103]\n"
    "v_cvt_pk_bf16_f32 v243, v102, v103\n"
    "s_waitcnt vmcnt(4)\n"
    "v_lshlrev_b32_e32 v120, 16, v16\n"
    "v_and_b32_e32 v121, 0xffff0000, v16\n"
    "v_lshlrev_b32_e32 v122, 16, v20\n"
    "v_and_b32_e32 v123, 0xffff0000, v20\n"
    "v_lshlrev_b32_e32 v124, 16, v24\n"
    "v_and_b32_e32 v125, 0xffff0000, v24\n"
    "v_lshlrev_b32_e32 v126, 16, v28\n"
    "v_and_b32_e32 v127, 0xffff0000, v28\n"
    "v_pk_add_f32 v[120:121], v[120:121], v[122:123]\n"
    "L_pad2_7_mk:\n"
    "s_cbranch_execz L_pad2_11_mk\n"
    "v_pk_add_f32 v[124:125], v[124:125], v[126:127]\n"
    "v_pk_add_f32 v[120:121], v[120:121], v[124:125]\n"
    "v_pk_fma_f32 v[104:105], v[120:121], v[52:53], v[104:105]\n"
    "v_cvt_pk_bf16_f32 v244, v104, v105\n"
    "v_lshlrev_b32_e32 v120, 16, v17\n"
    "v_and_b32_e32 v121, 0xffff0000, v17\n"
    "v_lshlrev_b32_e32 v122, 16, v21\n"
    "v_and_b32_e32 v123, 0xffff0000, v21\n"
    "v_lshlrev_b32_e32 v124, 16, v25\n"
    "v_and_b32_e32 v125, 0xffff0000, v25\n"
    "v_lshlrev_b32_e32 v126, 16, v29\n"
    "v_and_b32_e32 v127, 0xffff0000, v29\n"
    "v_pk_add_f32 v[120:121], v[120:121], v[122:123]\n"
    "v_pk_add_f32 v[124:125], v[124:125], v[126:127]\n"
    "v_pk_add_f32 v[120:121], v[120:121], v[124:125]\n"
    "v_pk_fma_f32 v[106:107], v[120:121], v[54:55], v[106:107]\n"
    "v_cvt_pk_bf16_f32 v245, v106, v107\n"
    "v_lshlrev_b32_e32 v120, 16, v18\n"
    "v_and_b32_e32 v121, 0xffff0000, v18\n"
    "v_lshlrev_b32_e32 v122, 16, v22\n"
    "v_and_b32_e32 v123, 0xffff0000, v22\n"
    "v_lshlrev_b32_e32 v124, 16, v26\n"
    "v_and_b32_e32 v125, 0xffff0000, v26\n"
    "v_lshlrev_b32_e32 v126, 16, v30\n"
    "v_and_b32_e32 v127, 0xffff0000, v30\n"
    "v_pk_add_f32 v[120:121], v[120:121], v[122:123]\n"
    "v_pk_add_f32 v[124:125], v[124:125], v[126:127]\n"
    "v_pk_add_f32 v[120:121], v[120:121], v[124:125]\n"
    "v_pk_fma_f32 v[108:109], v[120:121], v[52:53], v[108:109]\n"
    "v_cvt_pk_bf16_f32 v246, v108, v109\n"
    "v_lshlrev_b32_e32 v120, 16, v19\n"
    "v_and_b32_e32 v121, 0xffff0000, v19\n"
    "v_lshlrev_b32_e32 v122, 16, v23\n"
    "v_and_b32_e32 v123, 0xffff0000, v23\n"
    "v_lshlrev_b32_e32 v124, 16, v27\n"
    "v_and_b32_e32 v125, 0xffff0000, v27\n"
    "v_lshlrev_b32_e32 v126, 16, v31\n"
    "v_and_b32_e32 v127, 0xffff0000, v31\n"
    "v_pk_add_f32 v[120:121], v[120:121], v[122:123]\n"
    "v_pk_add_f32 v[124:125], v[124:125], v[126:127]\n"
    "v_pk_add_f32 v[120:121], v[120:121], v[124:125]\n"
    "v_pk_fma_f32 v[110:111], v[120:121], v[54:55], v[110:111]\n"
    "v_cvt_pk_bf16_f32 v247, v110, v111\n"
    "s_waitcnt vmcnt(0)\n"
    "v_lshlrev_b32_e32 v120, 16, v32\n"
    "v_and_b32_e32 v121, 0xffff0000, v32\n"
    "v_lshlrev_b32_e32 v122, 16, v36\n"
    "v_and_b32_e32 v123, 0xffff0000, v36\n"
    "v_lshlrev_b32_e32 v124, 16, v40\n"
    "v_and_b32_e32 v125, 0xffff0000, v40\n"
    "v_lshlrev_b32_e32 v126, 16, v44\n"
    "v_and_b32_e32 v127, 0xffff0000, v44\n"
    "v_pk_add_f32 v[120:121], v[120:121], v[122:123]\n"
    "v_pk_add_f32 v[124:125], v[124:125], v[126:127]\n"
    "v_pk_add_f32 v[120:121], v[120:121], v[124:125]\n"
    "v_pk_fma_f32 v[112:113], v[120:121], v[56:57], v[112:113]\n"
    "v_cvt_pk_bf16_f32 v248, v112, v113\n"
    "v_lshlrev_b32_e32 v120, 16, v33\n"
    "v_and_b32_e32 v121, 0xffff0000, v33\n"
    "v_lshlrev_b32_e32 v122, 16, v37\n"
    "v_and_b32_e32 v123, 0xffff0000, v37\n"
    "v_lshlrev_b32_e32 v124, 16, v41\n"
    "v_and_b32_e32 v125, 0xffff0000, v41\n"
    "v_lshlrev_b32_e32 v126, 16, v45\n"
    "v_and_b32_e32 v127, 0xffff0000, v45\n"
    "v_pk_add_f32 v[120:121], v[120:121], v[122:123]\n"
    "v_pk_add_f32 v[124:125], v[124:125], v[126:127]\n"
    "v_pk_add_f32 v[120:121], v[120:121], v[124:125]\n"
    "v_pk_fma_f32 v[114:115], v[120:121], v[58:59], v[114:115]\n"
    "v_cvt_pk_bf16_f32 v249, v114, v115\n"
    "v_lshlrev_b32_e32 v120, 16, v34\n"
    "v_and_b32_e32 v121, 0xffff0000, v34\n"
    "v_lshlrev_b32_e32 v122, 16, v38\n"
    "v_and_b32_e32 v123, 0xffff0000, v38\n"
    "v_lshlrev_b32_e32 v124, 16, v42\n"
    "v_and_b32_e32 v125, 0xffff0000, v42\n"
    "L_pad2_8_mk:\n"
    "s_cbranch_execz L_pad2_12_mk\n"
    "v_lshlrev_b32_e32 v126, 16, v46\n"
    "v_and_b32_e32 v127, 0xffff0000, v46\n"
    "v_pk_add_f32 v[120:121], v[120:121], v[122:123]\n"
    "v_pk_add_f32 v[124:125], v[124:125], v[126:127]\n"
    "v_pk_add_f32 v[120:121], v[120:121], v[124:125]\n"
    "v_pk_fma_f32 v[116:117], v[120:121], v[56:57], v[116:117]\n"
    "v_cvt_pk_bf16_f32 v250, v116, v117\n"
    "v_lshlrev_b32_e32 v120, 16, v35\n"
    "v_and_b32_e32 v121, 0xffff0000, v35\n"
    "v_lshlrev_b32_e32 v122, 16, v39\n"
    "v_and_b32_e32 v123, 0xffff0000, v39\n"
    "v_lshlrev_b32_e32 v124, 16, v43\n"
    "v_and_b32_e32 v125, 0xffff0000, v43\n"
    "v_lshlrev_b32_e32 v126, 16, v47\n"
    "v_and_b32_e32 v127, 0xffff0000, v47\n"
    "v_pk_add_f32 v[120:121], v[120:121], v[122:123]\n"
    "v_pk_add_f32 v[124:125], v[124:125], v[126:127]\n"
    "v_pk_add_f32 v[120:121], v[120:121], v[124:125]\n"
    "v_pk_fma_f32 v[118:119], v[120:121], v[58:59], v[118:119]\n"
    "v_cvt_pk_bf16_f32 v251, v118, v119\n"
    "v_pk_add_f32 v[128:129], v[96:97], v[98:99]\n"
    "v_pk_add_f32 v[130:131], v[100:101], v[102:103]\n"
    "v_pk_add_f32 v[128:129], v[128:129], v[104:105]\n"
    "v_pk_add_f32 v[130:131], v[130:131], v[108:109]\n"
    "v_pk_add_f32 v[128:129], v[128:129], v[106:107]\n"
    "v_pk_add_f32 v[130:131], v[130:131], v[110:111]\n"
    "v_pk_add_f32 v[128:129], v[128:129], v[112:113]\n"
    "v_pk_add_f32 v[130:131], v[130:131], v[116:117]\n"
    "v_pk_add_f32 v[128:129], v[128:129], v[114:115]\n"
    "v_pk_add_f32 v[130:131], v[130:131], v[118:119]\n"
    "v_add_f32_e32 v120, v128, v129\n"
    "v_add_f32_e32 v122, v130, v131\n"
    "s_nop 0\n"
    "s_nop 0\n"
    "v_add_f32_dpp v120, v120, v120 quad_perm:[1,0,3,2] row_mask:0xf bank_mask:0xf\n"
    "v_add_f32_dpp v122, v122, v122 quad_perm:[1,0,3,2] row_mask:0xf bank_mask:0xf\n"
    "s_nop 0\n"
    "v_add_f32_dpp v120, v120, v120 quad_perm:[2,3,0,1] row_mask:0xf bank_mask:0xf\n"
    "v_add_f32_dpp v122, v122, v122 quad_perm:[2,3,0,1] row_mask:0xf bank_mask:0xf\n"
    "s_nop 0\n"
    "v_add_f32_dpp v120, v120, v120 row_half_mirror row_mask:0xf bank_mask:0xf\n"
    "v_add_f32_dpp v122, v122, v122 row_half_mirror row_mask:0xf bank_mask:0xf\n"
    "s_nop 0\n"
    "v_add_f32_dpp v120, v120, v120 row_mirror row_mask:0xf bank_mask:0xf\n"
    "v_add_f32_dpp v122, v122, v122 row_mirror row_mask:0xf bank_mask:0xf\n"
    "v_mov_b32_e32 v124, v120\n"
    "v_mov_b32_e32 v125, v122\n"
    "s_nop 0\n"
    "v_permlane16_swap_b32_e32 v120, v124\n"
    "v_permlane16_swap_b32_e32 v122, v125\n"
    "v_add_f32_e32 v120, v120, v124\n"
    "v_add_f32_e32 v122, v122, v125\n"
    "v_mov_b32_e32 v124, v120\n"
    "v_mov_b32_e32 v125, v122\n"
    "s_nop 0\n"
    "v_permlane32_swap_b32_e32 v120, v124\n"
    "v_permlane32_swap_b32_e32 v122, v125\n"
    "v_add_f32_e32 v120, v120, v124\n"
    "v_add_f32_e32 v122, v122, v125\n"
    "v_mul_f32_e32 v120, 0xbae38e39, v120\n"
    "v_mul_f32_e32 v122, 0xbae38e39, v122\n"
    "v_pk_add_f32 v[96:97], v[96:97], v[120:121] op_sel_hi:[1,0]\n"
    "v_pk_add_f32 v[100:101], v[100:101], v[122:123] op_sel_hi:[1,0]\n"
    "v_pk_add_f32 v[98:99], v[98:99], v[120:121] op_sel_hi:[1,0]\n"
    "v_pk_add_f32 v[102:103], v[102:103], v[122:123] op_sel_hi:[1,0]\n"
    "v_pk_add_f32 v[104:105], v[104:105], v[120:121] op_sel_hi:[1,0]\n"
    "v_pk_add_f32 v[108:109], v[108:109], v[122:123] op_sel_hi:[1,0]\n"
    "v_pk_add_f32 v[106:107], v[106:107], v[120:121] op_sel_hi:[1,0]\n"
    "v_pk_add_f32 v[110:111], v[110:111], v[122:123] op_sel_hi:[1,0]\n"
    "s_mov_b64 exec, 0xffff\n"
    "v_pk_add_f32 v[112:113], v[112:113], v[120:121] op_sel_hi:[1,0]\n"
    "v_pk_add_f32 v[116:117], v[116:117], v[122:123] op_sel_hi:[1,0]\n"
    "v_pk_add_f32 v[114:115], v[114:115], v[120:121] op_sel_hi:[1,0]\n"
    "v_pk_add_f32 v[118:119], v[118:119], v[122:123] op_sel_hi:[1,0]\n"
    "s_mov_b64 exec, -1\n"
    "v_pk_mul_f32 v[128:129], v[96:97], v[96:97]\n"
    "v_pk_mul_f32 v[130:131], v[100:101], v[100:101]\n"
    "L_pad2_9_mk:\n"
    "s_cbranch_execz L_ret2_mk\n"
    "v_pk_fma_f32 v[128:129], v[98:99], v[98:99], v[128:129]\n"
    "v_pk_fma_f32 v[130:131], v[102:103], v[102:103], v[130:131]\n"
    "v_pk_fma_f32 v[128:129], v[104:105], v[104:105], v[128:129]\n"
    "v_pk_fma_f32 v[130:131], v[108:109], v[108:109], v[130:131]\n"
    "v_pk_fma_f32 v[128:129], v[106:107], v[106:107], v[128:129]\n"
    "v_pk_fma_f32 v[130:131], v[110:111], v[110:111], v[130:131]\n"
    "v_pk_fma_f32 v[128:129], v[112:113], v[112:113], v[128:129]\n"
    "v_pk_fma_f32 v[130:131], v[116:117], v[116:117], v[130:131]\n"
    "v_pk_fma_f32 v[128:129], v[114:115], v[114:115], v[128:129]\n"
    "v_pk_fma_f32 v[130:131], v[118:119], v[118:119], v[130:131]\n"
    "v_add_f32_e32 v120, v128, v129\n"
    "v_add_f32_e32 v122, v130, v131\n"
    "v_mov_b32_e32 v128, 0x3ae38e39\n"
    "s_nop 0\n"
    "v_add_f32_dpp v120, v120, v120 quad_perm:[1,0,3,2] row_mask:0xf bank_mask:0xf\n"
    "v_add_f32_dpp v122, v122, v122 quad_perm:[1,0,3,2] row_mask:0xf bank_mask:0xf\n"
    "s_nop 0\n"
    "v_add_f32_dpp v120, v120, v120 quad_perm:[2,3,0,1] row_mask:0xf bank_mask:0xf\n"
    "v_add_f32_dpp v122, v122, v122 quad_perm:[2,3,0,1] row_mask:0xf bank_mask:0xf\n"
    "s_nop 0\n"
    "v_add_f32_dpp v120, v120, v120 row_half_mirror row_mask:0xf bank_mask:0xf\n"
    "v_add_f32_dpp v122, v122, v122 row_half_mirror row_mask:0xf bank_mask:0xf\n"
    "s_nop 0\n"
    "v_add_f32_dpp v120, v120, v120 row_mirror row_mask:0xf bank_mask:0xf\n"
    "v_add_f32_dpp v122, v122, v122 row_mirror row_mask:0xf bank_mask:0xf\n"
    "v_mov_b32_e32 v124, v120\n"
    "v_mov_b32_e32 v125, v122\n"
    "s_nop 0\n"
    "v_permlane16_swap_b32_e32 v120, v124\n"
    "v_permlane16_swap_b32_e32 v122, v125\n"
    "v_add_f32_e32 v120, v120, v124\n"
    "v_add_f32_e32 v122, v122, v125\n"
    "v_mov_b32_e32 v124, v120\n"
    "v_mov_b32_e32 v125, v122\n"
    "s_nop 0\n"
    "v_permlane32_swap_b32_e32 v120, v124\n"
    "v_permlane32_swap_b32_e32 v122, v125\n"
    "v_add_f32_e32 v120, v120, v124\n"
    "v_add_f32_e32 v122, v122, v125\n"
    "v_fmaak_f32 v120, v120, v128, 0x3727c5ac\n"
    "v_fmaak_f32 v122, v122, v128, 0x3727c5ac\n"
    "v_rsq_f32_e32 v120, v120\n"
    "v_rsq_f32_e32 v122, v122\n"
    "s_nop 0\n"
    "v_pk_mul_f32 v[96:97], v[96:97], v[120:121] op_sel_hi:[1,0]\n"
    "v_pk_mul_f32 v[100:101], v[100:101], v[122:123] op_sel_hi:[1,0]\n"
    "v_pk_mul_f32 v[98:99], v[98:99], v[120:121] op_sel_hi:[1,0]\n"
    "v_pk_mul_f32 v[102:103], v[102:103], v[122:123] op_sel_hi:[1,0]\n"
    "v_pk_mul_f32 v[104:105], v[104:105], v[120:121] op_sel_hi:[1,0]\n"
    "v_pk_mul_f32 v[108:109], v[108:109], v[122:123] op_sel_hi:[1,0]\n"
    "v_pk_mul_f32 v[106:107], v[106:107], v[120:121] op_sel_hi:[1,0]\n"
    "v_pk_mul_f32 v[110:111], v[110:111], v[122:123] op_sel_hi:[1,0]\n"
    "v_pk_mul_f32 v[112:113], v[112:113], v[120:121] op_sel_hi:[1,0]\n"
    "v_pk_mul_f32 v[116:117], v[116:117], v[122:123] op_sel_hi:[1,0]\n"
    "v_pk_mul_f32 v[114:115], v[114:115], v[120:121] op_sel_hi:[1,0]\n"
    "v_pk_mul_f32 v[118:119], v[118:119], v[122:123] op_sel_hi:[1,0]\n"
    "v_pk_fma_f32 v[96:97], v[96:97], v[72:73], v[60:61]\n"
    "v_pk_fma_f32 v[98:99], v[98:99], v[74:75], v[62:63]\n"
    "v_cvt_pk_bf16_f32 v96, v96, v97\n"
    "v_cvt_pk_bf16_f32 v97, v98, v99\n"
    "ds_write_b64 v142, v[96:97] offset:0\n"
    "v_pk_fma_f32 v[100:101], v[100:101], v[72:73], v[60:61]\n"
    "v_pk_fma_f32 v[102:103], v[102:103], v[74:75], v[62:63]\n"
    "v_cvt_pk_bf16_f32 v100, v100, v101\n"
    "v_cvt_pk_bf16_f32 v101, v102, v103\n"
    "ds_write_b64 v142, v[100:101] offset:4608\n"
    "v_pk_fma_f32 v[104:105], v[104:105], v[76:77], v[64:65]\n"
    "v_pk_fma_f32 v[106:107], v[106:107], v[78:79], v[66:67]\n"
    "v_cvt_pk_bf16_f32 v104, v104, v105\n"
    "v_cvt_pk_bf16_f32 v105, v106, v107\n"
    "ds_write_b64 v142, v[104:105] offset:2048\n"
    "v_pk_fma_f32 v[108:109], v[108:109], v[76:77], v[64:65]\n"
    "v_pk_fma_f32 v[110:111], v[110:111], v[78:79], v[66:67]\n"
    "v_cvt_pk_bf16_f32 v108, v108, v109\n"
    "v_cvt_pk_bf16_f32 v109, v110, v111\n"
    "L_pad2_10_mk:\n"
    "s_cbranch_execz L_ret2_mk\n"
    "ds_write_b64 v142, v[108:109] offset:6656\n"
    "s_mov_b64 exec, 0xffff\n"
    "v_pk_fma_f32 v[112:113], v[112:113], v[80:81], v[68:69]\n"
    "v_pk_fma_f32 v[114:115], v[114:115], v[82:83], v[70:71]\n"
    "v_cvt_pk_bf16_f32 v112, v112, v113\n"
    "v_cvt_pk_bf16_f32 v113, v114, v115\n"
    "ds_write_b64 v142, v[112:113] offset:4096\n"
    "v_pk_fma_f32 v[116:117], v[116:117], v[80:81], v[68:69]\n"
    "v_pk_fma_f32 v[118:119], v[118:119], v[82:83], v[70:71]\n"
    "v_cvt_pk_bf16_f32 v116, v116, v117\n"
    "v_cvt_pk_bf16_f32 v117, v118, v119\n"
    "ds_write_b64 v142, v[116:117] offset:8704\n"
    "s_mov_b64 exec, -1\n"
    "s_waitcnt lgkmcnt(0)\n"
    "s_barrier\n"
    "ds_read_b128 v[0:3], v143 offset:0\n"
    "ds_read_b128 v[4:7], v143 offset:4096\n"
    "ds_read_b128 v[8:11], v143 offset:8192\n"
    "s_mov_b32 s40, 0\n"
    "s_waitcnt lgkmcnt(2)\n"
    "buffer_store_dwordx4 v[0:3], v144, s[32:35], s40 offen sc1\n"
    "s_mov_b32 s40, 16384\n"
    "s_waitcnt lgkmcnt(1)\n"
    "buffer_store_dwordx4 v[4:7], v144, s[32:35], s40 offen sc1\n"
    "s_mov_b32 s40, 32768\n"
    "s_waitcnt lgkmcnt(0)\n"
    "buffer_store_dwordx4 v[8:11], v144, s[32:35], s40 offen sc1\n"
    "s_lshl_b32 s42, s58, 2\n"
    "s_add_u32 s42, s42, s88\n"
    "s_mul_i32 s40, s42, 73728\n"
    "s_add_u32 s40, s40, 1048576\n"
    "s_add_u32 s24, s64, s40\n"
    "s_addc_u32 s25, s65, 0\n"
    "s_and_b32 s25, s25, 0xffff\n"
    "s_mov_b32 s26, 73728\n"
    "s_mov_b32 s27, 0x20000\n"
    "s_mul_i32 s40, s42, 589824\n"
    "s_lshr_b32 s41, s57, 1\n"
    "s_mul_i32 s41, s41, 9216\n"
    "s_add_u32 s40, s40, s41\n"
    "s_add_u32 s40, s40, 38797312\n"
    "s_add_u32 s28, s64, s40\n"
    "s_addc_u32 s29, s65, 0\n"
    "s_and_b32 s29, s29, 0xffff\n"
    "s_mov_b32 s30, 451584\n"
    "s_mov_b32 s31, 0x20000\n"
    "s_and_b32 s41, s57, 1\n"
    "s_mul_i32 s36, s41, 5120\n"
    "s_lshl_b32 s41, s41, 9\n"
    "v_lshrrev_b32_e32 v237, 1, v232\n"
    "v_add_u32_e32 v236, s41, v237\n"
    "s_mov_b32 s38, 0xbfb8aa3b\n"
    "s_mul_i32 s40, s55, 1327104\n"
    "s_mul_i32 s41, s54, 331776\n"
    "s_add_u32 s40, s40, s41\n"
    "s_add_u32 s40, s40, 75497472\n"
    "s_add_u32 s20, s64, s40\n"
    "s_addc_u32 s21, s65, 0\n"
    "s_and_b32 s21, s21, 0xffff\n"
    "s_mov_b32 s22, 331776\n"
    "s_mov_b32 s23, 0x20000\n"
    "v_lshrrev_b32_e32 v194, 4, v232\n"
    "s_and_b32 s43, s89, 0xe0\n"
    "s_and_b32 s40, s89, 31\n"
    "v_mul_u32_u24_e32 v194, 2048, v194\n"
    "s_mul_i32 s43, s43, 2048\n"
    "s_mul_i32 s40, s40, 64\n"
    "s_add_u32 s43, s43, s40\n"
    "s_add_u32 s43, s43, 524296\n"
    "v_mov_b32_e32 v192, 1\n"
    "s_waitcnt vmcnt(0)\n"
    "s_barrier\n"
    "s_cmp_lg_u32 s88, 1\n"
    "s_cbranch_scc1 L_psh40_mk\n"
    "s_mov_b32 exec_hi, 0\n"
    "buffer_store_dword v192, v194, s[96:99], s43 offen sc1\n"
    "s_mov_b64 exec, -1\n"
    "L_psh40_mk:\n"
    "s_cmp_eq_u32 s88, 0\n"
    "s_cbranch_scc1 L_w0a41_mk\n"
    "s_mul_i32 s40, s88, 24576\n"
    "s_add_u32 m0, s40, 49152\n"
    "L_pad2_11_mk:\n"
    "s_cbranch_execz L_ret2_mk\n"
    "s_add_u32 s53, s40, 49152\n"
    "buffer_load_dwordx4 v232, s[48:51], s53 offen offset:0 sc1 lds\n"
    "buffer_load_dwordx4 v232, s[48:51], s53 offen offset:1024 sc1 lds\n"
    "buffer_load_dwordx4 v232, s[48:51], s53 offen offset:2048 sc1 lds\n"
    "buffer_load_dwordx4 v232, s[48:51], s53 offen offset:3072 sc1 lds\n"
    "s_add_u32 m0, s40, 53248\n"
    "s_add_u32 s53, s40, 53248\n"
    "buffer_load_dwordx4 v232, s[48:51], s53 offen offset:0 sc1 lds\n"
    "buffer_load_dwordx4 v232, s[48:51], s53 offen offset:1024 sc1 lds\n"
    "buffer_load_dwordx4 v232, s[48:51], s53 offen offset:2048 sc1 lds\n"
    "buffer_load_dwordx4 v232, s[48:51], s53 offen offset:3072 sc1 lds\n"
    "s_add_u32 m0, s40, 57344\n"
    "s_add_u32 s53, s40, 57344\n"
    "buffer_load_dwordx4 v232, s[48:51], s53 offen offset:0 sc1 lds\n"
    "buffer_load_dwordx4 v232, s[48:51], s53 offen offset:1024 sc1 lds\n"
    "buffer_load_dwordx4 v232, s[48:51], s53 offen offset:2048 sc1 lds\n"
    "buffer_load_dwordx4 v232, s[48:51], s53 offen offset:3072 sc1 lds\n"
    "s_add_u32 m0, s40, 61440\n"
    "s_add_u32 s53, s40, 61440\n"
    "buffer_load_dwordx4 v232, s[48:51], s53 offen offset:0 sc1 lds\n"
    "buffer_load_dwordx4 v232, s[48:51], s53 offen offset:1024 sc1 lds\n"
    "buffer_load_dwordx4 v232, s[48:51], s53 offen offset:2048 sc1 lds\n"
    "buffer_load_dwordx4 v232, s[48:51], s53 offen offset:3072 sc1 lds\n"
    "s_add_u32 m0, s40, 65536\n"
    "s_add_u32 s53, s40, 65536\n"
    "buffer_load_dwordx4 v232, s[48:51], s53 offen offset:0 sc1 lds\n"
    "buffer_load_dwordx4 v232, s[48:51], s53 offen offset:1024 sc1 lds\n"
    "buffer_load_dwordx4 v232, s[48:51], s53 offen offset:2048 sc1 lds\n"
    "buffer_load_dwordx4 v232, s[48:51], s53 offen offset:3072 sc1 lds\n"
    "s_add_u32 m0, s40, 69632\n"
    "s_add_u32 s53, s40, 69632\n"
    "buffer_load_dwordx4 v232, s[48:51], s53 offen offset:0 sc1 lds\n"
    "buffer_load_dwordx4 v232, s[48:51], s53 offen offset:1024 sc1 lds\n"
    "buffer_load_dwordx4 v232, s[48:51], s53 offen offset:2048 sc1 lds\n"
    "buffer_load_dwordx4 v232, s[48:51], s53 offen offset:3072 sc1 lds\n"
    "L_w0a41_mk:\n"
    "v_lshrrev_b32_e32 v194, 5, v232\n"
    "v_lshlrev_b32_e32 v194, 3, v194\n"
    "v_bfe_u32 v192, v232, 4, 1\n"
    "v_add_u32_e32 v194, v194, v192\n"
    "s_and_b32 s43, s89, 0xe6\n"
    "s_bfe_u32 s40, s89, 0x20003\n"
    "s_lshl_b32 s40, s40, 1\n"
    "s_and_b32 s41, s89, 1\n"
    "s_add_u32 s40, s40, s41\n"
    "v_mul_u32_u24_e32 v194, 2048, v194\n"
    "s_mul_i32 s43, s43, 2048\n"
    "s_mul_i32 s40, s40, 4\n"
    "s_add_u32 s43, s43, s40\n"
    "s_add_u32 s43, s43, 524448\n"
    "v_mov_b32_e32 v192, 1\n"
    "s_mov_b32 s39, s43\n"
    "s_cmp_eq_u32 s88, 1\n"
    "s_cselect_b32 s40, 128, 0\n"
    "v_mov_b32_e32 v252, 0x7f000000\n"
    "v_cmp_gt_u32_e32 vcc, s40, v232\n"
    "s_mul_i32 s35, s89, 2048\n"
    "s_add_u32 s35, s35, 524448\n"
    "v_cndmask_b32_e32 v252, v252, v194, vcc\n"
    "v_and_b32_e32 v239, 0x70, v232\n"
    "v_lshrrev_b32_e32 v239, 2, v239\n"
    "s_lshl_b32 s40, s56, 2\n"
    "s_add_u32 s42, s40, s88\n"
    "s_mul_i32 s40, s42, 294912\n"
    "s_mul_i32 s41, s54, 36864\n"
    "s_add_u32 s40, s40, s41\n"
    "s_add_u32 s40, s40, 38797312\n"
    "s_add_u32 s60, s64, s40\n"
    "s_addc_u32 s61, s65, 0\n"
    "s_and_b32 s61, s61, 0xffff\n"
    "s_mov_b32 s62, 184320\n"
    "s_mov_b32 s63, 0x20000\n"
    "s_mul_i32 s40, s54, 2359296\n"
    "s_mul_i32 s41, s42, 36864\n"
    "L_pad2_12_mk:\n"
    "s_cbranch_execz L_ret2_mk\n"
    "s_add_u32 s40, s40, s41\n"
    "s_mul_i32 s41, s55, 576\n"
    "s_add_u32 s40, s40, s41\n"
    "s_add_u32 s40, s40, 22020096\n"
    "s_add_u32 s72, s64, s40\n"
    "s_addc_u32 s73, s65, 0\n"
    "s_and_b32 s73, s73, 0xffff\n"
    "s_mov_b32 s74, 35136\n"
    "s_mov_b32 s75, 0x20000\n"
    "s_mov_b64 exec, 0\n"
    "s_cmp_eq_u32 s88, 3\n"
    "s_cbranch_scc1 L_chain4x3_mk\n"
    "s_cmp_eq_u32 s88, 2\n"
    "s_cbranch_scc1 L_chain4x2_mk\n"
    "s_cmp_eq_u32 s88, 1\n"
    "s_cbranch_scc1 L_chain4x1_mk\n"
    "L_ret4_mk:\n"
    "s_mov_b64 exec, -1\n"
    "s_cmp_lg_u32 s88, 0\n"
    "s_cbranch_scc1 L_polled44_mk\n"
    "s_mul_i32 s41, s89, 2048\n"
    "s_add_u32 s41, s41, 524296\n"
    "v_and_b32_e32 v188, 496, v232\n"
    "v_lshlrev_b32_e32 v188, 2, v188\n"
    "s_mov_b32 s92, 0x40000\n"
    "L_poll43_mk:\n"
    "buffer_load_dword v186, v188, s[96:99], s41 offen sc1\n"
    "s_sub_u32 s92, s92, 1\n"
    "s_waitcnt vmcnt(0)\n"
    "v_cmp_gt_u32_e32 vcc, 1, v186\n"
    "s_cbranch_scc1 L_polled44_mk\n"
    "s_cbranch_vccnz L_poll43_mk\n"
    "L_polled44_mk:\n"
    "s_barrier\n"
    "ds_read_b128 v[128:131], v233 offset:0\n"
    "ds_read_b128 v[132:135], v233 offset:1024\n"
    "ds_read_b128 v[136:139], v233 offset:2048\n"
    "ds_read_b128 v[140:143], v233 offset:3072\n"
    "ds_read_b128 v[144:147], v233 offset:4096\n"
    "ds_read_b128 v[148:151], v233 offset:5120\n"
    "s_mov_b32 s33, 0\n"
    "buffer_load_dwordx4 a[0:3], v232, s[24:27], s33 offen offset:0 sc1\n"
    "s_mov_b32 s33, 18432\n"
    "buffer_load_dwordx4 a[4:7], v232, s[24:27], s33 offen offset:0 sc1\n"
    "s_mov_b32 s33, 36864\n"
    "buffer_load_dwordx4 a[8:11], v232, s[24:27], s33 offen offset:0 sc1\n"
    "s_mov_b32 s33, 55296\n"
    "buffer_load_dwordx4 a[12:15], v232, s[24:27], s33 offen offset:0 sc1\n"
    "s_mov_b32 s33, 0\n"
    "buffer_load_dwordx4 a[16:19], v232, s[24:27], s33 offen offset:1024 sc1\n"
    "s_mov_b32 s33, 18432\n"
    "buffer_load_dwordx4 a[20:23], v232, s[24:27], s33 offen offset:1024 sc1\n"
    "s_mov_b32 s33, 36864\n"
    "buffer_load_dwordx4 a[24:27], v232, s[24:27], s33 offen offset:1024 sc1\n"
    "s_mov_b32 s33, 55296\n"
    "buffer_load_dwordx4 a[28:31], v232, s[24:27], s33 offen offset:1024 sc1\n"
    "s_mov_b32 s33, 0\n"
    "buffer_load_dwordx4 a[32:35], v232, s[24:27], s33 offen offset:2048 sc1\n"
    "s_mov_b32 s33, 18432\n"
    "buffer_load_dwordx4 a[36:39], v232, s[24:27], s33 offen offset:2048 sc1\n"
    "s_mov_b32 s33, 36864\n"
    "buffer_load_dwordx4 a[40:43], v232, s[24:27], s33 offen offset:2048 sc1\n"
    "s_mov_b32 s33, 55296\n"
    "buffer_load_dwordx4 a[44:47], v232, s[24:27], s33 offen offset:2048 sc1\n"
    "s_waitcnt vmcnt(11) lgkmcnt(3)\n"
    "v_mfma_f32_16x16x32_bf16 v[0:3], v[128:131], a[0:3], 0\n"
    "ds_read_b128 v[152:155], v233 offset:6144\n"
    "ds_read_b128 v[156:159], v233 offset:7168\n"
    "v_mfma_f32_16x16x32_bf16 v[16:19], v[132:135], a[0:3], 0\n"
    "s_mov_b32 s33, 3072\n"
    "buffer_load_dwordx4 a[48:51], v232, s[24:27], s33 offen offset:0 sc1\n"
    "v_mfma_f32_16x16x32_bf16 v[32:35], v[136:139], a[0:3], 0\n"
    "ds_read_b128 v[160:163], v233 offset:8192\n"
    "s_waitcnt vmcnt(11)\n"
    "v_mfma_f32_16x16x32_bf16 v[36:39], v[136:139], a[4:7], 0\n"
    "buffer_load_dwordx4 a[64:67], v232, s[24:27], s33 offen offset:1024 sc1\n"
    "v_mfma_f32_16x16x32_bf16 v[20:23], v[132:135], a[4:7], 0\n"
    "v_mfma_f32_16x16x32_bf16 v[4:7], v[128:131], a[4:7], 0\n"
    "s_waitcnt vmcnt(11)\n"
    "v_mfma_f32_16x16x32_bf16 v[8:11], v[128:131], a[8:11], 0\n"
    "buffer_load_dwordx4 a[80:83], v232, s[24:27], s33 offen offset:2048 sc1\n"
    "v_mfma_f32_16x16x32_bf16 v[24:27], v[132:135], a[8:11], 0\n"
    "v_mfma_f32_16x16x32_bf16 v[40:43], v[136:139], a[8:11], 0\n"
    "s_waitcnt vmcnt(11)\n"
    "v_mfma_f32_16x16x32_bf16 v[44:47], v[136:139], a[12:15], 0\n"
    "s_mov_b32 s33, 21504\n"
    "buffer_load_dwordx4 a[52:55], v232, s[24:27], s33 offen offset:0 sc1\n"
    "v_mfma_f32_16x16x32_bf16 v[28:31], v[132:135], a[12:15], 0\n"
    "v_mfma_f32_16x16x32_bf16 v[12:15], v[128:131], a[12:15], 0\n"
    "s_waitcnt lgkmcnt(3)\n"
    "v_mfma_f32_16x16x32_bf16 v[48:51], v[140:143], a[0:3], 0\n"
    "ds_read_b128 v[164:167], v233 offset:9216\n"
    "ds_read_b128 v[168:171], v233 offset:10240\n"
    "v_mfma_f32_16x16x32_bf16 v[64:67], v[144:147], a[0:3], 0\n"
    "buffer_load_dwordx4 a[68:71], v232, s[24:27], s33 offen offset:1024 sc1\n"
    "v_mfma_f32_16x16x32_bf16 v[80:83], v[148:151], a[0:3], 0\n"
    "ds_read_b128 v[172:175], v233 offset:11264\n"
    "v_mfma_f32_16x16x32_bf16 v[84:87], v[148:151], a[4:7], 0\n"
    "buffer_load_dwordx4 a[84:87], v232, s[24:27], s33 offen offset:2048 sc1\n"
    "v_mfma_f32_16x16x32_bf16 v[68:71], v[144:147], a[4:7], 0\n"
    "v_mfma_f32_16x16x32_bf16 v[52:55], v[140:143], a[4:7], 0\n"
    "v_mfma_f32_16x16x32_bf16 v[56:59], v[140:143], a[8:11], 0\n"
    "s_mov_b32 s33, 39936\n"
    "buffer_load_dwordx4 a[56:59], v232, s[24:27], s33 offen offset:0 sc1\n"
    "v_mfma_f32_16x16x32_bf16 v[72:75], v[144:147], a[8:11], 0\n"
    "v_mfma_f32_16x16x32_bf16 v[88:91], v[148:151], a[8:11], 0\n"
    "v_mfma_f32_16x16x32_bf16 v[92:95], v[148:151], a[12:15], 0\n"
    "buffer_load_dwordx4 a[72:75], v232, s[24:27], s33 offen offset:1024 sc1\n"
    "v_mfma_f32_16x16x32_bf16 v[76:79], v[144:147], a[12:15], 0\n"
    "v_mfma_f32_16x16x32_bf16 v[60:63], v[140:143], a[12:15], 0\n"
    "s_waitcnt lgkmcnt(3)\n"
    "s_waitcnt vmcnt(15)\n"
    "v_mfma_f32_16x16x32_bf16 v[0:3], v[152:155], a[16:19], v[0:3]\n"
    "ds_read_b128 v[128:131], v233 offset:12288\n"
    "ds_read_b128 v[132:135], v233 offset:13312\n"
    "v_mfma_f32_16x16x32_bf16 v[16:19], v[156:159], a[16:19], v[16:19]\n"
    "buffer_load_dwordx4 a[88:91], v232, s[24:27], s33 offen offset:2048 sc1\n"
    "v_mfma_f32_16x16x32_bf16 v[32:35], v[160:163], a[16:19], v[32:35]\n"
    "ds_read_b128 v[136:139], v233 offset:14336\n"
    "s_waitcnt vmcnt(15)\n"
    "v_mfma_f32_16x16x32_bf16 v[36:39], v[160:163], a[20:23], v[36:39]\n"
    "s_mov_b32 s33, 58368\n"
    "buffer_load_dwordx4 a[60:63], v232, s[24:27], s33 offen offset:0 sc1\n"
    "v_mfma_f32_16x16x32_bf16 v[20:23], v[156:159], a[20:23], v[20:23]\n"
    "v_mfma_f32_16x16x32_bf16 v[4:7], v[152:155], a[20:23], v[4:7]\n"
    "s_waitcnt vmcnt(15)\n"
    "v_mfma_f32_16x16x32_bf16 v[8:11], v[152:155], a[24:27], v[8:11]\n"
    "buffer_load_dwordx4 a[76:79], v232, s[24:27], s33 offen offset:1024 sc1\n"
    "v_mfma_f32_16x16x32_bf16 v[24:27], v[156:159], a[24:27], v[24:27]\n"
    "v_mfma_f32_16x16x32_bf16 v[40:43], v[160:163], a[24:27], v[40:43]\n"
    "s_waitcnt vmcnt(15)\n"
    "v_mfma_f32_16x16x32_bf16 v[44:47], v[160:163], a[28:31], v[44:47]\n"
    "buffer_load_dwordx4 a[92:95], v232, s[24:27], s33 offen offset:2048 sc1\n"
    "v_mfma_f32_16x16x32_bf16 v[28:31], v[156:159], a[28:31], v[28:31]\n"
    "v_mfma_f32_16x16x32_bf16 v[12:15], v[152:155], a[28:31], v[12:15]\n"
    "s_waitcnt lgkmcnt(3)\n"
    "v_mfma_f32_16x16x32_bf16 v[48:51], v[164:167], a[16:19], v[48:51]\n"
    "ds_read_b128 v[140:143], v233 offset:15360\n"
    "ds_read_b128 v[144:147], v233 offset:16384\n"
    "v_mfma_f32_16x16x32_bf16 v[64:67], v[168:171], a[16:19], v[64:67]\n"
    "s_mov_b32 s33, 6144\n"
    "buffer_load_dwordx4 a[96:99], v232, s[24:27], s33 offen offset:0 sc1\n"
    "v_mfma_f32_16x16x32_bf16 v[80:83], v[172:175], a[16:19], v[80:83]\n"
    "ds_read_b128 v[148:151], v233 offset:17408\n"
    "v_mfma_f32_16x16x32_bf16 v[84:87], v[172:175], a[20:23], v[84:87]\n"
    "v_mfma_f32_16x16x32_bf16 v[68:71], v[168:171], a[20:23], v[68:71]\n"
    "buffer_load_dwordx4 a[112:115], v232, s[24:27], s33 offen offset:1024 sc1\n"
    "v_mfma_f32_16x16x32_bf16 v[52:55], v[164:167], a[20:23], v[52:55]\n"
    "v_mfma_f32_16x16x32_bf16 v[56:59], v[164:167], a[24:27], v[56:59]\n"
    "v_mfma_f32_16x16x32_bf16 v[72:75], v[168:171], a[24:27], v[72:75]\n"
    "v_mfma_f32_16x16x32_bf16 v[88:91], v[172:175], a[24:27], v[88:91]\n"
    "buffer_load_dwordx4 a[128:131], v232, s[24:27], s33 offen offset:2048 sc1\n"
    "v_mfma_f32_16x16x32_bf16 v[92:95], v[172:175], a[28:31], v[92:95]\n"
    "v_mfma_f32_16x16x32_bf16 v[76:79], v[168:171], a[28:31], v[76:79]\n"
    "v_mfma_f32_16x16x32_bf16 v[60:63], v[164:167], a[28:31], v[60:63]\n"
    "s_waitcnt lgkmcnt(3)\n"
    "s_waitcnt vmcnt(18)\n"
    "v_mfma_f32_16x16x32_bf16 v[0:3], v[128:131], a[32:35], v[0:3]\n"
    "ds_read_b128 v[152:155], v233 offset:18432\n"
    "ds_read_b128 v[156:159], v233 offset:24576\n"
    "v_mfma_f32_16x16x32_bf16 v[16:19], v[132:135], a[32:35], v[16:19]\n"
    "s_mov_b32 s33, 24576\n"
    "buffer_load_dwordx4 a[100:103], v232, s[24:27], s33 offen offset:0 sc1\n"
    "v_mfma_f32_16x16x32_bf16 v[32:35], v[136:139], a[32:35], v[32:35]\n"
    "ds_read_b128 v[160:163], v233 offset:30720\n"
    "s_waitcnt vmcnt(18)\n"
    "v_mfma_f32_16x16x32_bf16 v[36:39], v[136:139], a[36:39], v[36:39]\n"
    "v_mfma_f32_16x16x32_bf16 v[20:23], v[132:135], a[36:39], v[20:23]\n"
    "buffer_load_dwordx4 a[116:119], v232, s[24:27], s33 offen offset:1024 sc1\n"
    "v_mfma_f32_16x16x32_bf16 v[4:7], v[128:131], a[36:39], v[4:7]\n"
    "s_waitcnt vmcnt(18)\n"
    "v_mfma_f32_16x16x32_bf16 v[8:11], v[128:131], a[40:43], v[8:11]\n"
    "v_mfma_f32_16x16x32_bf16 v[24:27], v[132:135], a[40:43], v[24:27]\n"
    "v_mfma_f32_16x16x32_bf16 v[40:43], v[136:139], a[40:43], v[40:43]\n"
    "buffer_load_dwordx4 a[132:135], v232, s[24:27], s33 offen offset:2048 sc1\n"
    "s_waitcnt vmcnt(18)\n"
    "v_mfma_f32_16x16x32_bf16 v[44:47], v[136:139], a[44:47], v[44:47]\n"
    "v_mfma_f32_16x16x32_bf16 v[28:31], v[132:135], a[44:47], v[28:31]\n"
    "v_mfma_f32_16x16x32_bf16 v[12:15], v[128:131], a[44:47], v[12:15]\n"
    "s_waitcnt lgkmcnt(3)\n"
    "v_mfma_f32_16x16x32_bf16 v[48:51], v[140:143], a[32:35], v[48:51]\n"
    "ds_read_b128 v[164:167], v233 offset:19456\n"
    "ds_read_b128 v[168:171], v233 offset:25600\n"
    "v_mfma_f32_16x16x32_bf16 v[64:67], v[144:147], a[32:35], v[64:67]\n"
    "s_mov_b32 s33, 43008\n"
    "buffer_load_dwordx4 a[104:107], v232, s[24:27], s33 offen offset:0 sc1\n"
    "v_mfma_f32_16x16x32_bf16 v[80:83], v[148:151], a[32:35], v[80:83]\n"
    "ds_read_b128 v[172:175], v233 offset:31744\n"
    "v_mfma_f32_16x16x32_bf16 v[84:87], v[148:151], a[36:39], v[84:87]\n"
    "v_mfma_f32_16x16x32_bf16 v[68:71], v[144:147], a[36:39], v[68:71]\n"
    "v_mfma_f32_16x16x32_bf16 v[52:55], v[140:143], a[36:39], v[52:55]\n"
    "v_mfma_f32_16x16x32_bf16 v[56:59], v[140:143], a[40:43], v[56:59]\n"
    "buffer_load_dwordx4 a[120:123], v232, s[24:27], s33 offen offset:1024 sc1\n"
    "v_mfma_f32_16x16x32_bf16 v[72:75], v[144:147], a[40:43], v[72:75]\n"
    "v_mfma_f32_16x16x32_bf16 v[88:91], v[148:151], a[40:43], v[88:91]\n"
    "v_mfma_f32_16x16x32_bf16 v[92:95], v[148:151], a[44:47], v[92:95]\n"
    "v_mfma_f32_16x16x32_bf16 v[76:79], v[144:147], a[44:47], v[76:79]\n"
    "v_mfma_f32_16x16x32_bf16 v[60:63], v[140:143], a[44:47], v[60:63]\n"
    "s_waitcnt vmcnt(8) lgkmcnt(3)\n"
    "v_mfma_f32_16x16x32_bf16 v[0:3], v[152:155], a[48:51], v[0:3]\n"
    "ds_read_b128 v[128:131], v233 offset:20480\n"
    "ds_read_b128 v[132:135], v233 offset:26624\n"
    "v_mfma_f32_16x16x32_bf16 v[0:3], v[156:159], a[64:67], v[0:3]\n"
    "buffer_load_dwordx4 a[136:139], v232, s[24:27], s33 offen offset:2048 sc1\n"
    "v_mfma_f32_16x16x32_bf16 v[0:3], v[160:163], a[80:83], v[0:3]\n"
    "ds_read_b128 v[136:139], v233 offset:32768\n"
    "v_mfma_f32_16x16x32_bf16 v[4:7], v[160:163], a[84:87], v[4:7]\n"
    "v_mfma_f32_16x16x32_bf16 v[4:7], v[156:159], a[68:71], v[4:7]\n"
    "s_mov_b32 s33, 61440\n"
    "buffer_load_dwordx4 a[108:111], v232, s[24:27], s33 offen offset:0 sc1\n"
    "v_mfma_f32_16x16x32_bf16 v[4:7], v[152:155], a[52:55], v[4:7]\n"
    "v_mfma_f32_16x16x32_bf16 v[8:11], v[152:155], a[56:59], v[8:11]\n"
    "v_mfma_f32_16x16x32_bf16 v[8:11], v[156:159], a[72:75], v[8:11]\n"
    "v_mfma_f32_16x16x32_bf16 v[8:11], v[160:163], a[88:91], v[8:11]\n"
    "buffer_load_dwordx4 a[124:127], v232, s[24:27], s33 offen offset:1024 sc1\n"
    "L_chain3x3_mk:\n"
    "s_cbranch_execz L_pad3_3_mk\n"
    "v_mfma_f32_16x16x32_bf16 v[12:15], v[160:163], a[92:95], v[12:15]\n"
    "v_mfma_f32_16x16x32_bf16 v[12:15], v[156:159], a[76:79], v[12:15]\n"
    "v_mfma_f32_16x16x32_bf16 v[12:15], v[152:155], a[60:63], v[12:15]\n"
    "s_waitcnt lgkmcnt(3)\n"
    "v_mfma_f32_16x16x32_bf16 v[28:31], v[164:167], a[60:63], v[28:31]\n"
    "ds_read_b128 v[140:143], v233 offset:21504\n"
    "ds_read_b128 v[144:147], v233 offset:27648\n"
    "v_mfma_f32_16x16x32_bf16 v[28:31], v[168:171], a[76:79], v[28:31]\n"
    "buffer_load_dwordx4 a[140:143], v232, s[24:27], s33 offen offset:2048 sc1\n"
    "v_mfma_f32_16x16x32_bf16 v[28:31], v[172:175], a[92:95], v[28:31]\n"
    "ds_read_b128 v[148:151], v233 offset:33792\n"
    "v_mfma_f32_16x16x32_bf16 v[24:27], v[172:175], a[88:91], v[24:27]\n"
    "v_mfma_f32_16x16x32_bf16 v[24:27], v[168:171], a[72:75], v[24:27]\n"
    "s_mov_b32 s33, 9216\n"
    "buffer_load_dwordx4 a[144:147], v232, s[24:27], s33 offen offset:0 sc1\n"
    "v_mfma_f32_16x16x32_bf16 v[24:27], v[164:167], a[56:59], v[24:27]\n"
    "v_mfma_f32_16x16x32_bf16 v[20:23], v[164:167], a[52:55], v[20:23]\n"
    "v_mfma_f32_16x16x32_bf16 v[20:23], v[168:171], a[68:71], v[20:23]\n"
    "v_mfma_f32_16x16x32_bf16 v[20:23], v[172:175], a[84:87], v[20:23]\n"
    "buffer_load_dwordx4 a[160:163], v232, s[24:27], s33 offen offset:1024 sc1\n"
    "v_mfma_f32_16x16x32_bf16 v[16:19], v[172:175], a[80:83], v[16:19]\n"
    "v_mfma_f32_16x16x32_bf16 v[16:19], v[168:171], a[64:67], v[16:19]\n"
    "v_mfma_f32_16x16x32_bf16 v[16:19], v[164:167], a[48:51], v[16:19]\n"
    "s_waitcnt lgkmcnt(3)\n"
    "v_mfma_f32_16x16x32_bf16 v[32:35], v[128:131], a[48:51], v[32:35]\n"
    "ds_read_b128 v[152:155], v233 offset:22528\n"
    "ds_read_b128 v[156:159], v233 offset:28672\n"
    "v_mfma_f32_16x16x32_bf16 v[32:35], v[132:135], a[64:67], v[32:35]\n"
    "buffer_load_dwordx4 a[176:179], v232, s[24:27], s33 offen offset:2048 sc1\n"
    "v_mfma_f32_16x16x32_bf16 v[32:35], v[136:139], a[80:83], v[32:35]\n"
    "ds_read_b128 v[160:163], v233 offset:34816\n"
    "v_mfma_f32_16x16x32_bf16 v[36:39], v[136:139], a[84:87], v[36:39]\n"
    "v_mfma_f32_16x16x32_bf16 v[36:39], v[132:135], a[68:71], v[36:39]\n"
    "v_mfma_f32_16x16x32_bf16 v[36:39], v[128:131], a[52:55], v[36:39]\n"
    "v_mfma_f32_16x16x32_bf16 v[40:43], v[128:131], a[56:59], v[40:43]\n"
    "s_mov_b32 s33, 27648\n"
    "buffer_load_dwordx4 a[148:151], v232, s[24:27], s33 offen offset:0 sc1\n"
    "v_mfma_f32_16x16x32_bf16 v[40:43], v[132:135], a[72:75], v[40:43]\n"
    "v_mfma_f32_16x16x32_bf16 v[40:43], v[136:139], a[88:91], v[40:43]\n"
    "v_mfma_f32_16x16x32_bf16 v[44:47], v[136:139], a[92:95], v[44:47]\n"
    "v_mfma_f32_16x16x32_bf16 v[44:47], v[132:135], a[76:79], v[44:47]\n"
    "v_mfma_f32_16x16x32_bf16 v[44:47], v[128:131], a[60:63], v[44:47]\n"
    "s_waitcnt lgkmcnt(3)\n"
    "v_mfma_f32_16x16x32_bf16 v[60:63], v[140:143], a[60:63], v[60:63]\n"
    "ds_read_b128 v[164:167], v233 offset:23552\n"
    "ds_read_b128 v[168:171], v233 offset:29696\n"
    "v_mfma_f32_16x16x32_bf16 v[60:63], v[144:147], a[76:79], v[60:63]\n"
    "buffer_load_dwordx4 a[164:167], v232, s[24:27], s33 offen offset:1024 sc1\n"
    "v_mfma_f32_16x16x32_bf16 v[60:63], v[148:151], a[92:95], v[60:63]\n"
    "ds_read_b128 v[172:175], v233 offset:35840\n"
    "v_mfma_f32_16x16x32_bf16 v[56:59], v[148:151], a[88:91], v[56:59]\n"
    "v_mfma_f32_16x16x32_bf16 v[56:59], v[144:147], a[72:75], v[56:59]\n"
    "buffer_load_dwordx4 a[180:183], v232, s[24:27], s33 offen offset:2048 sc1\n"
    "v_mfma_f32_16x16x32_bf16 v[56:59], v[140:143], a[56:59], v[56:59]\n"
    "v_mfma_f32_16x16x32_bf16 v[52:55], v[140:143], a[52:55], v[52:55]\n"
    "v_mfma_f32_16x16x32_bf16 v[52:55], v[144:147], a[68:71], v[52:55]\n"
    "v_mfma_f32_16x16x32_bf16 v[52:55], v[148:151], a[84:87], v[52:55]\n"
    "s_mov_b32 s33, 46080\n"
    "buffer_load_dwordx4 a[152:155], v232, s[24:27], s33 offen offset:0 sc1\n"
    "v_mfma_f32_16x16x32_bf16 v[48:51], v[148:151], a[80:83], v[48:51]\n"
    "v_mfma_f32_16x16x32_bf16 v[48:51], v[144:147], a[64:67], v[48:51]\n"
    "v_mfma_f32_16x16x32_bf16 v[48:51], v[140:143], a[48:51], v[48:51]\n"
    "s_waitcnt vmcnt(63) lgkmcnt(0)\n"
    "v_mfma_f32_16x16x32_bf16 v[64:67], v[152:155], a[48:51], v[64:67]\n"
    "s_barrier\n"
    "v_mfma_f32_16x16x32_bf16 v[64:67], v[156:159], a[64:67], v[64:67]\n"
    "buffer_load_dwordx4 a[168:171], v232, s[24:27], s33 offen offset:1024 sc1\n"
    "v_mfma_f32_16x16x32_bf16 v[64:67], v[160:163], a[80:83], v[64:67]\n"
    "ds_read_b128 v[128:131], v233 offset:36864\n"
    "ds_read_b128 v[132:135], v233 offset:43008\n"
    "v_mfma_f32_16x16x32_bf16 v[68:71], v[160:163], a[84:87], v[68:71]\n"
    "ds_read_b128 v[136:139], v233 offset:49152\n"
    "v_mfma_f32_16x16x32_bf16 v[68:71], v[156:159], a[68:71], v[68:71]\n"
    "buffer_load_dwordx4 a[184:187], v232, s[24:27], s33 offen offset:2048 sc1\n"
    "v_mfma_f32_16x16x32_bf16 v[68:71], v[152:155], a[52:55], v[68:71]\n"
    "v_mfma_f32_16x16x32_bf16 v[72:75], v[152:155], a[56:59], v[72:75]\n"
    "v_mfma_f32_16x16x32_bf16 v[72:75], v[156:159], a[72:75], v[72:75]\n"
    "v_mfma_f32_16x16x32_bf16 v[72:75], v[160:163], a[88:91], v[72:75]\n"
    "s_mov_b32 s33, 64512\n"
    "buffer_load_dwordx4 a[156:159], v232, s[24:27], s33 offen offset:0 sc1\n"
    "v_mfma_f32_16x16x32_bf16 v[76:79], v[160:163], a[92:95], v[76:79]\n"
    "v_mfma_f32_16x16x32_bf16 v[76:79], v[156:159], a[76:79], v[76:79]\n"
    "v_mfma_f32_16x16x32_bf16 v[76:79], v[152:155], a[60:63], v[76:79]\n"
    "v_mfma_f32_16x16x32_bf16 v[92:95], v[164:167], a[60:63], v[92:95]\n"
    "ds_read_b128 v[140:143], v233 offset:37888\n"
    "ds_read_b128 v[144:147], v233 offset:44032\n"
    "v_mfma_f32_16x16x32_bf16 v[92:95], v[168:171], a[76:79], v[92:95]\n"
    "buffer_load_dwordx4 a[172:175], v232, s[24:27], s33 offen offset:1024 sc1\n"
    "v_mfma_f32_16x16x32_bf16 v[92:95], v[172:175], a[92:95], v[92:95]\n"
    "ds_read_b128 v[148:151], v233 offset:50176\n"
    "v_mfma_f32_16x16x32_bf16 v[88:91], v[172:175], a[88:91], v[88:91]\n"
    "v_mfma_f32_16x16x32_bf16 v[88:91], v[168:171], a[72:75], v[88:91]\n"
    "v_mfma_f32_16x16x32_bf16 v[88:91], v[164:167], a[56:59], v[88:91]\n"
    "v_mfma_f32_16x16x32_bf16 v[84:87], v[164:167], a[52:55], v[84:87]\n"
    "buffer_load_dwordx4 a[188:191], v232, s[24:27], s33 offen offset:2048 sc1\n"
    "v_mfma_f32_16x16x32_bf16 v[84:87], v[168:171], a[68:71], v[84:87]\n"
    "v_mfma_f32_16x16x32_bf16 v[84:87], v[172:175], a[84:87], v[84:87]\n"
    "v_mfma_f32_16x16x32_bf16 v[80:83], v[172:175], a[80:83], v[80:83]\n"
    "v_mfma_f32_16x16x32_bf16 v[80:83], v[168:171], a[64:67], v[80:83]\n"
    "v_mfma_f32_16x16x32_bf16 v[80:83], v[164:167], a[48:51], v[80:83]\n"
    "s_waitcnt vmcnt(12) lgkmcnt(3)\n"
    "v_mfma_f32_16x16x32_bf16 v[0:3], v[128:131], a[96:99], v[0:3]\n"
    "ds_read_b128 v[152:155], v233 offset:38912\n"
    "ds_read_b128 v[156:159], v233 offset:45056\n"
    "v_mfma_f32_16x16x32_bf16 v[0:3], v[132:135], a[112:115], v[0:3]\n"
    "s_mov_b32 s33, 12288\n"
    "buffer_load_dwordx4 a[192:195], v232, s[24:27], s33 offen offset:0 sc1\n"
    "v_mfma_f32_16x16x32_bf16 v[0:3], v[136:139], a[128:131], v[0:3]\n"
    "ds_read_b128 v[160:163], v233 offset:51200\n"
    "v_mfma_f32_16x16x32_bf16 v[4:7], v[136:139], a[132:135], v[4:7]\n"
    "v_mfma_f32_16x16x32_bf16 v[4:7], v[132:135], a[116:119], v[4:7]\n"
    "v_mfma_f32_16x16x32_bf16 v[4:7], v[128:131], a[100:103], v[4:7]\n"
    "v_mfma_f32_16x16x32_bf16 v[8:11], v[128:131], a[104:107], v[8:11]\n"
    "buffer_load_dwordx4 a[208:211], v232, s[24:27], s33 offen offset:1024 sc1\n"
    "v_mfma_f32_16x16x32_bf16 v[8:11], v[132:135], a[120:123], v[8:11]\n"
    "v_mfma_f32_16x16x32_bf16 v[8:11], v[136:139], a[136:139], v[8:11]\n"
    "v_mfma_f32_16x16x32_bf16 v[12:15], v[136:139], a[140:143], v[12:15]\n"
    "v_mfma_f32_16x16x32_bf16 v[12:15], v[132:135], a[124:127], v[12:15]\n"
    "v_mfma_f32_16x16x32_bf16 v[12:15], v[128:131], a[108:111], v[12:15]\n"
    "s_waitcnt lgkmcnt(3)\n"
    "v_mfma_f32_16x16x32_bf16 v[28:31], v[140:143], a[108:111], v[28:31]\n"
    "ds_read_b128 v[164:167], v233 offset:39936\n"
    "ds_read_b128 v[168:171], v233 offset:46080\n"
    "v_mfma_f32_16x16x32_bf16 v[28:31], v[144:147], a[124:127], v[28:31]\n"
    "buffer_load_dwordx4 a[224:227], v232, s[24:27], s33 offen offset:2048 sc1\n"
    "v_mfma_f32_16x16x32_bf16 v[28:31], v[148:151], a[140:143], v[28:31]\n"
    "ds_read_b128 v[172:175], v233 offset:52224\n"
    "v_mfma_f32_16x16x32_bf16 v[24:27], v[148:151], a[136:139], v[24:27]\n"
    "v_mfma_f32_16x16x32_bf16 v[24:27], v[144:147], a[120:123], v[24:27]\n"
    "v_mfma_f32_16x16x32_bf16 v[24:27], v[140:143], a[104:107], v[24:27]\n"
    "v_mfma_f32_16x16x32_bf16 v[20:23], v[140:143], a[100:103], v[20:23]\n"
    "s_mov_b32 s33, 30720\n"
    "buffer_load_dwordx4 a[196:199], v232, s[24:27], s33 offen offset:0 sc1\n"
    "v_mfma_f32_16x16x32_bf16 v[20:23], v[144:147], a[116:119], v[20:23]\n"
    "v_mfma_f32_16x16x32_bf16 v[20:23], v[148:151], a[132:135], v[20:23]\n"
    "v_mfma_f32_16x16x32_bf16 v[16:19], v[148:151], a[128:131], v[16:19]\n"
    "v_mfma_f32_16x16x32_bf16 v[16:19], v[144:147], a[112:115], v[16:19]\n"
    "v_mfma_f32_16x16x32_bf16 v[16:19], v[140:143], a[96:99], v[16:19]\n"
    "s_waitcnt lgkmcnt(3)\n"
    "v_mfma_f32_16x16x32_bf16 v[32:35], v[152:155], a[96:99], v[32:35]\n"
    "ds_read_b128 v[128:131], v233 offset:40960\n"
    "ds_read_b128 v[132:135], v233 offset:47104\n"
    "v_mfma_f32_16x16x32_bf16 v[32:35], v[156:159], a[112:115], v[32:35]\n"
    "buffer_load_dwordx4 a[212:215], v232, s[24:27], s33 offen offset:1024 sc1\n"
    "v_mfma_f32_16x16x32_bf16 v[32:35], v[160:163], a[128:131], v[32:35]\n"
    "ds_read_b128 v[136:139], v233 offset:53248\n"
    "v_mfma_f32_16x16x32_bf16 v[36:39], v[160:163], a[132:135], v[36:39]\n"
    "v_mfma_f32_16x16x32_bf16 v[36:39], v[156:159], a[116:119], v[36:39]\n"
    "v_mfma_f32_16x16x32_bf16 v[36:39], v[152:155], a[100:103], v[36:39]\n"
    "v_mfma_f32_16x16x32_bf16 v[40:43], v[152:155], a[104:107], v[40:43]\n"
    "buffer_load_dwordx4 a[228:231], v232, s[24:27], s33 offen offset:2048 sc1\n"
    "v_mfma_f32_16x16x32_bf16 v[40:43], v[156:159], a[120:123], v[40:43]\n"
    "v_mfma_f32_16x16x32_bf16 v[40:43], v[160:163], a[136:139], v[40:43]\n"
    "v_mfma_f32_16x16x32_bf16 v[44:47], v[160:163], a[140:143], v[44:47]\n"
    "v_mfma_f32_16x16x32_bf16 v[44:47], v[156:159], a[124:127], v[44:47]\n"
    "v_mfma_f32_16x16x32_bf16 v[44:47], v[152:155], a[108:111], v[44:47]\n"
    "s_waitcnt lgkmcnt(3)\n"
    "v_mfma_f32_16x16x32_bf16 v[60:63], v[164:167], a[108:111], v[60:63]\n"
    "ds_read_b128 v[140:143], v233 offset:41984\n"
    "ds_read_b128 v[144:147], v233 offset:48128\n"
    "v_mfma_f32_16x16x32_bf16 v[60:63], v[168:171], a[124:127], v[60:63]\n"
    "s_mov_b32 s33, 49152\n"
    "buffer_load_dwordx4 a[200:203], v232, s[24:27], s33 offen offset:0 sc1\n"
    "v_mfma_f32_16x16x32_bf16 v[60:63], v[172:175], a[140:143], v[60:63]\n"
    "ds_read_b128 v[148:151], v233 offset:54272\n"
    "v_mfma_f32_16x16x32_bf16 v[56:59], v[172:175], a[136:139], v[56:59]\n"
    "v_mfma_f32_16x16x32_bf16 v[56:59], v[168:171], a[120:123], v[56:59]\n"
    "v_mfma_f32_16x16x32_bf16 v[56:59], v[164:167], a[104:107], v[56:59]\n"
    "v_mfma_f32_16x16x32_bf16 v[52:55], v[164:167], a[100:103], v[52:55]\n"
    "buffer_load_dwordx4 a[216:219], v232, s[24:27], s33 offen offset:1024 sc1\n"
    "v_mfma_f32_16x16x32_bf16 v[52:55], v[168:171], a[116:119], v[52:55]\n"
    "v_mfma_f32_16x16x32_bf16 v[52:55], v[172:175], a[132:135], v[52:55]\n"
    "v_mfma_f32_16x16x32_bf16 v[48:51], v[172:175], a[128:131], v[48:51]\n"
    "v_mfma_f32_16x16x32_bf16 v[48:51], v[168:171], a[112:115], v[48:51]\n"
    "v_mfma_f32_16x16x32_bf16 v[48:51], v[164:167], a[96:99], v[48:51]\n"
    "s_waitcnt lgkmcnt(3)\n"
    "v_mfma_f32_16x16x32_bf16 v[64:67], v[128:131], a[96:99], v[64:67]\n"
    "ds_read_b128 v[152:155], v233 offset:55296\n"
    "ds_read_b128 v[156:159], v233 offset:61440\n"
    "v_mfma_f32_16x16x32_bf16 v[64:67], v[132:135], a[112:115], v[64:67]\n"
    "buffer_load_dwordx4 a[232:235], v232, s[24:27], s33 offen offset:2048 sc1\n"
    "v_mfma_f32_16x16x32_bf16 v[64:67], v[136:139], a[128:131], v[64:67]\n"
    "ds_read_b128 v[160:163], v234 offset:2048\n"
    "v_mfma_f32_16x16x32_bf16 v[68:71], v[136:139], a[132:135], v[68:71]\n"
    "v_mfma_f32_16x16x32_bf16 v[68:71], v[132:135], a[116:119], v[68:71]\n"
    "v_mfma_f32_16x16x32_bf16 v[68:71], v[128:131], a[100:103], v[68:71]\n"
    "v_mfma_f32_16x16x32_bf16 v[72:75], v[128:131], a[104:107], v[72:75]\n"
    "s_mov_b32 s33, 67584\n"
    "buffer_load_dwordx4 a[204:207], v232, s[24:27], s33 offen offset:0 sc1\n"
    "v_mfma_f32_16x16x32_bf16 v[72:75], v[132:135], a[120:123], v[72:75]\n"
    "v_mfma_f32_16x16x32_bf16 v[72:75], v[136:139], a[136:139], v[72:75]\n"
    "v_mfma_f32_16x16x32_bf16 v[76:79], v[136:139], a[140:143], v[76:79]\n"
    "v_mfma_f32_16x16x32_bf16 v[76:79], v[132:135], a[124:127], v[76:79]\n"
    "v_mfma_f32_16x16x32_bf16 v[76:79], v[128:131], a[108:111], v[76:79]\n"
    "s_waitcnt lgkmcnt(3)\n"
    "v_mfma_f32_16x16x32_bf16 v[92:95], v[140:143], a[108:111], v[92:95]\n"
    "ds_read_b128 v[164:167], v233 offset:56320\n"
    "ds_read_b128 v[168:171], v233 offset:62464\n"
    "v_mfma_f32_16x16x32_bf16 v[92:95], v[144:147], a[124:127], v[92:95]\n"
    "buffer_load_dwordx4 a[220:223], v232, s[24:27], s33 offen offset:1024 sc1\n"
    "v_mfma_f32_16x16x32_bf16 v[92:95], v[148:151], a[140:143], v[92:95]\n"
    "ds_read_b128 v[172:175], v234 offset:3072\n"
    "v_mfma_f32_16x16x32_bf16 v[88:91], v[148:151], a[136:139], v[88:91]\n"
    "v_mfma_f32_16x16x32_bf16 v[88:91], v[144:147], a[120:123], v[88:91]\n"
    "v_mfma_f32_16x16x32_bf16 v[88:91], v[140:143], a[104:107], v[88:91]\n"
    "v_mfma_f32_16x16x32_bf16 v[84:87], v[140:143], a[100:103], v[84:87]\n"
    "buffer_load_dwordx4 a[236:239], v232, s[24:27], s33 offen offset:2048 sc1\n"
    "v_mfma_f32_16x16x32_bf16 v[84:87], v[144:147], a[116:119], v[84:87]\n"
    "v_mfma_f32_16x16x32_bf16 v[84:87], v[148:151], a[132:135], v[84:87]\n"
    "v_mfma_f32_16x16x32_bf16 v[80:83], v[148:151], a[128:131], v[80:83]\n"
    "v_mfma_f32_16x16x32_bf16 v[80:83], v[144:147], a[112:115], v[80:83]\n"
    "v_mfma_f32_16x16x32_bf16 v[80:83], v[140:143], a[96:99], v[80:83]\n"
    "s_waitcnt vmcnt(12) lgkmcnt(3)\n"
    "v_mfma_f32_16x16x32_bf16 v[0:3], v[152:155], a[144:147], v[0:3]\n"
    "ds_read_b128 v[128:131], v233 offset:57344\n"
    "ds_read_b128 v[132:135], v233 offset:63488\n"
    "v_mfma_f32_16x16x32_bf16 v[0:3], v[156:159], a[160:163], v[0:3]\n"
    "s_mov_b32 s33, 15360\n"
    "buffer_load_dwordx4 a[240:243], v232, s[24:27], s33 offen offset:0 sc1\n"
    "v_mfma_f32_16x16x32_bf16 v[0:3], v[160:163], a[176:179], v[0:3]\n"
    "ds_read_b128 v[136:139], v234 offset:4096\n"
    "v_mfma_f32_16x16x32_bf16 v[4:7], v[160:163], a[180:183], v[4:7]\n"
    "v_mfma_f32_16x16x32_bf16 v[4:7], v[156:159], a[164:167], v[4:7]\n"
    "v_mfma_f32_16x16x32_bf16 v[4:7], v[152:155], a[148:151], v[4:7]\n"
    "v_mfma_f32_16x16x32_bf16 v[8:11], v[152:155], a[152:155], v[8:11]\n"
    "buffer_load_dwordx4 v[200:203], v232, s[24:27], s33 offen offset:1024 sc1\n"
    "v_mfma_f32_16x16x32_bf16 v[8:11], v[156:159], a[168:171], v[8:11]\n"
    "v_mfma_f32_16x16x32_bf16 v[8:11], v[160:163], a[184:187], v[8:11]\n"
    "v_mfma_f32_16x16x32_bf16 v[12:15], v[160:163], a[188:191], v[12:15]\n"
    "v_mfma_f32_16x16x32_bf16 v[12:15], v[156:159], a[172:175], v[12:15]\n"
    "v_mfma_f32_16x16x32_bf16 v[12:15], v[152:155], a[156:159], v[12:15]\n"
    "s_waitcnt lgkmcnt(3)\n"
    "v_mfma_f32_16x16x32_bf16 v[28:31], v[164:167], a[156:159], v[28:31]\n"
    "ds_read_b128 v[140:143], v233 offset:58368\n"
    "ds_read_b128 v[144:147], v233 offset:64512\n"
    "v_mfma_f32_16x16x32_bf16 v[28:31], v[168:171], a[172:175], v[28:31]\n"
    "buffer_load_dwordx4 v[216:219], v232, s[24:27], s33 offen offset:2048 sc1\n"
    "v_mfma_f32_16x16x32_bf16 v[28:31], v[172:175], a[188:191], v[28:31]\n"
    "ds_read_b128 v[148:151], v234 offset:5120\n"
    "v_mfma_f32_16x16x32_bf16 v[24:27], v[172:175], a[184:187], v[24:27]\n"
    "v_mfma_f32_16x16x32_bf16 v[24:27], v[168:171], a[168:171], v[24:27]\n"
    "v_mfma_f32_16x16x32_bf16 v[24:27], v[164:167], a[152:155], v[24:27]\n"
    "v_mfma_f32_16x16x32_bf16 v[20:23], v[164:167], a[148:151], v[20:23]\n"
    "s_mov_b32 s33, 33792\n"
    "buffer_load_dwordx4 a[244:247], v232, s[24:27], s33 offen offset:0 sc1\n"
    "v_mfma_f32_16x16x32_bf16 v[20:23], v[168:171], a[164:167], v[20:23]\n"
    "v_mfma_f32_16x16x32_bf16 v[20:23], v[172:175], a[180:183], v[20:23]\n"
    "v_mfma_f32_16x16x32_bf16 v[16:19], v[172:175], a[176:179], v[16:19]\n"
    "v_mfma_f32_16x16x32_bf16 v[16:19], v[168:171], a[160:163], v[16:19]\n"
    "v_mfma_f32_16x16x32_bf16 v[16:19], v[164:167], a[144:147], v[16:19]\n"
    "s_waitcnt lgkmcnt(3)\n"
    "v_mfma_f32_16x16x32_bf16 v[32:35], v[128:131], a[144:147], v[32:35]\n"
    "ds_read_b128 v[152:155], v233 offset:59392\n"
    "ds_read_b128 v[156:159], v234 offset:0\n"
    "v_mfma_f32_16x16x32_bf16 v[32:35], v[132:135], a[160:163], v[32:35]\n"
    "buffer_load_dwordx4 v[204:207], v232, s[24:27], s33 offen offset:1024 sc1\n"
    "v_mfma_f32_16x16x32_bf16 v[32:35], v[136:139], a[176:179], v[32:35]\n"
    "ds_read_b128 v[160:163], v234 offset:6144\n"
    "v_mfma_f32_16x16x32_bf16 v[36:39], v[136:139], a[180:183], v[36:39]\n"
    "v_mfma_f32_16x16x32_bf16 v[36:39], v[132:135], a[164:167], v[36:39]\n"
    "v_mfma_f32_16x16x32_bf16 v[36:39], v[128:131], a[148:151], v[36:39]\n"
    "v_mfma_f32_16x16x32_bf16 v[40:43], v[128:131], a[152:155], v[40:43]\n"
    "buffer_load_dwordx4 v[220:223], v232, s[24:27], s33 offen offset:2048 sc1\n"
    "v_mfma_f32_16x16x32_bf16 v[40:43], v[132:135], a[168:171], v[40:43]\n"
    "v_mfma_f32_16x16x32_bf16 v[40:43], v[136:139], a[184:187], v[40:43]\n"
    "v_mfma_f32_16x16x32_bf16 v[44:47], v[136:139], a[188:191], v[44:47]\n"
    "v_mfma_f32_16x16x32_bf16 v[44:47], v[132:135], a[172:175], v[44:47]\n"
    "v_mfma_f32_16x16x32_bf16 v[44:47], v[128:131], a[156:159], v[44:47]\n"
    "s_waitcnt lgkmcnt(3)\n"
    "v_mfma_f32_16x16x32_bf16 v[60:63], v[140:143], a[156:159], v[60:63]\n"
    "ds_read_b128 v[164:167], v233 offset:60416\n"
    "ds_read_b128 v[168:171], v234 offset:1024\n"
    "v_mfma_f32_16x16x32_bf16 v[60:63], v[144:147], a[172:175], v[60:63]\n"
    "s_mov_b32 s33, 52224\n"
    "buffer_load_dwordx4 a[248:251], v232, s[24:27], s33 offen offset:0 sc1\n"
    "v_mfma_f32_16x16x32_bf16 v[60:63], v[148:151], a[188:191], v[60:63]\n"
    "ds_read_b128 v[172:175], v234 offset:7168\n"
    "v_mfma_f32_16x16x32_bf16 v[56:59], v[148:151], a[184:187], v[56:59]\n"
    "v_mfma_f32_16x16x32_bf16 v[56:59], v[144:147], a[168:171], v[56:59]\n"
    "v_mfma_f32_16x16x32_bf16 v[56:59], v[140:143], a[152:155], v[56:59]\n"
    "v_mfma_f32_16x16x32_bf16 v[52:55], v[140:143], a[148:151], v[52:55]\n"
    "buffer_load_dwordx4 v[208:211], v232, s[24:27], s33 offen offset:1024 sc1\n"
    "v_mfma_f32_16x16x32_bf16 v[52:55], v[144:147], a[164:167], v[52:55]\n"
    "v_mfma_f32_16x16x32_bf16 v[52:55], v[148:151], a[180:183], v[52:55]\n"
    "v_mfma_f32_16x16x32_bf16 v[48:51], v[148:151], a[176:179], v[48:51]\n"
    "v_mfma_f32_16x16x32_bf16 v[48:51], v[144:147], a[160:163], v[48:51]\n"
    "v_mfma_f32_16x16x32_bf16 v[48:51], v[140:143], a[144:147], v[48:51]\n"
    "s_waitcnt vmcnt(63) lgkmcnt(0)\n"
    "v_mfma_f32_16x16x32_bf16 v[64:67], v[152:155], a[144:147], v[64:67]\n"
    "s_barrier\n"
    "v_mfma_f32_16x16x32_bf16 v[64:67], v[156:159], a[160:163], v[64:67]\n"
    "buffer_load_dwordx4 v[224:227], v232, s[24:27], s33 offen offset:2048 sc1\n"
    "v_mfma_f32_16x16x32_bf16 v[64:67], v[160:163], a[176:179], v[64:67]\n"
    "ds_read_b128 v[128:131], v234 offset:8192\n"
    "ds_read_b128 v[132:135], v234 offset:14336\n"
    "v_mfma_f32_16x16x32_bf16 v[68:71], v[160:163], a[180:183], v[68:71]\n"
    "ds_read_b128 v[136:139], v234 offset:20480\n"
    "v_mfma_f32_16x16x32_bf16 v[68:71], v[156:159], a[164:167], v[68:71]\n"
    "v_mfma_f32_16x16x32_bf16 v[68:71], v[152:155], a[148:151], v[68:71]\n"
    "v_mfma_f32_16x16x32_bf16 v[72:75], v[152:155], a[152:155], v[72:75]\n"
    "s_mov_b32 s33, 70656\n"
    "buffer_load_dwordx4 a[252:255], v232, s[24:27], s33 offen offset:0 sc1\n"
    "v_mfma_f32_16x16x32_bf16 v[72:75], v[156:159], a[168:171], v[72:75]\n"
    "v_mfma_f32_16x16x32_bf16 v[72:75], v[160:163], a[184:187], v[72:75]\n"
    "v_mfma_f32_16x16x32_bf16 v[76:79], v[160:163], a[188:191], v[76:79]\n"
    "v_mfma_f32_16x16x32_bf16 v[76:79], v[156:159], a[172:175], v[76:79]\n"
    "v_mfma_f32_16x16x32_bf16 v[76:79], v[152:155], a[156:159], v[76:79]\n"
    "v_mfma_f32_16x16x32_bf16 v[92:95], v[164:167], a[156:159], v[92:95]\n"
    "ds_read_b128 v[140:143], v234 offset:9216\n"
    "ds_read_b128 v[144:147], v234 offset:15360\n"
    "v_mfma_f32_16x16x32_bf16 v[92:95], v[168:171], a[172:175], v[92:95]\n"
    "buffer_load_dwordx4 v[212:215], v232, s[24:27], s33 offen offset:1024 sc1\n"
    "v_mfma_f32_16x16x32_bf16 v[92:95], v[172:175], a[188:191], v[92:95]\n"
    "ds_read_b128 v[148:151], v234 offset:21504\n"
    "v_mfma_f32_16x16x32_bf16 v[88:91], v[172:175], a[184:187], v[88:91]\n"
    "v_mfma_f32_16x16x32_bf16 v[88:91], v[168:171], a[168:171], v[88:91]\n"
    "v_mfma_f32_16x16x32_bf16 v[88:91], v[164:167], a[152:155], v[88:91]\n"
    "v_mfma_f32_16x16x32_bf16 v[84:87], v[164:167], a[148:151], v[84:87]\n"
    "buffer_load_dwordx4 v[228:231], v232, s[24:27], s33 offen offset:2048 sc1\n"
    "v_mfma_f32_16x16x32_bf16 v[84:87], v[168:171], a[164:167], v[84:87]\n"
    "v_mfma_f32_16x16x32_bf16 v[84:87], v[172:175], a[180:183], v[84:87]\n"
    "v_mfma_f32_16x16x32_bf16 v[80:83], v[172:175], a[176:179], v[80:83]\n"
    "v_mfma_f32_16x16x32_bf16 v[80:83], v[168:171], a[160:163], v[80:83]\n"
    "v_mfma_f32_16x16x32_bf16 v[80:83], v[164:167], a[144:147], v[80:83]\n"
    "s_waitcnt vmcnt(12) lgkmcnt(3)\n"
    "v_mfma_f32_16x16x32_bf16 v[0:3], v[128:131], a[192:195], v[0:3]\n"
    "s_add_u32 m0, s52, 0\n"
    "ds_read_b128 v[152:155], v234 offset:10240\n"
    "ds_read_b128 v[156:159], v234 offset:16384\n"
    "v_mfma_f32_16x16x32_bf16 v[0:3], v[132:135], a[208:211], v[0:3]\n"
    "s_add_u32 s53, s52, 147456\n"
    "buffer_load_dwordx4 v232, s[48:51], s53 offen offset:0 sc1 lds\n"
    "v_mfma_f32_16x16x32_bf16 v[0:3], v[136:139], a[224:227], v[0:3]\n"
    "ds_read_b128 v[160:163], v234 offset:22528\n"
    "v_mfma_f32_16x16x32_bf16 v[4:7], v[136:139], a[228:231], v[4:7]\n"
    "v_mfma_f32_16x16x32_bf16 v[4:7], v[132:135], a[212:215], v[4:7]\n"
    "v_mfma_f32_16x16x32_bf16 v[4:7], v[128:131], a[196:199], v[4:7]\n"
    "v_mfma_f32_16x16x32_bf16 v[8:11], v[128:131], a[200:203], v[8:11]\n"
    "buffer_load_dwordx4 v232, s[48:51], s53 offen offset:1024 sc1 lds\n"
    "v_mfma_f32_16x16x32_bf16 v[8:11], v[132:135], a[216:219], v[8:11]\n"
    "v_mfma_f32_16x16x32_bf16 v[8:11], v[136:139], a[232:235], v[8:11]\n"
    "v_mfma_f32_16x16x32_bf16 v[12:15], v[136:139], a[236:239], v[12:15]\n"
    "v_mfma_f32_16x16x32_bf16 v[12:15], v[132:135], a[220:223], v[12:15]\n"
    "v_mfma_f32_16x16x32_bf16 v[12:15], v[128:131], a[204:207], v[12:15]\n"
    "s_waitcnt lgkmcnt(3)\n"
    "v_mfma_f32_16x16x32_bf16 v[28:31], v[140:143], a[204:207], v[28:31]\n"
    "ds_read_b128 v[164:167], v234 offset:11264\n"
    "ds_read_b128 v[168:171], v234 offset:17408\n"
    "v_mfma_f32_16x16x32_bf16 v[28:31], v[144:147], a[220:223], v[28:31]\n"
    "buffer_load_dwordx4 v232, s[48:51], s53 offen offset:2048 sc1 lds\n"
    "v_mfma_f32_16x16x32_bf16 v[28:31], v[148:151], a[236:239], v[28:31]\n"
    "ds_read_b128 v[172:175], v234 offset:23552\n"
    "v_mfma_f32_16x16x32_bf16 v[24:27], v[148:151], a[232:235], v[24:27]\n"
    "v_mfma_f32_16x16x32_bf16 v[24:27], v[144:147], a[216:219], v[24:27]\n"
    "v_mfma_f32_16x16x32_bf16 v[24:27], v[140:143], a[200:203], v[24:27]\n"
    "v_mfma_f32_16x16x32_bf16 v[20:23], v[140:143], a[196:199], v[20:23]\n"
    "v_mfma_f32_16x16x32_bf16 v[20:23], v[144:147], a[212:215], v[20:23]\n"
    "v_mfma_f32_16x16x32_bf16 v[20:23], v[148:151], a[228:231], v[20:23]\n"
    "v_mfma_f32_16x16x32_bf16 v[16:19], v[148:151], a[224:227], v[16:19]\n"
    "v_mfma_f32_16x16x32_bf16 v[16:19], v[144:147], a[208:211], v[16:19]\n"
    "v_mfma_f32_16x16x32_bf16 v[16:19], v[140:143], a[192:195], v[16:19]\n"
    "s_waitcnt lgkmcnt(3)\n"
    "v_mfma_f32_16x16x32_bf16 v[32:35], v[152:155], a[192:195], v[32:35]\n"
    "ds_read_b128 v[128:131], v234 offset:12288\n"
    "ds_read_b128 v[132:135], v234 offset:18432\n"
    "v_mfma_f32_16x16x32_bf16 v[32:35], v[156:159], a[208:211], v[32:35]\n"
    "buffer_load_dwordx4 v232, s[48:51], s53 offen offset:3072 sc1 lds\n"
    "v_mfma_f32_16x16x32_bf16 v[32:35], v[160:163], a[224:227], v[32:35]\n"
    "ds_read_b128 v[136:139], v234 offset:24576\n"
    "v_mfma_f32_16x16x32_bf16 v[36:39], v[160:163], a[228:231], v[36:39]\n"
    "v_mfma_f32_16x16x32_bf16 v[36:39], v[156:159], a[212:215], v[36:39]\n"
    "v_mfma_f32_16x16x32_bf16 v[36:39], v[152:155], a[196:199], v[36:39]\n"
    "s_add_u32 m0, s52, 4096\n"
    "v_mfma_f32_16x16x32_bf16 v[40:43], v[152:155], a[200:203], v[40:43]\n"
    "s_add_u32 s53, s52, 151552\n"
    "buffer_load_dwordx4 v232, s[48:51], s53 offen offset:0 sc1 lds\n"
    "v_mfma_f32_16x16x32_bf16 v[40:43], v[156:159], a[216:219], v[40:43]\n"
    "v_mfma_f32_16x16x32_bf16 v[40:43], v[160:163], a[232:235], v[40:43]\n"
    "v_mfma_f32_16x16x32_bf16 v[44:47], v[160:163], a[236:239], v[44:47]\n"
    "v_mfma_f32_16x16x32_bf16 v[44:47], v[156:159], a[220:223], v[44:47]\n"
    "v_mfma_f32_16x16x32_bf16 v[44:47], v[152:155], a[204:207], v[44:47]\n"
    "s_waitcnt lgkmcnt(3)\n"
    "v_mfma_f32_16x16x32_bf16 v[60:63], v[164:167], a[204:207], v[60:63]\n"
    "ds_read_b128 v[140:143], v234 offset:13312\n"
    "ds_read_b128 v[144:147], v234 offset:19456\n"
    "v_mfma_f32_16x16x32_bf16 v[60:63], v[168:171], a[220:223], v[60:63]\n"
    "buffer_load_dwordx4 v232, s[48:51], s53 offen offset:1024 sc1 lds\n"
    "v_mfma_f32_16x16x32_bf16 v[60:63], v[172:175], a[236:239], v[60:63]\n"
    "ds_read_b128 v[148:151], v234 offset:25600\n"
    "v_mfma_f32_16x16x32_bf16 v[56:59], v[172:175], a[232:235], v[56:59]\n"
    "v_mfma_f32_16x16x32_bf16 v[56:59], v[168:171], a[216:219], v[56:59]\n"
    "v_mfma_f32_16x16x32_bf16 v[56:59], v[164:167], a[200:203], v[56:59]\n"
    "v_mfma_f32_16x16x32_bf16 v[52:55], v[164:167], a[196:199], v[52:55]\n"
    "v_mfma_f32_16x16x32_bf16 v[52:55], v[168:171], a[212:215], v[52:55]\n"
    "v_mfma_f32_16x16x32_bf16 v[52:55], v[172:175], a[228:231], v[52:55]\n"
    "v_mfma_f32_16x16x32_bf16 v[48:51], v[172:175], a[224:227], v[48:51]\n"
    "v_mfma_f32_16x16x32_bf16 v[48:51], v[168:171], a[208:211], v[48:51]\n"
    "v_mfma_f32_16x16x32_bf16 v[48:51], v[164:167], a[192:195], v[48:51]\n"
    "s_waitcnt lgkmcnt(3)\n"
    "v_mfma_f32_16x16x32_bf16 v[64:67], v[128:131], a[192:195], v[64:67]\n"
    "ds_read_b128 v[152:155], v234 offset:26624\n"
    "ds_read_b128 v[156:159], v234 offset:32768\n"
    "v_mfma_f32_16x16x32_bf16 v[64:67], v[132:135], a[208:211], v[64:67]\n"
    "buffer_load_dwordx4 v232, s[48:51], s53 offen offset:2048 sc1 lds\n"
    "v_mfma_f32_16x16x32_bf16 v[64:67], v[136:139], a[224:227], v[64:67]\n"
    "ds_read_b128 v[160:163], v234 offset:38912\n"
    "v_mfma_f32_16x16x32_bf16 v[68:71], v[136:139], a[228:231], v[68:71]\n"
    "v_mfma_f32_16x16x32_bf16 v[68:71], v[132:135], a[212:215], v[68:71]\n"
    "v_mfma_f32_16x16x32_bf16 v[68:71], v[128:131], a[196:199], v[68:71]\n"
    "v_mfma_f32_16x16x32_bf16 v[72:75], v[128:131], a[200:203], v[72:75]\n"
    "buffer_load_dwordx4 v232, s[48:51], s53 offen offset:3072 sc1 lds\n"
    "v_mfma_f32_16x16x32_bf16 v[72:75], v[132:135], a[216:219], v[72:75]\n"
    "v_mfma_f32_16x16x32_bf16 v[72:75], v[136:139], a[232:235], v[72:75]\n"
    "v_mfma_f32_16x16x32_bf16 v[76:79], v[136:139], a[236:239], v[76:79]\n"
    "v_mfma_f32_16x16x32_bf16 v[76:79], v[132:135], a[220:223], v[76:79]\n"
    "v_mfma_f32_16x16x32_bf16 v[76:79], v[128:131], a[204:207], v[76:79]\n"
    "s_waitcnt lgkmcnt(3)\n"
    "v_mfma_f32_16x16x32_bf16 v[92:95], v[140:143], a[204:207], v[92:95]\n"
    "s_add_u32 m0, s52, 8192\n"
    "ds_read_b128 v[164:167], v234 offset:27648\n"
    "ds_read_b128 v[168:171], v234 offset:33792\n"
    "v_mfma_f32_16x16x32_bf16 v[92:95], v[144:147], a[220:223], v[92:95]\n"
    "s_add_u32 s53, s52, 155648\n"
    "buffer_load_dwordx4 v232, s[48:51], s53 offen offset:0 sc1 lds\n"
    "v_mfma_f32_16x16x32_bf16 v[92:95], v[148:151], a[236:239], v[92:95]\n"
    "ds_read_b128 v[172:175], v234 offset:39936\n"
    "v_mfma_f32_16x16x32_bf16 v[88:91], v[148:151], a[232:235], v[88:91]\n"
    "v_mfma_f32_16x16x32_bf16 v[88:91], v[144:147], a[216:219], v[88:91]\n"
    "v_mfma_f32_16x16x32_bf16 v[88:91], v[140:143], a[200:203], v[88:91]\n"
    "v_mfma_f32_16x16x32_bf16 v[84:87], v[140:143], a[196:199], v[84:87]\n"
    "v_mfma_f32_16x16x32_bf16 v[84:87], v[144:147], a[212:215], v[84:87]\n"
    "v_mfma_f32_16x16x32_bf16 v[84:87], v[148:151], a[228:231], v[84:87]\n"
    "v_mfma_f32_16x16x32_bf16 v[80:83], v[148:151], a[224:227], v[80:83]\n"
    "v_mfma_f32_16x16x32_bf16 v[80:83], v[144:147], a[208:211], v[80:83]\n"
    "v_mfma_f32_16x16x32_bf16 v[80:83], v[140:143], a[192:195], v[80:83]\n"
    "s_waitcnt vmcnt(9) lgkmcnt(3)\n"
    "v_mfma_f32_16x16x32_bf16 v[0:3], v[152:155], a[240:243], v[0:3]\n"
    "s_add_u32 m0, s52, 36864\n"
    "ds_read_b128 v[128:131], v234 offset:28672\n"
    "ds_read_b128 v[132:135], v234 offset:34816\n"
    "v_mfma_f32_16x16x32_bf16 v[0:3], v[156:159], v[200:203], v[0:3]\n"
    "s_add_u32 s53, s52, 184320\n"
    "buffer_load_dwordx4 v232, s[48:51], s53 offen offset:0 sc1 lds\n"
    "v_mfma_f32_16x16x32_bf16 v[0:3], v[160:163], v[216:219], v[0:3]\n"
    "ds_read_b128 v[136:139], v234 offset:40960\n"
    "v_mfma_f32_16x16x32_bf16 v[4:7], v[160:163], v[220:223], v[4:7]\n"
    "v_mfma_f32_16x16x32_bf16 v[4:7], v[156:159], v[204:207], v[4:7]\n"
    "v_mfma_f32_16x16x32_bf16 v[4:7], v[152:155], a[244:247], v[4:7]\n"
    "v_mfma_f32_16x16x32_bf16 v[8:11], v[152:155], a[248:251], v[8:11]\n"
    "buffer_load_dwordx4 v232, s[48:51], s53 offen offset:1024 sc1 lds\n"
    "v_mfma_f32_16x16x32_bf16 v[8:11], v[156:159], v[208:211], v[8:11]\n"
    "v_mfma_f32_16x16x32_bf16 v[8:11], v[160:163], v[224:227], v[8:11]\n"
    "v_mfma_f32_16x16x32_bf16 v[12:15], v[160:163], v[228:231], v[12:15]\n"
    "v_mfma_f32_16x16x32_bf16 v[12:15], v[156:159], v[212:215], v[12:15]\n"
    "v_mfma_f32_16x16x32_bf16 v[12:15], v[152:155], a[252:255], v[12:15]\n"
    "s_waitcnt lgkmcnt(3)\n"
    "v_mfma_f32_16x16x32_bf16 v[28:31], v[164:167], a[252:255], v[28:31]\n"
    "ds_read_b128 v[140:143], v234 offset:29696\n"
    "ds_read_b128 v[144:147], v234 offset:35840\n"
    "v_mfma_f32_16x16x32_bf16 v[28:31], v[168:171], v[212:215], v[28:31]\n"
    "buffer_load_dwordx4 v232, s[48:51], s53 offen offset:2048 sc1 lds\n"
    "v_mfma_f32_16x16x32_bf16 v[28:31], v[172:175], v[228:231], v[28:31]\n"
    "ds_read_b128 v[148:151], v234 offset:41984\n"
    "v_mfma_f32_16x16x32_bf16 v[24:27], v[172:175], v[224:227], v[24:27]\n"
    "v_mfma_f32_16x16x32_bf16 v[24:27], v[168:171], v[208:211], v[24:27]\n"
    "v_mfma_f32_16x16x32_bf16 v[24:27], v[164:167], a[248:251], v[24:27]\n"
    "v_mfma_f32_16x16x32_bf16 v[20:23], v[164:167], a[244:247], v[20:23]\n"
    "v_mfma_f32_16x16x32_bf16 v[20:23], v[168:171], v[204:207], v[20:23]\n"
    "v_mfma_f32_16x16x32_bf16 v[20:23], v[172:175], v[220:223], v[20:23]\n"
    "v_mfma_f32_16x16x32_bf16 v[16:19], v[172:175], v[216:219], v[16:19]\n"
    "v_mfma_f32_16x16x32_bf16 v[16:19], v[168:171], v[200:203], v[16:19]\n"
    "v_mfma_f32_16x16x32_bf16 v[16:19], v[164:167], a[240:243], v[16:19]\n"
    "s_waitcnt lgkmcnt(3)\n"
    "v_mfma_f32_16x16x32_bf16 v[32:35], v[128:131], a[240:243], v[32:35]\n"
    "ds_read_b128 v[152:155], v234 offset:30720\n"
    "ds_read_b128 v[156:159], v234 offset:36864\n"
    "v_mfma_f32_16x16x32_bf16 v[32:35], v[132:135], v[200:203], v[32:35]\n"
    "buffer_load_dwordx4 v232, s[48:51], s53 offen offset:3072 sc1 lds\n"
    "v_mfma_f32_16x16x32_bf16 v[32:35], v[136:139], v[216:219], v[32:35]\n"
    "ds_read_b128 v[160:163], v234 offset:43008\n"
    "v_mfma_f32_16x16x32_bf16 v[36:39], v[136:139], v[220:223], v[36:39]\n"
    "v_mfma_f32_16x16x32_bf16 v[36:39], v[132:135], v[204:207], v[36:39]\n"
    "v_mfma_f32_16x16x32_bf16 v[36:39], v[128:131], a[244:247], v[36:39]\n"
    "s_add_u32 m0, s52, 40960\n"
    "v_mfma_f32_16x16x32_bf16 v[40:43], v[128:131], a[248:251], v[40:43]\n"
    "s_add_u32 s53, s52, 188416\n"
    "buffer_load_dwordx4 v232, s[48:51], s53 offen offset:0 sc1 lds\n"
    "v_mfma_f32_16x16x32_bf16 v[40:43], v[132:135], v[208:211], v[40:43]\n"
    "v_mfma_f32_16x16x32_bf16 v[40:43], v[136:139], v[224:227], v[40:43]\n"
    "v_mfma_f32_16x16x32_bf16 v[44:47], v[136:139], v[228:231], v[44:47]\n"
    "v_mfma_f32_16x16x32_bf16 v[44:47], v[132:135], v[212:215], v[44:47]\n"
    "v_mfma_f32_16x16x32_bf16 v[44:47], v[128:131], a[252:255], v[44:47]\n"
    "s_waitcnt lgkmcnt(3)\n"
    "v_mfma_f32_16x16x32_bf16 v[60:63], v[140:143], a[252:255], v[60:63]\n"
    "ds_read_b128 v[164:167], v234 offset:31744\n"
    "ds_read_b128 v[168:171], v234 offset:37888\n"
    "v_mfma_f32_16x16x32_bf16 v[60:63], v[144:147], v[212:215], v[60:63]\n"
    "buffer_load_dwordx4 v232, s[48:51], s53 offen offset:1024 sc1 lds\n"
    "v_mfma_f32_16x16x32_bf16 v[60:63], v[148:151], v[228:231], v[60:63]\n"
    "ds_read_b128 v[172:175], v234 offset:44032\n"
    "v_mfma_f32_16x16x32_bf16 v[56:59], v[148:151], v[224:227], v[56:59]\n"
    "v_mfma_f32_16x16x32_bf16 v[56:59], v[144:147], v[208:211], v[56:59]\n"
    "v_mfma_f32_16x16x32_bf16 v[56:59], v[140:143], a[248:251], v[56:59]\n"
    "v_mfma_f32_16x16x32_bf16 v[52:55], v[140:143], a[244:247], v[52:55]\n"
    "v_mfma_f32_16x16x32_bf16 v[52:55], v[144:147], v[204:207], v[52:55]\n"
    "v_mfma_f32_16x16x32_bf16 v[52:55], v[148:151], v[220:223], v[52:55]\n"
    "v_mfma_f32_16x16x32_bf16 v[48:51], v[148:151], v[216:219], v[48:51]\n"
    "v_mfma_f32_16x16x32_bf16 v[48:51], v[144:147], v[200:203], v[48:51]\n"
    "v_mfma_f32_16x16x32_bf16 v[48:51], v[140:143], a[240:243], v[48:51]\n"
    "s_waitcnt vmcnt(63) lgkmcnt(0)\n"
    "v_mfma_f32_16x16x32_bf16 v[64:67], v[152:155], a[240:243], v[64:67]\n"
    "s_barrier\n"
    "v_mfma_f32_16x16x32_bf16 v[64:67], v[156:159], v[200:203], v[64:67]\n"
    "buffer_load_dwordx4 v232, s[48:51], s53 offen offset:2048 sc1 lds\n"
    "v_mfma_f32_16x16x32_bf16 v[64:67], v[160:163], v[216:219], v[64:67]\n"
    "ds_read_b128 v[128:131], v234 offset:45056\n"
    "ds_read_b128 v[132:135], v234 offset:47104\n"
    "v_mfma_f32_16x16x32_bf16 v[68:71], v[160:163], v[220:223], v[68:71]\n"
    "ds_read_b128 v[136:139], v234 offset:49152\n"
    "v_mfma_f32_16x16x32_bf16 v[68:71], v[156:159], v[204:207], v[68:71]\n"
    "v_mfma_f32_16x16x32_bf16 v[68:71], v[152:155], a[244:247], v[68:71]\n"
    "v_mfma_f32_16x16x32_bf16 v[72:75], v[152:155], a[248:251], v[72:75]\n"
    "buffer_load_dwordx4 v232, s[48:51], s53 offen offset:3072 sc1 lds\n"
    "v_mfma_f32_16x16x32_bf16 v[72:75], v[156:159], v[208:211], v[72:75]\n"
    "v_mfma_f32_16x16x32_bf16 v[72:75], v[160:163], v[224:227], v[72:75]\n"
    "v_mfma_f32_16x16x32_bf16 v[76:79], v[160:163], v[228:231], v[76:79]\n"
    "L_chain3x2_mk:\n"
    "s_cbranch_execz L_pad3_4_mk\n"
    "v_mfma_f32_16x16x32_bf16 v[76:79], v[156:159], v[212:215], v[76:79]\n"
    "v_mfma_f32_16x16x32_bf16 v[76:79], v[152:155], a[252:255], v[76:79]\n"
    "v_mfma_f32_16x16x32_bf16 v[92:95], v[164:167], a[252:255], v[92:95]\n"
    "s_add_u32 m0, s52, 45056\n"
    "ds_read_b128 v[140:143], v234 offset:51200\n"
    "ds_read_b128 v[144:147], v234 offset:53248\n"
    "v_mfma_f32_16x16x32_bf16 v[92:95], v[168:171], v[212:215], v[92:95]\n"
    "s_add_u32 s53, s52, 192512\n"
    "buffer_load_dwordx4 v232, s[48:51], s53 offen offset:0 sc1 lds\n"
    "v_mfma_f32_16x16x32_bf16 v[92:95], v[172:175], v[228:231], v[92:95]\n"
    "ds_read_b128 v[148:151], v234 offset:55296\n"
    "v_mfma_f32_16x16x32_bf16 v[88:91], v[172:175], v[224:227], v[88:91]\n"
    "v_mfma_f32_16x16x32_bf16 v[88:91], v[168:171], v[208:211], v[88:91]\n"
    "v_mfma_f32_16x16x32_bf16 v[88:91], v[164:167], a[248:251], v[88:91]\n"
    "v_mfma_f32_16x16x32_bf16 v[84:87], v[164:167], a[244:247], v[84:87]\n"
    "v_mfma_f32_16x16x32_bf16 v[84:87], v[168:171], v[204:207], v[84:87]\n"
    "v_mfma_f32_16x16x32_bf16 v[84:87], v[172:175], v[220:223], v[84:87]\n"
    "v_mfma_f32_16x16x32_bf16 v[80:83], v[172:175], v[216:219], v[80:83]\n"
    "v_mfma_f32_16x16x32_bf16 v[80:83], v[168:171], v[200:203], v[80:83]\n"
    "v_mfma_f32_16x16x32_bf16 v[80:83], v[164:167], a[240:243], v[80:83]\n"
    "s_waitcnt lgkmcnt(3)\n"
    "v_mfma_f32_16x16x32_bf16 v[96:99], v[128:131], a[0:3], 0\n"
    "s_add_u32 m0, s52, 73728\n"
    "ds_read_b128 v[152:155], v234 offset:57344\n"
    "ds_read_b128 v[156:159], v234 offset:59392\n"
    "v_mfma_f32_16x16x32_bf16 v[96:99], v[132:135], a[16:19], v[96:99]\n"
    "s_add_u32 s53, s52, 221184\n"
    "buffer_load_dwordx4 v232, s[48:51], s53 offen offset:0 sc1 lds\n"
    "v_mfma_f32_16x16x32_bf16 v[96:99], v[136:139], a[32:35], v[96:99]\n"
    "ds_read_b128 v[160:163], v234 offset:61440\n"
    "v_mfma_f32_16x16x32_bf16 v[100:103], v[136:139], a[36:39], 0\n"
    "v_exp_f32_e64 v192, -v0\n"
    "v_mfma_f32_16x16x32_bf16 v[100:103], v[132:135], a[20:23], v[100:103]\n"
    "v_exp_f32_e64 v193, -v1\n"
    "v_mfma_f32_16x16x32_bf16 v[100:103], v[128:131], a[4:7], v[100:103]\n"
    "v_mul_f32_e32 v0, v0, v16\n"
    "v_add_f32_e32 v192, 1.0, v192\n"
    "v_mfma_f32_16x16x32_bf16 v[104:107], v[128:131], a[8:11], 0\n"
    "v_rcp_f32_e32 v192, v192\n"
    "v_mfma_f32_16x16x32_bf16 v[104:107], v[132:135], a[24:27], v[104:107]\n"
    "v_exp_f32_e64 v194, -v2\n"
    "v_mfma_f32_16x16x32_bf16 v[104:107], v[136:139], a[40:43], v[104:107]\n"
    "v_mul_f32_e32 v1, v1, v17\n"
    "v_add_f32_e32 v193, 1.0, v193\n"
    "v_mfma_f32_16x16x32_bf16 v[108:111], v[136:139], a[44:47], 0\n"
    "v_rcp_f32_e32 v193, v193\n"
    "v_mfma_f32_16x16x32_bf16 v[108:111], v[132:135], a[28:31], v[108:111]\n"
    "v_mul_f32_e32 v192, v192, v0\n"
    "v_mfma_f32_16x16x32_bf16 v[108:111], v[128:131], a[12:15], v[108:111]\n"
    "s_waitcnt lgkmcnt(3)\n"
    "v_exp_f32_e64 v195, -v3\n"
    "v_mfma_f32_16x16x32_bf16 v[108:111], v[140:143], a[60:63], v[108:111]\n"
    "ds_read_b128 v[164:167], v234 offset:63488\n"
    "ds_read_b128 v[168:171], v235 offset:0\n"
    "v_mfma_f32_16x16x32_bf16 v[108:111], v[144:147], a[76:79], v[108:111]\n"
    "buffer_load_dwordx4 v232, s[48:51], s53 offen offset:1024 sc1 lds\n"
    "v_mfma_f32_16x16x32_bf16 v[108:111], v[148:151], a[92:95], v[108:111]\n"
    "ds_read_b128 v[172:175], v235 offset:2048\n"
    "v_mul_f32_e32 v2, v2, v18\n"
    "v_mfma_f32_16x16x32_bf16 v[104:107], v[148:151], a[88:91], v[104:107]\n"
    "v_add_f32_e32 v194, 1.0, v194\n"
    "v_mfma_f32_16x16x32_bf16 v[104:107], v[144:147], a[72:75], v[104:107]\n"
    "v_rcp_f32_e32 v194, v194\n"
    "v_mfma_f32_16x16x32_bf16 v[104:107], v[140:143], a[56:59], v[104:107]\n"
    "v_mul_f32_e32 v193, v193, v1\n"
    "v_mfma_f32_16x16x32_bf16 v[100:103], v[140:143], a[52:55], v[100:103]\n"
    "v_exp_f32_e64 v196, -v4\n"
    "v_mfma_f32_16x16x32_bf16 v[100:103], v[144:147], a[68:71], v[100:103]\n"
    "v_mul_f32_e32 v3, v3, v19\n"
    "v_add_f32_e32 v195, 1.0, v195\n"
    "v_mfma_f32_16x16x32_bf16 v[100:103], v[148:151], a[84:87], v[100:103]\n"
    "v_rcp_f32_e32 v195, v195\n"
    "v_mfma_f32_16x16x32_bf16 v[96:99], v[148:151], a[80:83], v[96:99]\n"
    "v_mul_f32_e32 v194, v194, v2\n"
    "v_cvt_pk_bf16_f32 v176, v192, v193\n"
    "v_mfma_f32_16x16x32_bf16 v[96:99], v[144:147], a[64:67], v[96:99]\n"
    "v_exp_f32_e64 v197, -v5\n"
    "v_mfma_f32_16x16x32_bf16 v[96:99], v[140:143], a[48:51], v[96:99]\n"
    "s_waitcnt lgkmcnt(3)\n"
    "v_mul_f32_e32 v4, v4, v20\n"
    "v_add_f32_e32 v196, 1.0, v196\n"
    "v_mfma_f32_16x16x32_bf16 v[96:99], v[152:155], a[96:99], v[96:99]\n"
    "ds_read_b128 v[128:131], v235 offset:4096\n"
    "ds_read_b128 v[132:135], v235 offset:6144\n"
    "v_mfma_f32_16x16x32_bf16 v[96:99], v[156:159], a[112:115], v[96:99]\n"
    "buffer_load_dwordx4 v232, s[48:51], s53 offen offset:2048 sc1 lds\n"
    "v_mfma_f32_16x16x32_bf16 v[96:99], v[160:163], a[128:131], v[96:99]\n"
    "ds_read_b128 v[136:139], v235 offset:8192\n"
    "v_mfma_f32_16x16x32_bf16 v[100:103], v[160:163], a[132:135], v[100:103]\n"
    "v_rcp_f32_e32 v196, v196\n"
    "v_mfma_f32_16x16x32_bf16 v[100:103], v[156:159], a[116:119], v[100:103]\n"
    "v_mul_f32_e32 v195, v195, v3\n"
    "v_mfma_f32_16x16x32_bf16 v[100:103], v[152:155], a[100:103], v[100:103]\n"
    "v_exp_f32_e64 v192, -v6\n"
    "v_mfma_f32_16x16x32_bf16 v[104:107], v[152:155], a[104:107], v[104:107]\n"
    "v_mul_f32_e32 v5, v5, v21\n"
    "v_add_f32_e32 v197, 1.0, v197\n"
    "v_mfma_f32_16x16x32_bf16 v[104:107], v[156:159], a[120:123], v[104:107]\n"
    "v_rcp_f32_e32 v197, v197\n"
    "v_mfma_f32_16x16x32_bf16 v[104:107], v[160:163], a[136:139], v[104:107]\n"
    "v_mul_f32_e32 v196, v196, v4\n"
    "v_cvt_pk_bf16_f32 v177, v194, v195\n"
    "v_mfma_f32_16x16x32_bf16 v[108:111], v[160:163], a[140:143], v[108:111]\n"
    "s_mov_b32 s34, 4096\n"
    "buffer_store_dwordx2 v[176:177], v236, s[28:31], s34 offen sc1\n"
    "v_mfma_f32_16x16x32_bf16 v[108:111], v[156:159], a[124:127], v[108:111]\n"
    "v_exp_f32_e64 v193, -v7\n"
    "v_mfma_f32_16x16x32_bf16 v[108:111], v[152:155], a[108:111], v[108:111]\n"
    "s_waitcnt lgkmcnt(3)\n"
    "v_mul_f32_e32 v6, v6, v22\n"
    "v_add_f32_e32 v192, 1.0, v192\n"
    "v_mfma_f32_16x16x32_bf16 v[108:111], v[164:167], a[156:159], v[108:111]\n"
    "ds_read_b128 v[140:143], v235 offset:10240\n"
    "ds_read_b128 v[144:147], v235 offset:12288\n"
    "v_mfma_f32_16x16x32_bf16 v[108:111], v[168:171], a[172:175], v[108:111]\n"
    "ds_read_b128 v[148:151], v235 offset:14336\n"
    "v_mfma_f32_16x16x32_bf16 v[108:111], v[172:175], a[188:191], v[108:111]\n"
    "v_rcp_f32_e32 v192, v192\n"
    "v_mfma_f32_16x16x32_bf16 v[104:107], v[172:175], a[184:187], v[104:107]\n"
    "v_mul_f32_e32 v197, v197, v5\n"
    "v_mfma_f32_16x16x32_bf16 v[104:107], v[168:171], a[168:171], v[104:107]\n"
    "v_exp_f32_e64 v194, -v8\n"
    "v_mfma_f32_16x16x32_bf16 v[104:107], v[164:167], a[152:155], v[104:107]\n"
    "v_mul_f32_e32 v7, v7, v23\n"
    "v_add_f32_e32 v193, 1.0, v193\n"
    "v_mfma_f32_16x16x32_bf16 v[100:103], v[164:167], a[148:151], v[100:103]\n"
    "v_rcp_f32_e32 v193, v193\n"
    "v_mfma_f32_16x16x32_bf16 v[100:103], v[168:171], a[164:167], v[100:103]\n"
    "v_mul_f32_e32 v192, v192, v6\n"
    "v_cvt_pk_bf16_f32 v180, v196, v197\n"
    "v_mfma_f32_16x16x32_bf16 v[100:103], v[172:175], a[180:183], v[100:103]\n"
    "v_exp_f32_e64 v195, -v9\n"
    "v_mfma_f32_16x16x32_bf16 v[96:99], v[172:175], a[176:179], v[96:99]\n"
    "v_mul_f32_e32 v8, v8, v24\n"
    "v_add_f32_e32 v194, 1.0, v194\n"
    "v_mfma_f32_16x16x32_bf16 v[96:99], v[168:171], a[160:163], v[96:99]\n"
    "v_rcp_f32_e32 v194, v194\n"
    "v_mfma_f32_16x16x32_bf16 v[96:99], v[164:167], a[144:147], v[96:99]\n"
    "s_waitcnt lgkmcnt(3)\n"
    "v_mul_f32_e32 v193, v193, v7\n"
    "v_mfma_f32_16x16x32_bf16 v[96:99], v[128:131], a[192:195], v[96:99]\n"
    "ds_read_b128 v[152:155], v234 offset:46080\n"
    "ds_read_b128 v[156:159], v234 offset:48128\n"
    "v_mfma_f32_16x16x32_bf16 v[96:99], v[132:135], a[208:211], v[96:99]\n"
    "buffer_load_dwordx4 v232, s[48:51], s53 offen offset:3072 sc1 lds\n"
    "v_mfma_f32_16x16x32_bf16 v[96:99], v[136:139], a[224:227], v[96:99]\n"
    "ds_read_b128 v[160:163], v234 offset:50176\n"
    "v_mfma_f32_16x16x32_bf16 v[100:103], v[136:139], a[228:231], v[100:103]\n"
    "v_exp_f32_e64 v196, -v10\n"
    "v_mfma_f32_16x16x32_bf16 v[100:103], v[132:135], a[212:215], v[100:103]\n"
    "v_mul_f32_e32 v9, v9, v25\n"
    "v_add_f32_e32 v195, 1.0, v195\n"
    "v_mfma_f32_16x16x32_bf16 v[100:103], v[128:131], a[196:199], v[100:103]\n"
    "v_rcp_f32_e32 v195, v195\n"
    "v_mfma_f32_16x16x32_bf16 v[104:107], v[128:131], a[200:203], v[104:107]\n"
    "v_mul_f32_e32 v194, v194, v8\n"
    "v_cvt_pk_bf16_f32 v181, v192, v193\n"
    "v_mfma_f32_16x16x32_bf16 v[104:107], v[132:135], a[216:219], v[104:107]\n"
    "s_mov_b32 s34, 151552\n"
    "buffer_store_dwordx2 v[180:181], v236, s[28:31], s34 offen sc1\n"
    "v_mfma_f32_16x16x32_bf16 v[104:107], v[136:139], a[232:235], v[104:107]\n"
    "v_exp_f32_e64 v197, -v11\n"
    "v_mfma_f32_16x16x32_bf16 v[108:111], v[136:139], a[236:239], v[108:111]\n"
    "v_mul_f32_e32 v10, v10, v26\n"
    "v_add_f32_e32 v196, 1.0, v196\n"
    "v_mfma_f32_16x16x32_bf16 v[108:111], v[132:135], a[220:223], v[108:111]\n"
    "v_rcp_f32_e32 v196, v196\n"
    "v_mfma_f32_16x16x32_bf16 v[108:111], v[128:131], a[204:207], v[108:111]\n"
    "s_waitcnt lgkmcnt(3)\n"
    "v_mul_f32_e32 v195, v195, v9\n"
    "v_mfma_f32_16x16x32_bf16 v[108:111], v[140:143], a[252:255], v[108:111]\n"
    "s_add_u32 m0, s52, 77824\n"
    "ds_read_b128 v[164:167], v234 offset:52224\n"
    "ds_read_b128 v[168:171], v234 offset:54272\n"
    "v_mfma_f32_16x16x32_bf16 v[108:111], v[144:147], v[212:215], v[108:111]\n"
    "s_add_u32 s53, s52, 225280\n"
    "buffer_load_dwordx4 v232, s[48:51], s53 offen offset:0 sc1 lds\n"
    "v_mfma_f32_16x16x32_bf16 v[108:111], v[148:151], v[228:231], v[108:111]\n"
    "ds_read_b128 v[172:175], v234 offset:56320\n"
    "v_mfma_f32_16x16x32_bf16 v[104:107], v[148:151], v[224:227], v[104:107]\n"
    "v_exp_f32_e64 v192, -v12\n"
    "v_mfma_f32_16x16x32_bf16 v[104:107], v[144:147], v[208:211], v[104:107]\n"
    "v_mul_f32_e32 v11, v11, v27\n"
    "v_add_f32_e32 v197, 1.0, v197\n"
    "v_mfma_f32_16x16x32_bf16 v[104:107], v[140:143], a[248:251], v[104:107]\n"
    "v_rcp_f32_e32 v197, v197\n"
    "v_mfma_f32_16x16x32_bf16 v[100:103], v[140:143], a[244:247], v[100:103]\n"
    "v_mul_f32_e32 v196, v196, v10\n"
    "v_cvt_pk_bf16_f32 v184, v194, v195\n"
    "v_mfma_f32_16x16x32_bf16 v[100:103], v[144:147], v[204:207], v[100:103]\n"
    "v_exp_f32_e64 v193, -v13\n"
    "v_mfma_f32_16x16x32_bf16 v[100:103], v[148:151], v[220:223], v[100:103]\n"
    "v_mul_f32_e32 v12, v12, v28\n"
    "v_add_f32_e32 v192, 1.0, v192\n"
    "v_mfma_f32_16x16x32_bf16 v[96:99], v[148:151], v[216:219], v[96:99]\n"
    "v_rcp_f32_e32 v192, v192\n"
    "v_mfma_f32_16x16x32_bf16 v[96:99], v[144:147], v[200:203], v[96:99]\n"
    "v_mul_f32_e32 v197, v197, v11\n"
    "v_mfma_f32_16x16x32_bf16 v[96:99], v[140:143], a[240:243], v[96:99]\n"
    "s_waitcnt lgkmcnt(3)\n"
    "v_exp_f32_e64 v194, -v14\n"
    "v_mfma_f32_16x16x32_bf16 v[112:115], v[152:155], a[0:3], 0\n"
    "ds_read_b128 v[128:131], v234 offset:58368\n"
    "ds_read_b128 v[132:135], v234 offset:60416\n"
    "v_mfma_f32_16x16x32_bf16 v[112:115], v[156:159], a[16:19], v[112:115]\n"
    "buffer_load_dwordx4 v232, s[48:51], s53 offen offset:1024 sc1 lds\n"
    "v_mfma_f32_16x16x32_bf16 v[112:115], v[160:163], a[32:35], v[112:115]\n"
    "ds_read_b128 v[136:139], v234 offset:62464\n"
    "v_mul_f32_e32 v13, v13, v29\n"
    "v_mfma_f32_16x16x32_bf16 v[116:119], v[160:163], a[36:39], 0\n"
    "v_add_f32_e32 v193, 1.0, v193\n"
    "v_mfma_f32_16x16x32_bf16 v[116:119], v[156:159], a[20:23], v[116:119]\n"
    "v_rcp_f32_e32 v193, v193\n"
    "v_mfma_f32_16x16x32_bf16 v[116:119], v[152:155], a[4:7], v[116:119]\n"
    "v_mul_f32_e32 v192, v192, v12\n"
    "v_cvt_pk_bf16_f32 v185, v196, v197\n"
    "v_mfma_f32_16x16x32_bf16 v[120:123], v[152:155], a[8:11], 0\n"
    "s_mov_b32 s34, 299008\n"
    "buffer_store_dwordx2 v[184:185], v236, s[28:31], s34 offen sc1\n"
    "v_mfma_f32_16x16x32_bf16 v[120:123], v[156:159], a[24:27], v[120:123]\n"
    "v_exp_f32_e64 v195, -v15\n"
    "v_mfma_f32_16x16x32_bf16 v[120:123], v[160:163], a[40:43], v[120:123]\n"
    "v_mul_f32_e32 v14, v14, v30\n"
    "v_add_f32_e32 v194, 1.0, v194\n"
    "v_mfma_f32_16x16x32_bf16 v[124:127], v[160:163], a[44:47], 0\n"
    "v_rcp_f32_e32 v194, v194\n"
    "v_mfma_f32_16x16x32_bf16 v[124:127], v[156:159], a[28:31], v[124:127]\n"
    "v_mul_f32_e32 v193, v193, v13\n"
    "v_mul_f32_e32 v15, v15, v31\n"
    "v_mfma_f32_16x16x32_bf16 v[124:127], v[152:155], a[12:15], v[124:127]\n"
    "s_waitcnt lgkmcnt(3)\n"
    "v_add_f32_e32 v195, 1.0, v195\n"
    "v_mfma_f32_16x16x32_bf16 v[124:127], v[164:167], a[60:63], v[124:127]\n"
    "ds_read_b128 v[140:143], v234 offset:64512\n"
    "ds_read_b128 v[144:147], v235 offset:1024\n"
    "v_mfma_f32_16x16x32_bf16 v[124:127], v[168:171], a[76:79], v[124:127]\n"
    "ds_read_b128 v[148:151], v235 offset:3072\n"
    "v_mfma_f32_16x16x32_bf16 v[124:127], v[172:175], a[92:95], v[124:127]\n"
    "v_rcp_f32_e32 v195, v195\n"
    "v_mfma_f32_16x16x32_bf16 v[120:123], v[172:175], a[88:91], v[120:123]\n"
    "v_mul_f32_e32 v194, v194, v14\n"
    "v_cvt_pk_bf16_f32 v188, v192, v193\n"
    "v_mfma_f32_16x16x32_bf16 v[120:123], v[168:171], a[72:75], v[120:123]\n"
    "s_nop 0\n"
    "v_mul_f32_e32 v195, v195, v15\n"
    "v_cvt_pk_bf16_f32 v189, v194, v195\n"
    "v_mfma_f32_16x16x32_bf16 v[120:123], v[164:167], a[56:59], v[120:123]\n"
    "s_mov_b32 s34, 446464\n"
    "buffer_store_dwordx2 v[188:189], v236, s[28:31], s34 offen sc1\n"
    "v_mfma_f32_16x16x32_bf16 v[116:119], v[164:167], a[52:55], v[116:119]\n"
    "v_exp_f32_e64 v192, -v32\n"
    "v_mfma_f32_16x16x32_bf16 v[116:119], v[168:171], a[68:71], v[116:119]\n"
    "v_exp_f32_e64 v193, -v33\n"
    "v_mfma_f32_16x16x32_bf16 v[116:119], v[172:175], a[84:87], v[116:119]\n"
    "v_mul_f32_e32 v32, v32, v48\n"
    "v_add_f32_e32 v192, 1.0, v192\n"
    "v_mfma_f32_16x16x32_bf16 v[112:115], v[172:175], a[80:83], v[112:115]\n"
    "v_rcp_f32_e32 v192, v192\n"
    "v_mfma_f32_16x16x32_bf16 v[112:115], v[168:171], a[64:67], v[112:115]\n"
    "v_exp_f32_e64 v194, -v34\n"
    "v_mfma_f32_16x16x32_bf16 v[112:115], v[164:167], a[48:51], v[112:115]\n"
    "s_waitcnt lgkmcnt(3)\n"
    "v_mul_f32_e32 v33, v33, v49\n"
    "v_add_f32_e32 v193, 1.0, v193\n"
    "v_mfma_f32_16x16x32_bf16 v[112:115], v[128:131], a[96:99], v[112:115]\n"
    "ds_read_b128 v[152:155], v235 offset:5120\n"
    "ds_read_b128 v[156:159], v235 offset:7168\n"
    "v_mfma_f32_16x16x32_bf16 v[112:115], v[132:135], a[112:115], v[112:115]\n"
    "buffer_load_dwordx4 v232, s[48:51], s53 offen offset:2048 sc1 lds\n"
    "v_mfma_f32_16x16x32_bf16 v[112:115], v[136:139], a[128:131], v[112:115]\n"
    "ds_read_b128 v[160:163], v235 offset:9216\n"
    "v_mfma_f32_16x16x32_bf16 v[116:119], v[136:139], a[132:135], v[116:119]\n"
    "v_rcp_f32_e32 v193, v193\n"
    "v_mfma_f32_16x16x32_bf16 v[116:119], v[132:135], a[116:119], v[116:119]\n"
    "v_mul_f32_e32 v192, v192, v32\n"
    "v_mfma_f32_16x16x32_bf16 v[116:119], v[128:131], a[100:103], v[116:119]\n"
    "v_exp_f32_e64 v195, -v35\n"
    "v_mfma_f32_16x16x32_bf16 v[120:123], v[128:131], a[104:107], v[120:123]\n"
    "v_mul_f32_e32 v34, v34, v50\n"
    "v_add_f32_e32 v194, 1.0, v194\n"
    "v_mfma_f32_16x16x32_bf16 v[120:123], v[132:135], a[120:123], v[120:123]\n"
    "v_rcp_f32_e32 v194, v194\n"
    "v_mfma_f32_16x16x32_bf16 v[120:123], v[136:139], a[136:139], v[120:123]\n"
    "v_mul_f32_e32 v193, v193, v33\n"
    "v_mfma_f32_16x16x32_bf16 v[124:127], v[136:139], a[140:143], v[124:127]\n"
    "v_exp_f32_e64 v196, -v36\n"
    "v_mfma_f32_16x16x32_bf16 v[124:127], v[132:135], a[124:127], v[124:127]\n"
    "v_mul_f32_e32 v35, v35, v51\n"
    "v_add_f32_e32 v195, 1.0, v195\n"
    "v_mfma_f32_16x16x32_bf16 v[124:127], v[128:131], a[108:111], v[124:127]\n"
    "s_waitcnt lgkmcnt(3)\n"
    "v_rcp_f32_e32 v195, v195\n"
    "v_mfma_f32_16x16x32_bf16 v[124:127], v[140:143], a[156:159], v[124:127]\n"
    "ds_read_b128 v[164:167], v235 offset:11264\n"
    "ds_read_b128 v[168:171], v235 offset:13312\n"
    "v_mfma_f32_16x16x32_bf16 v[124:127], v[144:147], a[172:175], v[124:127]\n"
    "buffer_load_dwordx4 v232, s[48:51], s53 offen offset:3072 sc1 lds\n"
    "v_mfma_f32_16x16x32_bf16 v[124:127], v[148:151], a[188:191], v[124:127]\n"
    "ds_read_b128 v[172:175], v235 offset:15360\n"
    "v_mul_f32_e32 v194, v194, v34\n"
    "v_mfma_f32_16x16x32_bf16 v[120:123], v[148:151], a[184:187], v[120:123]\n"
    "v_cvt_pk_bf16_f32 v176, v192, v193\n"
    "v_mfma_f32_16x16x32_bf16 v[120:123], v[144:147], a[168:171], v[120:123]\n"
    "v_exp_f32_e64 v197, -v37\n"
    "v_mfma_f32_16x16x32_bf16 v[120:123], v[140:143], a[152:155], v[120:123]\n"
    "v_mul_f32_e32 v36, v36, v52\n"
    "v_add_f32_e32 v196, 1.0, v196\n"
    "v_mfma_f32_16x16x32_bf16 v[116:119], v[140:143], a[148:151], v[116:119]\n"
    "v_rcp_f32_e32 v196, v196\n"
    "v_mfma_f32_16x16x32_bf16 v[116:119], v[144:147], a[164:167], v[116:119]\n"
    "v_mul_f32_e32 v195, v195, v35\n"
    "v_mfma_f32_16x16x32_bf16 v[116:119], v[148:151], a[180:183], v[116:119]\n"
    "v_exp_f32_e64 v192, -v38\n"
    "v_mfma_f32_16x16x32_bf16 v[112:115], v[148:151], a[176:179], v[112:115]\n"
    "v_mul_f32_e32 v37, v37, v53\n"
    "v_add_f32_e32 v197, 1.0, v197\n"
    "v_mfma_f32_16x16x32_bf16 v[112:115], v[144:147], a[160:163], v[112:115]\n"
    "v_rcp_f32_e32 v197, v197\n"
    "v_mfma_f32_16x16x32_bf16 v[112:115], v[140:143], a[144:147], v[112:115]\n"
    "s_waitcnt vmcnt(21) lgkmcnt(0)\n"
    "v_mul_f32_e32 v196, v196, v36\n"
    "v_cvt_pk_bf16_f32 v177, v194, v195\n"
    "v_mfma_f32_16x16x32_bf16 v[112:115], v[152:155], a[192:195], v[112:115]\n"
    "s_barrier\n"
    "v_mfma_f32_16x16x32_bf16 v[112:115], v[156:159], a[208:211], v[112:115]\n"
    "s_add_u32 m0, s52, 81920\n"
    "s_add_u32 s53, s52, 229376\n"
    "ds_read_b128 v[128:131], v233 offset:0\n"
    "v_mfma_f32_16x16x32_bf16 v[112:115], v[160:163], a[224:227], v[112:115]\n"
    "buffer_load_dwordx4 v232, s[48:51], s53 offen offset:0 sc1 lds\n"
    "v_mfma_f32_16x16x32_bf16 v[116:119], v[160:163], a[228:231], v[116:119]\n"
    "ds_read_b128 v[132:135], v233 offset:2048\n"
    "ds_read_b128 v[136:139], v233 offset:4096\n"
    "v_mfma_f32_16x16x32_bf16 v[116:119], v[156:159], a[212:215], v[116:119]\n"
    "v_exp_f32_e64 v193, -v39\n"
    "v_mfma_f32_16x16x32_bf16 v[116:119], v[152:155], a[196:199], v[116:119]\n"
    "v_mul_f32_e32 v38, v38, v54\n"
    "v_add_f32_e32 v192, 1.0, v192\n"
    "v_mfma_f32_16x16x32_bf16 v[120:123], v[152:155], a[200:203], v[120:123]\n"
    "v_rcp_f32_e32 v192, v192\n"
    "v_mfma_f32_16x16x32_bf16 v[120:123], v[156:159], a[216:219], v[120:123]\n"
    "v_mul_f32_e32 v197, v197, v37\n"
    "v_mfma_f32_16x16x32_bf16 v[120:123], v[160:163], a[232:235], v[120:123]\n"
    "v_exp_f32_e64 v194, -v40\n"
    "v_mfma_f32_16x16x32_bf16 v[124:127], v[160:163], a[236:239], v[124:127]\n"
    "v_mul_f32_e32 v39, v39, v55\n"
    "v_add_f32_e32 v193, 1.0, v193\n"
    "v_mfma_f32_16x16x32_bf16 v[124:127], v[156:159], a[220:223], v[124:127]\n"
    "v_rcp_f32_e32 v193, v193\n"
    "v_mfma_f32_16x16x32_bf16 v[124:127], v[152:155], a[204:207], v[124:127]\n"
    "v_mul_f32_e32 v192, v192, v38\n"
    "v_cvt_pk_bf16_f32 v180, v196, v197\n"
    "v_mfma_f32_16x16x32_bf16 v[124:127], v[164:167], a[252:255], v[124:127]\n"
    "ds_read_b128 v[140:143], v233 offset:6144\n"
    "ds_read_b128 v[144:147], v233 offset:8192\n"
    "v_mfma_f32_16x16x32_bf16 v[124:127], v[168:171], v[212:215], v[124:127]\n"
    "ds_read_b128 v[148:151], v233 offset:10240\n"
    "v_mfma_f32_16x16x32_bf16 v[124:127], v[172:175], v[228:231], v[124:127]\n"
    "v_exp_f32_e64 v195, -v41\n"
    "v_mfma_f32_16x16x32_bf16 v[120:123], v[172:175], v[224:227], v[120:123]\n"
    "v_mul_f32_e32 v40, v40, v56\n"
    "v_add_f32_e32 v194, 1.0, v194\n"
    "v_mfma_f32_16x16x32_bf16 v[120:123], v[168:171], v[208:211], v[120:123]\n"
    "v_rcp_f32_e32 v194, v194\n"
    "v_mfma_f32_16x16x32_bf16 v[120:123], v[164:167], a[248:251], v[120:123]\n"
    "v_mul_f32_e32 v193, v193, v39\n"
    "v_mfma_f32_16x16x32_bf16 v[116:119], v[164:167], a[244:247], v[116:119]\n"
    "v_exp_f32_e64 v196, -v42\n"
    "v_mfma_f32_16x16x32_bf16 v[116:119], v[168:171], v[204:207], v[116:119]\n"
    "v_mul_f32_e32 v41, v41, v57\n"
    "v_add_f32_e32 v195, 1.0, v195\n"
    "v_mfma_f32_16x16x32_bf16 v[116:119], v[172:175], v[220:223], v[116:119]\n"
    "v_rcp_f32_e32 v195, v195\n"
    "v_mfma_f32_16x16x32_bf16 v[112:115], v[172:175], v[216:219], v[112:115]\n"
    "v_mul_f32_e32 v194, v194, v40\n"
    "v_cvt_pk_bf16_f32 v181, v192, v193\n"
    "v_mfma_f32_16x16x32_bf16 v[112:115], v[168:171], v[200:203], v[112:115]\n"
    "v_exp_f32_e64 v197, -v43\n"
    "v_mfma_f32_16x16x32_bf16 v[112:115], v[164:167], a[240:243], v[112:115]\n"
    "s_waitcnt lgkmcnt(3)\n"
    "v_mul_f32_e32 v42, v42, v58\n"
    "v_add_f32_e32 v196, 1.0, v196\n"
    "v_mfma_f32_16x16x32_bf16 v[0:3], v[128:131], a[0:3], 0\n"
    "s_add_u32 m0, s52, 110592\n"
    "ds_read_b128 v[152:155], v233 offset:12288\n"
    "ds_read_b128 v[156:159], v233 offset:14336\n"
    "v_mfma_f32_16x16x32_bf16 v[0:3], v[132:135], a[16:19], v[0:3]\n"
    "s_add_u32 s53, s52, 258048\n"
    "buffer_load_dwordx4 v232, s[48:51], s53 offen offset:0 sc1 lds\n"
    "v_mfma_f32_16x16x32_bf16 v[0:3], v[136:139], a[32:35], v[0:3]\n"
    "ds_read_b128 v[160:163], v233 offset:16384\n"
    "v_mfma_f32_16x16x32_bf16 v[4:7], v[136:139], a[36:39], 0\n"
    "v_rcp_f32_e32 v196, v196\n"
    "v_mfma_f32_16x16x32_bf16 v[4:7], v[132:135], a[20:23], v[4:7]\n"
    "v_mul_f32_e32 v195, v195, v41\n"
    "v_mfma_f32_16x16x32_bf16 v[4:7], v[128:131], a[4:7], v[4:7]\n"
    "v_exp_f32_e64 v192, -v44\n"
    "v_mfma_f32_16x16x32_bf16 v[8:11], v[128:131], a[8:11], 0\n"
    "v_mul_f32_e32 v43, v43, v59\n"
    "v_add_f32_e32 v197, 1.0, v197\n"
    "v_mfma_f32_16x16x32_bf16 v[8:11], v[132:135], a[24:27], v[8:11]\n"
    "v_rcp_f32_e32 v197, v197\n"
    "v_mfma_f32_16x16x32_bf16 v[8:11], v[136:139], a[40:43], v[8:11]\n"
    "v_mul_f32_e32 v196, v196, v42\n"
    "v_cvt_pk_bf16_f32 v184, v194, v195\n"
    "v_mfma_f32_16x16x32_bf16 v[12:15], v[136:139], a[44:47], 0\n"
    "v_exp_f32_e64 v193, -v45\n"
    "v_mfma_f32_16x16x32_bf16 v[12:15], v[132:135], a[28:31], v[12:15]\n"
    "v_mul_f32_e32 v44, v44, v60\n"
    "v_add_f32_e32 v192, 1.0, v192\n"
    "v_mfma_f32_16x16x32_bf16 v[12:15], v[128:131], a[12:15], v[12:15]\n"
    "s_waitcnt lgkmcnt(3)\n"
    "v_rcp_f32_e32 v192, v192\n"
    "v_mfma_f32_16x16x32_bf16 v[12:15], v[140:143], a[60:63], v[12:15]\n"
    "ds_read_b128 v[164:167], v233 offset:18432\n"
    "ds_read_b128 v[168:171], v233 offset:20480\n"
    "v_mfma_f32_16x16x32_bf16 v[12:15], v[144:147], a[76:79], v[12:15]\n"
    "buffer_load_dwordx4 v232, s[48:51], s53 offen offset:1024 sc1 lds\n"
    "v_mfma_f32_16x16x32_bf16 v[12:15], v[148:151], a[92:95], v[12:15]\n"
    "ds_read_b128 v[172:175], v233 offset:22528\n"
    "v_mul_f32_e32 v197, v197, v43\n"
    "v_mfma_f32_16x16x32_bf16 v[8:11], v[148:151], a[88:91], v[8:11]\n"
    "v_exp_f32_e64 v194, -v46\n"
    "v_mfma_f32_16x16x32_bf16 v[8:11], v[144:147], a[72:75], v[8:11]\n"
    "v_mul_f32_e32 v45, v45, v61\n"
    "v_add_f32_e32 v193, 1.0, v193\n"
    "v_mfma_f32_16x16x32_bf16 v[8:11], v[140:143], a[56:59], v[8:11]\n"
    "v_rcp_f32_e32 v193, v193\n"
    "v_mfma_f32_16x16x32_bf16 v[4:7], v[140:143], a[52:55], v[4:7]\n"
    "v_mul_f32_e32 v192, v192, v44\n"
    "v_cvt_pk_bf16_f32 v185, v196, v197\n"
    "v_mfma_f32_16x16x32_bf16 v[4:7], v[144:147], a[68:71], v[4:7]\n"
    "v_exp_f32_e64 v195, -v47\n"
    "v_mfma_f32_16x16x32_bf16 v[4:7], v[148:151], a[84:87], v[4:7]\n"
    "v_mul_f32_e32 v46, v46, v62\n"
    "v_add_f32_e32 v194, 1.0, v194\n"
    "v_mfma_f32_16x16x32_bf16 v[0:3], v[148:151], a[80:83], v[0:3]\n"
    "v_rcp_f32_e32 v194, v194\n"
    "v_mfma_f32_16x16x32_bf16 v[0:3], v[144:147], a[64:67], v[0:3]\n"
    "v_mul_f32_e32 v193, v193, v45\n"
    "v_mul_f32_e32 v47, v47, v63\n"
    "v_mfma_f32_16x16x32_bf16 v[0:3], v[140:143], a[48:51], v[0:3]\n"
    "s_waitcnt lgkmcnt(3)\n"
    "v_add_f32_e32 v195, 1.0, v195\n"
    "v_mfma_f32_16x16x32_bf16 v[0:3], v[152:155], a[96:99], v[0:3]\n"
    "ds_read_b128 v[128:131], v233 offset:24576\n"
    "ds_read_b128 v[132:135], v233 offset:26624\n"
    "v_mfma_f32_16x16x32_bf16 v[0:3], v[156:159], a[112:115], v[0:3]\n"
    "buffer_load_dwordx4 v232, s[48:51], s53 offen offset:2048 sc1 lds\n"
    "v_mfma_f32_16x16x32_bf16 v[0:3], v[160:163], a[128:131], v[0:3]\n"
    "ds_read_b128 v[136:139], v233 offset:28672\n"
    "v_mfma_f32_16x16x32_bf16 v[4:7], v[160:163], a[132:135], v[4:7]\n"
    "v_rcp_f32_e32 v195, v195\n"
    "v_mfma_f32_16x16x32_bf16 v[4:7], v[156:159], a[116:119], v[4:7]\n"
    "v_mul_f32_e32 v194, v194, v46\n"
    "v_cvt_pk_bf16_f32 v188, v192, v193\n"
    "v_mfma_f32_16x16x32_bf16 v[4:7], v[152:155], a[100:103], v[4:7]\n"
    "s_nop 0\n"
    "v_mul_f32_e32 v195, v195, v47\n"
    "v_cvt_pk_bf16_f32 v189, v194, v195\n"
    "v_mfma_f32_16x16x32_bf16 v[8:11], v[152:155], a[104:107], v[8:11]\n"
    "v_exp_f32_e64 v192, -v64\n"
    "v_mfma_f32_16x16x32_bf16 v[8:11], v[156:159], a[120:123], v[8:11]\n"
    "v_exp_f32_e64 v193, -v65\n"
    "v_mfma_f32_16x16x32_bf16 v[8:11], v[160:163], a[136:139], v[8:11]\n"
    "v_mul_f32_e32 v64, v64, v80\n"
    "v_add_f32_e32 v192, 1.0, v192\n"
    "v_mfma_f32_16x16x32_bf16 v[12:15], v[160:163], a[140:143], v[12:15]\n"
    "v_rcp_f32_e32 v192, v192\n"
    "v_mfma_f32_16x16x32_bf16 v[12:15], v[156:159], a[124:127], v[12:15]\n"
    "v_exp_f32_e64 v194, -v66\n"
    "v_mfma_f32_16x16x32_bf16 v[12:15], v[152:155], a[108:111], v[12:15]\n"
    "s_waitcnt lgkmcnt(3)\n"
    "v_mul_f32_e32 v65, v65, v81\n"
    "v_add_f32_e32 v193, 1.0, v193\n"
    "v_mfma_f32_16x16x32_bf16 v[12:15], v[164:167], a[156:159], v[12:15]\n"
    "ds_read_b128 v[140:143], v233 offset:30720\n"
    "ds_read_b128 v[144:147], v233 offset:32768\n"
    "v_mfma_f32_16x16x32_bf16 v[12:15], v[168:171], a[172:175], v[12:15]\n"
    "ds_read_b128 v[148:151], v233 offset:34816\n"
    "v_mfma_f32_16x16x32_bf16 v[12:15], v[172:175], a[188:191], v[12:15]\n"
    "v_rcp_f32_e32 v193, v193\n"
    "v_mfma_f32_16x16x32_bf16 v[8:11], v[172:175], a[184:187], v[8:11]\n"
    "v_mul_f32_e32 v192, v192, v64\n"
    "v_mfma_f32_16x16x32_bf16 v[8:11], v[168:171], a[168:171], v[8:11]\n"
    "v_exp_f32_e64 v195, -v67\n"
    "v_mfma_f32_16x16x32_bf16 v[8:11], v[164:167], a[152:155], v[8:11]\n"
    "v_mul_f32_e32 v66, v66, v82\n"
    "v_add_f32_e32 v194, 1.0, v194\n"
    "v_mfma_f32_16x16x32_bf16 v[4:7], v[164:167], a[148:151], v[4:7]\n"
    "v_rcp_f32_e32 v194, v194\n"
    "v_mfma_f32_16x16x32_bf16 v[4:7], v[168:171], a[164:167], v[4:7]\n"
    "v_mul_f32_e32 v193, v193, v65\n"
    "v_mfma_f32_16x16x32_bf16 v[4:7], v[172:175], a[180:183], v[4:7]\n"
    "v_exp_f32_e64 v196, -v68\n"
    "v_mfma_f32_16x16x32_bf16 v[0:3], v[172:175], a[176:179], v[0:3]\n"
    "v_mul_f32_e32 v67, v67, v83\n"
    "v_add_f32_e32 v195, 1.0, v195\n"
    "v_mfma_f32_16x16x32_bf16 v[0:3], v[168:171], a[160:163], v[0:3]\n"
    "v_rcp_f32_e32 v195, v195\n"
    "v_mfma_f32_16x16x32_bf16 v[0:3], v[164:167], a[144:147], v[0:3]\n"
    "s_waitcnt lgkmcnt(3)\n"
    "v_mul_f32_e32 v194, v194, v66\n"
    "v_cvt_pk_bf16_f32 v178, v192, v193\n"
    "v_mfma_f32_16x16x32_bf16 v[0:3], v[128:131], a[192:195], v[0:3]\n"
    "ds_read_b128 v[152:155], v233 offset:1024\n"
    "ds_read_b128 v[156:159], v233 offset:3072\n"
    "v_mfma_f32_16x16x32_bf16 v[0:3], v[132:135], a[208:211], v[0:3]\n"
    "buffer_load_dwordx4 v232, s[48:51], s53 offen offset:3072 sc1 lds\n"
    "v_mfma_f32_16x16x32_bf16 v[0:3], v[136:139], a[224:227], v[0:3]\n"
    "ds_read_b128 v[160:163], v233 offset:5120\n"
    "v_mfma_f32_16x16x32_bf16 v[4:7], v[136:139], a[228:231], v[4:7]\n"
    "v_exp_f32_e64 v197, -v69\n"
    "v_mfma_f32_16x16x32_bf16 v[4:7], v[132:135], a[212:215], v[4:7]\n"
    "v_mul_f32_e32 v68, v68, v84\n"
    "v_add_f32_e32 v196, 1.0, v196\n"
    "v_mfma_f32_16x16x32_bf16 v[4:7], v[128:131], a[196:199], v[4:7]\n"
    "v_rcp_f32_e32 v196, v196\n"
    "v_mfma_f32_16x16x32_bf16 v[8:11], v[128:131], a[200:203], v[8:11]\n"
    "v_mul_f32_e32 v195, v195, v67\n"
    "v_mfma_f32_16x16x32_bf16 v[8:11], v[132:135], a[216:219], v[8:11]\n"
    "v_exp_f32_e64 v192, -v70\n"
    "v_mfma_f32_16x16x32_bf16 v[8:11], v[136:139], a[232:235], v[8:11]\n"
    "v_mul_f32_e32 v69, v69, v85\n"
    "v_add_f32_e32 v197, 1.0, v197\n"
    "v_mfma_f32_16x16x32_bf16 v[12:15], v[136:139], a[236:239], v[12:15]\n"
    "v_rcp_f32_e32 v197, v197\n"
    "v_mfma_f32_16x16x32_bf16 v[12:15], v[132:135], a[220:223], v[12:15]\n"
    "v_mul_f32_e32 v196, v196, v68\n"
    "v_cvt_pk_bf16_f32 v179, v194, v195\n"
    "v_mfma_f32_16x16x32_bf16 v[12:15], v[128:131], a[204:207], v[12:15]\n"
    "s_waitcnt lgkmcnt(3)\n"
    "s_add_u32 s34, s36, 0\n"
    "v_mfma_f32_16x16x32_bf16 v[12:15], v[140:143], a[252:255], v[12:15]\n"
    "s_add_u32 m0, s52, 114688\n"
    "ds_read_b128 v[164:167], v233 offset:7168\n"
    "ds_read_b128 v[168:171], v233 offset:9216\n"
    "v_mfma_f32_16x16x32_bf16 v[12:15], v[144:147], v[212:215], v[12:15]\n"
    "s_add_u32 s53, s52, 262144\n"
    "buffer_load_dwordx4 v232, s[48:51], s53 offen offset:0 sc1 lds\n"
    "v_mfma_f32_16x16x32_bf16 v[12:15], v[148:151], v[228:231], v[12:15]\n"
    "ds_read_b128 v[172:175], v233 offset:11264\n"
    "v_mfma_f32_16x16x32_bf16 v[8:11], v[148:151], v[224:227], v[8:11]\n"
    "buffer_store_dwordx4 v[176:179], v232, s[28:31], s34 offen offset:0 sc1\n"
    "v_mfma_f32_16x16x32_bf16 v[8:11], v[144:147], v[208:211], v[8:11]\n"
    "v_exp_f32_e64 v193, -v71\n"
    "v_mfma_f32_16x16x32_bf16 v[8:11], v[140:143], a[248:251], v[8:11]\n"
    "v_mul_f32_e32 v70, v70, v86\n"
    "v_add_f32_e32 v192, 1.0, v192\n"
    "v_mfma_f32_16x16x32_bf16 v[4:7], v[140:143], a[244:247], v[4:7]\n"
    "v_rcp_f32_e32 v192, v192\n"
    "v_mfma_f32_16x16x32_bf16 v[4:7], v[144:147], v[204:207], v[4:7]\n"
    "v_mul_f32_e32 v197, v197, v69\n"
    "v_mfma_f32_16x16x32_bf16 v[4:7], v[148:151], v[220:223], v[4:7]\n"
    "v_exp_f32_e64 v194, -v72\n"
    "v_mfma_f32_16x16x32_bf16 v[0:3], v[148:151], v[216:219], v[0:3]\n"
    "v_mul_f32_e32 v71, v71, v87\n"
    "v_add_f32_e32 v193, 1.0, v193\n"
    "v_mfma_f32_16x16x32_bf16 v[0:3], v[144:147], v[200:203], v[0:3]\n"
    "v_rcp_f32_e32 v193, v193\n"
    "v_mfma_f32_16x16x32_bf16 v[0:3], v[140:143], a[240:243], v[0:3]\n"
    "s_waitcnt lgkmcnt(3)\n"
    "v_mul_f32_e32 v192, v192, v70\n"
    "v_cvt_pk_bf16_f32 v182, v196, v197\n"
    "v_mfma_f32_16x16x32_bf16 v[16:19], v[152:155], a[0:3], 0\n"
    "ds_read_b128 v[128:131], v233 offset:13312\n"
    "ds_read_b128 v[132:135], v233 offset:15360\n"
    "v_mfma_f32_16x16x32_bf16 v[16:19], v[156:159], a[16:19], v[16:19]\n"
    "buffer_load_dwordx4 v232, s[48:51], s53 offen offset:1024 sc1 lds\n"
    "v_mfma_f32_16x16x32_bf16 v[16:19], v[160:163], a[32:35], v[16:19]\n"
    "ds_read_b128 v[136:139], v233 offset:17408\n"
    "v_mfma_f32_16x16x32_bf16 v[20:23], v[160:163], a[36:39], 0\n"
    "v_exp_f32_e64 v195, -v73\n"
    "v_mfma_f32_16x16x32_bf16 v[20:23], v[156:159], a[20:23], v[20:23]\n"
    "v_mul_f32_e32 v72, v72, v88\n"
    "v_add_f32_e32 v194, 1.0, v194\n"
    "v_mfma_f32_16x16x32_bf16 v[20:23], v[152:155], a[4:7], v[20:23]\n"
    "v_rcp_f32_e32 v194, v194\n"
    "v_mfma_f32_16x16x32_bf16 v[24:27], v[152:155], a[8:11], 0\n"
    "v_mul_f32_e32 v193, v193, v71\n"
    "v_mfma_f32_16x16x32_bf16 v[24:27], v[156:159], a[24:27], v[24:27]\n"
    "v_exp_f32_e64 v196, -v74\n"
    "v_mfma_f32_16x16x32_bf16 v[24:27], v[160:163], a[40:43], v[24:27]\n"
    "v_mul_f32_e32 v73, v73, v89\n"
    "v_add_f32_e32 v195, 1.0, v195\n"
    "v_mfma_f32_16x16x32_bf16 v[28:31], v[160:163], a[44:47], 0\n"
    "v_rcp_f32_e32 v195, v195\n"
    "v_mfma_f32_16x16x32_bf16 v[28:31], v[156:159], a[28:31], v[28:31]\n"
    "v_mul_f32_e32 v194, v194, v72\n"
    "v_cvt_pk_bf16_f32 v183, v192, v193\n"
    "v_mfma_f32_16x16x32_bf16 v[28:31], v[152:155], a[12:15], v[28:31]\n"
    "s_waitcnt lgkmcnt(3)\n"
    "s_add_u32 s34, s36, 147456\n"
    "v_mfma_f32_16x16x32_bf16 v[28:31], v[164:167], a[60:63], v[28:31]\n"
    "ds_read_b128 v[140:143], v233 offset:19456\n"
    "ds_read_b128 v[144:147], v233 offset:21504\n"
    "v_mfma_f32_16x16x32_bf16 v[28:31], v[168:171], a[76:79], v[28:31]\n"
    "ds_read_b128 v[148:151], v233 offset:23552\n"
    "v_mfma_f32_16x16x32_bf16 v[28:31], v[172:175], a[92:95], v[28:31]\n"
    "buffer_store_dwordx4 v[180:183], v232, s[28:31], s34 offen offset:0 sc1\n"
    "v_mfma_f32_16x16x32_bf16 v[24:27], v[172:175], a[88:91], v[24:27]\n"
    "v_exp_f32_e64 v197, -v75\n"
    "v_mfma_f32_16x16x32_bf16 v[24:27], v[168:171], a[72:75], v[24:27]\n"
    "v_mul_f32_e32 v74, v74, v90\n"
    "v_add_f32_e32 v196, 1.0, v196\n"
    "v_mfma_f32_16x16x32_bf16 v[24:27], v[164:167], a[56:59], v[24:27]\n"
    "v_rcp_f32_e32 v196, v196\n"
    "v_mfma_f32_16x16x32_bf16 v[20:23], v[164:167], a[52:55], v[20:23]\n"
    "L_chain3x1_mk:\n"
    "s_cbranch_execz L_ret3_mk\n"
    "v_mul_f32_e32 v195, v195, v73\n"
    "v_mfma_f32_16x16x32_bf16 v[20:23], v[168:171], a[68:71], v[20:23]\n"
    "v_exp_f32_e64 v192, -v76\n"
    "v_mfma_f32_16x16x32_bf16 v[20:23], v[172:175], a[84:87], v[20:23]\n"
    "v_mul_f32_e32 v75, v75, v91\n"
    "v_add_f32_e32 v197, 1.0, v197\n"
    "v_mfma_f32_16x16x32_bf16 v[16:19], v[172:175], a[80:83], v[16:19]\n"
    "v_rcp_f32_e32 v197, v197\n"
    "v_mfma_f32_16x16x32_bf16 v[16:19], v[168:171], a[64:67], v[16:19]\n"
    "v_mul_f32_e32 v196, v196, v74\n"
    "v_cvt_pk_bf16_f32 v186, v194, v195\n"
    "v_mfma_f32_16x16x32_bf16 v[16:19], v[164:167], a[48:51], v[16:19]\n"
    "s_waitcnt lgkmcnt(3)\n"
    "v_exp_f32_e64 v193, -v77\n"
    "v_mfma_f32_16x16x32_bf16 v[16:19], v[128:131], a[96:99], v[16:19]\n"
    "ds_read_b128 v[152:155], v233 offset:25600\n"
    "ds_read_b128 v[156:159], v233 offset:27648\n"
    "v_mfma_f32_16x16x32_bf16 v[16:19], v[132:135], a[112:115], v[16:19]\n"
    "buffer_load_dwordx4 v232, s[48:51], s53 offen offset:2048 sc1 lds\n"
    "v_mfma_f32_16x16x32_bf16 v[16:19], v[136:139], a[128:131], v[16:19]\n"
    "ds_read_b128 v[160:163], v233 offset:29696\n"
    "v_mul_f32_e32 v76, v76, v92\n"
    "v_mfma_f32_16x16x32_bf16 v[20:23], v[136:139], a[132:135], v[20:23]\n"
    "v_add_f32_e32 v192, 1.0, v192\n"
    "v_mfma_f32_16x16x32_bf16 v[20:23], v[132:135], a[116:119], v[20:23]\n"
    "v_rcp_f32_e32 v192, v192\n"
    "v_mfma_f32_16x16x32_bf16 v[20:23], v[128:131], a[100:103], v[20:23]\n"
    "v_mul_f32_e32 v197, v197, v75\n"
    "v_mfma_f32_16x16x32_bf16 v[24:27], v[128:131], a[104:107], v[24:27]\n"
    "v_exp_f32_e64 v194, -v78\n"
    "v_mfma_f32_16x16x32_bf16 v[24:27], v[132:135], a[120:123], v[24:27]\n"
    "v_mul_f32_e32 v77, v77, v93\n"
    "v_add_f32_e32 v193, 1.0, v193\n"
    "v_mfma_f32_16x16x32_bf16 v[24:27], v[136:139], a[136:139], v[24:27]\n"
    "v_rcp_f32_e32 v193, v193\n"
    "v_mfma_f32_16x16x32_bf16 v[28:31], v[136:139], a[140:143], v[28:31]\n"
    "v_mul_f32_e32 v192, v192, v76\n"
    "v_cvt_pk_bf16_f32 v187, v196, v197\n"
    "v_mfma_f32_16x16x32_bf16 v[28:31], v[132:135], a[124:127], v[28:31]\n"
    "s_add_u32 s34, s36, 294912\n"
    "buffer_store_dwordx4 v[184:187], v232, s[28:31], s34 offen offset:0 sc1\n"
    "v_mfma_f32_16x16x32_bf16 v[28:31], v[128:131], a[108:111], v[28:31]\n"
    "s_waitcnt lgkmcnt(3)\n"
    "v_exp_f32_e64 v195, -v79\n"
    "v_mfma_f32_16x16x32_bf16 v[28:31], v[140:143], a[156:159], v[28:31]\n"
    "ds_read_b128 v[164:167], v233 offset:31744\n"
    "ds_read_b128 v[168:171], v233 offset:33792\n"
    "v_mfma_f32_16x16x32_bf16 v[28:31], v[144:147], a[172:175], v[28:31]\n"
    "buffer_load_dwordx4 v232, s[48:51], s53 offen offset:3072 sc1 lds\n"
    "v_mfma_f32_16x16x32_bf16 v[28:31], v[148:151], a[188:191], v[28:31]\n"
    "ds_read_b128 v[172:175], v233 offset:35840\n"
    "v_mul_f32_e32 v78, v78, v94\n"
    "v_mfma_f32_16x16x32_bf16 v[24:27], v[148:151], a[184:187], v[24:27]\n"
    "v_add_f32_e32 v194, 1.0, v194\n"
    "v_mfma_f32_16x16x32_bf16 v[24:27], v[144:147], a[168:171], v[24:27]\n"
    "v_rcp_f32_e32 v194, v194\n"
    "v_mfma_f32_16x16x32_bf16 v[24:27], v[140:143], a[152:155], v[24:27]\n"
    "v_mul_f32_e32 v193, v193, v77\n"
    "v_mul_f32_e32 v79, v79, v95\n"
    "v_mfma_f32_16x16x32_bf16 v[20:23], v[140:143], a[148:151], v[20:23]\n"
    "v_add_f32_e32 v195, 1.0, v195\n"
    "v_mfma_f32_16x16x32_bf16 v[20:23], v[144:147], a[164:167], v[20:23]\n"
    "v_rcp_f32_e32 v195, v195\n"
    "v_mfma_f32_16x16x32_bf16 v[20:23], v[148:151], a[180:183], v[20:23]\n"
    "v_mul_f32_e32 v194, v194, v78\n"
    "v_cvt_pk_bf16_f32 v190, v192, v193\n"
    "v_mfma_f32_16x16x32_bf16 v[16:19], v[148:151], a[176:179], v[16:19]\n"
    "s_nop 0\n"
    "v_mul_f32_e32 v195, v195, v79\n"
    "v_cvt_pk_bf16_f32 v191, v194, v195\n"
    "v_mfma_f32_16x16x32_bf16 v[16:19], v[144:147], a[160:163], v[16:19]\n"
    "s_add_u32 s34, s36, 442368\n"
    "buffer_store_dwordx4 v[188:191], v232, s[28:31], s34 offen offset:0 sc1\n"
    "v_mfma_f32_16x16x32_bf16 v[16:19], v[140:143], a[144:147], v[16:19]\n"
    "s_waitcnt vmcnt(25) lgkmcnt(0)\n"
    "v_exp_f32_e64 v192, -v96\n"
    "v_mfma_f32_16x16x32_bf16 v[16:19], v[152:155], a[192:195], v[16:19]\n"
    "s_barrier\n"
    "v_mfma_f32_16x16x32_bf16 v[16:19], v[156:159], a[208:211], v[16:19]\n"
    "s_add_u32 m0, s52, 118784\n"
    "s_add_u32 s53, s52, 266240\n"
    "ds_read_b128 v[128:131], v233 offset:36864\n"
    "v_mfma_f32_16x16x32_bf16 v[16:19], v[160:163], a[224:227], v[16:19]\n"
    "buffer_load_dwordx4 v232, s[48:51], s53 offen offset:0 sc1 lds\n"
    "v_mfma_f32_16x16x32_bf16 v[20:23], v[160:163], a[228:231], v[20:23]\n"
    "ds_read_b128 v[132:135], v233 offset:38912\n"
    "ds_read_b128 v[136:139], v233 offset:40960\n"
    "v_mfma_f32_16x16x32_bf16 v[20:23], v[156:159], a[212:215], v[20:23]\n"
    "v_exp_f32_e64 v193, -v97\n"
    "v_mfma_f32_16x16x32_bf16 v[20:23], v[152:155], a[196:199], v[20:23]\n"
    "v_mul_f32_e32 v96, v96, v112\n"
    "v_add_f32_e32 v192, 1.0, v192\n"
    "v_mfma_f32_16x16x32_bf16 v[24:27], v[152:155], a[200:203], v[24:27]\n"
    "v_rcp_f32_e32 v192, v192\n"
    "v_mfma_f32_16x16x32_bf16 v[24:27], v[156:159], a[216:219], v[24:27]\n"
    "v_exp_f32_e64 v194, -v98\n"
    "v_mfma_f32_16x16x32_bf16 v[24:27], v[160:163], a[232:235], v[24:27]\n"
    "v_mul_f32_e32 v97, v97, v113\n"
    "v_add_f32_e32 v193, 1.0, v193\n"
    "v_mfma_f32_16x16x32_bf16 v[28:31], v[160:163], a[236:239], v[28:31]\n"
    "v_rcp_f32_e32 v193, v193\n"
    "v_mfma_f32_16x16x32_bf16 v[28:31], v[156:159], a[220:223], v[28:31]\n"
    "v_mul_f32_e32 v192, v192, v96\n"
    "v_mfma_f32_16x16x32_bf16 v[28:31], v[152:155], a[204:207], v[28:31]\n"
    "v_exp_f32_e64 v195, -v99\n"
    "v_mfma_f32_16x16x32_bf16 v[28:31], v[164:167], a[252:255], v[28:31]\n"
    "ds_read_b128 v[140:143], v233 offset:43008\n"
    "ds_read_b128 v[144:147], v233 offset:45056\n"
    "v_mfma_f32_16x16x32_bf16 v[28:31], v[168:171], v[212:215], v[28:31]\n"
    "ds_read_b128 v[148:151], v233 offset:47104\n"
    "v_mul_f32_e32 v98, v98, v114\n"
    "v_mfma_f32_16x16x32_bf16 v[28:31], v[172:175], v[228:231], v[28:31]\n"
    "v_add_f32_e32 v194, 1.0, v194\n"
    "v_mfma_f32_16x16x32_bf16 v[24:27], v[172:175], v[224:227], v[24:27]\n"
    "v_rcp_f32_e32 v194, v194\n"
    "v_mfma_f32_16x16x32_bf16 v[24:27], v[168:171], v[208:211], v[24:27]\n"
    "v_mul_f32_e32 v193, v193, v97\n"
    "v_mfma_f32_16x16x32_bf16 v[24:27], v[164:167], a[248:251], v[24:27]\n"
    "v_exp_f32_e64 v196, -v100\n"
    "v_mfma_f32_16x16x32_bf16 v[20:23], v[164:167], a[244:247], v[20:23]\n"
    "v_mul_f32_e32 v99, v99, v115\n"
    "v_add_f32_e32 v195, 1.0, v195\n"
    "v_mfma_f32_16x16x32_bf16 v[20:23], v[168:171], v[204:207], v[20:23]\n"
    "v_rcp_f32_e32 v195, v195\n"
    "v_mfma_f32_16x16x32_bf16 v[20:23], v[172:175], v[220:223], v[20:23]\n"
    "v_mul_f32_e32 v194, v194, v98\n"
    "v_cvt_pk_bf16_f32 v176, v192, v193\n"
    "v_mfma_f32_16x16x32_bf16 v[16:19], v[172:175], v[216:219], v[16:19]\n"
    "v_exp_f32_e64 v197, -v101\n"
    "v_mfma_f32_16x16x32_bf16 v[16:19], v[168:171], v[200:203], v[16:19]\n"
    "v_mul_f32_e32 v100, v100, v116\n"
    "v_add_f32_e32 v196, 1.0, v196\n"
    "v_mfma_f32_16x16x32_bf16 v[16:19], v[164:167], a[240:243], v[16:19]\n"
    "s_waitcnt lgkmcnt(3)\n"
    "v_rcp_f32_e32 v196, v196\n"
    "v_mfma_f32_16x16x32_bf16 v[32:35], v[128:131], a[0:3], 0\n"
    "s_add_u32 m0, s52, 0\n"
    "ds_read_b128 v[152:155], v233 offset:49152\n"
    "ds_read_b128 v[156:159], v233 offset:51200\n"
    "v_mfma_f32_16x16x32_bf16 v[32:35], v[132:135], a[16:19], v[32:35]\n"
    "s_add_u32 s53, s52, 294912\n"
    "buffer_load_dwordx4 v232, s[48:51], s53 offen offset:0 sc1 lds\n"
    "v_mfma_f32_16x16x32_bf16 v[32:35], v[136:139], a[32:35], v[32:35]\n"
    "ds_read_b128 v[160:163], v233 offset:53248\n"
    "v_mul_f32_e32 v195, v195, v99\n"
    "v_mfma_f32_16x16x32_bf16 v[36:39], v[136:139], a[36:39], 0\n"
    "v_exp_f32_e64 v192, -v102\n"
    "v_mfma_f32_16x16x32_bf16 v[36:39], v[132:135], a[20:23], v[36:39]\n"
    "v_mul_f32_e32 v101, v101, v117\n"
    "v_add_f32_e32 v197, 1.0, v197\n"
    "v_mfma_f32_16x16x32_bf16 v[36:39], v[128:131], a[4:7], v[36:39]\n"
    "v_rcp_f32_e32 v197, v197\n"
    "v_mfma_f32_16x16x32_bf16 v[40:43], v[128:131], a[8:11], 0\n"
    "v_mul_f32_e32 v196, v196, v100\n"
    "v_cvt_pk_bf16_f32 v177, v194, v195\n"
    "v_mfma_f32_16x16x32_bf16 v[40:43], v[132:135], a[24:27], v[40:43]\n"
    "v_exp_f32_e64 v193, -v103\n"
    "v_mfma_f32_16x16x32_bf16 v[40:43], v[136:139], a[40:43], v[40:43]\n"
    "v_mul_f32_e32 v102, v102, v118\n"
    "v_add_f32_e32 v192, 1.0, v192\n"
    "v_mfma_f32_16x16x32_bf16 v[44:47], v[136:139], a[44:47], 0\n"
    "v_rcp_f32_e32 v192, v192\n"
    "v_mfma_f32_16x16x32_bf16 v[44:47], v[132:135], a[28:31], v[44:47]\n"
    "v_mul_f32_e32 v197, v197, v101\n"
    "v_mfma_f32_16x16x32_bf16 v[44:47], v[128:131], a[12:15], v[44:47]\n"
    "s_waitcnt lgkmcnt(3)\n"
    "v_exp_f32_e64 v194, -v104\n"
    "v_mfma_f32_16x16x32_bf16 v[44:47], v[140:143], a[60:63], v[44:47]\n"
    "ds_read_b128 v[164:167], v233 offset:55296\n"
    "ds_read_b128 v[168:171], v233 offset:57344\n"
    "v_mfma_f32_16x16x32_bf16 v[44:47], v[144:147], a[76:79], v[44:47]\n"
    "buffer_load_dwordx4 v232, s[48:51], s53 offen offset:1024 sc1 lds\n"
    "v_mfma_f32_16x16x32_bf16 v[44:47], v[148:151], a[92:95], v[44:47]\n"
    "ds_read_b128 v[172:175], v233 offset:59392\n"
    "v_mul_f32_e32 v103, v103, v119\n"
    "v_mfma_f32_16x16x32_bf16 v[40:43], v[148:151], a[88:91], v[40:43]\n"
    "v_add_f32_e32 v193, 1.0, v193\n"
    "v_mfma_f32_16x16x32_bf16 v[40:43], v[144:147], a[72:75], v[40:43]\n"
    "v_rcp_f32_e32 v193, v193\n"
    "v_mfma_f32_16x16x32_bf16 v[40:43], v[140:143], a[56:59], v[40:43]\n"
    "v_mul_f32_e32 v192, v192, v102\n"
    "v_cvt_pk_bf16_f32 v180, v196, v197\n"
    "v_mfma_f32_16x16x32_bf16 v[36:39], v[140:143], a[52:55], v[36:39]\n"
    "v_exp_f32_e64 v195, -v105\n"
    "v_mfma_f32_16x16x32_bf16 v[36:39], v[144:147], a[68:71], v[36:39]\n"
    "v_mul_f32_e32 v104, v104, v120\n"
    "v_add_f32_e32 v194, 1.0, v194\n"
    "v_mfma_f32_16x16x32_bf16 v[36:39], v[148:151], a[84:87], v[36:39]\n"
    "v_rcp_f32_e32 v194, v194\n"
    "v_mfma_f32_16x16x32_bf16 v[32:35], v[148:151], a[80:83], v[32:35]\n"
    "v_mul_f32_e32 v193, v193, v103\n"
    "v_mfma_f32_16x16x32_bf16 v[32:35], v[144:147], a[64:67], v[32:35]\n"
    "v_exp_f32_e64 v196, -v106\n"
    "v_mfma_f32_16x16x32_bf16 v[32:35], v[140:143], a[48:51], v[32:35]\n"
    "s_waitcnt lgkmcnt(3)\n"
    "v_mul_f32_e32 v105, v105, v121\n"
    "v_add_f32_e32 v195, 1.0, v195\n"
    "v_mfma_f32_16x16x32_bf16 v[32:35], v[152:155], a[96:99], v[32:35]\n"
    "ds_read_b128 v[128:131], v233 offset:61440\n"
    "ds_read_b128 v[132:135], v233 offset:63488\n"
    "v_mfma_f32_16x16x32_bf16 v[32:35], v[156:159], a[112:115], v[32:35]\n"
    "buffer_load_dwordx4 v232, s[48:51], s53 offen offset:2048 sc1 lds\n"
    "v_mfma_f32_16x16x32_bf16 v[32:35], v[160:163], a[128:131], v[32:35]\n"
    "ds_read_b128 v[136:139], v234 offset:0\n"
    "v_mfma_f32_16x16x32_bf16 v[36:39], v[160:163], a[132:135], v[36:39]\n"
    "v_rcp_f32_e32 v195, v195\n"
    "v_mfma_f32_16x16x32_bf16 v[36:39], v[156:159], a[116:119], v[36:39]\n"
    "v_mul_f32_e32 v194, v194, v104\n"
    "v_cvt_pk_bf16_f32 v181, v192, v193\n"
    "v_mfma_f32_16x16x32_bf16 v[36:39], v[152:155], a[100:103], v[36:39]\n"
    "v_exp_f32_e64 v197, -v107\n"
    "v_mfma_f32_16x16x32_bf16 v[40:43], v[152:155], a[104:107], v[40:43]\n"
    "v_mul_f32_e32 v106, v106, v122\n"
    "v_add_f32_e32 v196, 1.0, v196\n"
    "v_mfma_f32_16x16x32_bf16 v[40:43], v[156:159], a[120:123], v[40:43]\n"
    "v_rcp_f32_e32 v196, v196\n"
    "v_mfma_f32_16x16x32_bf16 v[40:43], v[160:163], a[136:139], v[40:43]\n"
    "v_mul_f32_e32 v195, v195, v105\n"
    "v_mfma_f32_16x16x32_bf16 v[44:47], v[160:163], a[140:143], v[44:47]\n"
    "v_exp_f32_e64 v192, -v108\n"
    "v_mfma_f32_16x16x32_bf16 v[44:47], v[156:159], a[124:127], v[44:47]\n"
    "v_mul_f32_e32 v107, v107, v123\n"
    "v_add_f32_e32 v197, 1.0, v197\n"
    "v_mfma_f32_16x16x32_bf16 v[44:47], v[152:155], a[108:111], v[44:47]\n"
    "s_waitcnt lgkmcnt(3)\n"
    "v_rcp_f32_e32 v197, v197\n"
    "v_mfma_f32_16x16x32_bf16 v[44:47], v[164:167], a[156:159], v[44:47]\n"
    "ds_read_b128 v[140:143], v234 offset:2048\n"
    "ds_read_b128 v[144:147], v234 offset:4096\n"
    "v_mfma_f32_16x16x32_bf16 v[44:47], v[168:171], a[172:175], v[44:47]\n"
    "ds_read_b128 v[148:151], v234 offset:6144\n"
    "v_mul_f32_e32 v196, v196, v106\n"
    "v_mfma_f32_16x16x32_bf16 v[44:47], v[172:175], a[188:191], v[44:47]\n"
    "v_cvt_pk_bf16_f32 v184, v194, v195\n"
    "v_mfma_f32_16x16x32_bf16 v[40:43], v[172:175], a[184:187], v[40:43]\n"
    "v_exp_f32_e64 v193, -v109\n"
    "v_mfma_f32_16x16x32_bf16 v[40:43], v[168:171], a[168:171], v[40:43]\n"
    "v_mul_f32_e32 v108, v108, v124\n"
    "v_add_f32_e32 v192, 1.0, v192\n"
    "v_mfma_f32_16x16x32_bf16 v[40:43], v[164:167], a[152:155], v[40:43]\n"
    "v_rcp_f32_e32 v192, v192\n"
    "v_mfma_f32_16x16x32_bf16 v[36:39], v[164:167], a[148:151], v[36:39]\n"
    "v_mul_f32_e32 v197, v197, v107\n"
    "v_mfma_f32_16x16x32_bf16 v[36:39], v[168:171], a[164:167], v[36:39]\n"
    "v_exp_f32_e64 v194, -v110\n"
    "v_mfma_f32_16x16x32_bf16 v[36:39], v[172:175], a[180:183], v[36:39]\n"
    "v_mul_f32_e32 v109, v109, v125\n"
    "v_add_f32_e32 v193, 1.0, v193\n"
    "v_mfma_f32_16x16x32_bf16 v[32:35], v[172:175], a[176:179], v[32:35]\n"
    "v_rcp_f32_e32 v193, v193\n"
    "v_mfma_f32_16x16x32_bf16 v[32:35], v[168:171], a[160:163], v[32:35]\n"
    "v_mul_f32_e32 v192, v192, v108\n"
    "v_cvt_pk_bf16_f32 v185, v196, v197\n"
    "v_mfma_f32_16x16x32_bf16 v[32:35], v[164:167], a[144:147], v[32:35]\n"
    "s_waitcnt lgkmcnt(3)\n"
    "v_exp_f32_e64 v195, -v111\n"
    "v_mfma_f32_16x16x32_bf16 v[32:35], v[128:131], a[192:195], v[32:35]\n"
    "ds_read_b128 v[152:155], v233 offset:37888\n"
    "ds_read_b128 v[156:159], v233 offset:39936\n"
    "v_mfma_f32_16x16x32_bf16 v[32:35], v[132:135], a[208:211], v[32:35]\n"
    "buffer_load_dwordx4 v232, s[48:51], s53 offen offset:3072 sc1 lds\n"
    "v_mfma_f32_16x16x32_bf16 v[32:35], v[136:139], a[224:227], v[32:35]\n"
    "ds_read_b128 v[160:163], v233 offset:41984\n"
    "v_mul_f32_e32 v110, v110, v126\n"
    "v_mfma_f32_16x16x32_bf16 v[36:39], v[136:139], a[228:231], v[36:39]\n"
    "v_add_f32_e32 v194, 1.0, v194\n"
    "v_mfma_f32_16x16x32_bf16 v[36:39], v[132:135], a[212:215], v[36:39]\n"
    "v_rcp_f32_e32 v194, v194\n"
    "v_mfma_f32_16x16x32_bf16 v[36:39], v[128:131], a[196:199], v[36:39]\n"
    "v_mul_f32_e32 v193, v193, v109\n"
    "v_mul_f32_e32 v111, v111, v127\n"
    "v_mfma_f32_16x16x32_bf16 v[40:43], v[128:131], a[200:203], v[40:43]\n"
    "v_add_f32_e32 v195, 1.0, v195\n"
    "v_mfma_f32_16x16x32_bf16 v[40:43], v[132:135], a[216:219], v[40:43]\n"
    "v_rcp_f32_e32 v195, v195\n"
    "v_mfma_f32_16x16x32_bf16 v[40:43], v[136:139], a[232:235], v[40:43]\n"
    "v_mul_f32_e32 v194, v194, v110\n"
    "v_cvt_pk_bf16_f32 v188, v192, v193\n"
    "v_mfma_f32_16x16x32_bf16 v[44:47], v[136:139], a[236:239], v[44:47]\n"
    "s_nop 0\n"
    "v_mul_f32_e32 v195, v195, v111\n"
    "v_cvt_pk_bf16_f32 v189, v194, v195\n"
    "v_mfma_f32_16x16x32_bf16 v[44:47], v[132:135], a[220:223], v[44:47]\n"
    "v_exp_f32_e64 v192, -v0\n"
    "v_mfma_f32_16x16x32_bf16 v[44:47], v[128:131], a[204:207], v[44:47]\n"
    "s_waitcnt lgkmcnt(3)\n"
    "v_exp_f32_e64 v193, -v1\n"
    "v_mfma_f32_16x16x32_bf16 v[44:47], v[140:143], a[252:255], v[44:47]\n"
    "s_add_u32 m0, s52, 4096\n"
    "ds_read_b128 v[164:167], v233 offset:44032\n"
    "ds_read_b128 v[168:171], v233 offset:46080\n"
    "v_mfma_f32_16x16x32_bf16 v[44:47], v[144:147], v[212:215], v[44:47]\n"
    "s_add_u32 s53, s52, 299008\n"
    "buffer_load_dwordx4 v232, s[48:51], s53 offen offset:0 sc1 lds\n"
    "v_mfma_f32_16x16x32_bf16 v[44:47], v[148:151], v[228:231], v[44:47]\n"
    "ds_read_b128 v[172:175], v233 offset:48128\n"
    "v_mul_f32_e32 v0, v0, v16\n"
    "v_mfma_f32_16x16x32_bf16 v[40:43], v[148:151], v[224:227], v[40:43]\n"
    "v_add_f32_e32 v192, 1.0, v192\n"
    "v_mfma_f32_16x16x32_bf16 v[40:43], v[144:147], v[208:211], v[40:43]\n"
    "v_rcp_f32_e32 v192, v192\n"
    "v_mfma_f32_16x16x32_bf16 v[40:43], v[140:143], a[248:251], v[40:43]\n"
    "v_exp_f32_e64 v194, -v2\n"
    "v_mfma_f32_16x16x32_bf16 v[36:39], v[140:143], a[244:247], v[36:39]\n"
    "v_mul_f32_e32 v1, v1, v17\n"
    "v_add_f32_e32 v193, 1.0, v193\n"
    "v_mfma_f32_16x16x32_bf16 v[36:39], v[144:147], v[204:207], v[36:39]\n"
    "v_rcp_f32_e32 v193, v193\n"
    "v_mfma_f32_16x16x32_bf16 v[36:39], v[148:151], v[220:223], v[36:39]\n"
    "v_mul_f32_e32 v192, v192, v0\n"
    "v_mfma_f32_16x16x32_bf16 v[32:35], v[148:151], v[216:219], v[32:35]\n"
    "v_exp_f32_e64 v195, -v3\n"
    "v_mfma_f32_16x16x32_bf16 v[32:35], v[144:147], v[200:203], v[32:35]\n"
    "v_mul_f32_e32 v2, v2, v18\n"
    "v_add_f32_e32 v194, 1.0, v194\n"
    "v_mfma_f32_16x16x32_bf16 v[32:35], v[140:143], a[240:243], v[32:35]\n"
    "s_waitcnt lgkmcnt(3)\n"
    "v_rcp_f32_e32 v194, v194\n"
    "v_mfma_f32_16x16x32_bf16 v[48:51], v[152:155], a[0:3], 0\n"
    "ds_read_b128 v[128:131], v233 offset:50176\n"
    "ds_read_b128 v[132:135], v233 offset:52224\n"
    "v_mfma_f32_16x16x32_bf16 v[48:51], v[156:159], a[16:19], v[48:51]\n"
    "buffer_load_dwordx4 v232, s[48:51], s53 offen offset:1024 sc1 lds\n"
    "v_mfma_f32_16x16x32_bf16 v[48:51], v[160:163], a[32:35], v[48:51]\n"
    "ds_read_b128 v[136:139], v233 offset:54272\n"
    "v_mul_f32_e32 v193, v193, v1\n"
    "v_mfma_f32_16x16x32_bf16 v[52:55], v[160:163], a[36:39], 0\n"
    "v_exp_f32_e64 v196, -v4\n"
    "v_mfma_f32_16x16x32_bf16 v[52:55], v[156:159], a[20:23], v[52:55]\n"
    "v_mul_f32_e32 v3, v3, v19\n"
    "v_add_f32_e32 v195, 1.0, v195\n"
    "v_mfma_f32_16x16x32_bf16 v[52:55], v[152:155], a[4:7], v[52:55]\n"
    "v_rcp_f32_e32 v195, v195\n"
    "v_mfma_f32_16x16x32_bf16 v[56:59], v[152:155], a[8:11], 0\n"
    "v_mul_f32_e32 v194, v194, v2\n"
    "v_cvt_pk_bf16_f32 v178, v192, v193\n"
    "v_mfma_f32_16x16x32_bf16 v[56:59], v[156:159], a[24:27], v[56:59]\n"
    "v_exp_f32_e64 v197, -v5\n"
    "v_mfma_f32_16x16x32_bf16 v[56:59], v[160:163], a[40:43], v[56:59]\n"
    "v_mul_f32_e32 v4, v4, v20\n"
    "v_add_f32_e32 v196, 1.0, v196\n"
    "v_mfma_f32_16x16x32_bf16 v[60:63], v[160:163], a[44:47], 0\n"
    "v_rcp_f32_e32 v196, v196\n"
    "v_mfma_f32_16x16x32_bf16 v[60:63], v[156:159], a[28:31], v[60:63]\n"
    "v_mul_f32_e32 v195, v195, v3\n"
    "v_mfma_f32_16x16x32_bf16 v[60:63], v[152:155], a[12:15], v[60:63]\n"
    "s_waitcnt lgkmcnt(3)\n"
    "v_exp_f32_e64 v192, -v6\n"
    "v_mfma_f32_16x16x32_bf16 v[60:63], v[164:167], a[60:63], v[60:63]\n"
    "ds_read_b128 v[140:143], v233 offset:56320\n"
    "ds_read_b128 v[144:147], v233 offset:58368\n"
    "v_mfma_f32_16x16x32_bf16 v[60:63], v[168:171], a[76:79], v[60:63]\n"
    "ds_read_b128 v[148:151], v233 offset:60416\n"
    "v_mul_f32_e32 v5, v5, v21\n"
    "v_mfma_f32_16x16x32_bf16 v[60:63], v[172:175], a[92:95], v[60:63]\n"
    "v_add_f32_e32 v197, 1.0, v197\n"
    "v_mfma_f32_16x16x32_bf16 v[56:59], v[172:175], a[88:91], v[56:59]\n"
    "v_rcp_f32_e32 v197, v197\n"
    "v_mfma_f32_16x16x32_bf16 v[56:59], v[168:171], a[72:75], v[56:59]\n"
    "v_mul_f32_e32 v196, v196, v4\n"
    "v_cvt_pk_bf16_f32 v179, v194, v195\n"
    "v_mfma_f32_16x16x32_bf16 v[56:59], v[164:167], a[56:59], v[56:59]\n"
    "s_add_u32 s34, s36, 0\n"
    "buffer_store_dwordx4 v[176:179], v232, s[28:31], s34 offen offset:1024 sc1\n"
    "v_mfma_f32_16x16x32_bf16 v[52:55], v[164:167], a[52:55], v[52:55]\n"
    "v_exp_f32_e64 v193, -v7\n"
    "v_mfma_f32_16x16x32_bf16 v[52:55], v[168:171], a[68:71], v[52:55]\n"
    "v_mul_f32_e32 v6, v6, v22\n"
    "v_add_f32_e32 v192, 1.0, v192\n"
    "v_mfma_f32_16x16x32_bf16 v[52:55], v[172:175], a[84:87], v[52:55]\n"
    "v_rcp_f32_e32 v192, v192\n"
    "v_mfma_f32_16x16x32_bf16 v[48:51], v[172:175], a[80:83], v[48:51]\n"
    "v_mul_f32_e32 v197, v197, v5\n"
    "v_mfma_f32_16x16x32_bf16 v[48:51], v[168:171], a[64:67], v[48:51]\n"
    "v_exp_f32_e64 v194, -v8\n"
    "v_mfma_f32_16x16x32_bf16 v[48:51], v[164:167], a[48:51], v[48:51]\n"
    "s_waitcnt lgkmcnt(3)\n"
    "v_mul_f32_e32 v7, v7, v23\n"
    "v_add_f32_e32 v193, 1.0, v193\n"
    "v_mfma_f32_16x16x32_bf16 v[48:51], v[128:131], a[96:99], v[48:51]\n"
    "ds_read_b128 v[152:155], v233 offset:62464\n"
    "ds_read_b128 v[156:159], v233 offset:64512\n"
    "v_mfma_f32_16x16x32_bf16 v[48:51], v[132:135], a[112:115], v[48:51]\n"
    "buffer_load_dwordx4 v232, s[48:51], s53 offen offset:2048 sc1 lds\n"
    "v_mfma_f32_16x16x32_bf16 v[48:51], v[136:139], a[128:131], v[48:51]\n"
    "ds_read_b128 v[160:163], v234 offset:1024\n"
    "v_mfma_f32_16x16x32_bf16 v[52:55], v[136:139], a[132:135], v[52:55]\n"
    "v_rcp_f32_e32 v193, v193\n"
    "v_mfma_f32_16x16x32_bf16 v[52:55], v[132:135], a[116:119], v[52:55]\n"
    "v_mul_f32_e32 v192, v192, v6\n"
    "v_cvt_pk_bf16_f32 v182, v196, v197\n"
    "v_mfma_f32_16x16x32_bf16 v[52:55], v[128:131], a[100:103], v[52:55]\n"
    "v_exp_f32_e64 v195, -v9\n"
    "v_mfma_f32_16x16x32_bf16 v[56:59], v[128:131], a[104:107], v[56:59]\n"
    "v_mul_f32_e32 v8, v8, v24\n"
    "v_add_f32_e32 v194, 1.0, v194\n"
    "v_mfma_f32_16x16x32_bf16 v[56:59], v[132:135], a[120:123], v[56:59]\n"
    "v_rcp_f32_e32 v194, v194\n"
    "v_mfma_f32_16x16x32_bf16 v[56:59], v[136:139], a[136:139], v[56:59]\n"
    "v_mul_f32_e32 v193, v193, v7\n"
    "v_mfma_f32_16x16x32_bf16 v[60:63], v[136:139], a[140:143], v[60:63]\n"
    "v_exp_f32_e64 v196, -v10\n"
    "v_mfma_f32_16x16x32_bf16 v[60:63], v[132:135], a[124:127], v[60:63]\n"
    "v_mul_f32_e32 v9, v9, v25\n"
    "v_add_f32_e32 v195, 1.0, v195\n"
    "v_mfma_f32_16x16x32_bf16 v[60:63], v[128:131], a[108:111], v[60:63]\n"
    "s_waitcnt lgkmcnt(3)\n"
    "v_rcp_f32_e32 v195, v195\n"
    "v_mfma_f32_16x16x32_bf16 v[60:63], v[140:143], a[156:159], v[60:63]\n"
    "ds_read_b128 v[164:167], v234 offset:3072\n"
    "ds_read_b128 v[168:171], v234 offset:5120\n"
    "v_mfma_f32_16x16x32_bf16 v[60:63], v[144:147], a[172:175], v[60:63]\n"
    "buffer_load_dwordx4 v232, s[48:51], s53 offen offset:3072 sc1 lds\n"
    "v_mfma_f32_16x16x32_bf16 v[60:63], v[148:151], a[188:191], v[60:63]\n"
    "ds_read_b128 v[172:175], v234 offset:7168\n"
    "v_mul_f32_e32 v194, v194, v8\n"
    "v_mfma_f32_16x16x32_bf16 v[56:59], v[148:151], a[184:187], v[56:59]\n"
    "v_cvt_pk_bf16_f32 v183, v192, v193\n"
    "s_add_u32 s34, s36, 147456\n"
    "v_mfma_f32_16x16x32_bf16 v[56:59], v[144:147], a[168:171], v[56:59]\n"
    "buffer_store_dwordx4 v[180:183], v232, s[28:31], s34 offen offset:1024 sc1\n"
    "v_mfma_f32_16x16x32_bf16 v[56:59], v[140:143], a[152:155], v[56:59]\n"
    "v_exp_f32_e64 v197, -v11\n"
    "v_mfma_f32_16x16x32_bf16 v[52:55], v[140:143], a[148:151], v[52:55]\n"
    "v_mul_f32_e32 v10, v10, v26\n"
    "v_add_f32_e32 v196, 1.0, v196\n"
    "v_mfma_f32_16x16x32_bf16 v[52:55], v[144:147], a[164:167], v[52:55]\n"
    "v_rcp_f32_e32 v196, v196\n"
    "v_mfma_f32_16x16x32_bf16 v[52:55], v[148:151], a[180:183], v[52:55]\n"
    "v_mul_f32_e32 v195, v195, v9\n"
    "v_mfma_f32_16x16x32_bf16 v[48:51], v[148:151], a[176:179], v[48:51]\n"
    "v_exp_f32_e64 v192, -v12\n"
    "v_mfma_f32_16x16x32_bf16 v[48:51], v[144:147], a[160:163], v[48:51]\n"
    "v_mul_f32_e32 v11, v11, v27\n"
    "v_add_f32_e32 v197, 1.0, v197\n"
    "v_mfma_f32_16x16x32_bf16 v[48:51], v[140:143], a[144:147], v[48:51]\n"
    "s_waitcnt vmcnt(11) lgkmcnt(0)\n"
    "v_rcp_f32_e32 v197, v197\n"
    "v_mfma_f32_16x16x32_bf16 v[48:51], v[152:155], a[192:195], v[48:51]\n"
    "s_barrier\n"
    "v_mfma_f32_16x16x32_bf16 v[48:51], v[156:159], a[208:211], v[48:51]\n"
    "s_add_u32 m0, s52, 8192\n"
    "s_add_u32 s53, s52, 303104\n"
    "v_mov_b32_e32 v253, 1\n"
    "v_mfma_f32_16x16x32_bf16 v[48:51], v[160:163], a[224:227], v[48:51]\n"
    "buffer_load_dwordx4 v232, s[48:51], s53 offen offset:0 sc1 lds\n"
    "v_mfma_f32_16x16x32_bf16 v[52:55], v[160:163], a[228:231], v[52:55]\n"
    "buffer_store_dword v253, v252, s[96:99], s39 offen sc1\n"
    "v_mfma_f32_16x16x32_bf16 v[52:55], v[156:159], a[212:215], v[52:55]\n"
    "ds_read_b128 v[128:131], v234 offset:8192\n"
    "ds_read_b128 v[132:135], v234 offset:10240\n"
    "v_mfma_f32_16x16x32_bf16 v[52:55], v[152:155], a[196:199], v[52:55]\n"
    "ds_read_b128 v[136:139], v234 offset:12288\n"
    "v_mul_f32_e32 v196, v196, v10\n"
    "v_mfma_f32_16x16x32_bf16 v[56:59], v[152:155], a[200:203], v[56:59]\n"
    "v_cvt_pk_bf16_f32 v186, v194, v195\n"
    "v_mfma_f32_16x16x32_bf16 v[56:59], v[156:159], a[216:219], v[56:59]\n"
    "v_exp_f32_e64 v193, -v13\n"
    "v_mfma_f32_16x16x32_bf16 v[56:59], v[160:163], a[232:235], v[56:59]\n"
    "v_mul_f32_e32 v12, v12, v28\n"
    "v_add_f32_e32 v192, 1.0, v192\n"
    "v_mfma_f32_16x16x32_bf16 v[60:63], v[160:163], a[236:239], v[60:63]\n"
    "v_rcp_f32_e32 v192, v192\n"
    "v_mfma_f32_16x16x32_bf16 v[60:63], v[156:159], a[220:223], v[60:63]\n"
    "v_mul_f32_e32 v197, v197, v11\n"
    "v_mfma_f32_16x16x32_bf16 v[60:63], v[152:155], a[204:207], v[60:63]\n"
    "v_exp_f32_e64 v194, -v14\n"
    "v_mfma_f32_16x16x32_bf16 v[60:63], v[164:167], a[252:255], v[60:63]\n"
    "ds_read_b128 v[140:143], v234 offset:14336\n"
    "ds_read_b128 v[144:147], v234 offset:16384\n"
    "v_mfma_f32_16x16x32_bf16 v[60:63], v[168:171], v[212:215], v[60:63]\n"
    "ds_read_b128 v[148:151], v234 offset:18432\n"
    "v_mul_f32_e32 v13, v13, v29\n"
    "v_mfma_f32_16x16x32_bf16 v[60:63], v[172:175], v[228:231], v[60:63]\n"
    "v_add_f32_e32 v193, 1.0, v193\n"
    "v_mfma_f32_16x16x32_bf16 v[56:59], v[172:175], v[224:227], v[56:59]\n"
    "v_rcp_f32_e32 v193, v193\n"
    "v_mfma_f32_16x16x32_bf16 v[56:59], v[168:171], v[208:211], v[56:59]\n"
    "v_mul_f32_e32 v192, v192, v12\n"
    "v_cvt_pk_bf16_f32 v187, v196, v197\n"
    "v_mfma_f32_16x16x32_bf16 v[56:59], v[164:167], a[248:251], v[56:59]\n"
    "s_add_u32 s34, s36, 294912\n"
    "buffer_store_dwordx4 v[184:187], v232, s[28:31], s34 offen offset:1024 sc1\n"
    "v_mfma_f32_16x16x32_bf16 v[52:55], v[164:167], a[244:247], v[52:55]\n"
    "v_exp_f32_e64 v195, -v15\n"
    "v_mfma_f32_16x16x32_bf16 v[52:55], v[168:171], v[204:207], v[52:55]\n"
    "v_mul_f32_e32 v14, v14, v30\n"
    "v_add_f32_e32 v194, 1.0, v194\n"
    "v_mfma_f32_16x16x32_bf16 v[52:55], v[172:175], v[220:223], v[52:55]\n"
    "v_rcp_f32_e32 v194, v194\n"
    "v_mfma_f32_16x16x32_bf16 v[48:51], v[172:175], v[216:219], v[48:51]\n"
    "v_mul_f32_e32 v193, v193, v13\n"
    "v_mul_f32_e32 v15, v15, v31\n"
    "v_mfma_f32_16x16x32_bf16 v[48:51], v[168:171], v[200:203], v[48:51]\n"
    "v_add_f32_e32 v195, 1.0, v195\n"
    "v_mfma_f32_16x16x32_bf16 v[48:51], v[164:167], a[240:243], v[48:51]\n"
    "s_waitcnt lgkmcnt(3)\n"
    "v_rcp_f32_e32 v195, v195\n"
    "v_mfma_f32_16x16x32_bf16 v[64:67], v[128:131], a[0:3], 0\n"
    "s_mov_b64 s[48:49], s[20:21]\n"
    "s_mov_b64 s[50:51], s[22:23]\n"
    "s_add_u32 m0, s52, 36864\n"
    "v_mfma_f32_16x16x32_bf16 v[64:67], v[132:135], a[16:19], v[64:67]\n"
    "s_add_u32 s53, s52, 0\n"
    "buffer_load_dwordx4 v232, s[48:51], s53 offen offset:0 sc1 lds\n"
    "v_mfma_f32_16x16x32_bf16 v[64:67], v[136:139], a[32:35], v[64:67]\n"
    "ds_read_b128 v[152:155], v234 offset:20480\n"
    "ds_read_b128 v[156:159], v234 offset:22528\n"
    "v_mfma_f32_16x16x32_bf16 v[68:71], v[136:139], a[36:39], 0\n"
    "ds_read_b128 v[160:163], v234 offset:24576\n"
    "v_mul_f32_e32 v194, v194, v14\n"
    "v_mfma_f32_16x16x32_bf16 v[68:71], v[132:135], a[20:23], v[68:71]\n"
    "v_cvt_pk_bf16_f32 v190, v192, v193\n"
    "s_nop 0\n"
    "v_mfma_f32_16x16x32_bf16 v[68:71], v[128:131], a[4:7], v[68:71]\n"
    "v_mul_f32_e32 v195, v195, v15\n"
    "v_cvt_pk_bf16_f32 v191, v194, v195\n"
    "v_mfma_f32_16x16x32_bf16 v[72:75], v[128:131], a[8:11], 0\n"
    "s_add_u32 s34, s36, 442368\n"
    "buffer_store_dwordx4 v[188:191], v232, s[28:31], s34 offen offset:1024 sc1\n"
    "v_mfma_f32_16x16x32_bf16 v[72:75], v[132:135], a[24:27], v[72:75]\n"
    "v_exp_f32_e64 v192, -v32\n"
    "v_mfma_f32_16x16x32_bf16 v[72:75], v[136:139], a[40:43], v[72:75]\n"
    "v_exp_f32_e64 v193, -v33\n"
    "v_mfma_f32_16x16x32_bf16 v[76:79], v[136:139], a[44:47], 0\n"
    "v_mul_f32_e32 v32, v32, v48\n"
    "v_add_f32_e32 v192, 1.0, v192\n"
    "v_mfma_f32_16x16x32_bf16 v[76:79], v[132:135], a[28:31], v[76:79]\n"
    "v_rcp_f32_e32 v192, v192\n"
    "v_mfma_f32_16x16x32_bf16 v[76:79], v[128:131], a[12:15], v[76:79]\n"
    "s_waitcnt lgkmcnt(3)\n"
    "v_exp_f32_e64 v194, -v34\n"
    "v_mfma_f32_16x16x32_bf16 v[76:79], v[140:143], a[60:63], v[76:79]\n"
    "ds_read_b128 v[164:167], v234 offset:26624\n"
    "ds_read_b128 v[168:171], v234 offset:28672\n"
    "v_mfma_f32_16x16x32_bf16 v[76:79], v[144:147], a[76:79], v[76:79]\n"
    "buffer_load_dwordx4 v232, s[48:51], s53 offen offset:1024 sc1 lds\n"
    "v_mfma_f32_16x16x32_bf16 v[76:79], v[148:151], a[92:95], v[76:79]\n"
    "ds_read_b128 v[172:175], v234 offset:30720\n"
    "v_mul_f32_e32 v33, v33, v49\n"
    "v_mfma_f32_16x16x32_bf16 v[72:75], v[148:151], a[88:91], v[72:75]\n"
    "v_add_f32_e32 v193, 1.0, v193\n"
    "v_mfma_f32_16x16x32_bf16 v[72:75], v[144:147], a[72:75], v[72:75]\n"
    "v_rcp_f32_e32 v193, v193\n"
    "v_mfma_f32_16x16x32_bf16 v[72:75], v[140:143], a[56:59], v[72:75]\n"
    "v_mul_f32_e32 v192, v192, v32\n"
    "v_mfma_f32_16x16x32_bf16 v[68:71], v[140:143], a[52:55], v[68:71]\n"
    "v_exp_f32_e64 v195, -v35\n"
    "v_mfma_f32_16x16x32_bf16 v[68:71], v[144:147], a[68:71], v[68:71]\n"
    "v_mul_f32_e32 v34, v34, v50\n"
    "v_add_f32_e32 v194, 1.0, v194\n"
    "v_mfma_f32_16x16x32_bf16 v[68:71], v[148:151], a[84:87], v[68:71]\n"
    "v_rcp_f32_e32 v194, v194\n"
    "v_mfma_f32_16x16x32_bf16 v[64:67], v[148:151], a[80:83], v[64:67]\n"
    "v_mul_f32_e32 v193, v193, v33\n"
    "v_mfma_f32_16x16x32_bf16 v[64:67], v[144:147], a[64:67], v[64:67]\n"
    "v_exp_f32_e64 v196, -v36\n"
    "v_mfma_f32_16x16x32_bf16 v[64:67], v[140:143], a[48:51], v[64:67]\n"
    "s_waitcnt lgkmcnt(3)\n"
    "v_mul_f32_e32 v35, v35, v51\n"
    "v_add_f32_e32 v195, 1.0, v195\n"
    "v_mfma_f32_16x16x32_bf16 v[64:67], v[152:155], a[96:99], v[64:67]\n"
    "ds_read_b128 v[128:131], v234 offset:32768\n"
    "ds_read_b128 v[132:135], v234 offset:34816\n"
    "v_mfma_f32_16x16x32_bf16 v[64:67], v[156:159], a[112:115], v[64:67]\n"
    "buffer_load_dwordx4 v232, s[48:51], s53 offen offset:2048 sc1 lds\n"
    "v_mfma_f32_16x16x32_bf16 v[64:67], v[160:163], a[128:131], v[64:67]\n"
    "ds_read_b128 v[136:139], v234 offset:36864\n"
    "v_mfma_f32_16x16x32_bf16 v[68:71], v[160:163], a[132:135], v[68:71]\n"
    "v_rcp_f32_e32 v195, v195\n"
    "v_mfma_f32_16x16x32_bf16 v[68:71], v[156:159], a[116:119], v[68:71]\n"
    "v_mul_f32_e32 v194, v194, v34\n"
    "v_cvt_pk_bf16_f32 v176, v192, v193\n"
    "v_mfma_f32_16x16x32_bf16 v[68:71], v[152:155], a[100:103], v[68:71]\n"
    "v_exp_f32_e64 v197, -v37\n"
    "v_mfma_f32_16x16x32_bf16 v[72:75], v[152:155], a[104:107], v[72:75]\n"
    "v_mul_f32_e32 v36, v36, v52\n"
    "v_add_f32_e32 v196, 1.0, v196\n"
    "v_mfma_f32_16x16x32_bf16 v[72:75], v[156:159], a[120:123], v[72:75]\n"
    "v_rcp_f32_e32 v196, v196\n"
    "v_mfma_f32_16x16x32_bf16 v[72:75], v[160:163], a[136:139], v[72:75]\n"
    "v_mul_f32_e32 v195, v195, v35\n"
    "v_mfma_f32_16x16x32_bf16 v[76:79], v[160:163], a[140:143], v[76:79]\n"
    "v_exp_f32_e64 v192, -v38\n"
    "v_mfma_f32_16x16x32_bf16 v[76:79], v[156:159], a[124:127], v[76:79]\n"
    "v_mul_f32_e32 v37, v37, v53\n"
    "v_add_f32_e32 v197, 1.0, v197\n"
    "v_mfma_f32_16x16x32_bf16 v[76:79], v[152:155], a[108:111], v[76:79]\n"
    "s_waitcnt lgkmcnt(3)\n"
    "v_rcp_f32_e32 v197, v197\n"
    "v_mfma_f32_16x16x32_bf16 v[76:79], v[164:167], a[156:159], v[76:79]\n"
    "ds_read_b128 v[140:143], v234 offset:38912\n"
    "ds_read_b128 v[144:147], v234 offset:40960\n"
    "v_mfma_f32_16x16x32_bf16 v[76:79], v[168:171], a[172:175], v[76:79]\n"
    "ds_read_b128 v[148:151], v234 offset:43008\n"
    "v_mul_f32_e32 v196, v196, v36\n"
    "v_mfma_f32_16x16x32_bf16 v[76:79], v[172:175], a[188:191], v[76:79]\n"
    "v_cvt_pk_bf16_f32 v177, v194, v195\n"
    "v_mfma_f32_16x16x32_bf16 v[72:75], v[172:175], a[184:187], v[72:75]\n"
    "v_exp_f32_e64 v193, -v39\n"
    "v_mfma_f32_16x16x32_bf16 v[72:75], v[168:171], a[168:171], v[72:75]\n"
    "v_mul_f32_e32 v38, v38, v54\n"
    "v_add_f32_e32 v192, 1.0, v192\n"
    "v_mfma_f32_16x16x32_bf16 v[72:75], v[164:167], a[152:155], v[72:75]\n"
    "v_rcp_f32_e32 v192, v192\n"
    "v_mfma_f32_16x16x32_bf16 v[68:71], v[164:167], a[148:151], v[68:71]\n"
    "v_mul_f32_e32 v197, v197, v37\n"
    "v_mfma_f32_16x16x32_bf16 v[68:71], v[168:171], a[164:167], v[68:71]\n"
    "v_exp_f32_e64 v194, -v40\n"
    "v_mfma_f32_16x16x32_bf16 v[68:71], v[172:175], a[180:183], v[68:71]\n"
    "v_mul_f32_e32 v39, v39, v55\n"
    "v_add_f32_e32 v193, 1.0, v193\n"
    "v_mfma_f32_16x16x32_bf16 v[64:67], v[172:175], a[176:179], v[64:67]\n"
    "v_rcp_f32_e32 v193, v193\n"
    "v_mfma_f32_16x16x32_bf16 v[64:67], v[168:171], a[160:163], v[64:67]\n"
    "v_mul_f32_e32 v192, v192, v38\n"
    "v_cvt_pk_bf16_f32 v180, v196, v197\n"
    "L_pad3_3_mk:\n"
    "s_cbranch_execz L_ret3_mk\n"
    "v_mfma_f32_16x16x32_bf16 v[64:67], v[164:167], a[144:147], v[64:67]\n"
    "s_waitcnt lgkmcnt(3)\n"
    "v_exp_f32_e64 v195, -v41\n"
    "v_mfma_f32_16x16x32_bf16 v[64:67], v[128:131], a[192:195], v[64:67]\n"
    "ds_read_b128 v[152:155], v234 offset:9216\n"
    "ds_read_b128 v[156:159], v234 offset:11264\n"
    "v_mfma_f32_16x16x32_bf16 v[64:67], v[132:135], a[208:211], v[64:67]\n"
    "buffer_load_dwordx4 v232, s[48:51], s53 offen offset:3072 sc1 lds\n"
    "v_mfma_f32_16x16x32_bf16 v[64:67], v[136:139], a[224:227], v[64:67]\n"
    "ds_read_b128 v[160:163], v234 offset:13312\n"
    "v_mul_f32_e32 v40, v40, v56\n"
    "v_mfma_f32_16x16x32_bf16 v[68:71], v[136:139], a[228:231], v[68:71]\n"
    "v_add_f32_e32 v194, 1.0, v194\n"
    "v_mfma_f32_16x16x32_bf16 v[68:71], v[132:135], a[212:215], v[68:71]\n"
    "v_rcp_f32_e32 v194, v194\n"
    "v_mfma_f32_16x16x32_bf16 v[68:71], v[128:131], a[196:199], v[68:71]\n"
    "v_mul_f32_e32 v193, v193, v39\n"
    "v_mfma_f32_16x16x32_bf16 v[72:75], v[128:131], a[200:203], v[72:75]\n"
    "v_exp_f32_e64 v196, -v42\n"
    "v_mfma_f32_16x16x32_bf16 v[72:75], v[132:135], a[216:219], v[72:75]\n"
    "v_mul_f32_e32 v41, v41, v57\n"
    "v_add_f32_e32 v195, 1.0, v195\n"
    "v_mfma_f32_16x16x32_bf16 v[72:75], v[136:139], a[232:235], v[72:75]\n"
    "v_rcp_f32_e32 v195, v195\n"
    "v_mfma_f32_16x16x32_bf16 v[76:79], v[136:139], a[236:239], v[76:79]\n"
    "v_mul_f32_e32 v194, v194, v40\n"
    "v_cvt_pk_bf16_f32 v181, v192, v193\n"
    "v_mfma_f32_16x16x32_bf16 v[76:79], v[132:135], a[220:223], v[76:79]\n"
    "v_exp_f32_e64 v197, -v43\n"
    "v_mfma_f32_16x16x32_bf16 v[76:79], v[128:131], a[204:207], v[76:79]\n"
    "s_waitcnt lgkmcnt(3)\n"
    "v_mul_f32_e32 v42, v42, v58\n"
    "v_add_f32_e32 v196, 1.0, v196\n"
    "v_mfma_f32_16x16x32_bf16 v[76:79], v[140:143], a[252:255], v[76:79]\n"
    "s_add_u32 m0, s52, 40960\n"
    "ds_read_b128 v[164:167], v234 offset:15360\n"
    "ds_read_b128 v[168:171], v234 offset:17408\n"
    "v_mfma_f32_16x16x32_bf16 v[76:79], v[144:147], v[212:215], v[76:79]\n"
    "s_add_u32 s53, s52, 4096\n"
    "buffer_load_dwordx4 v232, s[48:51], s53 offen offset:0 sc1 lds\n"
    "v_mfma_f32_16x16x32_bf16 v[76:79], v[148:151], v[228:231], v[76:79]\n"
    "ds_read_b128 v[172:175], v234 offset:19456\n"
    "v_mfma_f32_16x16x32_bf16 v[72:75], v[148:151], v[224:227], v[72:75]\n"
    "v_rcp_f32_e32 v196, v196\n"
    "v_mfma_f32_16x16x32_bf16 v[72:75], v[144:147], v[208:211], v[72:75]\n"
    "v_mul_f32_e32 v195, v195, v41\n"
    "v_mfma_f32_16x16x32_bf16 v[72:75], v[140:143], a[248:251], v[72:75]\n"
    "v_exp_f32_e64 v192, -v44\n"
    "v_mfma_f32_16x16x32_bf16 v[68:71], v[140:143], a[244:247], v[68:71]\n"
    "v_mul_f32_e32 v43, v43, v59\n"
    "v_add_f32_e32 v197, 1.0, v197\n"
    "v_mfma_f32_16x16x32_bf16 v[68:71], v[144:147], v[204:207], v[68:71]\n"
    "v_rcp_f32_e32 v197, v197\n"
    "v_mfma_f32_16x16x32_bf16 v[68:71], v[148:151], v[220:223], v[68:71]\n"
    "v_mul_f32_e32 v196, v196, v42\n"
    "v_cvt_pk_bf16_f32 v184, v194, v195\n"
    "v_mfma_f32_16x16x32_bf16 v[64:67], v[148:151], v[216:219], v[64:67]\n"
    "v_exp_f32_e64 v193, -v45\n"
    "v_mfma_f32_16x16x32_bf16 v[64:67], v[144:147], v[200:203], v[64:67]\n"
    "v_mul_f32_e32 v44, v44, v60\n"
    "v_add_f32_e32 v192, 1.0, v192\n"
    "v_mfma_f32_16x16x32_bf16 v[64:67], v[140:143], a[240:243], v[64:67]\n"
    "s_waitcnt lgkmcnt(3)\n"
    "v_rcp_f32_e32 v192, v192\n"
    "v_mfma_f32_16x16x32_bf16 v[80:83], v[152:155], a[0:3], 0\n"
    "ds_read_b128 v[128:131], v234 offset:21504\n"
    "ds_read_b128 v[132:135], v234 offset:23552\n"
    "v_mfma_f32_16x16x32_bf16 v[80:83], v[156:159], a[16:19], v[80:83]\n"
    "buffer_load_dwordx4 v232, s[48:51], s53 offen offset:1024 sc1 lds\n"
    "v_mfma_f32_16x16x32_bf16 v[80:83], v[160:163], a[32:35], v[80:83]\n"
    "ds_read_b128 v[136:139], v234 offset:25600\n"
    "v_mul_f32_e32 v197, v197, v43\n"
    "v_mfma_f32_16x16x32_bf16 v[84:87], v[160:163], a[36:39], 0\n"
    "v_exp_f32_e64 v194, -v46\n"
    "v_mfma_f32_16x16x32_bf16 v[84:87], v[156:159], a[20:23], v[84:87]\n"
    "v_mul_f32_e32 v45, v45, v61\n"
    "v_add_f32_e32 v193, 1.0, v193\n"
    "v_mfma_f32_16x16x32_bf16 v[84:87], v[152:155], a[4:7], v[84:87]\n"
    "v_rcp_f32_e32 v193, v193\n"
    "v_mfma_f32_16x16x32_bf16 v[88:91], v[152:155], a[8:11], 0\n"
    "v_mul_f32_e32 v192, v192, v44\n"
    "v_cvt_pk_bf16_f32 v185, v196, v197\n"
    "v_mfma_f32_16x16x32_bf16 v[88:91], v[156:159], a[24:27], v[88:91]\n"
    "v_exp_f32_e64 v195, -v47\n"
    "v_mfma_f32_16x16x32_bf16 v[88:91], v[160:163], a[40:43], v[88:91]\n"
    "v_mul_f32_e32 v46, v46, v62\n"
    "v_add_f32_e32 v194, 1.0, v194\n"
    "v_mfma_f32_16x16x32_bf16 v[92:95], v[160:163], a[44:47], 0\n"
    "v_rcp_f32_e32 v194, v194\n"
    "v_mfma_f32_16x16x32_bf16 v[92:95], v[156:159], a[28:31], v[92:95]\n"
    "v_mul_f32_e32 v193, v193, v45\n"
    "v_mul_f32_e32 v47, v47, v63\n"
    "v_mfma_f32_16x16x32_bf16 v[92:95], v[152:155], a[12:15], v[92:95]\n"
    "s_waitcnt lgkmcnt(3)\n"
    "v_add_f32_e32 v195, 1.0, v195\n"
    "v_mfma_f32_16x16x32_bf16 v[92:95], v[164:167], a[60:63], v[92:95]\n"
    "ds_read_b128 v[140:143], v234 offset:27648\n"
    "ds_read_b128 v[144:147], v234 offset:29696\n"
    "v_mfma_f32_16x16x32_bf16 v[92:95], v[168:171], a[76:79], v[92:95]\n"
    "ds_read_b128 v[148:151], v234 offset:31744\n"
    "v_mfma_f32_16x16x32_bf16 v[92:95], v[172:175], a[92:95], v[92:95]\n"
    "v_rcp_f32_e32 v195, v195\n"
    "v_mfma_f32_16x16x32_bf16 v[88:91], v[172:175], a[88:91], v[88:91]\n"
    "v_mul_f32_e32 v194, v194, v46\n"
    "v_cvt_pk_bf16_f32 v188, v192, v193\n"
    "v_mfma_f32_16x16x32_bf16 v[88:91], v[168:171], a[72:75], v[88:91]\n"
    "s_nop 0\n"
    "v_mul_f32_e32 v195, v195, v47\n"
    "v_cvt_pk_bf16_f32 v189, v194, v195\n"
    "v_mfma_f32_16x16x32_bf16 v[88:91], v[164:167], a[56:59], v[88:91]\n"
    "v_mfma_f32_16x16x32_bf16 v[84:87], v[164:167], a[52:55], v[84:87]\n"
    "v_mfma_f32_16x16x32_bf16 v[84:87], v[168:171], a[68:71], v[84:87]\n"
    "v_mfma_f32_16x16x32_bf16 v[84:87], v[172:175], a[84:87], v[84:87]\n"
    "v_mfma_f32_16x16x32_bf16 v[80:83], v[172:175], a[80:83], v[80:83]\n"
    "v_mfma_f32_16x16x32_bf16 v[80:83], v[168:171], a[64:67], v[80:83]\n"
    "v_mfma_f32_16x16x32_bf16 v[80:83], v[164:167], a[48:51], v[80:83]\n"
    "s_waitcnt lgkmcnt(3)\n"
    "v_mfma_f32_16x16x32_bf16 v[80:83], v[128:131], a[96:99], v[80:83]\n"
    "ds_read_b128 v[152:155], v234 offset:33792\n"
    "ds_read_b128 v[156:159], v234 offset:35840\n"
    "v_mfma_f32_16x16x32_bf16 v[80:83], v[132:135], a[112:115], v[80:83]\n"
    "buffer_load_dwordx4 v232, s[48:51], s53 offen offset:2048 sc1 lds\n"
    "v_mfma_f32_16x16x32_bf16 v[80:83], v[136:139], a[128:131], v[80:83]\n"
    "ds_read_b128 v[160:163], v234 offset:37888\n"
    "v_mfma_f32_16x16x32_bf16 v[84:87], v[136:139], a[132:135], v[84:87]\n"
    "v_mfma_f32_16x16x32_bf16 v[84:87], v[132:135], a[116:119], v[84:87]\n"
    "v_mfma_f32_16x16x32_bf16 v[84:87], v[128:131], a[100:103], v[84:87]\n"
    "v_mfma_f32_16x16x32_bf16 v[88:91], v[128:131], a[104:107], v[88:91]\n"
    "v_mfma_f32_16x16x32_bf16 v[88:91], v[132:135], a[120:123], v[88:91]\n"
    "v_mfma_f32_16x16x32_bf16 v[88:91], v[136:139], a[136:139], v[88:91]\n"
    "v_mfma_f32_16x16x32_bf16 v[92:95], v[136:139], a[140:143], v[92:95]\n"
    "v_mfma_f32_16x16x32_bf16 v[92:95], v[132:135], a[124:127], v[92:95]\n"
    "v_mfma_f32_16x16x32_bf16 v[92:95], v[128:131], a[108:111], v[92:95]\n"
    "s_waitcnt lgkmcnt(3)\n"
    "v_mfma_f32_16x16x32_bf16 v[92:95], v[140:143], a[156:159], v[92:95]\n"
    "ds_read_b128 v[164:167], v234 offset:39936\n"
    "ds_read_b128 v[168:171], v234 offset:41984\n"
    "v_mfma_f32_16x16x32_bf16 v[92:95], v[144:147], a[172:175], v[92:95]\n"
    "buffer_load_dwordx4 v232, s[48:51], s53 offen offset:3072 sc1 lds\n"
    "v_mfma_f32_16x16x32_bf16 v[92:95], v[148:151], a[188:191], v[92:95]\n"
    "ds_read_b128 v[172:175], v234 offset:44032\n"
    "v_mfma_f32_16x16x32_bf16 v[88:91], v[148:151], a[184:187], v[88:91]\n"
    "v_mfma_f32_16x16x32_bf16 v[88:91], v[144:147], a[168:171], v[88:91]\n"
    "v_mfma_f32_16x16x32_bf16 v[88:91], v[140:143], a[152:155], v[88:91]\n"
    "v_mfma_f32_16x16x32_bf16 v[84:87], v[140:143], a[148:151], v[84:87]\n"
    "v_mfma_f32_16x16x32_bf16 v[84:87], v[144:147], a[164:167], v[84:87]\n"
    "v_mfma_f32_16x16x32_bf16 v[84:87], v[148:151], a[180:183], v[84:87]\n"
    "v_mfma_f32_16x16x32_bf16 v[80:83], v[148:151], a[176:179], v[80:83]\n"
    "v_mfma_f32_16x16x32_bf16 v[80:83], v[144:147], a[160:163], v[80:83]\n"
    "v_mfma_f32_16x16x32_bf16 v[80:83], v[140:143], a[144:147], v[80:83]\n"
    "s_waitcnt vmcnt(7) lgkmcnt(0)\n"
    "v_mfma_f32_16x16x32_bf16 v[80:83], v[152:155], a[192:195], v[80:83]\n"
    "s_barrier\n"
    "v_mfma_f32_16x16x32_bf16 v[80:83], v[156:159], a[208:211], v[80:83]\n"
    "s_add_u32 m0, s52, 45056\n"
    "s_add_u32 s53, s52, 8192\n"
    "v_mov_b32_e32 v253, 2\n"
    "v_mfma_f32_16x16x32_bf16 v[80:83], v[160:163], a[224:227], v[80:83]\n"
    "buffer_load_dwordx4 v232, s[48:51], s53 offen offset:0 sc1 lds\n"
    "v_mfma_f32_16x16x32_bf16 v[84:87], v[160:163], a[228:231], v[84:87]\n"
    "buffer_store_dword v253, v252, s[96:99], s39 offen sc1\n"
    "v_mfma_f32_16x16x32_bf16 v[84:87], v[156:159], a[212:215], v[84:87]\n"
    "ds_read_b128 v[128:131], v234 offset:45056\n"
    "ds_read_b128 v[132:135], v234 offset:49152\n"
    "v_mfma_f32_16x16x32_bf16 v[84:87], v[152:155], a[196:199], v[84:87]\n"
    "ds_read_b128 v[136:139], v234 offset:53248\n"
    "v_mfma_f32_16x16x32_bf16 v[88:91], v[152:155], a[200:203], v[88:91]\n"
    "v_mfma_f32_16x16x32_bf16 v[88:91], v[156:159], a[216:219], v[88:91]\n"
    "v_mfma_f32_16x16x32_bf16 v[88:91], v[160:163], a[232:235], v[88:91]\n"
    "v_mfma_f32_16x16x32_bf16 v[92:95], v[160:163], a[236:239], v[92:95]\n"
    "v_mfma_f32_16x16x32_bf16 v[92:95], v[156:159], a[220:223], v[92:95]\n"
    "v_mfma_f32_16x16x32_bf16 v[92:95], v[152:155], a[204:207], v[92:95]\n"
    "v_mfma_f32_16x16x32_bf16 v[92:95], v[164:167], a[252:255], v[92:95]\n"
    "ds_read_b128 v[140:143], v234 offset:46080\n"
    "ds_read_b128 v[144:147], v234 offset:50176\n"
    "v_mfma_f32_16x16x32_bf16 v[92:95], v[168:171], v[212:215], v[92:95]\n"
    "ds_read_b128 v[148:151], v234 offset:54272\n"
    "v_mfma_f32_16x16x32_bf16 v[92:95], v[172:175], v[228:231], v[92:95]\n"
    "v_mfma_f32_16x16x32_bf16 v[88:91], v[172:175], v[224:227], v[88:91]\n"
    "v_mfma_f32_16x16x32_bf16 v[88:91], v[168:171], v[208:211], v[88:91]\n"
    "v_mfma_f32_16x16x32_bf16 v[88:91], v[164:167], a[248:251], v[88:91]\n"
    "v_mfma_f32_16x16x32_bf16 v[84:87], v[164:167], a[244:247], v[84:87]\n"
    "v_mfma_f32_16x16x32_bf16 v[84:87], v[168:171], v[204:207], v[84:87]\n"
    "v_mfma_f32_16x16x32_bf16 v[84:87], v[172:175], v[220:223], v[84:87]\n"
    "v_mfma_f32_16x16x32_bf16 v[80:83], v[172:175], v[216:219], v[80:83]\n"
    "v_mfma_f32_16x16x32_bf16 v[80:83], v[168:171], v[200:203], v[80:83]\n"
    "v_mfma_f32_16x16x32_bf16 v[80:83], v[164:167], a[240:243], v[80:83]\n"
    "s_waitcnt lgkmcnt(3)\n"
    "v_mfma_f32_16x16x32_bf16 v[96:99], v[128:131], a[0:3], 0\n"
    "s_add_u32 m0, s52, 73728\n"
    "ds_read_b128 v[152:155], v234 offset:47104\n"
    "ds_read_b128 v[156:159], v234 offset:51200\n"
    "v_mfma_f32_16x16x32_bf16 v[96:99], v[132:135], a[16:19], v[96:99]\n"
    "s_add_u32 s53, s52, 36864\n"
    "buffer_load_dwordx4 v232, s[48:51], s53 offen offset:0 sc1 lds\n"
    "v_mfma_f32_16x16x32_bf16 v[96:99], v[136:139], a[32:35], v[96:99]\n"
    "ds_read_b128 v[160:163], v234 offset:55296\n"
    "v_mfma_f32_16x16x32_bf16 v[100:103], v[136:139], a[36:39], 0\n"
    "buffer_load_dword v238, v239, s[96:99], s35 offen sc1\n"
    "v_mfma_f32_16x16x32_bf16 v[100:103], v[132:135], a[20:23], v[100:103]\n"
    "buffer_load_dwordx4 v232, s[48:51], s53 offen offset:1024 sc1 lds\n"
    "v_mfma_f32_16x16x32_bf16 v[100:103], v[128:131], a[4:7], v[100:103]\n"
    "v_mfma_f32_16x16x32_bf16 v[104:107], v[128:131], a[8:11], 0\n"
    "v_mfma_f32_16x16x32_bf16 v[104:107], v[132:135], a[24:27], v[104:107]\n"
    "v_mfma_f32_16x16x32_bf16 v[104:107], v[136:139], a[40:43], v[104:107]\n"
    "buffer_load_dwordx4 v232, s[48:51], s53 offen offset:2048 sc1 lds\n"
    "v_mfma_f32_16x16x32_bf16 v[108:111], v[136:139], a[44:47], 0\n"
    "v_mfma_f32_16x16x32_bf16 v[108:111], v[132:135], a[28:31], v[108:111]\n"
    "v_mfma_f32_16x16x32_bf16 v[108:111], v[128:131], a[12:15], v[108:111]\n"
    "s_waitcnt lgkmcnt(3)\n"
    "v_mfma_f32_16x16x32_bf16 v[124:127], v[140:143], a[12:15], 0\n"
    "ds_read_b128 v[164:167], v234 offset:48128\n"
    "ds_read_b128 v[168:171], v234 offset:52224\n"
    "v_mfma_f32_16x16x32_bf16 v[124:127], v[144:147], a[28:31], v[124:127]\n"
    "buffer_load_dwordx4 v232, s[48:51], s53 offen offset:3072 sc1 lds\n"
    "v_mfma_f32_16x16x32_bf16 v[124:127], v[148:151], a[44:47], v[124:127]\n"
    "ds_read_b128 v[172:175], v234 offset:56320\n"
    "v_mfma_f32_16x16x32_bf16 v[120:123], v[148:151], a[40:43], 0\n"
    "v_exp_f32_e64 v192, -v64\n"
    "v_mfma_f32_16x16x32_bf16 v[120:123], v[144:147], a[24:27], v[120:123]\n"
    "v_exp_f32_e64 v193, -v65\n"
    "v_mfma_f32_16x16x32_bf16 v[120:123], v[140:143], a[8:11], v[120:123]\n"
    "s_add_u32 m0, s52, 77824\n"
    "v_mul_f32_e32 v64, v64, v80\n"
    "v_add_f32_e32 v192, 1.0, v192\n"
    "v_mfma_f32_16x16x32_bf16 v[116:119], v[140:143], a[4:7], 0\n"
    "s_add_u32 s53, s52, 40960\n"
    "buffer_load_dwordx4 v232, s[48:51], s53 offen offset:0 sc1 lds\n"
    "v_mfma_f32_16x16x32_bf16 v[116:119], v[144:147], a[20:23], v[116:119]\n"
    "v_rcp_f32_e32 v192, v192\n"
    "v_mfma_f32_16x16x32_bf16 v[116:119], v[148:151], a[36:39], v[116:119]\n"
    "v_exp_f32_e64 v194, -v66\n"
    "v_mfma_f32_16x16x32_bf16 v[112:115], v[148:151], a[32:35], 0\n"
    "v_mul_f32_e32 v65, v65, v81\n"
    "v_add_f32_e32 v193, 1.0, v193\n"
    "v_mfma_f32_16x16x32_bf16 v[112:115], v[144:147], a[16:19], v[112:115]\n"
    "v_rcp_f32_e32 v193, v193\n"
    "v_mfma_f32_16x16x32_bf16 v[112:115], v[140:143], a[0:3], v[112:115]\n"
    "s_waitcnt lgkmcnt(3)\n"
    "v_mul_f32_e32 v192, v192, v64\n"
    "v_mfma_f32_16x16x32_bf16 v[0:3], v[152:155], a[0:3], 0\n"
    "ds_read_b128 v[128:131], v234 offset:57344\n"
    "ds_read_b128 v[132:135], v234 offset:61440\n"
    "v_mfma_f32_16x16x32_bf16 v[0:3], v[156:159], a[16:19], v[0:3]\n"
    "buffer_load_dwordx4 v232, s[48:51], s53 offen offset:1024 sc1 lds\n"
    "v_mfma_f32_16x16x32_bf16 v[0:3], v[160:163], a[32:35], v[0:3]\n"
    "ds_read_b128 v[136:139], v235 offset:0\n"
    "v_mfma_f32_16x16x32_bf16 v[4:7], v[160:163], a[36:39], 0\n"
    "v_exp_f32_e64 v195, -v67\n"
    "v_mfma_f32_16x16x32_bf16 v[4:7], v[156:159], a[20:23], v[4:7]\n"
    "v_mul_f32_e32 v66, v66, v82\n"
    "v_add_f32_e32 v194, 1.0, v194\n"
    "v_mfma_f32_16x16x32_bf16 v[4:7], v[152:155], a[4:7], v[4:7]\n"
    "v_rcp_f32_e32 v194, v194\n"
    "v_mfma_f32_16x16x32_bf16 v[8:11], v[152:155], a[8:11], 0\n"
    "buffer_load_dwordx4 v232, s[48:51], s53 offen offset:2048 sc1 lds\n"
    "v_mfma_f32_16x16x32_bf16 v[8:11], v[156:159], a[24:27], v[8:11]\n"
    "v_mul_f32_e32 v193, v193, v65\n"
    "v_mfma_f32_16x16x32_bf16 v[8:11], v[160:163], a[40:43], v[8:11]\n"
    "v_exp_f32_e64 v196, -v68\n"
    "v_mfma_f32_16x16x32_bf16 v[12:15], v[160:163], a[44:47], 0\n"
    "v_mul_f32_e32 v67, v67, v83\n"
    "v_add_f32_e32 v195, 1.0, v195\n"
    "v_mfma_f32_16x16x32_bf16 v[12:15], v[156:159], a[28:31], v[12:15]\n"
    "v_rcp_f32_e32 v195, v195\n"
    "v_mfma_f32_16x16x32_bf16 v[12:15], v[152:155], a[12:15], v[12:15]\n"
    "s_waitcnt lgkmcnt(3)\n"
    "v_mul_f32_e32 v194, v194, v66\n"
    "v_cvt_pk_bf16_f32 v178, v192, v193\n"
    "v_mfma_f32_16x16x32_bf16 v[28:31], v[164:167], a[12:15], 0\n"
    "ds_read_b128 v[140:143], v234 offset:58368\n"
    "ds_read_b128 v[144:147], v234 offset:62464\n"
    "v_mfma_f32_16x16x32_bf16 v[28:31], v[168:171], a[28:31], v[28:31]\n"
    "buffer_load_dwordx4 v232, s[48:51], s53 offen offset:3072 sc1 lds\n"
    "v_mfma_f32_16x16x32_bf16 v[28:31], v[172:175], a[44:47], v[28:31]\n"
    "ds_read_b128 v[148:151], v235 offset:1024\n"
    "v_mfma_f32_16x16x32_bf16 v[24:27], v[172:175], a[40:43], 0\n"
    "v_exp_f32_e64 v197, -v69\n"
    "v_mfma_f32_16x16x32_bf16 v[24:27], v[168:171], a[24:27], v[24:27]\n"
    "v_mul_f32_e32 v68, v68, v84\n"
    "v_add_f32_e32 v196, 1.0, v196\n"
    "v_mfma_f32_16x16x32_bf16 v[24:27], v[164:167], a[8:11], v[24:27]\n"
    "s_add_u32 m0, s52, 81920\n"
    "v_rcp_f32_e32 v196, v196\n"
    "v_mfma_f32_16x16x32_bf16 v[20:23], v[164:167], a[4:7], 0\n"
    "s_add_u32 s53, s52, 45056\n"
    "buffer_load_dwordx4 v232, s[48:51], s53 offen offset:0 sc1 lds\n"
    "v_mfma_f32_16x16x32_bf16 v[20:23], v[168:171], a[20:23], v[20:23]\n"
    "v_mul_f32_e32 v195, v195, v67\n"
    "v_mfma_f32_16x16x32_bf16 v[20:23], v[172:175], a[36:39], v[20:23]\n"
    "v_exp_f32_e64 v192, -v70\n"
    "v_mfma_f32_16x16x32_bf16 v[16:19], v[172:175], a[32:35], 0\n"
    "v_mul_f32_e32 v69, v69, v85\n"
    "v_add_f32_e32 v197, 1.0, v197\n"
    "v_mfma_f32_16x16x32_bf16 v[16:19], v[168:171], a[16:19], v[16:19]\n"
    "v_rcp_f32_e32 v197, v197\n"
    "v_mfma_f32_16x16x32_bf16 v[16:19], v[164:167], a[0:3], v[16:19]\n"
    "s_waitcnt lgkmcnt(3)\n"
    "v_mul_f32_e32 v196, v196, v68\n"
    "v_cvt_pk_bf16_f32 v179, v194, v195\n"
    "v_mfma_f32_16x16x32_bf16 v[96:99], v[128:131], a[48:51], v[96:99]\n"
    "s_waitcnt vmcnt(8)\n"
    "v_cmp_gt_u32_e32 vcc, 1, v238\n"
    "s_nop 0\n"
    "v_mfma_f32_16x16x32_bf16 v[96:99], v[132:135], a[64:67], v[96:99]\n"
    "s_cbranch_vccnz L_spn45_mk\n"
    "L_spb46_mk:\n"
    "s_mov_b32 s33, 4096\n"
    "v_mfma_f32_16x16x32_bf16 v[96:99], v[136:139], a[80:83], v[96:99]\n"
    "buffer_load_dwordx2 a[0:1], v237, s[60:63], s33 offen offset:0 sc1\n"
    "v_mfma_f32_16x16x32_bf16 v[100:103], v[136:139], a[84:87], v[100:103]\n"
    "buffer_load_dwordx2 a[2:3], v237, s[60:63], s33 offen offset:512 sc1\n"
    "v_mfma_f32_16x16x32_bf16 v[100:103], v[132:135], a[68:71], v[100:103]\n"
    "ds_read_b128 v[152:155], v234 offset:59392\n"
    "ds_read_b128 v[156:159], v234 offset:63488\n"
    "v_mfma_f32_16x16x32_bf16 v[100:103], v[128:131], a[52:55], v[100:103]\n"
    "ds_read_b128 v[160:163], v235 offset:2048\n"
    "s_add_u32 s34, s36, 0\n"
    "v_mfma_f32_16x16x32_bf16 v[104:107], v[128:131], a[56:59], v[104:107]\n"
    "s_mov_b32 s33, 13312\n"
    "buffer_load_dwordx2 a[8:9], v237, s[60:63], s33 offen offset:0 sc1\n"
    "v_mfma_f32_16x16x32_bf16 v[104:107], v[132:135], a[72:75], v[104:107]\n"
    "buffer_store_dwordx4 v[176:179], v232, s[28:31], s34 offen offset:2048 sc1\n"
    "v_mfma_f32_16x16x32_bf16 v[104:107], v[136:139], a[88:91], v[104:107]\n"
    "v_exp_f32_e64 v193, -v71\n"
    "v_mfma_f32_16x16x32_bf16 v[108:111], v[136:139], a[92:95], v[108:111]\n"
    "buffer_load_dwordx2 a[10:11], v237, s[60:63], s33 offen offset:512 sc1\n"
    "v_mfma_f32_16x16x32_bf16 v[108:111], v[132:135], a[76:79], v[108:111]\n"
    "v_mul_f32_e32 v70, v70, v86\n"
    "v_add_f32_e32 v192, 1.0, v192\n"
    "v_mfma_f32_16x16x32_bf16 v[108:111], v[128:131], a[60:63], v[108:111]\n"
    "s_waitcnt lgkmcnt(3)\n"
    "v_rcp_f32_e32 v192, v192\n"
    "v_mfma_f32_16x16x32_bf16 v[124:127], v[140:143], a[60:63], v[124:127]\n"
    "ds_read_b128 v[164:167], v234 offset:60416\n"
    "ds_read_b128 v[168:171], v234 offset:64512\n"
    "v_mfma_f32_16x16x32_bf16 v[124:127], v[144:147], a[76:79], v[124:127]\n"
    "s_mov_b32 s33, 22528\n"
    "buffer_load_dwordx2 a[16:17], v237, s[60:63], s33 offen offset:0 sc1\n"
    "v_mfma_f32_16x16x32_bf16 v[124:127], v[148:151], a[92:95], v[124:127]\n"
    "ds_read_b128 v[172:175], v235 offset:3072\n"
    "v_mul_f32_e32 v197, v197, v69\n"
    "v_mfma_f32_16x16x32_bf16 v[120:123], v[148:151], a[88:91], v[120:123]\n"
    "buffer_load_dwordx2 a[18:19], v237, s[60:63], s33 offen offset:512 sc1\n"
    "v_mfma_f32_16x16x32_bf16 v[120:123], v[144:147], a[72:75], v[120:123]\n"
    "v_exp_f32_e64 v194, -v72\n"
    "v_mfma_f32_16x16x32_bf16 v[120:123], v[140:143], a[56:59], v[120:123]\n"
    "v_mul_f32_e32 v71, v71, v87\n"
    "v_add_f32_e32 v193, 1.0, v193\n"
    "v_mfma_f32_16x16x32_bf16 v[116:119], v[140:143], a[52:55], v[116:119]\n"
    "s_mov_b32 s33, 31744\n"
    "buffer_load_dwordx2 a[24:25], v237, s[60:63], s33 offen offset:0 sc1\n"
    "v_mfma_f32_16x16x32_bf16 v[116:119], v[144:147], a[68:71], v[116:119]\n"
    "v_rcp_f32_e32 v193, v193\n"
    "v_mfma_f32_16x16x32_bf16 v[116:119], v[148:151], a[84:87], v[116:119]\n"
    "v_mul_f32_e32 v192, v192, v70\n"
    "v_cvt_pk_bf16_f32 v182, v196, v197\n"
    "v_mfma_f32_16x16x32_bf16 v[112:115], v[148:151], a[80:83], v[112:115]\n"
    "buffer_load_dwordx2 a[26:27], v237, s[60:63], s33 offen offset:512 sc1\n"
    "v_mfma_f32_16x16x32_bf16 v[112:115], v[144:147], a[64:67], v[112:115]\n"
    "v_exp_f32_e64 v195, -v73\n"
    "v_mfma_f32_16x16x32_bf16 v[112:115], v[140:143], a[48:51], v[112:115]\n"
    "s_waitcnt lgkmcnt(3)\n"
    "v_mul_f32_e32 v72, v72, v88\n"
    "v_add_f32_e32 v194, 1.0, v194\n"
    "v_mfma_f32_16x16x32_bf16 v[0:3], v[152:155], a[48:51], v[0:3]\n"
    "ds_read_b128 v[128:131], v235 offset:4096\n"
    "ds_read_b128 v[132:135], v235 offset:8192\n"
    "v_mfma_f32_16x16x32_bf16 v[0:3], v[156:159], a[64:67], v[0:3]\n"
    "s_mov_b32 s33, 151552\n"
    "buffer_load_dwordx2 a[4:5], v237, s[60:63], s33 offen offset:0 sc1\n"
    "v_mfma_f32_16x16x32_bf16 v[0:3], v[160:163], a[80:83], v[0:3]\n"
    "ds_read_b128 v[136:139], v235 offset:12288\n"
    "v_mfma_f32_16x16x32_bf16 v[4:7], v[160:163], a[84:87], v[4:7]\n"
    "buffer_load_dwordx2 a[6:7], v237, s[60:63], s33 offen offset:512 sc1\n"
    "v_mfma_f32_16x16x32_bf16 v[4:7], v[156:159], a[68:71], v[4:7]\n"
    "v_rcp_f32_e32 v194, v194\n"
    "v_mfma_f32_16x16x32_bf16 v[4:7], v[152:155], a[52:55], v[4:7]\n"
    "v_mul_f32_e32 v193, v193, v71\n"
    "v_mfma_f32_16x16x32_bf16 v[8:11], v[152:155], a[56:59], v[8:11]\n"
    "s_mov_b32 s33, 160768\n"
    "buffer_load_dwordx2 a[12:13], v237, s[60:63], s33 offen offset:0 sc1\n"
    "v_mfma_f32_16x16x32_bf16 v[8:11], v[156:159], a[72:75], v[8:11]\n"
    "v_exp_f32_e64 v196, -v74\n"
    "v_mfma_f32_16x16x32_bf16 v[8:11], v[160:163], a[88:91], v[8:11]\n"
    "v_mul_f32_e32 v73, v73, v89\n"
    "v_add_f32_e32 v195, 1.0, v195\n"
    "v_mfma_f32_16x16x32_bf16 v[12:15], v[160:163], a[92:95], v[12:15]\n"
    "buffer_load_dwordx2 a[14:15], v237, s[60:63], s33 offen offset:512 sc1\n"
    "v_mfma_f32_16x16x32_bf16 v[12:15], v[156:159], a[76:79], v[12:15]\n"
    "v_rcp_f32_e32 v195, v195\n"
    "v_mfma_f32_16x16x32_bf16 v[12:15], v[152:155], a[60:63], v[12:15]\n"
    "s_waitcnt lgkmcnt(3)\n"
    "v_mul_f32_e32 v194, v194, v72\n"
    "v_cvt_pk_bf16_f32 v183, v192, v193\n"
    "v_mfma_f32_16x16x32_bf16 v[28:31], v[164:167], a[60:63], v[28:31]\n"
    "ds_read_b128 v[140:143], v235 offset:5120\n"
    "ds_read_b128 v[144:147], v235 offset:9216\n"
    "v_mfma_f32_16x16x32_bf16 v[28:31], v[168:171], a[76:79], v[28:31]\n"
    "s_mov_b32 s33, 169984\n"
    "buffer_load_dwordx2 a[20:21], v237, s[60:63], s33 offen offset:0 sc1\n"
    "v_mfma_f32_16x16x32_bf16 v[28:31], v[172:175], a[92:95], v[28:31]\n"
    "ds_read_b128 v[148:151], v235 offset:13312\n"
    "s_add_u32 s34, s36, 147456\n"
    "v_mfma_f32_16x16x32_bf16 v[24:27], v[172:175], a[88:91], v[24:27]\n"
    "buffer_load_dwordx2 a[22:23], v237, s[60:63], s33 offen offset:512 sc1\n"
    "v_mfma_f32_16x16x32_bf16 v[24:27], v[168:171], a[72:75], v[24:27]\n"
    "buffer_store_dwordx4 v[180:183], v232, s[28:31], s34 offen offset:2048 sc1\n"
    "v_mfma_f32_16x16x32_bf16 v[24:27], v[164:167], a[56:59], v[24:27]\n"
    "v_exp_f32_e64 v197, -v75\n"
    "v_mfma_f32_16x16x32_bf16 v[20:23], v[164:167], a[52:55], v[20:23]\n"
    "s_mov_b32 s33, 179200\n"
    "buffer_load_dwordx2 a[28:29], v237, s[60:63], s33 offen offset:0 sc1\n"
    "v_mfma_f32_16x16x32_bf16 v[20:23], v[168:171], a[68:71], v[20:23]\n"
    "v_mul_f32_e32 v74, v74, v90\n"
    "v_add_f32_e32 v196, 1.0, v196\n"
    "v_mfma_f32_16x16x32_bf16 v[20:23], v[172:175], a[84:87], v[20:23]\n"
    "v_rcp_f32_e32 v196, v196\n"
    "v_mfma_f32_16x16x32_bf16 v[16:19], v[172:175], a[80:83], v[16:19]\n"
    "buffer_load_dwordx2 a[30:31], v237, s[60:63], s33 offen offset:512 sc1\n"
    "v_mfma_f32_16x16x32_bf16 v[16:19], v[168:171], a[64:67], v[16:19]\n"
    "v_mul_f32_e32 v195, v195, v73\n"
    "v_mfma_f32_16x16x32_bf16 v[16:19], v[164:167], a[48:51], v[16:19]\n"
    "s_waitcnt lgkmcnt(3)\n"
    "v_exp_f32_e64 v192, -v76\n"
    "v_mfma_f32_16x16x32_bf16 v[96:99], v[128:131], a[96:99], v[96:99]\n"
    "ds_read_b128 v[152:155], v235 offset:6144\n"
    "ds_read_b128 v[156:159], v235 offset:10240\n"
    "v_mfma_f32_16x16x32_bf16 v[96:99], v[132:135], a[112:115], v[96:99]\n"
    "s_mov_b32 s33, 0\n"
    "buffer_load_dwordx4 a[32:35], v232, s[60:63], s33 offen sc1\n"
    "v_mfma_f32_16x16x32_bf16 v[96:99], v[136:139], a[128:131], v[96:99]\n"
    "ds_read_b128 v[160:163], v235 offset:14336\n"
    "v_mul_f32_e32 v75, v75, v91\n"
    "v_mfma_f32_16x16x32_bf16 v[100:103], v[136:139], a[132:135], v[100:103]\n"
    "v_add_f32_e32 v197, 1.0, v197\n"
    "v_mfma_f32_16x16x32_bf16 v[100:103], v[132:135], a[116:119], v[100:103]\n"
    "v_rcp_f32_e32 v197, v197\n"
    "v_mfma_f32_16x16x32_bf16 v[100:103], v[128:131], a[100:103], v[100:103]\n"
    "v_mul_f32_e32 v196, v196, v74\n"
    "v_cvt_pk_bf16_f32 v186, v194, v195\n"
    "v_mfma_f32_16x16x32_bf16 v[104:107], v[128:131], a[104:107], v[104:107]\n"
    "s_mov_b32 s33, 9216\n"
    "buffer_load_dwordx4 a[40:43], v232, s[60:63], s33 offen sc1\n"
    "v_mfma_f32_16x16x32_bf16 v[104:107], v[132:135], a[120:123], v[104:107]\n"
    "v_exp_f32_e64 v193, -v77\n"
    "v_mfma_f32_16x16x32_bf16 v[104:107], v[136:139], a[136:139], v[104:107]\n"
    "v_mul_f32_e32 v76, v76, v92\n"
    "v_add_f32_e32 v192, 1.0, v192\n"
    "v_mfma_f32_16x16x32_bf16 v[108:111], v[136:139], a[140:143], v[108:111]\n"
    "v_rcp_f32_e32 v192, v192\n"
    "v_mfma_f32_16x16x32_bf16 v[108:111], v[132:135], a[124:127], v[108:111]\n"
    "v_mul_f32_e32 v197, v197, v75\n"
    "v_mfma_f32_16x16x32_bf16 v[108:111], v[128:131], a[108:111], v[108:111]\n"
    "s_waitcnt lgkmcnt(3)\n"
    "v_exp_f32_e64 v194, -v78\n"
    "v_mfma_f32_16x16x32_bf16 v[124:127], v[140:143], a[108:111], v[124:127]\n"
    "ds_read_b128 v[164:167], v235 offset:7168\n"
    "ds_read_b128 v[168:171], v235 offset:11264\n"
    "v_mfma_f32_16x16x32_bf16 v[124:127], v[144:147], a[124:127], v[124:127]\n"
    "s_mov_b32 s33, 18432\n"
    "buffer_load_dwordx4 a[48:51], v232, s[60:63], s33 offen sc1\n"
    "v_mfma_f32_16x16x32_bf16 v[124:127], v[148:151], a[140:143], v[124:127]\n"
    "ds_read_b128 v[172:175], v235 offset:15360\n"
    "v_mul_f32_e32 v77, v77, v93\n"
    "v_mfma_f32_16x16x32_bf16 v[120:123], v[148:151], a[136:139], v[120:123]\n"
    "v_add_f32_e32 v193, 1.0, v193\n"
    "v_mfma_f32_16x16x32_bf16 v[120:123], v[144:147], a[120:123], v[120:123]\n"
    "v_rcp_f32_e32 v193, v193\n"
    "v_mfma_f32_16x16x32_bf16 v[120:123], v[140:143], a[104:107], v[120:123]\n"
    "v_mul_f32_e32 v192, v192, v76\n"
    "v_cvt_pk_bf16_f32 v187, v196, v197\n"
    "v_mfma_f32_16x16x32_bf16 v[116:119], v[140:143], a[100:103], v[116:119]\n"
    "s_mov_b32 s33, 27648\n"
    "buffer_load_dwordx4 a[56:59], v232, s[60:63], s33 offen sc1\n"
    "v_mfma_f32_16x16x32_bf16 v[116:119], v[144:147], a[116:119], v[116:119]\n"
    "s_add_u32 s34, s36, 294912\n"
    "buffer_store_dwordx4 v[184:187], v232, s[28:31], s34 offen offset:2048 sc1\n"
    "v_mfma_f32_16x16x32_bf16 v[116:119], v[148:151], a[132:135], v[116:119]\n"
    "v_exp_f32_e64 v195, -v79\n"
    "v_mfma_f32_16x16x32_bf16 v[112:115], v[148:151], a[128:131], v[112:115]\n"
    "v_mul_f32_e32 v78, v78, v94\n"
    "v_add_f32_e32 v194, 1.0, v194\n"
    "v_mfma_f32_16x16x32_bf16 v[112:115], v[144:147], a[112:115], v[112:115]\n"
    "v_rcp_f32_e32 v194, v194\n"
    "v_mfma_f32_16x16x32_bf16 v[112:115], v[140:143], a[96:99], v[112:115]\n"
    "s_waitcnt vmcnt(46) lgkmcnt(0)\n"
    "v_mul_f32_e32 v193, v193, v77\n"
    "v_mul_f32_e32 v79, v79, v95\n"
    "v_mfma_f32_16x16x32_bf16 v[0:3], v[152:155], a[96:99], v[0:3]\n"
    "s_barrier\n"
    "v_mfma_f32_16x16x32_bf16 v[0:3], v[156:159], a[112:115], v[0:3]\n"
    "s_mov_b32 s33, 147456\n"
    "buffer_load_dwordx4 a[36:39], v232, s[60:63], s33 offen sc1\n"
    "v_mfma_f32_16x16x32_bf16 v[0:3], v[160:163], a[128:131], v[0:3]\n"
    "ds_read_b128 v[128:131], v233 offset:0\n"
    "ds_read_b128 v[132:135], v233 offset:4096\n"
    "v_mfma_f32_16x16x32_bf16 v[4:7], v[160:163], a[132:135], v[4:7]\n"
    "ds_read_b128 v[136:139], v233 offset:8192\n"
    "v_add_f32_e32 v195, 1.0, v195\n"
    "v_mfma_f32_16x16x32_bf16 v[4:7], v[156:159], a[116:119], v[4:7]\n"
    "v_rcp_f32_e32 v195, v195\n"
    "v_mfma_f32_16x16x32_bf16 v[4:7], v[152:155], a[100:103], v[4:7]\n"
    "v_mul_f32_e32 v194, v194, v78\n"
    "v_cvt_pk_bf16_f32 v190, v192, v193\n"
    "v_mfma_f32_16x16x32_bf16 v[8:11], v[152:155], a[104:107], v[8:11]\n"
    "s_mov_b32 s33, 156672\n"
    "buffer_load_dwordx4 a[44:47], v232, s[60:63], s33 offen sc1\n"
    "v_mfma_f32_16x16x32_bf16 v[8:11], v[156:159], a[120:123], v[8:11]\n"
    "s_nop 0\n"
    "v_mul_f32_e32 v195, v195, v79\n"
    "v_cvt_pk_bf16_f32 v191, v194, v195\n"
    "v_mfma_f32_16x16x32_bf16 v[8:11], v[160:163], a[136:139], v[8:11]\n"
    "s_add_u32 s34, s36, 442368\n"
    "buffer_store_dwordx4 v[188:191], v232, s[28:31], s34 offen offset:2048 sc1\n"
    "v_mfma_f32_16x16x32_bf16 v[12:15], v[160:163], a[140:143], v[12:15]\n"
    "v_mfma_f32_16x16x32_bf16 v[12:15], v[156:159], a[124:127], v[12:15]\n"
    "v_mfma_f32_16x16x32_bf16 v[12:15], v[152:155], a[108:111], v[12:15]\n"
    "v_mfma_f32_16x16x32_bf16 v[28:31], v[164:167], a[108:111], v[28:31]\n"
    "ds_read_b128 v[140:143], v233 offset:12288\n"
    "ds_read_b128 v[144:147], v233 offset:16384\n"
    "v_mfma_f32_16x16x32_bf16 v[28:31], v[168:171], a[124:127], v[28:31]\n"
    "s_mov_b32 s33, 165888\n"
    "buffer_load_dwordx4 a[52:55], v232, s[60:63], s33 offen sc1\n"
    "v_mfma_f32_16x16x32_bf16 v[28:31], v[172:175], a[140:143], v[28:31]\n"
    "ds_read_b128 v[148:151], v233 offset:20480\n"
    "v_mfma_f32_16x16x32_bf16 v[24:27], v[172:175], a[136:139], v[24:27]\n"
    "buffer_load_dword v238, v239, s[96:99], s35 offen sc1\n"
    "v_mfma_f32_16x16x32_bf16 v[24:27], v[168:171], a[120:123], v[24:27]\n"
    "v_mfma_f32_16x16x32_bf16 v[24:27], v[164:167], a[104:107], v[24:27]\n"
    "v_mfma_f32_16x16x32_bf16 v[20:23], v[164:167], a[100:103], v[20:23]\n"
    "s_mov_b32 s33, 175104\n"
    "buffer_load_dwordx4 a[60:63], v232, s[60:63], s33 offen sc1\n"
    "v_mfma_f32_16x16x32_bf16 v[20:23], v[168:171], a[116:119], v[20:23]\n"
    "v_mfma_f32_16x16x32_bf16 v[20:23], v[172:175], a[132:135], v[20:23]\n"
    "v_mfma_f32_16x16x32_bf16 v[16:19], v[172:175], a[128:131], v[16:19]\n"
    "v_mfma_f32_16x16x32_bf16 v[16:19], v[168:171], a[112:115], v[16:19]\n"
    "v_mfma_f32_16x16x32_bf16 v[16:19], v[164:167], a[96:99], v[16:19]\n"
    "s_waitcnt lgkmcnt(3)\n"
    "v_mfma_f32_16x16x32_bf16 v[96:99], v[128:131], a[144:147], v[96:99]\n"
    "s_add_u32 m0, s52, 110592\n"
    "ds_read_b128 v[152:155], v233 offset:24576\n"
    "ds_read_b128 v[156:159], v233 offset:28672\n"
    "v_mfma_f32_16x16x32_bf16 v[96:99], v[132:135], a[160:163], v[96:99]\n"
    "s_add_u32 s53, s52, 73728\n"
    "buffer_load_dwordx4 v232, s[48:51], s53 offen offset:0 sc1 lds\n"
    "v_mfma_f32_16x16x32_bf16 v[96:99], v[136:139], a[176:179], v[96:99]\n"
    "ds_read_b128 v[160:163], v233 offset:32768\n"
    "v_mfma_f32_16x16x32_bf16 v[100:103], v[136:139], a[180:183], v[100:103]\n"
    "buffer_load_dwordx4 v232, s[48:51], s53 offen offset:1024 sc1 lds\n"
    "v_mfma_f32_16x16x32_bf16 v[100:103], v[132:135], a[164:167], v[100:103]\n"
    "v_mfma_f32_16x16x32_bf16 v[100:103], v[128:131], a[148:151], v[100:103]\n"
    "v_mfma_f32_16x16x32_bf16 v[104:107], v[128:131], a[152:155], v[104:107]\n"
    "s_mov_b32 s33, 5120\n"
    "buffer_load_dwordx4 a[64:67], v232, s[60:63], s33 offen sc1\n"
    "v_mfma_f32_16x16x32_bf16 v[104:107], v[132:135], a[168:171], v[104:107]\n"
    "v_mfma_f32_16x16x32_bf16 v[104:107], v[136:139], a[184:187], v[104:107]\n"
    "v_mfma_f32_16x16x32_bf16 v[108:111], v[136:139], a[188:191], v[108:111]\n"
    "s_mov_b32 s33, 14336\n"
    "buffer_load_dwordx4 a[72:75], v232, s[60:63], s33 offen sc1\n"
    "v_mfma_f32_16x16x32_bf16 v[108:111], v[132:135], a[172:175], v[108:111]\n"
    "v_mfma_f32_16x16x32_bf16 v[108:111], v[128:131], a[156:159], v[108:111]\n"
    "s_waitcnt lgkmcnt(3)\n"
    "v_mfma_f32_16x16x32_bf16 v[108:111], v[140:143], a[204:207], v[108:111]\n"
    "ds_read_b128 v[164:167], v233 offset:1024\n"
    "ds_read_b128 v[168:171], v233 offset:5120\n"
    "v_mfma_f32_16x16x32_bf16 v[108:111], v[144:147], a[220:223], v[108:111]\n"
    "buffer_load_dwordx4 v232, s[48:51], s53 offen offset:2048 sc1 lds\n"
    "v_mfma_f32_16x16x32_bf16 v[108:111], v[148:151], a[236:239], v[108:111]\n"
    "ds_read_b128 v[172:175], v233 offset:9216\n"
    "v_mfma_f32_16x16x32_bf16 v[104:107], v[148:151], a[232:235], v[104:107]\n"
    "v_mfma_f32_16x16x32_bf16 v[104:107], v[144:147], a[216:219], v[104:107]\n"
    "s_mov_b32 s33, 23552\n"
    "buffer_load_dwordx4 a[80:83], v232, s[60:63], s33 offen sc1\n"
    "v_mfma_f32_16x16x32_bf16 v[104:107], v[140:143], a[200:203], v[104:107]\n"
    "v_mfma_f32_16x16x32_bf16 v[100:103], v[140:143], a[196:199], v[100:103]\n"
    "L_pad3_4_mk:\n"
    "s_cbranch_execz L_ret3_mk\n"
    "v_mfma_f32_16x16x32_bf16 v[100:103], v[144:147], a[212:215], v[100:103]\n"
    "v_mfma_f32_16x16x32_bf16 v[100:103], v[148:151], a[228:231], v[100:103]\n"
    "s_mov_b32 s33, 32768\n"
    "buffer_load_dwordx4 a[88:91], v232, s[60:63], s33 offen sc1\n"
    "v_mfma_f32_16x16x32_bf16 v[96:99], v[148:151], a[224:227], v[96:99]\n"
    "v_mfma_f32_16x16x32_bf16 v[96:99], v[144:147], a[208:211], v[96:99]\n"
    "v_mfma_f32_16x16x32_bf16 v[96:99], v[140:143], a[192:195], v[96:99]\n"
    "s_waitcnt lgkmcnt(3)\n"
    "v_mfma_f32_16x16x32_bf16 v[96:99], v[152:155], a[240:243], v[96:99]\n"
    "ds_read_b128 v[128:131], v233 offset:13312\n"
    "ds_read_b128 v[132:135], v233 offset:17408\n"
    "v_mfma_f32_16x16x32_bf16 v[96:99], v[156:159], v[200:203], v[96:99]\n"
    "buffer_load_dwordx4 v232, s[48:51], s53 offen offset:3072 sc1 lds\n"
    "v_mfma_f32_16x16x32_bf16 v[96:99], v[160:163], v[216:219], v[96:99]\n"
    "ds_read_b128 v[136:139], v233 offset:21504\n"
    "v_mfma_f32_16x16x32_bf16 v[100:103], v[160:163], v[220:223], v[100:103]\n"
    "v_mfma_f32_16x16x32_bf16 v[100:103], v[156:159], v[204:207], v[100:103]\n"
    "s_mov_b32 s33, 152576\n"
    "buffer_load_dwordx4 a[68:71], v232, s[60:63], s33 offen sc1\n"
    "v_mfma_f32_16x16x32_bf16 v[100:103], v[152:155], a[244:247], v[100:103]\n"
    "v_mfma_f32_16x16x32_bf16 v[104:107], v[152:155], a[248:251], v[104:107]\n"
    "v_mfma_f32_16x16x32_bf16 v[104:107], v[156:159], v[208:211], v[104:107]\n"
    "v_mfma_f32_16x16x32_bf16 v[104:107], v[160:163], v[224:227], v[104:107]\n"
    "s_mov_b32 s33, 161792\n"
    "buffer_load_dwordx4 a[76:79], v232, s[60:63], s33 offen sc1\n"
    "v_mfma_f32_16x16x32_bf16 v[108:111], v[160:163], v[228:231], v[108:111]\n"
    "v_mfma_f32_16x16x32_bf16 v[108:111], v[156:159], v[212:215], v[108:111]\n"
    "v_mfma_f32_16x16x32_bf16 v[108:111], v[152:155], a[252:255], v[108:111]\n"
    "s_waitcnt lgkmcnt(3)\n"
    "v_mfma_f32_16x16x32_bf16 v[124:127], v[164:167], a[156:159], v[124:127]\n"
    "s_add_u32 m0, s52, 114688\n"
    "ds_read_b128 v[140:143], v233 offset:25600\n"
    "ds_read_b128 v[144:147], v233 offset:29696\n"
    "v_mfma_f32_16x16x32_bf16 v[124:127], v[168:171], a[172:175], v[124:127]\n"
    "s_add_u32 s53, s52, 77824\n"
    "buffer_load_dwordx4 v232, s[48:51], s53 offen offset:0 sc1 lds\n"
    "v_mfma_f32_16x16x32_bf16 v[124:127], v[172:175], a[188:191], v[124:127]\n"
    "ds_read_b128 v[148:151], v233 offset:33792\n"
    "v_mfma_f32_16x16x32_bf16 v[120:123], v[172:175], a[184:187], v[120:123]\n"
    "buffer_load_dwordx4 v232, s[48:51], s53 offen offset:1024 sc1 lds\n"
    "v_mfma_f32_16x16x32_bf16 v[120:123], v[168:171], a[168:171], v[120:123]\n"
    "v_mfma_f32_16x16x32_bf16 v[120:123], v[164:167], a[152:155], v[120:123]\n"
    "v_mfma_f32_16x16x32_bf16 v[116:119], v[164:167], a[148:151], v[116:119]\n"
    "s_mov_b32 s33, 171008\n"
    "buffer_load_dwordx4 a[84:87], v232, s[60:63], s33 offen sc1\n"
    "v_mfma_f32_16x16x32_bf16 v[116:119], v[168:171], a[164:167], v[116:119]\n"
    "v_mfma_f32_16x16x32_bf16 v[116:119], v[172:175], a[180:183], v[116:119]\n"
    "v_mfma_f32_16x16x32_bf16 v[112:115], v[172:175], a[176:179], v[112:115]\n"
    "s_mov_b32 s33, 180224\n"
    "buffer_load_dwordx4 a[92:95], v232, s[60:63], s33 offen sc1\n"
    "v_mfma_f32_16x16x32_bf16 v[112:115], v[168:171], a[160:163], v[112:115]\n"
    "v_mfma_f32_16x16x32_bf16 v[112:115], v[164:167], a[144:147], v[112:115]\n"
    "s_waitcnt lgkmcnt(3)\n"
    "v_mfma_f32_16x16x32_bf16 v[112:115], v[128:131], a[192:195], v[112:115]\n"
    "ds_read_b128 v[152:155], v233 offset:2048\n"
    "ds_read_b128 v[156:159], v233 offset:6144\n"
    "v_mfma_f32_16x16x32_bf16 v[112:115], v[132:135], a[208:211], v[112:115]\n"
    "buffer_load_dwordx4 v232, s[48:51], s53 offen offset:2048 sc1 lds\n"
    "v_mfma_f32_16x16x32_bf16 v[112:115], v[136:139], a[224:227], v[112:115]\n"
    "ds_read_b128 v[160:163], v233 offset:10240\n"
    "v_mfma_f32_16x16x32_bf16 v[116:119], v[136:139], a[228:231], v[116:119]\n"
    "s_waitcnt vmcnt(16)\n"
    "v_cmp_gt_u32_e32 vcc, 2, v238\n"
    "s_nop 0\n"
    "v_mfma_f32_16x16x32_bf16 v[116:119], v[132:135], a[212:215], v[116:119]\n"
    "s_cbranch_vccnz L_spn47_mk\n"
    "L_spb48_mk:\n"
    "s_mov_b32 s33, 1024\n"
    "v_mfma_f32_16x16x32_bf16 v[116:119], v[128:131], a[196:199], v[116:119]\n"
    "buffer_load_dwordx4 a[96:99], v232, s[60:63], s33 offen sc1\n"
    "v_mfma_f32_16x16x32_bf16 v[120:123], v[128:131], a[200:203], v[120:123]\n"
    "v_mfma_f32_16x16x32_bf16 v[120:123], v[132:135], a[216:219], v[120:123]\n"
    "v_mfma_f32_16x16x32_bf16 v[120:123], v[136:139], a[232:235], v[120:123]\n"
    "s_mov_b32 s33, 10240\n"
    "buffer_load_dwordx4 a[104:107], v232, s[60:63], s33 offen sc1\n"
    "v_mfma_f32_16x16x32_bf16 v[124:127], v[136:139], a[236:239], v[124:127]\n"
    "v_mfma_f32_16x16x32_bf16 v[124:127], v[132:135], a[220:223], v[124:127]\n"
    "v_mfma_f32_16x16x32_bf16 v[124:127], v[128:131], a[204:207], v[124:127]\n"
    "s_waitcnt lgkmcnt(3)\n"
    "v_mfma_f32_16x16x32_bf16 v[124:127], v[140:143], a[252:255], v[124:127]\n"
    "ds_read_b128 v[164:167], v233 offset:3072\n"
    "ds_read_b128 v[168:171], v233 offset:7168\n"
    "v_mfma_f32_16x16x32_bf16 v[124:127], v[144:147], v[212:215], v[124:127]\n"
    "buffer_load_dwordx4 v232, s[48:51], s53 offen offset:3072 sc1 lds\n"
    "v_mfma_f32_16x16x32_bf16 v[124:127], v[148:151], v[228:231], v[124:127]\n"
    "ds_read_b128 v[172:175], v233 offset:11264\n"
    "v_mfma_f32_16x16x32_bf16 v[120:123], v[148:151], v[224:227], v[120:123]\n"
    "v_mfma_f32_16x16x32_bf16 v[120:123], v[144:147], v[208:211], v[120:123]\n"
    "s_mov_b32 s33, 19456\n"
    "buffer_load_dwordx4 a[112:115], v232, s[60:63], s33 offen sc1\n"
    "v_mfma_f32_16x16x32_bf16 v[120:123], v[140:143], a[248:251], v[120:123]\n"
    "v_mfma_f32_16x16x32_bf16 v[116:119], v[140:143], a[244:247], v[116:119]\n"
    "v_mfma_f32_16x16x32_bf16 v[116:119], v[144:147], v[204:207], v[116:119]\n"
    "v_mfma_f32_16x16x32_bf16 v[116:119], v[148:151], v[220:223], v[116:119]\n"
    "s_mov_b32 s33, 28672\n"
    "buffer_load_dwordx4 a[120:123], v232, s[60:63], s33 offen sc1\n"
    "v_mfma_f32_16x16x32_bf16 v[112:115], v[148:151], v[216:219], v[112:115]\n"
    "v_mfma_f32_16x16x32_bf16 v[112:115], v[144:147], v[200:203], v[112:115]\n"
    "v_mfma_f32_16x16x32_bf16 v[112:115], v[140:143], a[240:243], v[112:115]\n"
    "s_waitcnt lgkmcnt(3)\n"
    "v_mfma_f32_16x16x32_bf16 v[0:3], v[152:155], a[144:147], v[0:3]\n"
    "s_add_u32 m0, s52, 118784\n"
    "ds_read_b128 v[128:131], v233 offset:14336\n"
    "ds_read_b128 v[132:135], v233 offset:18432\n"
    "v_mfma_f32_16x16x32_bf16 v[0:3], v[156:159], a[160:163], v[0:3]\n"
    "s_add_u32 s53, s52, 81920\n"
    "buffer_load_dwordx4 v232, s[48:51], s53 offen offset:0 sc1 lds\n"
    "v_mfma_f32_16x16x32_bf16 v[0:3], v[160:163], a[176:179], v[0:3]\n"
    "ds_read_b128 v[136:139], v233 offset:22528\n"
    "v_mfma_f32_16x16x32_bf16 v[4:7], v[160:163], a[180:183], v[4:7]\n"
    "v_mfma_f32_16x16x32_bf16 v[4:7], v[156:159], a[164:167], v[4:7]\n"
    "s_mov_b32 s33, 148480\n"
    "buffer_load_dwordx4 a[100:103], v232, s[60:63], s33 offen sc1\n"
    "v_mfma_f32_16x16x32_bf16 v[4:7], v[152:155], a[148:151], v[4:7]\n"
    "v_mfma_f32_16x16x32_bf16 v[8:11], v[152:155], a[152:155], v[8:11]\n"
    "v_mfma_f32_16x16x32_bf16 v[8:11], v[156:159], a[168:171], v[8:11]\n"
    "v_mfma_f32_16x16x32_bf16 v[8:11], v[160:163], a[184:187], v[8:11]\n"
    "s_mov_b32 s33, 157696\n"
    "buffer_load_dwordx4 a[108:111], v232, s[60:63], s33 offen sc1\n"
    "v_mfma_f32_16x16x32_bf16 v[12:15], v[160:163], a[188:191], v[12:15]\n"
    "v_mfma_f32_16x16x32_bf16 v[12:15], v[156:159], a[172:175], v[12:15]\n"
    "v_mfma_f32_16x16x32_bf16 v[12:15], v[152:155], a[156:159], v[12:15]\n"
    "s_waitcnt lgkmcnt(3)\n"
    "v_mfma_f32_16x16x32_bf16 v[28:31], v[164:167], a[156:159], v[28:31]\n"
    "ds_read_b128 v[140:143], v233 offset:15360\n"
    "ds_read_b128 v[144:147], v233 offset:19456\n"
    "v_mfma_f32_16x16x32_bf16 v[28:31], v[168:171], a[172:175], v[28:31]\n"
    "s_mov_b32 s33, 166912\n"
    "buffer_load_dwordx4 a[116:119], v232, s[60:63], s33 offen sc1\n"
    "v_mfma_f32_16x16x32_bf16 v[28:31], v[172:175], a[188:191], v[28:31]\n"
    "ds_read_b128 v[148:151], v233 offset:23552\n"
    "v_mfma_f32_16x16x32_bf16 v[24:27], v[172:175], a[184:187], v[24:27]\n"
    "v_exp_f32_e64 v192, -v96\n"
    "v_mfma_f32_16x16x32_bf16 v[24:27], v[168:171], a[168:171], v[24:27]\n"
    "v_exp_f32_e64 v193, -v97\n"
    "v_mfma_f32_16x16x32_bf16 v[24:27], v[164:167], a[152:155], v[24:27]\n"
    "v_mul_f32_e32 v96, v96, v112\n"
    "v_add_f32_e32 v192, 1.0, v192\n"
    "v_mfma_f32_16x16x32_bf16 v[20:23], v[164:167], a[148:151], v[20:23]\n"
    "s_mov_b32 s33, 176128\n"
    "buffer_load_dwordx4 a[124:127], v232, s[60:63], s33 offen sc1\n"
    "v_mfma_f32_16x16x32_bf16 v[20:23], v[168:171], a[164:167], v[20:23]\n"
    "v_rcp_f32_e32 v192, v192\n"
    "v_mfma_f32_16x16x32_bf16 v[20:23], v[172:175], a[180:183], v[20:23]\n"
    "v_exp_f32_e64 v194, -v98\n"
    "v_mfma_f32_16x16x32_bf16 v[16:19], v[172:175], a[176:179], v[16:19]\n"
    "v_mul_f32_e32 v97, v97, v113\n"
    "v_add_f32_e32 v193, 1.0, v193\n"
    "v_mfma_f32_16x16x32_bf16 v[16:19], v[168:171], a[160:163], v[16:19]\n"
    "v_rcp_f32_e32 v193, v193\n"
    "v_mfma_f32_16x16x32_bf16 v[16:19], v[164:167], a[144:147], v[16:19]\n"
    "s_waitcnt lgkmcnt(3)\n"
    "v_mul_f32_e32 v192, v192, v96\n"
    "v_mfma_f32_16x16x32_bf16 v[0:3], v[128:131], a[192:195], v[0:3]\n"
    "ds_read_b128 v[152:155], v233 offset:26624\n"
    "ds_read_b128 v[156:159], v233 offset:30720\n"
    "v_mfma_f32_16x16x32_bf16 v[0:3], v[132:135], a[208:211], v[0:3]\n"
    "s_mov_b32 s33, 6144\n"
    "buffer_load_dwordx4 a[128:131], v232, s[60:63], s33 offen sc1\n"
    "v_mfma_f32_16x16x32_bf16 v[0:3], v[136:139], a[224:227], v[0:3]\n"
    "ds_read_b128 v[160:163], v233 offset:34816\n"
    "v_mfma_f32_16x16x32_bf16 v[4:7], v[136:139], a[228:231], v[4:7]\n"
    "v_exp_f32_e64 v195, -v99\n"
    "v_mfma_f32_16x16x32_bf16 v[4:7], v[132:135], a[212:215], v[4:7]\n"
    "v_mul_f32_e32 v98, v98, v114\n"
    "v_add_f32_e32 v194, 1.0, v194\n"
    "v_mfma_f32_16x16x32_bf16 v[4:7], v[128:131], a[196:199], v[4:7]\n"
    "v_rcp_f32_e32 v194, v194\n"
    "v_mfma_f32_16x16x32_bf16 v[8:11], v[128:131], a[200:203], v[8:11]\n"
    "s_mov_b32 s33, 15360\n"
    "buffer_load_dwordx4 a[136:139], v232, s[60:63], s33 offen sc1\n"
    "v_mfma_f32_16x16x32_bf16 v[8:11], v[132:135], a[216:219], v[8:11]\n"
    "v_mul_f32_e32 v193, v193, v97\n"
    "v_mfma_f32_16x16x32_bf16 v[8:11], v[136:139], a[232:235], v[8:11]\n"
    "v_exp_f32_e64 v196, -v100\n"
    "v_mfma_f32_16x16x32_bf16 v[12:15], v[136:139], a[236:239], v[12:15]\n"
    "v_mul_f32_e32 v99, v99, v115\n"
    "v_add_f32_e32 v195, 1.0, v195\n"
    "v_mfma_f32_16x16x32_bf16 v[12:15], v[132:135], a[220:223], v[12:15]\n"
    "v_rcp_f32_e32 v195, v195\n"
    "v_mfma_f32_16x16x32_bf16 v[12:15], v[128:131], a[204:207], v[12:15]\n"
    "s_waitcnt lgkmcnt(3)\n"
    "v_mul_f32_e32 v194, v194, v98\n"
    "v_cvt_pk_bf16_f32 v176, v192, v193\n"
    "v_mfma_f32_16x16x32_bf16 v[28:31], v[140:143], a[204:207], v[28:31]\n"
    "ds_read_b128 v[164:167], v233 offset:27648\n"
    "ds_read_b128 v[168:171], v233 offset:31744\n"
    "v_mfma_f32_16x16x32_bf16 v[28:31], v[144:147], a[220:223], v[28:31]\n"
    "s_mov_b32 s33, 24576\n"
    "buffer_load_dwordx4 a[144:147], v232, s[60:63], s33 offen sc1\n"
    "v_mfma_f32_16x16x32_bf16 v[28:31], v[148:151], a[236:239], v[28:31]\n"
    "ds_read_b128 v[172:175], v233 offset:35840\n"
    "v_mfma_f32_16x16x32_bf16 v[24:27], v[148:151], a[232:235], v[24:27]\n"
    "v_exp_f32_e64 v197, -v101\n"
    "v_mfma_f32_16x16x32_bf16 v[24:27], v[144:147], a[216:219], v[24:27]\n"
    "v_mul_f32_e32 v100, v100, v116\n"
    "v_add_f32_e32 v196, 1.0, v196\n"
    "v_mfma_f32_16x16x32_bf16 v[24:27], v[140:143], a[200:203], v[24:27]\n"
    "v_rcp_f32_e32 v196, v196\n"
    "v_mfma_f32_16x16x32_bf16 v[20:23], v[140:143], a[196:199], v[20:23]\n"
    "s_mov_b32 s33, 33792\n"
    "buffer_load_dwordx4 a[152:155], v232, s[60:63], s33 offen sc1\n"
    "v_mfma_f32_16x16x32_bf16 v[20:23], v[144:147], a[212:215], v[20:23]\n"
    "v_mul_f32_e32 v195, v195, v99\n"
    "v_mfma_f32_16x16x32_bf16 v[20:23], v[148:151], a[228:231], v[20:23]\n"
    "v_exp_f32_e64 v192, -v102\n"
    "v_mfma_f32_16x16x32_bf16 v[16:19], v[148:151], a[224:227], v[16:19]\n"
    "v_mul_f32_e32 v101, v101, v117\n"
    "v_add_f32_e32 v197, 1.0, v197\n"
    "v_mfma_f32_16x16x32_bf16 v[16:19], v[144:147], a[208:211], v[16:19]\n"
    "v_rcp_f32_e32 v197, v197\n"
    "v_mfma_f32_16x16x32_bf16 v[16:19], v[140:143], a[192:195], v[16:19]\n"
    "s_waitcnt vmcnt(32) lgkmcnt(0)\n"
    "v_mul_f32_e32 v196, v196, v100\n"
    "v_cvt_pk_bf16_f32 v177, v194, v195\n"
    "v_mfma_f32_16x16x32_bf16 v[0:3], v[152:155], a[240:243], v[0:3]\n"
    "s_barrier\n"
    "v_mfma_f32_16x16x32_bf16 v[0:3], v[156:159], v[200:203], v[0:3]\n"
    "s_mov_b32 s33, 153600\n"
    "buffer_load_dwordx4 a[132:135], v232, s[60:63], s33 offen sc1\n"
    "v_mfma_f32_16x16x32_bf16 v[0:3], v[160:163], v[216:219], v[0:3]\n"
    "v_mov_b32_e32 v253, 3\n"
    "v_mfma_f32_16x16x32_bf16 v[4:7], v[160:163], v[220:223], v[4:7]\n"
    "buffer_store_dword v253, v252, s[96:99], s39 offen sc1\n"
    "v_mfma_f32_16x16x32_bf16 v[4:7], v[156:159], v[204:207], v[4:7]\n"
    "v_exp_f32_e64 v193, -v103\n"
    "v_mfma_f32_16x16x32_bf16 v[4:7], v[152:155], a[244:247], v[4:7]\n"
    "v_mul_f32_e32 v102, v102, v118\n"
    "v_add_f32_e32 v192, 1.0, v192\n"
    "v_mfma_f32_16x16x32_bf16 v[8:11], v[152:155], a[248:251], v[8:11]\n"
    "s_mov_b32 s33, 162816\n"
    "buffer_load_dwordx4 a[140:143], v232, s[60:63], s33 offen sc1\n"
    "v_mfma_f32_16x16x32_bf16 v[8:11], v[156:159], v[208:211], v[8:11]\n"
    "v_rcp_f32_e32 v192, v192\n"
    "v_mfma_f32_16x16x32_bf16 v[8:11], v[160:163], v[224:227], v[8:11]\n"
    "v_mul_f32_e32 v197, v197, v101\n"
    "v_mfma_f32_16x16x32_bf16 v[12:15], v[160:163], v[228:231], v[12:15]\n"
    "v_exp_f32_e64 v194, -v104\n"
    "v_mfma_f32_16x16x32_bf16 v[12:15], v[156:159], v[212:215], v[12:15]\n"
    "v_mul_f32_e32 v103, v103, v119\n"
    "v_add_f32_e32 v193, 1.0, v193\n"
    "v_mfma_f32_16x16x32_bf16 v[12:15], v[152:155], a[252:255], v[12:15]\n"
    "v_rcp_f32_e32 v193, v193\n"
    "v_mfma_f32_16x16x32_bf16 v[28:31], v[164:167], a[252:255], v[28:31]\n"
    "v_mul_f32_e32 v192, v192, v102\n"
    "v_cvt_pk_bf16_f32 v180, v196, v197\n"
    "v_mfma_f32_16x16x32_bf16 v[28:31], v[168:171], v[212:215], v[28:31]\n"
    "s_mov_b32 s33, 172032\n"
    "buffer_load_dwordx4 a[148:151], v232, s[60:63], s33 offen sc1\n"
    "v_mfma_f32_16x16x32_bf16 v[28:31], v[172:175], v[228:231], v[28:31]\n"
    "v_exp_f32_e64 v195, -v105\n"
    "v_mfma_f32_16x16x32_bf16 v[24:27], v[172:175], v[224:227], v[24:27]\n"
    "v_mul_f32_e32 v104, v104, v120\n"
    "v_add_f32_e32 v194, 1.0, v194\n"
    "v_mfma_f32_16x16x32_bf16 v[24:27], v[168:171], v[208:211], v[24:27]\n"
    "v_rcp_f32_e32 v194, v194\n"
    "v_mfma_f32_16x16x32_bf16 v[24:27], v[164:167], a[248:251], v[24:27]\n"
    "v_mul_f32_e32 v193, v193, v103\n"
    "v_mfma_f32_16x16x32_bf16 v[20:23], v[164:167], a[244:247], v[20:23]\n"
    "s_mov_b32 s33, 181248\n"
    "buffer_load_dwordx4 a[156:159], v232, s[60:63], s33 offen sc1\n"
    "v_mfma_f32_16x16x32_bf16 v[20:23], v[168:171], v[204:207], v[20:23]\n"
    "v_exp_f32_e64 v196, -v106\n"
    "v_mfma_f32_16x16x32_bf16 v[20:23], v[172:175], v[220:223], v[20:23]\n"
    "v_mul_f32_e32 v105, v105, v121\n"
    "v_add_f32_e32 v195, 1.0, v195\n"
    "v_mfma_f32_16x16x32_bf16 v[16:19], v[172:175], v[216:219], v[16:19]\n"
    "v_rcp_f32_e32 v195, v195\n"
    "v_mfma_f32_16x16x32_bf16 v[16:19], v[168:171], v[200:203], v[16:19]\n"
    "v_mul_f32_e32 v194, v194, v104\n"
    "v_cvt_pk_bf16_f32 v181, v192, v193\n"
    "v_mfma_f32_16x16x32_bf16 v[16:19], v[164:167], a[240:243], v[16:19]\n"
    "v_exp_f32_e64 v197, -v107\n"
    "s_lshl_b32 s40, s56, 2\n"
    "s_add_u32 s42, s40, s88\n"
    "v_bfe_u32 v236, v232, 4, 4\n"
    "v_mul_u32_u24_e32 v236, 2304, v236\n"
    "v_lshrrev_b32_e32 v237, 8, v232\n"
    "v_lshl_add_u32 v236, v237, 4, v236\n"
    "v_mul_f32_e32 v106, v106, v122\n"
    "v_add_f32_e32 v196, 1.0, v196\n"
    "v_rcp_f32_e32 v196, v196\n"
    "v_mul_f32_e32 v195, v195, v105\n"
    "v_exp_f32_e64 v192, -v108\n"
    "v_mul_f32_e32 v107, v107, v123\n"
    "v_add_f32_e32 v197, 1.0, v197\n"
    "v_rcp_f32_e32 v197, v197\n"
    "v_mul_f32_e32 v196, v196, v106\n"
    "v_cvt_pk_bf16_f32 v184, v194, v195\n"
    "v_exp_f32_e64 v193, -v109\n"
    "v_mul_f32_e32 v108, v108, v124\n"
    "v_add_f32_e32 v192, 1.0, v192\n"
    "v_rcp_f32_e32 v192, v192\n"
    "v_mul_f32_e32 v197, v197, v107\n"
    "v_exp_f32_e64 v194, -v110\n"
    "v_mul_f32_e32 v109, v109, v125\n"
    "v_add_f32_e32 v193, 1.0, v193\n"
    "v_rcp_f32_e32 v193, v193\n"
    "v_mul_f32_e32 v192, v192, v108\n"
    "v_cvt_pk_bf16_f32 v185, v196, v197\n"
    "v_exp_f32_e64 v195, -v111\n"
    "v_mul_f32_e32 v110, v110, v126\n"
    "v_add_f32_e32 v194, 1.0, v194\n"
    "v_rcp_f32_e32 v194, v194\n"
    "v_mul_f32_e32 v193, v193, v109\n"
    "v_mul_f32_e32 v111, v111, v127\n"
    "v_add_f32_e32 v195, 1.0, v195\n"
    "v_rcp_f32_e32 v195, v195\n"
    "v_mul_f32_e32 v194, v194, v110\n"
    "v_cvt_pk_bf16_f32 v188, v192, v193\n"
    "s_nop 0\n"
    "v_mul_f32_e32 v195, v195, v111\n"
    "v_cvt_pk_bf16_f32 v189, v194, v195\n"
    "v_exp_f32_e64 v192, -v0\n"
    "v_exp_f32_e64 v193, -v1\n"
    "v_mul_f32_e32 v0, v0, v16\n"
    "v_add_f32_e32 v192, 1.0, v192\n"
    "v_rcp_f32_e32 v192, v192\n"
    "v_exp_f32_e64 v194, -v2\n"
    "ds_read_b128 v[128:131], v233 offset:36864\n"
    "ds_read_b128 v[132:135], v233 offset:37888\n"
    "ds_read_b128 v[136:139], v233 offset:38912\n"
    "ds_read_b128 v[140:143], v233 offset:39936\n"
    "ds_read_b128 v[144:147], v233 offset:40960\n"
    "ds_read_b128 v[148:151], v233 offset:41984\n"
    "ds_read_b128 v[152:155], v233 offset:43008\n"
    "ds_read_b128 v[156:159], v233 offset:44032\n"
    "s_waitcnt vmcnt(45) lgkmcnt(4)\n"
    "v_mfma_f32_16x16x32_bf16 v[32:35], v[128:131], a[0:3], 0\n"
    "ds_read_b128 v[160:163], v233 offset:45056\n"
    "ds_read_b128 v[164:167], v233 offset:46080\n"
    "v_mfma_f32_16x16x32_bf16 v[32:35], v[132:135], a[8:11], v[32:35]\n"
    "v_mul_f32_e32 v1, v1, v17\n"
    "v_add_f32_e32 v193, 1.0, v193\n"
    "v_mfma_f32_16x16x32_bf16 v[32:35], v[136:139], a[16:19], v[32:35]\n"
    "ds_read_b128 v[168:171], v233 offset:47104\n"
    "ds_read_b128 v[172:175], v233 offset:48128\n"
    "v_mfma_f32_16x16x32_bf16 v[32:35], v[140:143], a[24:27], v[32:35]\n"
    "v_rcp_f32_e32 v193, v193\n"
    "v_mfma_f32_16x16x32_bf16 v[36:39], v[140:143], a[28:31], 0\n"
    "v_mul_f32_e32 v192, v192, v0\n"
    "v_mfma_f32_16x16x32_bf16 v[36:39], v[136:139], a[20:23], v[36:39]\n"
    "v_exp_f32_e64 v195, -v3\n"
    "v_mfma_f32_16x16x32_bf16 v[36:39], v[132:135], a[12:15], v[36:39]\n"
    "v_mul_f32_e32 v2, v2, v18\n"
    "v_add_f32_e32 v194, 1.0, v194\n"
    "v_mfma_f32_16x16x32_bf16 v[36:39], v[128:131], a[4:7], v[36:39]\n"
    "s_waitcnt lgkmcnt(4)\n"
    "v_rcp_f32_e32 v194, v194\n"
    "v_mfma_f32_16x16x32_bf16 v[44:47], v[144:147], a[4:7], 0\n"
    "ds_read_b128 v[128:131], v233 offset:49152\n"
    "ds_read_b128 v[132:135], v233 offset:50176\n"
    "v_mfma_f32_16x16x32_bf16 v[44:47], v[148:151], a[12:15], v[44:47]\n"
    "v_mul_f32_e32 v193, v193, v1\n"
    "v_mfma_f32_16x16x32_bf16 v[44:47], v[152:155], a[20:23], v[44:47]\n"
    "ds_read_b128 v[136:139], v233 offset:51200\n"
    "ds_read_b128 v[140:143], v233 offset:52224\n"
    "v_mfma_f32_16x16x32_bf16 v[44:47], v[156:159], a[28:31], v[44:47]\n"
    "v_exp_f32_e64 v196, -v4\n"
    "v_mfma_f32_16x16x32_bf16 v[40:43], v[156:159], a[24:27], 0\n"
    "v_mul_f32_e32 v3, v3, v19\n"
    "v_add_f32_e32 v195, 1.0, v195\n"
    "v_mfma_f32_16x16x32_bf16 v[40:43], v[152:155], a[16:19], v[40:43]\n"
    "v_rcp_f32_e32 v195, v195\n"
    "v_mfma_f32_16x16x32_bf16 v[40:43], v[148:151], a[8:11], v[40:43]\n"
    "v_mul_f32_e32 v194, v194, v2\n"
    "v_cvt_pk_bf16_f32 v178, v192, v193\n"
    "v_mfma_f32_16x16x32_bf16 v[40:43], v[144:147], a[0:3], v[40:43]\n"
    "s_waitcnt lgkmcnt(4)\n"
    "v_exp_f32_e64 v197, -v5\n"
    "v_mfma_f32_16x16x32_bf16 v[48:51], v[160:163], a[0:3], 0\n"
    "ds_read_b128 v[144:147], v233 offset:53248\n"
    "ds_read_b128 v[148:151], v233 offset:54272\n"
    "v_mfma_f32_16x16x32_bf16 v[48:51], v[164:167], a[8:11], v[48:51]\n"
    "v_mul_f32_e32 v4, v4, v20\n"
    "v_add_f32_e32 v196, 1.0, v196\n"
    "v_mfma_f32_16x16x32_bf16 v[48:51], v[168:171], a[16:19], v[48:51]\n"
    "ds_read_b128 v[152:155], v233 offset:55296\n"
    "ds_read_b128 v[156:159], v233 offset:56320\n"
    "v_mfma_f32_16x16x32_bf16 v[48:51], v[172:175], a[24:27], v[48:51]\n"
    "v_rcp_f32_e32 v196, v196\n"
    "v_mfma_f32_16x16x32_bf16 v[52:55], v[172:175], a[28:31], 0\n"
    "v_mul_f32_e32 v195, v195, v3\n"
    "v_mfma_f32_16x16x32_bf16 v[52:55], v[168:171], a[20:23], v[52:55]\n"
    "v_exp_f32_e64 v192, -v6\n"
    "v_mfma_f32_16x16x32_bf16 v[52:55], v[164:167], a[12:15], v[52:55]\n"
    "v_mul_f32_e32 v5, v5, v21\n"
    "v_add_f32_e32 v197, 1.0, v197\n"
    "v_mfma_f32_16x16x32_bf16 v[52:55], v[160:163], a[4:7], v[52:55]\n"
    "s_waitcnt lgkmcnt(4)\n"
    "v_rcp_f32_e32 v197, v197\n"
    "v_mfma_f32_16x16x32_bf16 v[60:63], v[128:131], a[4:7], 0\n"
    "s_add_u32 m0, s52, 0\n"
    "ds_read_b128 v[160:163], v233 offset:57344\n"
    "ds_read_b128 v[164:167], v233 offset:58368\n"
    "v_mfma_f32_16x16x32_bf16 v[60:63], v[132:135], a[12:15], v[60:63]\n"
    "s_add_u32 s53, s52, 110592\n"
    "buffer_load_dwordx4 v232, s[48:51], s53 offen offset:0 sc1 lds\n"
    "v_mfma_f32_16x16x32_bf16 v[60:63], v[136:139], a[20:23], v[60:63]\n"
    "ds_read_b128 v[168:171], v233 offset:59392\n"
    "ds_read_b128 v[172:175], v233 offset:60416\n"
    "v_mfma_f32_16x16x32_bf16 v[60:63], v[140:143], a[28:31], v[60:63]\n"
    "v_mul_f32_e32 v196, v196, v4\n"
    "v_cvt_pk_bf16_f32 v179, v194, v195\n"
    "v_mfma_f32_16x16x32_bf16 v[56:59], v[140:143], a[24:27], 0\n"
    "s_add_u32 s34, s36, 0\n"
    "buffer_store_dwordx4 v[176:179], v232, s[28:31], s34 offen offset:3072 sc1\n"
    "v_mfma_f32_16x16x32_bf16 v[56:59], v[136:139], a[16:19], v[56:59]\n"
    "buffer_load_dwordx4 v232, s[48:51], s53 offen offset:1024 sc1 lds\n"
    "v_mfma_f32_16x16x32_bf16 v[56:59], v[132:135], a[8:11], v[56:59]\n"
    "v_exp_f32_e64 v193, -v7\n"
    "v_mfma_f32_16x16x32_bf16 v[56:59], v[128:131], a[0:3], v[56:59]\n"
    "s_waitcnt lgkmcnt(4)\n"
    "v_mul_f32_e32 v6, v6, v22\n"
    "v_add_f32_e32 v192, 1.0, v192\n"
    "v_mfma_f32_16x16x32_bf16 v[64:67], v[144:147], a[0:3], 0\n"
    "ds_read_b128 v[128:131], v233 offset:61440\n"
    "ds_read_b128 v[132:135], v233 offset:62464\n"
    "v_mfma_f32_16x16x32_bf16 v[64:67], v[148:151], a[8:11], v[64:67]\n"
    "buffer_load_dwordx4 v232, s[48:51], s53 offen offset:2048 sc1 lds\n"
    "v_mfma_f32_16x16x32_bf16 v[64:67], v[152:155], a[16:19], v[64:67]\n"
    "ds_read_b128 v[136:139], v233 offset:63488\n"
    "ds_read_b128 v[140:143], v233 offset:64512\n"
    "v_mfma_f32_16x16x32_bf16 v[64:67], v[156:159], a[24:27], v[64:67]\n"
    "v_rcp_f32_e32 v192, v192\n"
    "v_mfma_f32_16x16x32_bf16 v[68:71], v[156:159], a[28:31], 0\n"
    "v_mul_f32_e32 v197, v197, v5\n"
    "v_mfma_f32_16x16x32_bf16 v[68:71], v[152:155], a[20:23], v[68:71]\n"
    "buffer_load_dwordx4 v232, s[48:51], s53 offen offset:3072 sc1 lds\n"
    "v_mfma_f32_16x16x32_bf16 v[68:71], v[148:151], a[12:15], v[68:71]\n"
    "v_exp_f32_e64 v194, -v8\n"
    "v_mfma_f32_16x16x32_bf16 v[68:71], v[144:147], a[4:7], v[68:71]\n"
    "s_waitcnt lgkmcnt(4)\n"
    "v_mul_f32_e32 v7, v7, v23\n"
    "v_add_f32_e32 v193, 1.0, v193\n"
    "v_mfma_f32_16x16x32_bf16 v[76:79], v[160:163], a[4:7], 0\n"
    "s_add_u32 m0, s52, 4096\n"
    "ds_read_b128 v[144:147], v234 offset:0\n"
    "ds_read_b128 v[148:151], v234 offset:1024\n"
    "v_mfma_f32_16x16x32_bf16 v[76:79], v[164:167], a[12:15], v[76:79]\n"
    "s_add_u32 s53, s52, 114688\n"
    "buffer_load_dwordx4 v232, s[48:51], s53 offen offset:0 sc1 lds\n"
    "v_mfma_f32_16x16x32_bf16 v[76:79], v[168:171], a[20:23], v[76:79]\n"
    "ds_read_b128 v[152:155], v234 offset:2048\n"
    "ds_read_b128 v[156:159], v234 offset:3072\n"
    "v_mfma_f32_16x16x32_bf16 v[76:79], v[172:175], a[28:31], v[76:79]\n"
    "v_rcp_f32_e32 v193, v193\n"
    "v_mfma_f32_16x16x32_bf16 v[72:75], v[172:175], a[24:27], 0\n"
    "v_mul_f32_e32 v192, v192, v6\n"
    "v_cvt_pk_bf16_f32 v182, v196, v197\n"
    "v_mfma_f32_16x16x32_bf16 v[72:75], v[168:171], a[16:19], v[72:75]\n"
    "v_exp_f32_e64 v195, -v9\n"
    "v_mfma_f32_16x16x32_bf16 v[72:75], v[164:167], a[8:11], v[72:75]\n"
    "v_mul_f32_e32 v8, v8, v24\n"
    "v_add_f32_e32 v194, 1.0, v194\n"
    "v_mfma_f32_16x16x32_bf16 v[72:75], v[160:163], a[0:3], v[72:75]\n"
    "s_waitcnt lgkmcnt(4)\n"
    "v_rcp_f32_e32 v194, v194\n"
    "v_mfma_f32_16x16x32_bf16 v[80:83], v[128:131], a[0:3], 0\n"
    "ds_read_b128 v[160:163], v234 offset:4096\n"
    "ds_read_b128 v[164:167], v234 offset:5120\n"
    "v_mfma_f32_16x16x32_bf16 v[80:83], v[132:135], a[8:11], v[80:83]\n"
    "buffer_load_dwordx4 v232, s[48:51], s53 offen offset:1024 sc1 lds\n"
    "v_mfma_f32_16x16x32_bf16 v[80:83], v[136:139], a[16:19], v[80:83]\n"
    "ds_read_b128 v[168:171], v234 offset:6144\n"
    "ds_read_b128 v[172:175], v234 offset:7168\n"
    "v_mfma_f32_16x16x32_bf16 v[80:83], v[140:143], a[24:27], v[80:83]\n"
    "v_mul_f32_e32 v193, v193, v7\n"
    "v_mfma_f32_16x16x32_bf16 v[84:87], v[140:143], a[28:31], 0\n"
    "v_exp_f32_e64 v196, -v10\n"
    "v_mfma_f32_16x16x32_bf16 v[84:87], v[136:139], a[20:23], v[84:87]\n"
    "buffer_load_dwordx4 v232, s[48:51], s53 offen offset:2048 sc1 lds\n"
    "v_mfma_f32_16x16x32_bf16 v[84:87], v[132:135], a[12:15], v[84:87]\n"
    "v_mul_f32_e32 v9, v9, v25\n"
    "v_add_f32_e32 v195, 1.0, v195\n"
    "v_mfma_f32_16x16x32_bf16 v[84:87], v[128:131], a[4:7], v[84:87]\n"
    "s_waitcnt vmcnt(42) lgkmcnt(0)\n"
    "v_rcp_f32_e32 v195, v195\n"
    "v_mfma_f32_16x16x32_bf16 v[36:39], v[144:147], a[36:39], v[36:39]\n"
    "s_barrier\n"
    "v_mfma_f32_16x16x32_bf16 v[36:39], v[148:151], a[44:47], v[36:39]\n"
    "buffer_load_dwordx4 v232, s[48:51], s53 offen offset:3072 sc1 lds\n"
    "v_mfma_f32_16x16x32_bf16 v[36:39], v[152:155], a[52:55], v[36:39]\n"
    "ds_read_b128 v[128:131], v234 offset:8192\n"
    "ds_read_b128 v[132:135], v234 offset:9216\n"
    "v_mfma_f32_16x16x32_bf16 v[36:39], v[156:159], a[60:63], v[36:39]\n"
    "ds_read_b128 v[136:139], v234 offset:10240\n"
    "ds_read_b128 v[140:143], v234 offset:11264\n"
    "v_mfma_f32_16x16x32_bf16 v[32:35], v[156:159], a[56:59], v[32:35]\n"
    "s_add_u32 m0, s52, 8192\n"
    "v_mul_f32_e32 v194, v194, v8\n"
    "v_cvt_pk_bf16_f32 v183, v192, v193\n"
    "v_mfma_f32_16x16x32_bf16 v[32:35], v[152:155], a[48:51], v[32:35]\n"
    "s_add_u32 s53, s52, 118784\n"
    "buffer_load_dwordx4 v232, s[48:51], s53 offen offset:0 sc1 lds\n"
    "v_mfma_f32_16x16x32_bf16 v[32:35], v[148:151], a[40:43], v[32:35]\n"
    "s_add_u32 s34, s36, 147456\n"
    "buffer_store_dwordx4 v[180:183], v232, s[28:31], s34 offen offset:3072 sc1\n"
    "v_mfma_f32_16x16x32_bf16 v[32:35], v[144:147], a[32:35], v[32:35]\n"
    "v_exp_f32_e64 v197, -v11\n"
    "v_mfma_f32_16x16x32_bf16 v[40:43], v[160:163], a[32:35], v[40:43]\n"
    "s_add_u32 m0, s52, 36864\n"
    "ds_read_b128 v[144:147], v234 offset:12288\n"
    "ds_read_b128 v[148:151], v234 offset:13312\n"
    "v_mfma_f32_16x16x32_bf16 v[40:43], v[164:167], a[40:43], v[40:43]\n"
    "s_add_u32 s53, s52, 147456\n"
    "buffer_load_dwordx4 v232, s[48:51], s53 offen offset:0 sc1 lds\n"
    "v_mfma_f32_16x16x32_bf16 v[40:43], v[168:171], a[48:51], v[40:43]\n"
    "ds_read_b128 v[152:155], v234 offset:14336\n"
    "ds_read_b128 v[156:159], v234 offset:15360\n"
    "v_mfma_f32_16x16x32_bf16 v[40:43], v[172:175], a[56:59], v[40:43]\n"
    "v_mul_f32_e32 v10, v10, v26\n"
    "v_add_f32_e32 v196, 1.0, v196\n"
    "v_mfma_f32_16x16x32_bf16 v[44:47], v[172:175], a[60:63], v[44:47]\n"
    "v_rcp_f32_e32 v196, v196\n"
    "v_mfma_f32_16x16x32_bf16 v[44:47], v[168:171], a[52:55], v[44:47]\n"
    "v_mul_f32_e32 v195, v195, v9\n"
    "v_mfma_f32_16x16x32_bf16 v[44:47], v[164:167], a[44:47], v[44:47]\n"
    "v_exp_f32_e64 v192, -v12\n"
    "v_mfma_f32_16x16x32_bf16 v[44:47], v[160:163], a[36:39], v[44:47]\n"
    "s_waitcnt lgkmcnt(4)\n"
    "v_mul_f32_e32 v11, v11, v27\n"
    "v_add_f32_e32 v197, 1.0, v197\n"
    "v_mfma_f32_16x16x32_bf16 v[52:55], v[128:131], a[36:39], v[52:55]\n"
    "ds_read_b128 v[160:163], v234 offset:16384\n"
    "ds_read_b128 v[164:167], v234 offset:17408\n"
    "v_mfma_f32_16x16x32_bf16 v[52:55], v[132:135], a[44:47], v[52:55]\n"
    "buffer_load_dwordx4 v232, s[48:51], s53 offen offset:1024 sc1 lds\n"
    "v_mfma_f32_16x16x32_bf16 v[52:55], v[136:139], a[52:55], v[52:55]\n"
    "ds_read_b128 v[168:171], v234 offset:18432\n"
    "ds_read_b128 v[172:175], v234 offset:19456\n"
    "v_mfma_f32_16x16x32_bf16 v[52:55], v[140:143], a[60:63], v[52:55]\n"
    "v_rcp_f32_e32 v197, v197\n"
    "v_mfma_f32_16x16x32_bf16 v[48:51], v[140:143], a[56:59], v[48:51]\n"
    "v_mul_f32_e32 v196, v196, v10\n"
    "v_cvt_pk_bf16_f32 v186, v194, v195\n"
    "v_mfma_f32_16x16x32_bf16 v[48:51], v[136:139], a[48:51], v[48:51]\n"
    "buffer_load_dwordx4 v232, s[48:51], s53 offen offset:2048 sc1 lds\n"
    "v_mfma_f32_16x16x32_bf16 v[48:51], v[132:135], a[40:43], v[48:51]\n"
    "v_exp_f32_e64 v193, -v13\n"
    "v_mfma_f32_16x16x32_bf16 v[48:51], v[128:131], a[32:35], v[48:51]\n"
    "s_waitcnt lgkmcnt(4)\n"
    "v_mul_f32_e32 v12, v12, v28\n"
    "v_add_f32_e32 v192, 1.0, v192\n"
    "v_mfma_f32_16x16x32_bf16 v[56:59], v[144:147], a[32:35], v[56:59]\n"
    "ds_read_b128 v[128:131], v234 offset:20480\n"
    "ds_read_b128 v[132:135], v234 offset:21504\n"
    "v_mfma_f32_16x16x32_bf16 v[56:59], v[148:151], a[40:43], v[56:59]\n"
    "buffer_load_dwordx4 v232, s[48:51], s53 offen offset:3072 sc1 lds\n"
    "v_mfma_f32_16x16x32_bf16 v[56:59], v[152:155], a[48:51], v[56:59]\n"
    "ds_read_b128 v[136:139], v234 offset:22528\n"
    "ds_read_b128 v[140:143], v234 offset:23552\n"
    "v_mfma_f32_16x16x32_bf16 v[56:59], v[156:159], a[56:59], v[56:59]\n"
    "v_rcp_f32_e32 v192, v192\n"
    "v_mfma_f32_16x16x32_bf16 v[60:63], v[156:159], a[60:63], v[60:63]\n"
    "v_mul_f32_e32 v197, v197, v11\n"
    "v_mfma_f32_16x16x32_bf16 v[60:63], v[152:155], a[52:55], v[60:63]\n"
    "buffer_load_dword v238, v239, s[96:99], s35 offen sc1\n"
    "v_mfma_f32_16x16x32_bf16 v[60:63], v[148:151], a[44:47], v[60:63]\n"
    "v_exp_f32_e64 v194, -v14\n"
    "v_mfma_f32_16x16x32_bf16 v[60:63], v[144:147], a[36:39], v[60:63]\n"
    "s_waitcnt lgkmcnt(4)\n"
    "v_mul_f32_e32 v13, v13, v29\n"
    "v_add_f32_e32 v193, 1.0, v193\n"
    "v_mfma_f32_16x16x32_bf16 v[68:71], v[160:163], a[36:39], v[68:71]\n"
    "s_add_u32 m0, s52, 40960\n"
    "ds_read_b128 v[144:147], v234 offset:24576\n"
    "ds_read_b128 v[148:151], v234 offset:25600\n"
    "v_mfma_f32_16x16x32_bf16 v[68:71], v[164:167], a[44:47], v[68:71]\n"
    "s_add_u32 s53, s52, 151552\n"
    "buffer_load_dwordx4 v232, s[48:51], s53 offen offset:0 sc1 lds\n"
    "v_mfma_f32_16x16x32_bf16 v[68:71], v[168:171], a[52:55], v[68:71]\n"
    "ds_read_b128 v[152:155], v234 offset:26624\n"
    "ds_read_b128 v[156:159], v234 offset:27648\n"
    "v_mfma_f32_16x16x32_bf16 v[68:71], v[172:175], a[60:63], v[68:71]\n"
    "v_rcp_f32_e32 v193, v193\n"
    "v_mfma_f32_16x16x32_bf16 v[64:67], v[172:175], a[56:59], v[64:67]\n"
    "v_mul_f32_e32 v192, v192, v12\n"
    "v_cvt_pk_bf16_f32 v187, v196, v197\n"
    "v_mfma_f32_16x16x32_bf16 v[64:67], v[168:171], a[48:51], v[64:67]\n"
    "s_add_u32 s34, s36, 294912\n"
    "buffer_store_dwordx4 v[184:187], v232, s[28:31], s34 offen offset:3072 sc1\n"
    "v_mfma_f32_16x16x32_bf16 v[64:67], v[164:167], a[40:43], v[64:67]\n"
    "v_exp_f32_e64 v195, -v15\n"
    "v_mfma_f32_16x16x32_bf16 v[64:67], v[160:163], a[32:35], v[64:67]\n"
    "s_waitcnt lgkmcnt(4)\n"
    "v_mul_f32_e32 v14, v14, v30\n"
    "v_add_f32_e32 v194, 1.0, v194\n"
    "v_mfma_f32_16x16x32_bf16 v[72:75], v[128:131], a[32:35], v[72:75]\n"
    "ds_read_b128 v[160:163], v234 offset:28672\n"
    "L_chain4x3_mk:\n"
    "s_cbranch_execz L_pad4_3_mk\n"
    "ds_read_b128 v[164:167], v234 offset:29696\n"
    "v_mfma_f32_16x16x32_bf16 v[72:75], v[132:135], a[40:43], v[72:75]\n"
    "buffer_load_dwordx4 v232, s[48:51], s53 offen offset:1024 sc1 lds\n"
    "v_mfma_f32_16x16x32_bf16 v[72:75], v[136:139], a[48:51], v[72:75]\n"
    "ds_read_b128 v[168:171], v234 offset:30720\n"
    "ds_read_b128 v[172:175], v234 offset:31744\n"
    "v_mfma_f32_16x16x32_bf16 v[72:75], v[140:143], a[56:59], v[72:75]\n"
    "v_rcp_f32_e32 v194, v194\n"
    "v_mfma_f32_16x16x32_bf16 v[76:79], v[140:143], a[60:63], v[76:79]\n"
    "v_mul_f32_e32 v193, v193, v13\n"
    "v_mul_f32_e32 v15, v15, v31\n"
    "v_mfma_f32_16x16x32_bf16 v[76:79], v[136:139], a[52:55], v[76:79]\n"
    "buffer_load_dwordx4 v232, s[48:51], s53 offen offset:2048 sc1 lds\n"
    "v_mfma_f32_16x16x32_bf16 v[76:79], v[132:135], a[44:47], v[76:79]\n"
    "v_add_f32_e32 v195, 1.0, v195\n"
    "v_mfma_f32_16x16x32_bf16 v[76:79], v[128:131], a[36:39], v[76:79]\n"
    "s_waitcnt lgkmcnt(4)\n"
    "v_rcp_f32_e32 v195, v195\n"
    "v_mfma_f32_16x16x32_bf16 v[84:87], v[144:147], a[36:39], v[84:87]\n"
    "ds_read_b128 v[128:131], v234 offset:32768\n"
    "ds_read_b128 v[132:135], v234 offset:33792\n"
    "v_mfma_f32_16x16x32_bf16 v[84:87], v[148:151], a[44:47], v[84:87]\n"
    "buffer_load_dwordx4 v232, s[48:51], s53 offen offset:3072 sc1 lds\n"
    "v_mfma_f32_16x16x32_bf16 v[84:87], v[152:155], a[52:55], v[84:87]\n"
    "ds_read_b128 v[136:139], v234 offset:34816\n"
    "ds_read_b128 v[140:143], v234 offset:35840\n"
    "v_mfma_f32_16x16x32_bf16 v[84:87], v[156:159], a[60:63], v[84:87]\n"
    "v_mul_f32_e32 v194, v194, v14\n"
    "v_cvt_pk_bf16_f32 v190, v192, v193\n"
    "v_mfma_f32_16x16x32_bf16 v[80:83], v[156:159], a[56:59], v[80:83]\n"
    "s_add_u32 m0, s52, 45056\n"
    "s_nop 0\n"
    "v_mul_f32_e32 v195, v195, v15\n"
    "v_mfma_f32_16x16x32_bf16 v[80:83], v[152:155], a[48:51], v[80:83]\n"
    "s_add_u32 s53, s52, 155648\n"
    "buffer_load_dwordx4 v232, s[48:51], s53 offen offset:0 sc1 lds\n"
    "v_mfma_f32_16x16x32_bf16 v[80:83], v[148:151], a[40:43], v[80:83]\n"
    "v_cvt_pk_bf16_f32 v191, v194, v195\n"
    "s_add_u32 s34, s36, 442368\n"
    "v_mfma_f32_16x16x32_bf16 v[80:83], v[144:147], a[32:35], v[80:83]\n"
    "s_waitcnt vmcnt(42) lgkmcnt(4)\n"
    "v_mfma_f32_16x16x32_bf16 v[32:35], v[160:163], a[64:67], v[32:35]\n"
    "ds_read_b128 v[144:147], v234 offset:36864\n"
    "ds_read_b128 v[148:151], v234 offset:37888\n"
    "v_mfma_f32_16x16x32_bf16 v[32:35], v[164:167], a[72:75], v[32:35]\n"
    "buffer_store_dwordx4 v[188:191], v232, s[28:31], s34 offen offset:3072 sc1\n"
    "v_mfma_f32_16x16x32_bf16 v[32:35], v[168:171], a[80:83], v[32:35]\n"
    "ds_read_b128 v[152:155], v234 offset:38912\n"
    "ds_read_b128 v[156:159], v234 offset:39936\n"
    "v_mfma_f32_16x16x32_bf16 v[32:35], v[172:175], a[88:91], v[32:35]\n"
    "v_mfma_f32_16x16x32_bf16 v[36:39], v[172:175], a[92:95], v[36:39]\n"
    "v_mfma_f32_16x16x32_bf16 v[36:39], v[168:171], a[84:87], v[36:39]\n"
    "v_mfma_f32_16x16x32_bf16 v[36:39], v[164:167], a[76:79], v[36:39]\n"
    "v_mfma_f32_16x16x32_bf16 v[36:39], v[160:163], a[68:71], v[36:39]\n"
    "s_waitcnt lgkmcnt(4)\n"
    "v_mfma_f32_16x16x32_bf16 v[44:47], v[128:131], a[68:71], v[44:47]\n"
    "ds_read_b128 v[160:163], v234 offset:40960\n"
    "ds_read_b128 v[164:167], v234 offset:41984\n"
    "v_mfma_f32_16x16x32_bf16 v[44:47], v[132:135], a[76:79], v[44:47]\n"
    "v_mfma_f32_16x16x32_bf16 v[44:47], v[136:139], a[84:87], v[44:47]\n"
    "ds_read_b128 v[168:171], v234 offset:43008\n"
    "ds_read_b128 v[172:175], v234 offset:44032\n"
    "v_mfma_f32_16x16x32_bf16 v[44:47], v[140:143], a[92:95], v[44:47]\n"
    "v_mfma_f32_16x16x32_bf16 v[40:43], v[140:143], a[88:91], v[40:43]\n"
    "v_mfma_f32_16x16x32_bf16 v[40:43], v[136:139], a[80:83], v[40:43]\n"
    "v_mfma_f32_16x16x32_bf16 v[40:43], v[132:135], a[72:75], v[40:43]\n"
    "v_mfma_f32_16x16x32_bf16 v[40:43], v[128:131], a[64:67], v[40:43]\n"
    "s_waitcnt vmcnt(36) lgkmcnt(0)\n"
    "v_mfma_f32_16x16x32_bf16 v[48:51], v[144:147], a[64:67], v[48:51]\n"
    "s_barrier\n"
    "v_mfma_f32_16x16x32_bf16 v[48:51], v[148:151], a[72:75], v[48:51]\n"
    "s_waitcnt vmcnt(7)\n"
    "v_cmp_gt_u32_e32 vcc, 3, v238\n"
    "s_nop 0\n"
    "v_mfma_f32_16x16x32_bf16 v[48:51], v[152:155], a[80:83], v[48:51]\n"
    "s_cbranch_vccnz L_spn50_mk\n"
    "L_spb51_mk:\n"
    "s_mov_b32 s33, 2048\n"
    "v_mfma_f32_16x16x32_bf16 v[48:51], v[156:159], a[88:91], v[48:51]\n"
    "buffer_load_dwordx4 a[160:163], v232, s[60:63], s33 offen sc1\n"
    "v_mfma_f32_16x16x32_bf16 v[52:55], v[156:159], a[92:95], v[52:55]\n"
    "ds_read_b128 v[128:131], v234 offset:45056\n"
    "ds_read_b128 v[132:135], v234 offset:46080\n"
    "v_mfma_f32_16x16x32_bf16 v[52:55], v[152:155], a[84:87], v[52:55]\n"
    "s_mov_b32 s33, 11264\n"
    "buffer_load_dwordx4 a[168:171], v232, s[60:63], s33 offen sc1\n"
    "v_mfma_f32_16x16x32_bf16 v[52:55], v[148:151], a[76:79], v[52:55]\n"
    "ds_read_b128 v[136:139], v234 offset:47104\n"
    "ds_read_b128 v[140:143], v234 offset:48128\n"
    "v_mfma_f32_16x16x32_bf16 v[52:55], v[144:147], a[68:71], v[52:55]\n"
    "v_mfma_f32_16x16x32_bf16 v[60:63], v[160:163], a[68:71], v[60:63]\n"
    "ds_read_b128 v[144:147], v234 offset:49152\n"
    "ds_read_b128 v[148:151], v234 offset:50176\n"
    "v_mfma_f32_16x16x32_bf16 v[60:63], v[164:167], a[76:79], v[60:63]\n"
    "s_mov_b32 s33, 20480\n"
    "buffer_load_dwordx4 a[176:179], v232, s[60:63], s33 offen sc1\n"
    "v_mfma_f32_16x16x32_bf16 v[60:63], v[168:171], a[84:87], v[60:63]\n"
    "ds_read_b128 v[152:155], v234 offset:51200\n"
    "ds_read_b128 v[156:159], v234 offset:52224\n"
    "v_mfma_f32_16x16x32_bf16 v[60:63], v[172:175], a[92:95], v[60:63]\n"
    "v_mfma_f32_16x16x32_bf16 v[56:59], v[172:175], a[88:91], v[56:59]\n"
    "v_mfma_f32_16x16x32_bf16 v[56:59], v[168:171], a[80:83], v[56:59]\n"
    "s_mov_b32 s33, 29696\n"
    "buffer_load_dwordx4 a[184:187], v232, s[60:63], s33 offen sc1\n"
    "v_mfma_f32_16x16x32_bf16 v[56:59], v[164:167], a[72:75], v[56:59]\n"
    "v_mfma_f32_16x16x32_bf16 v[56:59], v[160:163], a[64:67], v[56:59]\n"
    "s_waitcnt lgkmcnt(4)\n"
    "v_mfma_f32_16x16x32_bf16 v[64:67], v[128:131], a[64:67], v[64:67]\n"
    "ds_read_b128 v[160:163], v234 offset:53248\n"
    "ds_read_b128 v[164:167], v234 offset:54272\n"
    "v_mfma_f32_16x16x32_bf16 v[64:67], v[132:135], a[72:75], v[64:67]\n"
    "s_mov_b32 s33, 149504\n"
    "buffer_load_dwordx4 a[164:167], v232, s[60:63], s33 offen sc1\n"
    "v_mfma_f32_16x16x32_bf16 v[64:67], v[136:139], a[80:83], v[64:67]\n"
    "ds_read_b128 v[168:171], v234 offset:55296\n"
    "ds_read_b128 v[172:175], v234 offset:56320\n"
    "v_mfma_f32_16x16x32_bf16 v[64:67], v[140:143], a[88:91], v[64:67]\n"
    "v_mfma_f32_16x16x32_bf16 v[68:71], v[140:143], a[92:95], v[68:71]\n"
    "v_mfma_f32_16x16x32_bf16 v[68:71], v[136:139], a[84:87], v[68:71]\n"
    "v_mfma_f32_16x16x32_bf16 v[68:71], v[132:135], a[76:79], v[68:71]\n"
    "v_mfma_f32_16x16x32_bf16 v[68:71], v[128:131], a[68:71], v[68:71]\n"
    "s_waitcnt lgkmcnt(4)\n"
    "v_mfma_f32_16x16x32_bf16 v[76:79], v[144:147], a[68:71], v[76:79]\n"
    "ds_read_b128 v[128:131], v234 offset:57344\n"
    "ds_read_b128 v[132:135], v234 offset:58368\n"
    "v_mfma_f32_16x16x32_bf16 v[76:79], v[148:151], a[76:79], v[76:79]\n"
    "s_mov_b32 s33, 158720\n"
    "buffer_load_dwordx4 a[172:175], v232, s[60:63], s33 offen sc1\n"
    "v_mfma_f32_16x16x32_bf16 v[76:79], v[152:155], a[84:87], v[76:79]\n"
    "ds_read_b128 v[136:139], v234 offset:59392\n"
    "ds_read_b128 v[140:143], v234 offset:60416\n"
    "v_mfma_f32_16x16x32_bf16 v[76:79], v[156:159], a[92:95], v[76:79]\n"
    "v_mfma_f32_16x16x32_bf16 v[72:75], v[156:159], a[88:91], v[72:75]\n"
    "v_mfma_f32_16x16x32_bf16 v[72:75], v[152:155], a[80:83], v[72:75]\n"
    "s_mov_b32 s33, 167936\n"
    "buffer_load_dwordx4 a[180:183], v232, s[60:63], s33 offen sc1\n"
    "v_mfma_f32_16x16x32_bf16 v[72:75], v[148:151], a[72:75], v[72:75]\n"
    "v_mfma_f32_16x16x32_bf16 v[72:75], v[144:147], a[64:67], v[72:75]\n"
    "s_waitcnt vmcnt(7) lgkmcnt(4)\n"
    "v_mfma_f32_16x16x32_bf16 v[80:83], v[160:163], a[64:67], v[80:83]\n"
    "s_barrier\n"
    "v_mfma_f32_16x16x32_bf16 v[80:83], v[164:167], a[72:75], v[80:83]\n"
    "s_mov_b32 s33, 177152\n"
    "buffer_load_dwordx4 a[188:191], v232, s[60:63], s33 offen sc1\n"
    "v_mfma_f32_16x16x32_bf16 v[80:83], v[168:171], a[80:83], v[80:83]\n"
    "v_mov_b32_e32 v253, 4\n"
    "v_mfma_f32_16x16x32_bf16 v[80:83], v[172:175], a[88:91], v[80:83]\n"
    "buffer_store_dword v253, v252, s[96:99], s39 offen sc1\n"
    "v_mfma_f32_16x16x32_bf16 v[84:87], v[172:175], a[92:95], v[84:87]\n"
    "s_add_u32 m0, s52, 73728\n"
    "ds_read_b128 v[144:147], v234 offset:61440\n"
    "ds_read_b128 v[148:151], v234 offset:62464\n"
    "v_mfma_f32_16x16x32_bf16 v[84:87], v[168:171], a[84:87], v[84:87]\n"
    "s_add_u32 s53, s52, 184320\n"
    "buffer_load_dwordx4 v232, s[48:51], s53 offen offset:0 sc1 lds\n"
    "v_mfma_f32_16x16x32_bf16 v[84:87], v[164:167], a[76:79], v[84:87]\n"
    "ds_read_b128 v[152:155], v234 offset:63488\n"
    "ds_read_b128 v[156:159], v234 offset:64512\n"
    "v_mfma_f32_16x16x32_bf16 v[84:87], v[160:163], a[68:71], v[84:87]\n"
    "s_waitcnt vmcnt(42) lgkmcnt(4)\n"
    "v_mfma_f32_16x16x32_bf16 v[36:39], v[128:131], a[100:103], v[36:39]\n"
    "ds_read_b128 v[160:163], v235 offset:0\n"
    "ds_read_b128 v[164:167], v235 offset:1024\n"
    "v_mfma_f32_16x16x32_bf16 v[36:39], v[132:135], a[108:111], v[36:39]\n"
    "buffer_load_dwordx4 v232, s[48:51], s53 offen offset:1024 sc1 lds\n"
    "v_mfma_f32_16x16x32_bf16 v[36:39], v[136:139], a[116:119], v[36:39]\n"
    "ds_read_b128 v[168:171], v235 offset:2048\n"
    "ds_read_b128 v[172:175], v235 offset:3072\n"
    "v_mfma_f32_16x16x32_bf16 v[36:39], v[140:143], a[124:127], v[36:39]\n"
    "v_mfma_f32_16x16x32_bf16 v[32:35], v[140:143], a[120:123], v[32:35]\n"
    "v_mfma_f32_16x16x32_bf16 v[32:35], v[136:139], a[112:115], v[32:35]\n"
    "v_mfma_f32_16x16x32_bf16 v[32:35], v[132:135], a[104:107], v[32:35]\n"
    "v_mfma_f32_16x16x32_bf16 v[32:35], v[128:131], a[96:99], v[32:35]\n"
    "s_waitcnt lgkmcnt(4)\n"
    "v_mfma_f32_16x16x32_bf16 v[40:43], v[144:147], a[96:99], v[40:43]\n"
    "ds_read_b128 v[128:131], v235 offset:4096\n"
    "ds_read_b128 v[132:135], v235 offset:5120\n"
    "v_mfma_f32_16x16x32_bf16 v[40:43], v[148:151], a[104:107], v[40:43]\n"
    "buffer_load_dwordx4 v232, s[48:51], s53 offen offset:2048 sc1 lds\n"
    "v_mfma_f32_16x16x32_bf16 v[40:43], v[152:155], a[112:115], v[40:43]\n"
    "ds_read_b128 v[136:139], v235 offset:6144\n"
    "ds_read_b128 v[140:143], v235 offset:7168\n"
    "v_mfma_f32_16x16x32_bf16 v[40:43], v[156:159], a[120:123], v[40:43]\n"
    "v_mfma_f32_16x16x32_bf16 v[44:47], v[156:159], a[124:127], v[44:47]\n"
    "v_mfma_f32_16x16x32_bf16 v[44:47], v[152:155], a[116:119], v[44:47]\n"
    "buffer_load_dwordx4 v232, s[48:51], s53 offen offset:3072 sc1 lds\n"
    "v_mfma_f32_16x16x32_bf16 v[44:47], v[148:151], a[108:111], v[44:47]\n"
    "v_mfma_f32_16x16x32_bf16 v[44:47], v[144:147], a[100:103], v[44:47]\n"
    "s_waitcnt lgkmcnt(4)\n"
    "v_mfma_f32_16x16x32_bf16 v[52:55], v[160:163], a[100:103], v[52:55]\n"
    "s_add_u32 m0, s52, 77824\n"
    "ds_read_b128 v[144:147], v235 offset:8192\n"
    "ds_read_b128 v[148:151], v235 offset:9216\n"
    "v_mfma_f32_16x16x32_bf16 v[52:55], v[164:167], a[108:111], v[52:55]\n"
    "s_add_u32 s53, s52, 188416\n"
    "buffer_load_dwordx4 v232, s[48:51], s53 offen offset:0 sc1 lds\n"
    "v_mfma_f32_16x16x32_bf16 v[52:55], v[168:171], a[116:119], v[52:55]\n"
    "ds_read_b128 v[152:155], v235 offset:10240\n"
    "ds_read_b128 v[156:159], v235 offset:11264\n"
    "v_mfma_f32_16x16x32_bf16 v[52:55], v[172:175], a[124:127], v[52:55]\n"
    "v_mfma_f32_16x16x32_bf16 v[48:51], v[172:175], a[120:123], v[48:51]\n"
    "v_mfma_f32_16x16x32_bf16 v[48:51], v[168:171], a[112:115], v[48:51]\n"
    "buffer_load_dwordx4 v232, s[48:51], s53 offen offset:1024 sc1 lds\n"
    "v_mfma_f32_16x16x32_bf16 v[48:51], v[164:167], a[104:107], v[48:51]\n"
    "v_mfma_f32_16x16x32_bf16 v[48:51], v[160:163], a[96:99], v[48:51]\n"
    "s_waitcnt lgkmcnt(4)\n"
    "v_mfma_f32_16x16x32_bf16 v[56:59], v[128:131], a[96:99], v[56:59]\n"
    "ds_read_b128 v[160:163], v235 offset:12288\n"
    "ds_read_b128 v[164:167], v235 offset:13312\n"
    "v_mfma_f32_16x16x32_bf16 v[56:59], v[132:135], a[104:107], v[56:59]\n"
    "buffer_load_dwordx4 v232, s[48:51], s53 offen offset:2048 sc1 lds\n"
    "v_mfma_f32_16x16x32_bf16 v[56:59], v[136:139], a[112:115], v[56:59]\n"
    "ds_read_b128 v[168:171], v235 offset:14336\n"
    "ds_read_b128 v[172:175], v235 offset:15360\n"
    "v_mfma_f32_16x16x32_bf16 v[56:59], v[140:143], a[120:123], v[56:59]\n"
    "v_mfma_f32_16x16x32_bf16 v[60:63], v[140:143], a[124:127], v[60:63]\n"
    "v_mfma_f32_16x16x32_bf16 v[60:63], v[136:139], a[116:119], v[60:63]\n"
    "v_mfma_f32_16x16x32_bf16 v[60:63], v[132:135], a[108:111], v[60:63]\n"
    "v_mfma_f32_16x16x32_bf16 v[60:63], v[128:131], a[100:103], v[60:63]\n"
    "s_waitcnt vmcnt(29) lgkmcnt(0)\n"
    "v_mfma_f32_16x16x32_bf16 v[68:71], v[144:147], a[100:103], v[68:71]\n"
    "s_barrier\n"
    "v_mfma_f32_16x16x32_bf16 v[68:71], v[148:151], a[108:111], v[68:71]\n"
    "buffer_load_dwordx4 v232, s[48:51], s53 offen offset:3072 sc1 lds\n"
    "v_mfma_f32_16x16x32_bf16 v[68:71], v[152:155], a[116:119], v[68:71]\n"
    "ds_read_b128 v[128:131], v233 offset:0\n"
    "ds_read_b128 v[132:135], v233 offset:1024\n"
    "v_mfma_f32_16x16x32_bf16 v[68:71], v[156:159], a[124:127], v[68:71]\n"
    "ds_read_b128 v[136:139], v233 offset:2048\n"
    "ds_read_b128 v[140:143], v233 offset:3072\n"
    "v_mfma_f32_16x16x32_bf16 v[64:67], v[156:159], a[120:123], v[64:67]\n"
    "s_add_u32 m0, s52, 81920\n"
    "v_mfma_f32_16x16x32_bf16 v[64:67], v[152:155], a[112:115], v[64:67]\n"
    "s_add_u32 s53, s52, 192512\n"
    "buffer_load_dwordx4 v232, s[48:51], s53 offen offset:0 sc1 lds\n"
    "v_mfma_f32_16x16x32_bf16 v[64:67], v[148:151], a[104:107], v[64:67]\n"
    "v_mfma_f32_16x16x32_bf16 v[64:67], v[144:147], a[96:99], v[64:67]\n"
    "v_mfma_f32_16x16x32_bf16 v[72:75], v[160:163], a[96:99], v[72:75]\n"
    "ds_read_b128 v[144:147], v233 offset:4096\n"
    "ds_read_b128 v[148:151], v233 offset:5120\n"
    "v_mfma_f32_16x16x32_bf16 v[72:75], v[164:167], a[104:107], v[72:75]\n"
    "s_mov_b32 s33, 7168\n"
    "buffer_load_dwordx4 a[192:195], v232, s[60:63], s33 offen sc1\n"
    "v_mfma_f32_16x16x32_bf16 v[72:75], v[168:171], a[112:115], v[72:75]\n"
    "ds_read_b128 v[152:155], v233 offset:6144\n"
    "ds_read_b128 v[156:159], v233 offset:7168\n"
    "v_mfma_f32_16x16x32_bf16 v[72:75], v[172:175], a[120:123], v[72:75]\n"
    "v_mfma_f32_16x16x32_bf16 v[76:79], v[172:175], a[124:127], v[76:79]\n"
    "v_mfma_f32_16x16x32_bf16 v[76:79], v[168:171], a[116:119], v[76:79]\n"
    "s_mov_b32 s33, 16384\n"
    "buffer_load_dwordx4 a[200:203], v232, s[60:63], s33 offen sc1\n"
    "v_mfma_f32_16x16x32_bf16 v[76:79], v[164:167], a[108:111], v[76:79]\n"
    "v_mfma_f32_16x16x32_bf16 v[76:79], v[160:163], a[100:103], v[76:79]\n"
    "s_waitcnt lgkmcnt(4)\n"
    "v_mfma_f32_16x16x32_bf16 v[84:87], v[128:131], a[100:103], v[84:87]\n"
    "ds_read_b128 v[160:163], v233 offset:8192\n"
    "ds_read_b128 v[164:167], v233 offset:9216\n"
    "v_mfma_f32_16x16x32_bf16 v[84:87], v[132:135], a[108:111], v[84:87]\n"
    "s_mov_b32 s33, 25600\n"
    "buffer_load_dwordx4 a[208:211], v232, s[60:63], s33 offen sc1\n"
    "v_mfma_f32_16x16x32_bf16 v[84:87], v[136:139], a[116:119], v[84:87]\n"
    "ds_read_b128 v[168:171], v233 offset:10240\n"
    "ds_read_b128 v[172:175], v233 offset:11264\n"
    "v_mfma_f32_16x16x32_bf16 v[84:87], v[140:143], a[124:127], v[84:87]\n"
    "v_mfma_f32_16x16x32_bf16 v[80:83], v[140:143], a[120:123], v[80:83]\n"
    "v_mfma_f32_16x16x32_bf16 v[80:83], v[136:139], a[112:115], v[80:83]\n"
    "v_mfma_f32_16x16x32_bf16 v[80:83], v[132:135], a[104:107], v[80:83]\n"
    "v_mfma_f32_16x16x32_bf16 v[80:83], v[128:131], a[96:99], v[80:83]\n"
    "s_waitcnt vmcnt(44) lgkmcnt(4)\n"
    "v_mfma_f32_16x16x32_bf16 v[32:35], v[144:147], a[128:131], v[32:35]\n"
    "ds_read_b128 v[128:131], v233 offset:12288\n"
    "ds_read_b128 v[132:135], v233 offset:13312\n"
    "v_mfma_f32_16x16x32_bf16 v[32:35], v[148:151], a[136:139], v[32:35]\n"
    "s_mov_b32 s33, 34816\n"
    "buffer_load_dwordx4 a[216:219], v232, s[60:63], s33 offen sc1\n"
    "v_mfma_f32_16x16x32_bf16 v[32:35], v[152:155], a[144:147], v[32:35]\n"
    "ds_read_b128 v[136:139], v233 offset:14336\n"
    "ds_read_b128 v[140:143], v233 offset:15360\n"
    "v_mfma_f32_16x16x32_bf16 v[32:35], v[156:159], a[152:155], v[32:35]\n"
    "v_mfma_f32_16x16x32_bf16 v[36:39], v[156:159], a[156:159], v[36:39]\n"
    "v_mfma_f32_16x16x32_bf16 v[36:39], v[152:155], a[148:151], v[36:39]\n"
    "s_mov_b32 s33, 154624\n"
    "buffer_load_dwordx4 a[196:199], v232, s[60:63], s33 offen sc1\n"
    "v_mfma_f32_16x16x32_bf16 v[36:39], v[148:151], a[140:143], v[36:39]\n"
    "v_mfma_f32_16x16x32_bf16 v[36:39], v[144:147], a[132:135], v[36:39]\n"
    "s_waitcnt lgkmcnt(4)\n"
    "v_mfma_f32_16x16x32_bf16 v[44:47], v[160:163], a[132:135], v[44:47]\n"
    "ds_read_b128 v[144:147], v233 offset:16384\n"
    "ds_read_b128 v[148:151], v233 offset:17408\n"
    "v_mfma_f32_16x16x32_bf16 v[44:47], v[164:167], a[140:143], v[44:47]\n"
    "s_mov_b32 s33, 163840\n"
    "buffer_load_dwordx4 a[204:207], v232, s[60:63], s33 offen sc1\n"
    "v_mfma_f32_16x16x32_bf16 v[44:47], v[168:171], a[148:151], v[44:47]\n"
    "ds_read_b128 v[152:155], v233 offset:18432\n"
    "ds_read_b128 v[156:159], v233 offset:19456\n"
    "v_mfma_f32_16x16x32_bf16 v[44:47], v[172:175], a[156:159], v[44:47]\n"
    "v_mfma_f32_16x16x32_bf16 v[40:43], v[172:175], a[152:155], v[40:43]\n"
    "v_mfma_f32_16x16x32_bf16 v[40:43], v[168:171], a[144:147], v[40:43]\n"
    "s_mov_b32 s33, 173056\n"
    "buffer_load_dwordx4 a[212:215], v232, s[60:63], s33 offen sc1\n"
    "v_mfma_f32_16x16x32_bf16 v[40:43], v[164:167], a[136:139], v[40:43]\n"
    "v_mfma_f32_16x16x32_bf16 v[40:43], v[160:163], a[128:131], v[40:43]\n"
    "s_waitcnt lgkmcnt(4)\n"
    "v_mfma_f32_16x16x32_bf16 v[48:51], v[128:131], a[128:131], v[48:51]\n"
    "ds_read_b128 v[160:163], v233 offset:20480\n"
    "ds_read_b128 v[164:167], v233 offset:21504\n"
    "v_mfma_f32_16x16x32_bf16 v[48:51], v[132:135], a[136:139], v[48:51]\n"
    "s_mov_b32 s33, 182272\n"
    "buffer_load_dwordx4 a[220:223], v232, s[60:63], s33 offen sc1\n"
    "v_mfma_f32_16x16x32_bf16 v[48:51], v[136:139], a[144:147], v[48:51]\n"
    "ds_read_b128 v[168:171], v233 offset:22528\n"
    "ds_read_b128 v[172:175], v233 offset:23552\n"
    "v_mfma_f32_16x16x32_bf16 v[48:51], v[140:143], a[152:155], v[48:51]\n"
    "v_mfma_f32_16x16x32_bf16 v[52:55], v[140:143], a[156:159], v[52:55]\n"
    "v_mfma_f32_16x16x32_bf16 v[52:55], v[136:139], a[148:151], v[52:55]\n"
    "v_mfma_f32_16x16x32_bf16 v[52:55], v[132:135], a[140:143], v[52:55]\n"
    "v_mfma_f32_16x16x32_bf16 v[52:55], v[128:131], a[132:135], v[52:55]\n"
    "s_waitcnt lgkmcnt(4)\n"
    "v_mfma_f32_16x16x32_bf16 v[60:63], v[144:147], a[132:135], v[60:63]\n"
    "s_add_u32 m0, s52, 110592\n"
    "ds_read_b128 v[128:131], v233 offset:24576\n"
    "ds_read_b128 v[132:135], v233 offset:25600\n"
    "v_mfma_f32_16x16x32_bf16 v[60:63], v[148:151], a[140:143], v[60:63]\n"
    "s_add_u32 s53, s52, 221184\n"
    "buffer_load_dwordx4 v232, s[48:51], s53 offen offset:0 sc1 lds\n"
    "v_mfma_f32_16x16x32_bf16 v[60:63], v[152:155], a[148:151], v[60:63]\n"
    "ds_read_b128 v[136:139], v233 offset:26624\n"
    "ds_read_b128 v[140:143], v233 offset:27648\n"
    "v_mfma_f32_16x16x32_bf16 v[60:63], v[156:159], a[156:159], v[60:63]\n"
    "v_mfma_f32_16x16x32_bf16 v[56:59], v[156:159], a[152:155], v[56:59]\n"
    "v_mfma_f32_16x16x32_bf16 v[56:59], v[152:155], a[144:147], v[56:59]\n"
    "buffer_load_dwordx4 v232, s[48:51], s53 offen offset:1024 sc1 lds\n"
    "v_mfma_f32_16x16x32_bf16 v[56:59], v[148:151], a[136:139], v[56:59]\n"
    "v_mfma_f32_16x16x32_bf16 v[56:59], v[144:147], a[128:131], v[56:59]\n"
    "s_waitcnt lgkmcnt(4)\n"
    "v_mfma_f32_16x16x32_bf16 v[64:67], v[160:163], a[128:131], v[64:67]\n"
    "ds_read_b128 v[144:147], v233 offset:28672\n"
    "ds_read_b128 v[148:151], v233 offset:29696\n"
    "v_mfma_f32_16x16x32_bf16 v[64:67], v[164:167], a[136:139], v[64:67]\n"
    "buffer_load_dwordx4 v232, s[48:51], s53 offen offset:2048 sc1 lds\n"
    "v_mfma_f32_16x16x32_bf16 v[64:67], v[168:171], a[144:147], v[64:67]\n"
    "ds_read_b128 v[152:155], v233 offset:30720\n"
    "ds_read_b128 v[156:159], v233 offset:31744\n"
    "v_mfma_f32_16x16x32_bf16 v[64:67], v[172:175], a[152:155], v[64:67]\n"
    "buffer_load_dwordx4 v232, s[48:51], s53 offen offset:3072 sc1 lds\n"
    "v_mfma_f32_16x16x32_bf16 v[68:71], v[172:175], a[156:159], v[68:71]\n"
    "v_mfma_f32_16x16x32_bf16 v[68:71], v[168:171], a[148:151], v[68:71]\n"
    "buffer_load_dword v238, v239, s[96:99], s35 offen sc1\n"
    "v_mfma_f32_16x16x32_bf16 v[68:71], v[164:167], a[140:143], v[68:71]\n"
    "v_mfma_f32_16x16x32_bf16 v[68:71], v[160:163], a[132:135], v[68:71]\n"
    "s_waitcnt lgkmcnt(4)\n"
    "v_mfma_f32_16x16x32_bf16 v[76:79], v[128:131], a[132:135], v[76:79]\n"
    "ds_read_b128 v[160:163], v233 offset:32768\n"
    "ds_read_b128 v[164:167], v233 offset:33792\n"
    "v_mfma_f32_16x16x32_bf16 v[76:79], v[132:135], a[140:143], v[76:79]\n"
    "v_mfma_f32_16x16x32_bf16 v[76:79], v[136:139], a[148:151], v[76:79]\n"
    "ds_read_b128 v[168:171], v233 offset:34816\n"
    "ds_read_b128 v[172:175], v233 offset:35840\n"
    "v_mfma_f32_16x16x32_bf16 v[76:79], v[140:143], a[156:159], v[76:79]\n"
    "v_mfma_f32_16x16x32_bf16 v[72:75], v[140:143], a[152:155], v[72:75]\n"
    "v_mfma_f32_16x16x32_bf16 v[72:75], v[136:139], a[144:147], v[72:75]\n"
    "v_mfma_f32_16x16x32_bf16 v[72:75], v[132:135], a[136:139], v[72:75]\n"
    "v_mfma_f32_16x16x32_bf16 v[72:75], v[128:131], a[128:131], v[72:75]\n"
    "s_waitcnt vmcnt(32) lgkmcnt(0)\n"
    "v_mfma_f32_16x16x32_bf16 v[80:83], v[144:147], a[128:131], v[80:83]\n"
    "s_barrier\n"
    "v_mfma_f32_16x16x32_bf16 v[80:83], v[148:151], a[136:139], v[80:83]\n"
    "s_add_u32 m0, s52, 114688\n"
    "s_add_u32 s53, s52, 225280\n"
    "ds_read_b128 v[128:131], v233 offset:36864\n"
    "v_mfma_f32_16x16x32_bf16 v[80:83], v[152:155], a[144:147], v[80:83]\n"
    "buffer_load_dwordx4 v232, s[48:51], s53 offen offset:0 sc1 lds\n"
    "v_mfma_f32_16x16x32_bf16 v[80:83], v[156:159], a[152:155], v[80:83]\n"
    "ds_read_b128 v[132:135], v233 offset:37888\n"
    "ds_read_b128 v[136:139], v233 offset:38912\n"
    "v_mfma_f32_16x16x32_bf16 v[84:87], v[156:159], a[156:159], v[84:87]\n"
    "ds_read_b128 v[140:143], v233 offset:39936\n"
    "v_mfma_f32_16x16x32_bf16 v[84:87], v[152:155], a[148:151], v[84:87]\n"
    "buffer_load_dwordx4 v232, s[48:51], s53 offen offset:1024 sc1 lds\n"
    "v_mfma_f32_16x16x32_bf16 v[84:87], v[148:151], a[140:143], v[84:87]\n"
    "v_mfma_f32_16x16x32_bf16 v[84:87], v[144:147], a[132:135], v[84:87]\n"
    "s_waitcnt vmcnt(25) lgkmcnt(4)\n"
    "v_mfma_f32_16x16x32_bf16 v[36:39], v[160:163], a[164:167], v[36:39]\n"
    "ds_read_b128 v[144:147], v233 offset:40960\n"
    "ds_read_b128 v[148:151], v233 offset:41984\n"
    "v_mfma_f32_16x16x32_bf16 v[36:39], v[164:167], a[172:175], v[36:39]\n"
    "buffer_load_dwordx4 v232, s[48:51], s53 offen offset:2048 sc1 lds\n"
    "v_mfma_f32_16x16x32_bf16 v[36:39], v[168:171], a[180:183], v[36:39]\n"
    "ds_read_b128 v[152:155], v233 offset:43008\n"
    "ds_read_b128 v[156:159], v233 offset:44032\n"
    "v_mfma_f32_16x16x32_bf16 v[36:39], v[172:175], a[188:191], v[36:39]\n"
    "v_mfma_f32_16x16x32_bf16 v[32:35], v[172:175], a[184:187], v[32:35]\n"
    "v_mfma_f32_16x16x32_bf16 v[32:35], v[168:171], a[176:179], v[32:35]\n"
    "buffer_load_dwordx4 v232, s[48:51], s53 offen offset:3072 sc1 lds\n"
    "v_mfma_f32_16x16x32_bf16 v[32:35], v[164:167], a[168:171], v[32:35]\n"
    "v_mfma_f32_16x16x32_bf16 v[32:35], v[160:163], a[160:163], v[32:35]\n"
    "s_waitcnt lgkmcnt(4)\n"
    "v_mfma_f32_16x16x32_bf16 v[40:43], v[128:131], a[160:163], v[40:43]\n"
    "s_add_u32 m0, s52, 118784\n"
    "ds_read_b128 v[160:163], v233 offset:45056\n"
    "ds_read_b128 v[164:167], v233 offset:46080\n"
    "v_mfma_f32_16x16x32_bf16 v[40:43], v[132:135], a[168:171], v[40:43]\n"
    "s_add_u32 s53, s52, 229376\n"
    "buffer_load_dwordx4 v232, s[48:51], s53 offen offset:0 sc1 lds\n"
    "v_mfma_f32_16x16x32_bf16 v[40:43], v[136:139], a[176:179], v[40:43]\n"
    "ds_read_b128 v[168:171], v233 offset:47104\n"
    "ds_read_b128 v[172:175], v233 offset:48128\n"
    "v_mfma_f32_16x16x32_bf16 v[40:43], v[140:143], a[184:187], v[40:43]\n"
    "v_mfma_f32_16x16x32_bf16 v[44:47], v[140:143], a[188:191], v[44:47]\n"
    "v_mfma_f32_16x16x32_bf16 v[44:47], v[136:139], a[180:183], v[44:47]\n"
    "v_mfma_f32_16x16x32_bf16 v[44:47], v[132:135], a[172:175], v[44:47]\n"
    "v_mfma_f32_16x16x32_bf16 v[44:47], v[128:131], a[164:167], v[44:47]\n"
    "s_waitcnt lgkmcnt(4)\n"
    "v_mfma_f32_16x16x32_bf16 v[52:55], v[144:147], a[164:167], v[52:55]\n"
    "s_add_u32 m0, s52, 0\n"
    "ds_read_b128 v[128:131], v233 offset:49152\n"
    "ds_read_b128 v[132:135], v233 offset:50176\n"
    "v_mfma_f32_16x16x32_bf16 v[52:55], v[148:151], a[172:175], v[52:55]\n"
    "s_add_u32 s53, s52, 258048\n"
    "buffer_load_dwordx4 v232, s[48:51], s53 offen offset:0 sc1 lds\n"
    "v_mfma_f32_16x16x32_bf16 v[52:55], v[152:155], a[180:183], v[52:55]\n"
    "ds_read_b128 v[136:139], v233 offset:51200\n"
    "ds_read_b128 v[140:143], v233 offset:52224\n"
    "v_mfma_f32_16x16x32_bf16 v[52:55], v[156:159], a[188:191], v[52:55]\n"
    "v_mfma_f32_16x16x32_bf16 v[48:51], v[156:159], a[184:187], v[48:51]\n"
    "v_mfma_f32_16x16x32_bf16 v[48:51], v[152:155], a[176:179], v[48:51]\n"
    "buffer_load_dwordx4 v232, s[48:51], s53 offen offset:1024 sc1 lds\n"
    "v_mfma_f32_16x16x32_bf16 v[48:51], v[148:151], a[168:171], v[48:51]\n"
    "v_mfma_f32_16x16x32_bf16 v[48:51], v[144:147], a[160:163], v[48:51]\n"
    "s_waitcnt lgkmcnt(4)\n"
    "v_mfma_f32_16x16x32_bf16 v[56:59], v[160:163], a[160:163], v[56:59]\n"
    "ds_read_b128 v[144:147], v233 offset:53248\n"
    "ds_read_b128 v[148:151], v233 offset:54272\n"
    "v_mfma_f32_16x16x32_bf16 v[56:59], v[164:167], a[168:171], v[56:59]\n"
    "buffer_load_dwordx4 v232, s[48:51], s53 offen offset:2048 sc1 lds\n"
    "v_mfma_f32_16x16x32_bf16 v[56:59], v[168:171], a[176:179], v[56:59]\n"
    "ds_read_b128 v[152:155], v233 offset:55296\n"
    "ds_read_b128 v[156:159], v233 offset:56320\n"
    "v_mfma_f32_16x16x32_bf16 v[56:59], v[172:175], a[184:187], v[56:59]\n"
    "v_mfma_f32_16x16x32_bf16 v[60:63], v[172:175], a[188:191], v[60:63]\n"
    "v_mfma_f32_16x16x32_bf16 v[60:63], v[168:171], a[180:183], v[60:63]\n"
    "buffer_load_dwordx4 v232, s[48:51], s53 offen offset:3072 sc1 lds\n"
    "v_mfma_f32_16x16x32_bf16 v[60:63], v[164:167], a[172:175], v[60:63]\n"
    "s_waitcnt vmcnt(9)\n"
    "v_cmp_gt_u32_e32 vcc, 4, v238\n"
    "s_nop 0\n"
    "s_cbranch_vccnz L_spn52_mk\n"
    "L_spb53_mk:\n"
    "v_mfma_f32_16x16x32_bf16 v[60:63], v[160:163], a[164:167], v[60:63]\n"
    "s_waitcnt lgkmcnt(4)\n"
    "v_mfma_f32_16x16x32_bf16 v[68:71], v[128:131], a[164:167], v[68:71]\n"
    "ds_read_b128 v[160:163], v233 offset:57344\n"
    "ds_read_b128 v[164:167], v233 offset:58368\n"
    "v_mfma_f32_16x16x32_bf16 v[68:71], v[132:135], a[172:175], v[68:71]\n"
    "s_mov_b32 s33, 3072\n"
    "buffer_load_dwordx4 a[224:227], v232, s[60:63], s33 offen sc1\n"
    "v_mfma_f32_16x16x32_bf16 v[68:71], v[136:139], a[180:183], v[68:71]\n"
    "ds_read_b128 v[168:171], v233 offset:59392\n"
    "ds_read_b128 v[172:175], v233 offset:60416\n"
    "v_mfma_f32_16x16x32_bf16 v[68:71], v[140:143], a[188:191], v[68:71]\n"
    "v_mfma_f32_16x16x32_bf16 v[64:67], v[140:143], a[184:187], v[64:67]\n"
    "v_mfma_f32_16x16x32_bf16 v[64:67], v[136:139], a[176:179], v[64:67]\n"
    "v_mfma_f32_16x16x32_bf16 v[64:67], v[132:135], a[168:171], v[64:67]\n"
    "v_mfma_f32_16x16x32_bf16 v[64:67], v[128:131], a[160:163], v[64:67]\n"
    "s_waitcnt lgkmcnt(4)\n"
    "v_mfma_f32_16x16x32_bf16 v[72:75], v[144:147], a[160:163], v[72:75]\n"
    "ds_read_b128 v[128:131], v233 offset:61440\n"
    "ds_read_b128 v[132:135], v233 offset:62464\n"
    "v_mfma_f32_16x16x32_bf16 v[72:75], v[148:151], a[168:171], v[72:75]\n"
    "s_mov_b32 s33, 12288\n"
    "buffer_load_dwordx4 a[232:235], v232, s[60:63], s33 offen sc1\n"
    "v_mfma_f32_16x16x32_bf16 v[72:75], v[152:155], a[176:179], v[72:75]\n"
    "ds_read_b128 v[136:139], v233 offset:63488\n"
    "ds_read_b128 v[140:143], v233 offset:64512\n"
    "v_mfma_f32_16x16x32_bf16 v[72:75], v[156:159], a[184:187], v[72:75]\n"
    "v_mfma_f32_16x16x32_bf16 v[76:79], v[156:159], a[188:191], v[76:79]\n"
    "v_mfma_f32_16x16x32_bf16 v[76:79], v[152:155], a[180:183], v[76:79]\n"
    "s_mov_b32 s33, 21504\n"
    "buffer_load_dwordx4 a[240:243], v232, s[60:63], s33 offen sc1\n"
    "v_mfma_f32_16x16x32_bf16 v[76:79], v[148:151], a[172:175], v[76:79]\n"
    "v_mfma_f32_16x16x32_bf16 v[76:79], v[144:147], a[164:167], v[76:79]\n"
    "s_waitcnt lgkmcnt(4)\n"
    "v_mfma_f32_16x16x32_bf16 v[84:87], v[160:163], a[164:167], v[84:87]\n"
    "ds_read_b128 v[144:147], v234 offset:0\n"
    "ds_read_b128 v[148:151], v234 offset:1024\n"
    "v_mfma_f32_16x16x32_bf16 v[84:87], v[164:167], a[172:175], v[84:87]\n"
    "s_mov_b32 s33, 30720\n"
    "buffer_load_dwordx4 a[248:251], v232, s[60:63], s33 offen sc1\n"
    "v_mfma_f32_16x16x32_bf16 v[84:87], v[168:171], a[180:183], v[84:87]\n"
    "ds_read_b128 v[152:155], v234 offset:2048\n"
    "ds_read_b128 v[156:159], v234 offset:3072\n"
    "v_mfma_f32_16x16x32_bf16 v[84:87], v[172:175], a[188:191], v[84:87]\n"
    "v_mfma_f32_16x16x32_bf16 v[80:83], v[172:175], a[184:187], v[80:83]\n"
    "v_mfma_f32_16x16x32_bf16 v[80:83], v[168:171], a[176:179], v[80:83]\n"
    "s_mov_b32 s33, 150528\n"
    "buffer_load_dwordx4 a[228:231], v232, s[60:63], s33 offen sc1\n"
    "v_mfma_f32_16x16x32_bf16 v[80:83], v[164:167], a[168:171], v[80:83]\n"
    "v_mfma_f32_16x16x32_bf16 v[80:83], v[160:163], a[160:163], v[80:83]\n"
    "s_waitcnt vmcnt(19) lgkmcnt(4)\n"
    "v_mfma_f32_16x16x32_bf16 v[32:35], v[128:131], a[192:195], v[32:35]\n"
    "ds_read_b128 v[160:163], v234 offset:4096\n"
    "ds_read_b128 v[164:167], v234 offset:5120\n"
    "v_mfma_f32_16x16x32_bf16 v[32:35], v[132:135], a[200:203], v[32:35]\n"
    "s_mov_b32 s33, 159744\n"
    "buffer_load_dwordx4 a[236:239], v232, s[60:63], s33 offen sc1\n"
    "v_mfma_f32_16x16x32_bf16 v[32:35], v[136:139], a[208:211], v[32:35]\n"
    "ds_read_b128 v[168:171], v234 offset:6144\n"
    "ds_read_b128 v[172:175], v234 offset:7168\n"
    "v_mfma_f32_16x16x32_bf16 v[32:35], v[140:143], a[216:219], v[32:35]\n"
    "v_mfma_f32_16x16x32_bf16 v[36:39], v[140:143], a[220:223], v[36:39]\n"
    "v_mfma_f32_16x16x32_bf16 v[36:39], v[136:139], a[212:215], v[36:39]\n"
    "v_mfma_f32_16x16x32_bf16 v[36:39], v[132:135], a[204:207], v[36:39]\n"
    "v_mfma_f32_16x16x32_bf16 v[36:39], v[128:131], a[196:199], v[36:39]\n"
    "s_waitcnt vmcnt(28) lgkmcnt(0)\n"
    "v_mfma_f32_16x16x32_bf16 v[44:47], v[144:147], a[196:199], v[44:47]\n"
    "s_barrier\n"
    "v_mfma_f32_16x16x32_bf16 v[44:47], v[148:151], a[204:207], v[44:47]\n"
    "s_mov_b32 s33, 168960\n"
    "buffer_load_dwordx4 a[244:247], v232, s[60:63], s33 offen sc1\n"
    "v_mfma_f32_16x16x32_bf16 v[44:47], v[152:155], a[212:215], v[44:47]\n"
    "ds_read_b128 v[128:131], v234 offset:8192\n"
    "ds_read_b128 v[132:135], v234 offset:9216\n"
    "v_mfma_f32_16x16x32_bf16 v[44:47], v[156:159], a[220:223], v[44:47]\n"
    "ds_read_b128 v[136:139], v234 offset:10240\n"
    "ds_read_b128 v[140:143], v234 offset:11264\n"
    "v_mfma_f32_16x16x32_bf16 v[40:43], v[156:159], a[216:219], v[40:43]\n"
    "v_mfma_f32_16x16x32_bf16 v[40:43], v[152:155], a[208:211], v[40:43]\n"
    "s_mov_b32 s33, 178176\n"
    "buffer_load_dwordx4 a[252:255], v232, s[60:63], s33 offen sc1\n"
    "v_mfma_f32_16x16x32_bf16 v[40:43], v[148:151], a[200:203], v[40:43]\n"
    "v_mfma_f32_16x16x32_bf16 v[40:43], v[144:147], a[192:195], v[40:43]\n"
    "v_mfma_f32_16x16x32_bf16 v[48:51], v[160:163], a[192:195], v[48:51]\n"
    "ds_read_b128 v[144:147], v234 offset:12288\n"
    "ds_read_b128 v[148:151], v234 offset:13312\n"
    "v_mfma_f32_16x16x32_bf16 v[48:51], v[164:167], a[200:203], v[48:51]\n"
    "s_mov_b32 s33, 8192\n"
    "buffer_load_dwordx4 v[200:203], v232, s[60:63], s33 offen sc1\n"
    "v_mfma_f32_16x16x32_bf16 v[48:51], v[168:171], a[208:211], v[48:51]\n"
    "ds_read_b128 v[152:155], v234 offset:14336\n"
    "ds_read_b128 v[156:159], v234 offset:15360\n"
    "L_chain4x2_mk:\n"
    "s_cbranch_execz L_pad4_4_mk\n"
    "v_mfma_f32_16x16x32_bf16 v[48:51], v[172:175], a[216:219], v[48:51]\n"
    "v_mfma_f32_16x16x32_bf16 v[52:55], v[172:175], a[220:223], v[52:55]\n"
    "v_mfma_f32_16x16x32_bf16 v[52:55], v[168:171], a[212:215], v[52:55]\n"
    "s_mov_b32 s33, 17408\n"
    "buffer_load_dwordx4 v[208:211], v232, s[60:63], s33 offen sc1\n"
    "v_mfma_f32_16x16x32_bf16 v[52:55], v[164:167], a[204:207], v[52:55]\n"
    "v_mfma_f32_16x16x32_bf16 v[52:55], v[160:163], a[196:199], v[52:55]\n"
    "s_waitcnt lgkmcnt(4)\n"
    "v_mfma_f32_16x16x32_bf16 v[60:63], v[128:131], a[196:199], v[60:63]\n"
    "ds_read_b128 v[160:163], v234 offset:16384\n"
    "ds_read_b128 v[164:167], v234 offset:17408\n"
    "v_mfma_f32_16x16x32_bf16 v[60:63], v[132:135], a[204:207], v[60:63]\n"
    "s_mov_b32 s33, 26624\n"
    "buffer_load_dwordx4 v[216:219], v232, s[60:63], s33 offen sc1\n"
    "v_mfma_f32_16x16x32_bf16 v[60:63], v[136:139], a[212:215], v[60:63]\n"
    "ds_read_b128 v[168:171], v234 offset:18432\n"
    "ds_read_b128 v[172:175], v234 offset:19456\n"
    "v_mfma_f32_16x16x32_bf16 v[60:63], v[140:143], a[220:223], v[60:63]\n"
    "v_mfma_f32_16x16x32_bf16 v[56:59], v[140:143], a[216:219], v[56:59]\n"
    "v_mfma_f32_16x16x32_bf16 v[56:59], v[136:139], a[208:211], v[56:59]\n"
    "v_mfma_f32_16x16x32_bf16 v[56:59], v[132:135], a[200:203], v[56:59]\n"
    "v_mfma_f32_16x16x32_bf16 v[56:59], v[128:131], a[192:195], v[56:59]\n"
    "s_waitcnt lgkmcnt(4)\n"
    "v_mfma_f32_16x16x32_bf16 v[64:67], v[144:147], a[192:195], v[64:67]\n"
    "ds_read_b128 v[128:131], v234 offset:20480\n"
    "ds_read_b128 v[132:135], v234 offset:21504\n"
    "v_mfma_f32_16x16x32_bf16 v[64:67], v[148:151], a[200:203], v[64:67]\n"
    "s_mov_b32 s33, 35840\n"
    "buffer_load_dwordx4 v[224:227], v232, s[60:63], s33 offen sc1\n"
    "v_mfma_f32_16x16x32_bf16 v[64:67], v[152:155], a[208:211], v[64:67]\n"
    "ds_read_b128 v[136:139], v234 offset:22528\n"
    "ds_read_b128 v[140:143], v234 offset:23552\n"
    "v_mfma_f32_16x16x32_bf16 v[64:67], v[156:159], a[216:219], v[64:67]\n"
    "v_mfma_f32_16x16x32_bf16 v[68:71], v[156:159], a[220:223], v[68:71]\n"
    "v_mfma_f32_16x16x32_bf16 v[68:71], v[152:155], a[212:215], v[68:71]\n"
    "s_mov_b32 s33, 155648\n"
    "buffer_load_dwordx4 v[204:207], v232, s[60:63], s33 offen sc1\n"
    "v_mfma_f32_16x16x32_bf16 v[68:71], v[148:151], a[204:207], v[68:71]\n"
    "v_mfma_f32_16x16x32_bf16 v[68:71], v[144:147], a[196:199], v[68:71]\n"
    "s_waitcnt lgkmcnt(4)\n"
    "v_mfma_f32_16x16x32_bf16 v[76:79], v[160:163], a[196:199], v[76:79]\n"
    "ds_read_b128 v[144:147], v234 offset:24576\n"
    "ds_read_b128 v[148:151], v234 offset:25600\n"
    "v_mfma_f32_16x16x32_bf16 v[76:79], v[164:167], a[204:207], v[76:79]\n"
    "s_mov_b32 s33, 164864\n"
    "buffer_load_dwordx4 v[212:215], v232, s[60:63], s33 offen sc1\n"
    "v_mfma_f32_16x16x32_bf16 v[76:79], v[168:171], a[212:215], v[76:79]\n"
    "ds_read_b128 v[152:155], v234 offset:26624\n"
    "ds_read_b128 v[156:159], v234 offset:27648\n"
    "v_mfma_f32_16x16x32_bf16 v[76:79], v[172:175], a[220:223], v[76:79]\n"
    "v_mfma_f32_16x16x32_bf16 v[72:75], v[172:175], a[216:219], v[72:75]\n"
    "v_mfma_f32_16x16x32_bf16 v[72:75], v[168:171], a[208:211], v[72:75]\n"
    "s_mov_b32 s33, 174080\n"
    "buffer_load_dwordx4 v[220:223], v232, s[60:63], s33 offen sc1\n"
    "v_mfma_f32_16x16x32_bf16 v[72:75], v[164:167], a[200:203], v[72:75]\n"
    "v_mfma_f32_16x16x32_bf16 v[72:75], v[160:163], a[192:195], v[72:75]\n"
    "s_waitcnt lgkmcnt(4)\n"
    "v_mfma_f32_16x16x32_bf16 v[80:83], v[128:131], a[192:195], v[80:83]\n"
    "ds_read_b128 v[160:163], v234 offset:28672\n"
    "ds_read_b128 v[164:167], v234 offset:29696\n"
    "v_mfma_f32_16x16x32_bf16 v[80:83], v[132:135], a[200:203], v[80:83]\n"
    "s_mov_b32 s33, 183296\n"
    "buffer_load_dwordx4 v[228:231], v232, s[60:63], s33 offen sc1\n"
    "v_mfma_f32_16x16x32_bf16 v[80:83], v[136:139], a[208:211], v[80:83]\n"
    "ds_read_b128 v[168:171], v234 offset:30720\n"
    "ds_read_b128 v[172:175], v234 offset:31744\n"
    "v_mfma_f32_16x16x32_bf16 v[80:83], v[140:143], a[216:219], v[80:83]\n"
    "v_mfma_f32_16x16x32_bf16 v[84:87], v[140:143], a[220:223], v[84:87]\n"
    "v_mfma_f32_16x16x32_bf16 v[84:87], v[136:139], a[212:215], v[84:87]\n"
    "v_mfma_f32_16x16x32_bf16 v[84:87], v[132:135], a[204:207], v[84:87]\n"
    "v_mfma_f32_16x16x32_bf16 v[84:87], v[128:131], a[196:199], v[84:87]\n"
    "s_waitcnt vmcnt(8) lgkmcnt(4)\n"
    "v_mfma_f32_16x16x32_bf16 v[36:39], v[144:147], a[228:231], v[36:39]\n"
    "s_add_u32 m0, s52, 4096\n"
    "ds_read_b128 v[128:131], v234 offset:32768\n"
    "ds_read_b128 v[132:135], v234 offset:33792\n"
    "v_mfma_f32_16x16x32_bf16 v[36:39], v[148:151], a[236:239], v[36:39]\n"
    "s_add_u32 s53, s52, 262144\n"
    "buffer_load_dwordx4 v232, s[48:51], s53 offen offset:0 sc1 lds\n"
    "v_mfma_f32_16x16x32_bf16 v[36:39], v[152:155], a[244:247], v[36:39]\n"
    "ds_read_b128 v[136:139], v234 offset:34816\n"
    "ds_read_b128 v[140:143], v234 offset:35840\n"
    "v_mfma_f32_16x16x32_bf16 v[36:39], v[156:159], a[252:255], v[36:39]\n"
    "v_mfma_f32_16x16x32_bf16 v[32:35], v[156:159], a[248:251], v[32:35]\n"
    "v_mfma_f32_16x16x32_bf16 v[32:35], v[152:155], a[240:243], v[32:35]\n"
    "buffer_load_dwordx4 v232, s[48:51], s53 offen offset:1024 sc1 lds\n"
    "v_mfma_f32_16x16x32_bf16 v[32:35], v[148:151], a[232:235], v[32:35]\n"
    "v_mfma_f32_16x16x32_bf16 v[32:35], v[144:147], a[224:227], v[32:35]\n"
    "s_waitcnt lgkmcnt(4)\n"
    "v_mfma_f32_16x16x32_bf16 v[40:43], v[160:163], a[224:227], v[40:43]\n"
    "ds_read_b128 v[144:147], v234 offset:36864\n"
    "ds_read_b128 v[148:151], v234 offset:37888\n"
    "v_mfma_f32_16x16x32_bf16 v[40:43], v[164:167], a[232:235], v[40:43]\n"
    "buffer_load_dwordx4 v232, s[48:51], s53 offen offset:2048 sc1 lds\n"
    "v_mfma_f32_16x16x32_bf16 v[40:43], v[168:171], a[240:243], v[40:43]\n"
    "ds_read_b128 v[152:155], v234 offset:38912\n"
    "ds_read_b128 v[156:159], v234 offset:39936\n"
    "v_mfma_f32_16x16x32_bf16 v[40:43], v[172:175], a[248:251], v[40:43]\n"
    "v_mfma_f32_16x16x32_bf16 v[44:47], v[172:175], a[252:255], v[44:47]\n"
    "v_mfma_f32_16x16x32_bf16 v[44:47], v[168:171], a[244:247], v[44:47]\n"
    "buffer_load_dwordx4 v232, s[48:51], s53 offen offset:3072 sc1 lds\n"
    "v_mfma_f32_16x16x32_bf16 v[44:47], v[164:167], a[236:239], v[44:47]\n"
    "v_mfma_f32_16x16x32_bf16 v[44:47], v[160:163], a[228:231], v[44:47]\n"
    "s_waitcnt lgkmcnt(4)\n"
    "v_mfma_f32_16x16x32_bf16 v[52:55], v[128:131], a[228:231], v[52:55]\n"
    "s_add_u32 m0, s52, 8192\n"
    "ds_read_b128 v[160:163], v234 offset:40960\n"
    "ds_read_b128 v[164:167], v234 offset:41984\n"
    "v_mfma_f32_16x16x32_bf16 v[52:55], v[132:135], a[236:239], v[52:55]\n"
    "s_add_u32 s53, s52, 266240\n"
    "buffer_load_dwordx4 v232, s[48:51], s53 offen offset:0 sc1 lds\n"
    "v_mfma_f32_16x16x32_bf16 v[52:55], v[136:139], a[244:247], v[52:55]\n"
    "ds_read_b128 v[168:171], v234 offset:43008\n"
    "ds_read_b128 v[172:175], v234 offset:44032\n"
    "v_mfma_f32_16x16x32_bf16 v[52:55], v[140:143], a[252:255], v[52:55]\n"
    "v_mfma_f32_16x16x32_bf16 v[48:51], v[140:143], a[248:251], v[48:51]\n"
    "v_mfma_f32_16x16x32_bf16 v[48:51], v[136:139], a[240:243], v[48:51]\n"
    "v_mfma_f32_16x16x32_bf16 v[48:51], v[132:135], a[232:235], v[48:51]\n"
    "v_mfma_f32_16x16x32_bf16 v[48:51], v[128:131], a[224:227], v[48:51]\n"
    "s_waitcnt vmcnt(25) lgkmcnt(0)\n"
    "v_mfma_f32_16x16x32_bf16 v[56:59], v[144:147], a[224:227], v[56:59]\n"
    "s_barrier\n"
    "v_mfma_f32_16x16x32_bf16 v[56:59], v[148:151], a[232:235], v[56:59]\n"
    "s_add_u32 m0, s52, 36864\n"
    "s_add_u32 s53, s52, 294912\n"
    "ds_read_b128 v[128:131], v234 offset:45056\n"
    "v_mfma_f32_16x16x32_bf16 v[56:59], v[152:155], a[240:243], v[56:59]\n"
    "buffer_load_dwordx4 v232, s[48:51], s53 offen offset:0 sc1 lds\n"
    "v_mfma_f32_16x16x32_bf16 v[56:59], v[156:159], a[248:251], v[56:59]\n"
    "ds_read_b128 v[132:135], v234 offset:46080\n"
    "ds_read_b128 v[136:139], v234 offset:47104\n"
    "v_mfma_f32_16x16x32_bf16 v[60:63], v[156:159], a[252:255], v[60:63]\n"
    "ds_read_b128 v[140:143], v234 offset:48128\n"
    "v_mfma_f32_16x16x32_bf16 v[60:63], v[152:155], a[244:247], v[60:63]\n"
    "buffer_load_dwordx4 v232, s[48:51], s53 offen offset:1024 sc1 lds\n"
    "v_mfma_f32_16x16x32_bf16 v[60:63], v[148:151], a[236:239], v[60:63]\n"
    "v_mfma_f32_16x16x32_bf16 v[60:63], v[144:147], a[228:231], v[60:63]\n"
    "v_mfma_f32_16x16x32_bf16 v[68:71], v[160:163], a[228:231], v[68:71]\n"
    "ds_read_b128 v[144:147], v234 offset:49152\n"
    "ds_read_b128 v[148:151], v234 offset:50176\n"
    "v_mfma_f32_16x16x32_bf16 v[68:71], v[164:167], a[236:239], v[68:71]\n"
    "buffer_load_dwordx4 v232, s[48:51], s53 offen offset:2048 sc1 lds\n"
    "v_mfma_f32_16x16x32_bf16 v[68:71], v[168:171], a[244:247], v[68:71]\n"
    "ds_read_b128 v[152:155], v234 offset:51200\n"
    "ds_read_b128 v[156:159], v234 offset:52224\n"
    "v_mfma_f32_16x16x32_bf16 v[68:71], v[172:175], a[252:255], v[68:71]\n"
    "v_mfma_f32_16x16x32_bf16 v[64:67], v[172:175], a[248:251], v[64:67]\n"
    "v_mfma_f32_16x16x32_bf16 v[64:67], v[168:171], a[240:243], v[64:67]\n"
    "buffer_load_dwordx4 v232, s[48:51], s53 offen offset:3072 sc1 lds\n"
    "v_mfma_f32_16x16x32_bf16 v[64:67], v[164:167], a[232:235], v[64:67]\n"
    "v_mfma_f32_16x16x32_bf16 v[64:67], v[160:163], a[224:227], v[64:67]\n"
    "s_waitcnt lgkmcnt(4)\n"
    "v_mfma_f32_16x16x32_bf16 v[72:75], v[128:131], a[224:227], v[72:75]\n"
    "s_add_u32 m0, s52, 40960\n"
    "ds_read_b128 v[160:163], v234 offset:53248\n"
    "ds_read_b128 v[164:167], v234 offset:54272\n"
    "v_mfma_f32_16x16x32_bf16 v[72:75], v[132:135], a[232:235], v[72:75]\n"
    "s_add_u32 s53, s52, 299008\n"
    "buffer_load_dwordx4 v232, s[48:51], s53 offen offset:0 sc1 lds\n"
    "v_mfma_f32_16x16x32_bf16 v[72:75], v[136:139], a[240:243], v[72:75]\n"
    "ds_read_b128 v[168:171], v234 offset:55296\n"
    "ds_read_b128 v[172:175], v234 offset:56320\n"
    "v_mfma_f32_16x16x32_bf16 v[72:75], v[140:143], a[248:251], v[72:75]\n"
    "v_mfma_f32_16x16x32_bf16 v[76:79], v[140:143], a[252:255], v[76:79]\n"
    "v_mfma_f32_16x16x32_bf16 v[76:79], v[136:139], a[244:247], v[76:79]\n"
    "v_mfma_f32_16x16x32_bf16 v[76:79], v[132:135], a[236:239], v[76:79]\n"
    "v_mfma_f32_16x16x32_bf16 v[76:79], v[128:131], a[228:231], v[76:79]\n"
    "s_waitcnt lgkmcnt(4)\n"
    "v_mfma_f32_16x16x32_bf16 v[84:87], v[144:147], a[228:231], v[84:87]\n"
    "ds_read_b128 v[128:131], v234 offset:57344\n"
    "ds_read_b128 v[132:135], v234 offset:58368\n"
    "v_mfma_f32_16x16x32_bf16 v[84:87], v[148:151], a[236:239], v[84:87]\n"
    "buffer_load_dwordx4 v232, s[48:51], s53 offen offset:1024 sc1 lds\n"
    "v_mfma_f32_16x16x32_bf16 v[84:87], v[152:155], a[244:247], v[84:87]\n"
    "ds_read_b128 v[136:139], v234 offset:59392\n"
    "ds_read_b128 v[140:143], v234 offset:60416\n"
    "v_mfma_f32_16x16x32_bf16 v[84:87], v[156:159], a[252:255], v[84:87]\n"
    "v_mfma_f32_16x16x32_bf16 v[80:83], v[156:159], a[248:251], v[80:83]\n"
    "v_mfma_f32_16x16x32_bf16 v[80:83], v[152:155], a[240:243], v[80:83]\n"
    "buffer_load_dwordx4 v232, s[48:51], s53 offen offset:2048 sc1 lds\n"
    "v_mfma_f32_16x16x32_bf16 v[80:83], v[148:151], a[232:235], v[80:83]\n"
    "v_mfma_f32_16x16x32_bf16 v[80:83], v[144:147], a[224:227], v[80:83]\n"
    "s_waitcnt vmcnt(12) lgkmcnt(4)\n"
    "v_mfma_f32_16x16x32_bf16 v[32:35], v[160:163], v[200:203], v[32:35]\n"
    "ds_read_b128 v[144:147], v234 offset:61440\n"
    "ds_read_b128 v[148:151], v234 offset:62464\n"
    "v_mfma_f32_16x16x32_bf16 v[32:35], v[164:167], v[208:211], v[32:35]\n"
    "buffer_load_dwordx4 v232, s[48:51], s53 offen offset:3072 sc1 lds\n"
    "v_mfma_f32_16x16x32_bf16 v[32:35], v[168:171], v[216:219], v[32:35]\n"
    "ds_read_b128 v[152:155], v234 offset:63488\n"
    "ds_read_b128 v[156:159], v234 offset:64512\n"
    "v_mfma_f32_16x16x32_bf16 v[32:35], v[172:175], v[224:227], v[32:35]\n"
    "v_mfma_f32_16x16x32_bf16 v[36:39], v[172:175], v[228:231], v[36:39]\n"
    "s_add_u32 m0, s52, 45056\n"
    "v_mfma_f32_16x16x32_bf16 v[36:39], v[168:171], v[220:223], v[36:39]\n"
    "s_add_u32 s53, s52, 303104\n"
    "buffer_load_dwordx4 v232, s[48:51], s53 offen offset:0 sc1 lds\n"
    "v_mfma_f32_16x16x32_bf16 v[36:39], v[164:167], v[212:215], v[36:39]\n"
    "v_mfma_f32_16x16x32_bf16 v[36:39], v[160:163], v[204:207], v[36:39]\n"
    "s_waitcnt lgkmcnt(4)\n"
    "v_mfma_f32_16x16x32_bf16 v[44:47], v[128:131], v[204:207], v[44:47]\n"
    "ds_read_b128 v[160:163], v235 offset:0\n"
    "ds_read_b128 v[164:167], v235 offset:1024\n"
    "v_mfma_f32_16x16x32_bf16 v[44:47], v[132:135], v[212:215], v[44:47]\n"
    "v_mfma_f32_16x16x32_bf16 v[44:47], v[136:139], v[220:223], v[44:47]\n"
    "ds_read_b128 v[168:171], v235 offset:2048\n"
    "ds_read_b128 v[172:175], v235 offset:3072\n"
    "v_mfma_f32_16x16x32_bf16 v[44:47], v[140:143], v[228:231], v[44:47]\n"
    "v_mfma_f32_16x16x32_bf16 v[40:43], v[140:143], v[224:227], v[40:43]\n"
    "v_mfma_f32_16x16x32_bf16 v[40:43], v[136:139], v[216:219], v[40:43]\n"
    "v_mfma_f32_16x16x32_bf16 v[40:43], v[132:135], v[208:211], v[40:43]\n"
    "v_mfma_f32_16x16x32_bf16 v[40:43], v[128:131], v[200:203], v[40:43]\n"
    "s_waitcnt lgkmcnt(4)\n"
    "v_mfma_f32_16x16x32_bf16 v[48:51], v[144:147], v[200:203], v[48:51]\n"
    "v_cvt_pk_bf16_f32 v32, v32, v33\n"
    "v_cvt_pk_bf16_f32 v33, v34, v35\n"
    "v_mfma_f32_16x16x32_bf16 v[48:51], v[148:151], v[208:211], v[48:51]\n"
    "v_cvt_pk_bf16_f32 v34, v36, v37\n"
    "v_cvt_pk_bf16_f32 v35, v38, v39\n"
    "v_mfma_f32_16x16x32_bf16 v[48:51], v[152:155], v[216:219], v[48:51]\n"
    "buffer_store_dwordx4 v[32:35], v236, s[72:75], 0 offen offset:0 sc1\n"
    "v_mfma_f32_16x16x32_bf16 v[48:51], v[156:159], v[224:227], v[48:51]\n"
    "ds_read_b128 v[128:131], v235 offset:4096\n"
    "ds_read_b128 v[132:135], v235 offset:5120\n"
    "v_mfma_f32_16x16x32_bf16 v[52:55], v[156:159], v[228:231], v[52:55]\n"
    "ds_read_b128 v[136:139], v235 offset:6144\n"
    "ds_read_b128 v[140:143], v235 offset:7168\n"
    "v_mfma_f32_16x16x32_bf16 v[52:55], v[152:155], v[220:223], v[52:55]\n"
    "v_mfma_f32_16x16x32_bf16 v[52:55], v[148:151], v[212:215], v[52:55]\n"
    "v_mfma_f32_16x16x32_bf16 v[52:55], v[144:147], v[204:207], v[52:55]\n"
    "s_waitcnt lgkmcnt(4)\n"
    "v_mfma_f32_16x16x32_bf16 v[60:63], v[160:163], v[204:207], v[60:63]\n"
    "ds_read_b128 v[144:147], v235 offset:8192\n"
    "ds_read_b128 v[148:151], v235 offset:9216\n"
    "v_mfma_f32_16x16x32_bf16 v[60:63], v[164:167], v[212:215], v[60:63]\n"
    "v_mfma_f32_16x16x32_bf16 v[60:63], v[168:171], v[220:223], v[60:63]\n"
    "ds_read_b128 v[152:155], v235 offset:10240\n"
    "ds_read_b128 v[156:159], v235 offset:11264\n"
    "v_mfma_f32_16x16x32_bf16 v[60:63], v[172:175], v[228:231], v[60:63]\n"
    "v_mfma_f32_16x16x32_bf16 v[56:59], v[172:175], v[224:227], v[56:59]\n"
    "v_mfma_f32_16x16x32_bf16 v[56:59], v[168:171], v[216:219], v[56:59]\n"
    "v_mfma_f32_16x16x32_bf16 v[56:59], v[164:167], v[208:211], v[56:59]\n"
    "v_mfma_f32_16x16x32_bf16 v[56:59], v[160:163], v[200:203], v[56:59]\n"
    "s_waitcnt lgkmcnt(4)\n"
    "v_mfma_f32_16x16x32_bf16 v[64:67], v[128:131], v[200:203], v[64:67]\n"
    "v_cvt_pk_bf16_f32 v40, v40, v41\n"
    "v_cvt_pk_bf16_f32 v41, v42, v43\n"
    "v_mfma_f32_16x16x32_bf16 v[64:67], v[132:135], v[208:211], v[64:67]\n"
    "v_cvt_pk_bf16_f32 v42, v44, v45\n"
    "v_cvt_pk_bf16_f32 v43, v46, v47\n"
    "v_mfma_f32_16x16x32_bf16 v[64:67], v[136:139], v[216:219], v[64:67]\n"
    "buffer_store_dwordx4 v[40:43], v236, s[72:75], 0 offen offset:64 sc1\n"
    "v_mfma_f32_16x16x32_bf16 v[64:67], v[140:143], v[224:227], v[64:67]\n"
    "ds_read_b128 v[160:163], v235 offset:12288\n"
    "ds_read_b128 v[164:167], v235 offset:13312\n"
    "v_mfma_f32_16x16x32_bf16 v[68:71], v[140:143], v[228:231], v[68:71]\n"
    "ds_read_b128 v[168:171], v235 offset:14336\n"
    "ds_read_b128 v[172:175], v235 offset:15360\n"
    "v_mfma_f32_16x16x32_bf16 v[68:71], v[136:139], v[220:223], v[68:71]\n"
    "v_mfma_f32_16x16x32_bf16 v[68:71], v[132:135], v[212:215], v[68:71]\n"
    "v_mfma_f32_16x16x32_bf16 v[68:71], v[128:131], v[204:207], v[68:71]\n"
    "s_waitcnt vmcnt(11) lgkmcnt(0)\n"
    "v_mfma_f32_16x16x32_bf16 v[76:79], v[144:147], v[204:207], v[76:79]\n"
    "s_barrier\n"
    "v_mfma_f32_16x16x32_bf16 v[76:79], v[148:151], v[212:215], v[76:79]\n"
    "ds_read_b128 v[128:131], v233 offset:0\n"
    "ds_read_b128 v[132:135], v233 offset:1024\n"
    "v_mfma_f32_16x16x32_bf16 v[76:79], v[152:155], v[220:223], v[76:79]\n"
    "ds_read_b128 v[136:139], v233 offset:2048\n"
    "ds_read_b128 v[140:143], v233 offset:3072\n"
    "v_mfma_f32_16x16x32_bf16 v[76:79], v[156:159], v[228:231], v[76:79]\n"
    "v_mfma_f32_16x16x32_bf16 v[72:75], v[156:159], v[224:227], v[72:75]\n"
    "v_mfma_f32_16x16x32_bf16 v[72:75], v[152:155], v[216:219], v[72:75]\n"
    "v_mfma_f32_16x16x32_bf16 v[72:75], v[148:151], v[208:211], v[72:75]\n"
    "v_mfma_f32_16x16x32_bf16 v[72:75], v[144:147], v[200:203], v[72:75]\n"
    "v_mfma_f32_16x16x32_bf16 v[80:83], v[160:163], v[200:203], v[80:83]\n"
    "v_cvt_pk_bf16_f32 v48, v48, v49\n"
    "v_cvt_pk_bf16_f32 v49, v50, v51\n"
    "v_mfma_f32_16x16x32_bf16 v[80:83], v[164:167], v[208:211], v[80:83]\n"
    "v_cvt_pk_bf16_f32 v50, v52, v53\n"
    "v_cvt_pk_bf16_f32 v51, v54, v55\n"
    "v_mfma_f32_16x16x32_bf16 v[80:83], v[168:171], v[216:219], v[80:83]\n"
    "buffer_store_dwordx4 v[48:51], v236, s[72:75], 0 offen offset:128 sc1\n"
    "v_mfma_f32_16x16x32_bf16 v[80:83], v[172:175], v[224:227], v[80:83]\n"
    "ds_read_b128 v[144:147], v233 offset:4096\n"
    "ds_read_b128 v[148:151], v233 offset:5120\n"
    "v_mfma_f32_16x16x32_bf16 v[84:87], v[172:175], v[228:231], v[84:87]\n"
    "ds_read_b128 v[152:155], v233 offset:6144\n"
    "ds_read_b128 v[156:159], v233 offset:7168\n"
    "v_mfma_f32_16x16x32_bf16 v[84:87], v[168:171], v[220:223], v[84:87]\n"
    "v_mfma_f32_16x16x32_bf16 v[84:87], v[164:167], v[212:215], v[84:87]\n"
    "v_mfma_f32_16x16x32_bf16 v[84:87], v[160:163], v[204:207], v[84:87]\n"
    "s_waitcnt lgkmcnt(4)\n"
    "v_mfma_f32_16x16x32_bf16 v[92:95], v[128:131], a[4:7], 0\n"
    "ds_read_b128 v[160:163], v233 offset:8192\n"
    "ds_read_b128 v[164:167], v233 offset:9216\n"
    "v_mfma_f32_16x16x32_bf16 v[92:95], v[132:135], a[12:15], v[92:95]\n"
    "v_mfma_f32_16x16x32_bf16 v[92:95], v[136:139], a[20:23], v[92:95]\n"
    "ds_read_b128 v[168:171], v233 offset:10240\n"
    "ds_read_b128 v[172:175], v233 offset:11264\n"
    "v_mfma_f32_16x16x32_bf16 v[92:95], v[140:143], a[28:31], v[92:95]\n"
    "v_mfma_f32_16x16x32_bf16 v[88:91], v[140:143], a[24:27], 0\n"
    "v_mfma_f32_16x16x32_bf16 v[88:91], v[136:139], a[16:19], v[88:91]\n"
    "v_mfma_f32_16x16x32_bf16 v[88:91], v[132:135], a[8:11], v[88:91]\n"
    "v_mfma_f32_16x16x32_bf16 v[88:91], v[128:131], a[0:3], v[88:91]\n"
    "s_waitcnt lgkmcnt(4)\n"
    "v_mfma_f32_16x16x32_bf16 v[88:91], v[144:147], a[32:35], v[88:91]\n"
    "v_cvt_pk_bf16_f32 v56, v56, v57\n"
    "v_cvt_pk_bf16_f32 v57, v58, v59\n"
    "v_mfma_f32_16x16x32_bf16 v[88:91], v[148:151], a[40:43], v[88:91]\n"
    "v_cvt_pk_bf16_f32 v58, v60, v61\n"
    "v_cvt_pk_bf16_f32 v59, v62, v63\n"
    "v_mfma_f32_16x16x32_bf16 v[88:91], v[152:155], a[48:51], v[88:91]\n"
    "buffer_store_dwordx4 v[56:59], v236, s[72:75], 0 offen offset:192 sc1\n"
    "v_mfma_f32_16x16x32_bf16 v[88:91], v[156:159], a[56:59], v[88:91]\n"
    "ds_read_b128 v[128:131], v233 offset:12288\n"
    "ds_read_b128 v[132:135], v233 offset:13312\n"
    "v_mfma_f32_16x16x32_bf16 v[92:95], v[156:159], a[60:63], v[92:95]\n"
    "ds_read_b128 v[136:139], v233 offset:14336\n"
    "ds_read_b128 v[140:143], v233 offset:15360\n"
    "v_mfma_f32_16x16x32_bf16 v[92:95], v[152:155], a[52:55], v[92:95]\n"
    "v_mfma_f32_16x16x32_bf16 v[92:95], v[148:151], a[44:47], v[92:95]\n"
    "v_mfma_f32_16x16x32_bf16 v[92:95], v[144:147], a[36:39], v[92:95]\n"
    "s_waitcnt lgkmcnt(4)\n"
    "v_mfma_f32_16x16x32_bf16 v[92:95], v[160:163], a[68:71], v[92:95]\n"
    "ds_read_b128 v[144:147], v233 offset:16384\n"
    "ds_read_b128 v[148:151], v233 offset:17408\n"
    "v_mfma_f32_16x16x32_bf16 v[92:95], v[164:167], a[76:79], v[92:95]\n"
    "v_mfma_f32_16x16x32_bf16 v[92:95], v[168:171], a[84:87], v[92:95]\n"
    "ds_read_b128 v[152:155], v233 offset:18432\n"
    "ds_read_b128 v[156:159], v233 offset:19456\n"
    "v_mfma_f32_16x16x32_bf16 v[92:95], v[172:175], a[92:95], v[92:95]\n"
    "v_mfma_f32_16x16x32_bf16 v[88:91], v[172:175], a[88:91], v[88:91]\n"
    "v_mfma_f32_16x16x32_bf16 v[88:91], v[168:171], a[80:83], v[88:91]\n"
    "v_mfma_f32_16x16x32_bf16 v[88:91], v[164:167], a[72:75], v[88:91]\n"
    "v_mfma_f32_16x16x32_bf16 v[88:91], v[160:163], a[64:67], v[88:91]\n"
    "s_waitcnt lgkmcnt(4)\n"
    "v_mfma_f32_16x16x32_bf16 v[88:91], v[128:131], a[96:99], v[88:91]\n"
    "v_cvt_pk_bf16_f32 v64, v64, v65\n"
    "v_cvt_pk_bf16_f32 v65, v66, v67\n"
    "v_mfma_f32_16x16x32_bf16 v[88:91], v[132:135], a[104:107], v[88:91]\n"
    "v_cvt_pk_bf16_f32 v66, v68, v69\n"
    "v_cvt_pk_bf16_f32 v67, v70, v71\n"
    "v_mfma_f32_16x16x32_bf16 v[88:91], v[136:139], a[112:115], v[88:91]\n"
    "buffer_store_dwordx4 v[64:67], v236, s[72:75], 0 offen offset:256 sc1\n"
    "v_mfma_f32_16x16x32_bf16 v[88:91], v[140:143], a[120:123], v[88:91]\n"
    "ds_read_b128 v[160:163], v233 offset:20480\n"
    "ds_read_b128 v[164:167], v233 offset:21504\n"
    "v_mfma_f32_16x16x32_bf16 v[92:95], v[140:143], a[124:127], v[92:95]\n"
    "ds_read_b128 v[168:171], v233 offset:22528\n"
    "ds_read_b128 v[172:175], v233 offset:23552\n"
    "v_mfma_f32_16x16x32_bf16 v[92:95], v[136:139], a[116:119], v[92:95]\n"
    "v_mfma_f32_16x16x32_bf16 v[92:95], v[132:135], a[108:111], v[92:95]\n"
    "v_mfma_f32_16x16x32_bf16 v[92:95], v[128:131], a[100:103], v[92:95]\n"
    "s_waitcnt lgkmcnt(4)\n"
    "v_mfma_f32_16x16x32_bf16 v[92:95], v[144:147], a[132:135], v[92:95]\n"
    "ds_read_b128 v[128:131], v233 offset:24576\n"
    "ds_read_b128 v[132:135], v233 offset:25600\n"
    "v_mfma_f32_16x16x32_bf16 v[92:95], v[148:151], a[140:143], v[92:95]\n"
    "v_mfma_f32_16x16x32_bf16 v[92:95], v[152:155], a[148:151], v[92:95]\n"
    "ds_read_b128 v[136:139], v233 offset:26624\n"
    "ds_read_b128 v[140:143], v233 offset:27648\n"
    "v_mfma_f32_16x16x32_bf16 v[92:95], v[156:159], a[156:159], v[92:95]\n"
    "v_mfma_f32_16x16x32_bf16 v[88:91], v[156:159], a[152:155], v[88:91]\n"
    "v_mfma_f32_16x16x32_bf16 v[88:91], v[152:155], a[144:147], v[88:91]\n"
    "v_mfma_f32_16x16x32_bf16 v[88:91], v[148:151], a[136:139], v[88:91]\n"
    "v_mfma_f32_16x16x32_bf16 v[88:91], v[144:147], a[128:131], v[88:91]\n"
    "s_waitcnt lgkmcnt(4)\n"
    "v_mfma_f32_16x16x32_bf16 v[88:91], v[160:163], a[160:163], v[88:91]\n"
    "v_cvt_pk_bf16_f32 v72, v72, v73\n"
    "v_cvt_pk_bf16_f32 v73, v74, v75\n"
    "v_mfma_f32_16x16x32_bf16 v[88:91], v[164:167], a[168:171], v[88:91]\n"
    "v_cvt_pk_bf16_f32 v74, v76, v77\n"
    "v_cvt_pk_bf16_f32 v75, v78, v79\n"
    "v_mfma_f32_16x16x32_bf16 v[88:91], v[168:171], a[176:179], v[88:91]\n"
    "buffer_store_dwordx4 v[72:75], v236, s[72:75], 0 offen offset:320 sc1\n"
    "v_mfma_f32_16x16x32_bf16 v[88:91], v[172:175], a[184:187], v[88:91]\n"
    "ds_read_b128 v[144:147], v233 offset:28672\n"
    "ds_read_b128 v[148:151], v233 offset:29696\n"
    "v_mfma_f32_16x16x32_bf16 v[92:95], v[172:175], a[188:191], v[92:95]\n"
    "ds_read_b128 v[152:155], v233 offset:30720\n"
    "ds_read_b128 v[156:159], v233 offset:31744\n"
    "v_mfma_f32_16x16x32_bf16 v[92:95], v[168:171], a[180:183], v[92:95]\n"
    "v_mfma_f32_16x16x32_bf16 v[92:95], v[164:167], a[172:175], v[92:95]\n"
    "v_mfma_f32_16x16x32_bf16 v[92:95], v[160:163], a[164:167], v[92:95]\n"
    "s_waitcnt lgkmcnt(4)\n"
    "v_mfma_f32_16x16x32_bf16 v[92:95], v[128:131], a[196:199], v[92:95]\n"
    "ds_read_b128 v[160:163], v233 offset:32768\n"
    "ds_read_b128 v[164:167], v233 offset:33792\n"
    "v_mfma_f32_16x16x32_bf16 v[92:95], v[132:135], a[204:207], v[92:95]\n"
    "v_mfma_f32_16x16x32_bf16 v[92:95], v[136:139], a[212:215], v[92:95]\n"
    "ds_read_b128 v[168:171], v233 offset:34816\n"
    "ds_read_b128 v[172:175], v233 offset:35840\n"
    "v_mfma_f32_16x16x32_bf16 v[92:95], v[140:143], a[220:223], v[92:95]\n"
    "v_mfma_f32_16x16x32_bf16 v[88:91], v[140:143], a[216:219], v[88:91]\n"
    "v_mfma_f32_16x16x32_bf16 v[88:91], v[136:139], a[208:211], v[88:91]\n"
    "v_mfma_f32_16x16x32_bf16 v[88:91], v[132:135], a[200:203], v[88:91]\n"
    "v_mfma_f32_16x16x32_bf16 v[88:91], v[128:131], a[192:195], v[88:91]\n"
    "s_waitcnt vmcnt(6) lgkmcnt(0)\n"
    "v_mfma_f32_16x16x32_bf16 v[88:91], v[144:147], a[224:227], v[88:91]\n"
    "s_barrier\n"
    "v_mfma_f32_16x16x32_bf16 v[88:91], v[148:151], a[232:235], v[88:91]\n"
    "v_cvt_pk_bf16_f32 v80, v80, v81\n"
    "v_cvt_pk_bf16_f32 v81, v82, v83\n"
    "v_mfma_f32_16x16x32_bf16 v[88:91], v[152:155], a[240:243], v[88:91]\n"
    "v_cvt_pk_bf16_f32 v82, v84, v85\n"
    "v_cvt_pk_bf16_f32 v83, v86, v87\n"
    "v_mfma_f32_16x16x32_bf16 v[88:91], v[156:159], a[248:251], v[88:91]\n"
    "buffer_store_dwordx4 v[80:83], v236, s[72:75], 0 offen offset:384 sc1\n"
    "v_mfma_f32_16x16x32_bf16 v[92:95], v[156:159], a[252:255], v[92:95]\n"
    "ds_read_b128 v[128:131], v233 offset:36864\n"
    "ds_read_b128 v[132:135], v233 offset:37888\n"
    "v_mfma_f32_16x16x32_bf16 v[92:95], v[152:155], a[244:247], v[92:95]\n"
    "ds_read_b128 v[136:139], v233 offset:38912\n"
    "ds_read_b128 v[140:143], v233 offset:39936\n"
    "v_mfma_f32_16x16x32_bf16 v[92:95], v[148:151], a[236:239], v[92:95]\n"
    "v_mfma_f32_16x16x32_bf16 v[92:95], v[144:147], a[228:231], v[92:95]\n"
    "v_mfma_f32_16x16x32_bf16 v[92:95], v[160:163], v[204:207], v[92:95]\n"
    "ds_read_b128 v[144:147], v233 offset:40960\n"
    "ds_read_b128 v[148:151], v233 offset:41984\n"
    "v_mfma_f32_16x16x32_bf16 v[92:95], v[164:167], v[212:215], v[92:95]\n"
    "v_mfma_f32_16x16x32_bf16 v[92:95], v[168:171], v[220:223], v[92:95]\n"
    "ds_read_b128 v[152:155], v233 offset:43008\n"
    "ds_read_b128 v[156:159], v233 offset:44032\n"
    "v_mfma_f32_16x16x32_bf16 v[92:95], v[172:175], v[228:231], v[92:95]\n"
    "v_mfma_f32_16x16x32_bf16 v[88:91], v[172:175], v[224:227], v[88:91]\n"
    "v_mfma_f32_16x16x32_bf16 v[88:91], v[168:171], v[216:219], v[88:91]\n"
    "v_mfma_f32_16x16x32_bf16 v[88:91], v[164:167], v[208:211], v[88:91]\n"
    "v_mfma_f32_16x16x32_bf16 v[88:91], v[160:163], v[200:203], v[88:91]\n"
    "s_waitcnt lgkmcnt(4)\n"
    "v_mfma_f32_16x16x32_bf16 v[0:3], v[128:131], a[0:3], 0\n"
    "ds_read_b128 v[160:163], v233 offset:45056\n"
    "ds_read_b128 v[164:167], v233 offset:46080\n"
    "v_mfma_f32_16x16x32_bf16 v[0:3], v[132:135], a[8:11], v[0:3]\n"
    "v_mfma_f32_16x16x32_bf16 v[0:3], v[136:139], a[16:19], v[0:3]\n"
    "ds_read_b128 v[168:171], v233 offset:47104\n"
    "ds_read_b128 v[172:175], v233 offset:48128\n"
    "v_mfma_f32_16x16x32_bf16 v[0:3], v[140:143], a[24:27], v[0:3]\n"
    "v_mfma_f32_16x16x32_bf16 v[4:7], v[140:143], a[28:31], 0\n"
    "v_mfma_f32_16x16x32_bf16 v[4:7], v[136:139], a[20:23], v[4:7]\n"
    "v_mfma_f32_16x16x32_bf16 v[4:7], v[132:135], a[12:15], v[4:7]\n"
    "v_mfma_f32_16x16x32_bf16 v[4:7], v[128:131], a[4:7], v[4:7]\n"
    "s_waitcnt lgkmcnt(4)\n"
    "v_mfma_f32_16x16x32_bf16 v[4:7], v[144:147], a[36:39], v[4:7]\n"
    "v_cvt_pk_bf16_f32 v88, v88, v89\n"
    "v_cvt_pk_bf16_f32 v89, v90, v91\n"
    "v_mfma_f32_16x16x32_bf16 v[4:7], v[148:151], a[44:47], v[4:7]\n"
    "v_cvt_pk_bf16_f32 v90, v92, v93\n"
    "v_cvt_pk_bf16_f32 v91, v94, v95\n"
    "v_mfma_f32_16x16x32_bf16 v[4:7], v[152:155], a[52:55], v[4:7]\n"
    "buffer_store_dwordx4 v[88:91], v236, s[72:75], 0 offen offset:448 sc1\n"
    "v_mfma_f32_16x16x32_bf16 v[4:7], v[156:159], a[60:63], v[4:7]\n"
    "ds_read_b128 v[128:131], v233 offset:49152\n"
    "ds_read_b128 v[132:135], v233 offset:50176\n"
    "v_mfma_f32_16x16x32_bf16 v[0:3], v[156:159], a[56:59], v[0:3]\n"
    "ds_read_b128 v[136:139], v233 offset:51200\n"
    "ds_read_b128 v[140:143], v233 offset:52224\n"
    "v_mfma_f32_16x16x32_bf16 v[0:3], v[152:155], a[48:51], v[0:3]\n"
    "v_mfma_f32_16x16x32_bf16 v[0:3], v[148:151], a[40:43], v[0:3]\n"
    "v_mfma_f32_16x16x32_bf16 v[0:3], v[144:147], a[32:35], v[0:3]\n"
    "s_waitcnt lgkmcnt(4)\n"
    "v_mfma_f32_16x16x32_bf16 v[0:3], v[160:163], a[64:67], v[0:3]\n"
    "ds_read_b128 v[144:147], v233 offset:53248\n"
    "ds_read_b128 v[148:151], v233 offset:54272\n"
    "v_mfma_f32_16x16x32_bf16 v[0:3], v[164:167], a[72:75], v[0:3]\n"
    "v_mfma_f32_16x16x32_bf16 v[0:3], v[168:171], a[80:83], v[0:3]\n"
    "ds_read_b128 v[152:155], v233 offset:55296\n"
    "ds_read_b128 v[156:159], v233 offset:56320\n"
    "v_mfma_f32_16x16x32_bf16 v[0:3], v[172:175], a[88:91], v[0:3]\n"
    "v_mfma_f32_16x16x32_bf16 v[4:7], v[172:175], a[92:95], v[4:7]\n"
    "v_mfma_f32_16x16x32_bf16 v[4:7], v[168:171], a[84:87], v[4:7]\n"
    "v_mfma_f32_16x16x32_bf16 v[4:7], v[164:167], a[76:79], v[4:7]\n"
    "v_mfma_f32_16x16x32_bf16 v[4:7], v[160:163], a[68:71], v[4:7]\n"
    "s_waitcnt lgkmcnt(4)\n"
    "v_mfma_f32_16x16x32_bf16 v[4:7], v[128:131], a[100:103], v[4:7]\n"
    "ds_read_b128 v[160:163], v233 offset:57344\n"
    "ds_read_b128 v[164:167], v233 offset:58368\n"
    "v_mfma_f32_16x16x32_bf16 v[4:7], v[132:135], a[108:111], v[4:7]\n"
    "v_mfma_f32_16x16x32_bf16 v[4:7], v[136:139], a[116:119], v[4:7]\n"
    "ds_read_b128 v[168:171], v233 offset:59392\n"
    "ds_read_b128 v[172:175], v233 offset:60416\n"
    "v_mfma_f32_16x16x32_bf16 v[4:7], v[140:143], a[124:127], v[4:7]\n"
    "v_mfma_f32_16x16x32_bf16 v[0:3], v[140:143], a[120:123], v[0:3]\n"
    "v_mfma_f32_16x16x32_bf16 v[0:3], v[136:139], a[112:115], v[0:3]\n"
    "v_mfma_f32_16x16x32_bf16 v[0:3], v[132:135], a[104:107], v[0:3]\n"
    "v_mfma_f32_16x16x32_bf16 v[0:3], v[128:131], a[96:99], v[0:3]\n"
    "s_waitcnt lgkmcnt(4)\n"
    "v_mfma_f32_16x16x32_bf16 v[0:3], v[144:147], a[128:131], v[0:3]\n"
    "ds_read_b128 v[128:131], v233 offset:61440\n"
    "ds_read_b128 v[132:135], v233 offset:62464\n"
    "v_mfma_f32_16x16x32_bf16 v[0:3], v[148:151], a[136:139], v[0:3]\n"
    "v_mfma_f32_16x16x32_bf16 v[0:3], v[152:155], a[144:147], v[0:3]\n"
    "ds_read_b128 v[136:139], v233 offset:63488\n"
    "ds_read_b128 v[140:143], v233 offset:64512\n"
    "v_mfma_f32_16x16x32_bf16 v[0:3], v[156:159], a[152:155], v[0:3]\n"
    "v_mfma_f32_16x16x32_bf16 v[4:7], v[156:159], a[156:159], v[4:7]\n"
    "v_mfma_f32_16x16x32_bf16 v[4:7], v[152:155], a[148:151], v[4:7]\n"
    "v_mfma_f32_16x16x32_bf16 v[4:7], v[148:151], a[140:143], v[4:7]\n"
    "v_mfma_f32_16x16x32_bf16 v[4:7], v[144:147], a[132:135], v[4:7]\n"
    "s_waitcnt lgkmcnt(4)\n"
    "v_mfma_f32_16x16x32_bf16 v[4:7], v[160:163], a[164:167], v[4:7]\n"
    "ds_read_b128 v[144:147], v234 offset:0\n"
    "ds_read_b128 v[148:151], v234 offset:1024\n"
    "v_mfma_f32_16x16x32_bf16 v[4:7], v[164:167], a[172:175], v[4:7]\n"
    "v_mfma_f32_16x16x32_bf16 v[4:7], v[168:171], a[180:183], v[4:7]\n"
    "ds_read_b128 v[152:155], v234 offset:2048\n"
    "ds_read_b128 v[156:159], v234 offset:3072\n"
    "v_mfma_f32_16x16x32_bf16 v[4:7], v[172:175], a[188:191], v[4:7]\n"
    "v_mfma_f32_16x16x32_bf16 v[0:3], v[172:175], a[184:187], v[0:3]\n"
    "v_mfma_f32_16x16x32_bf16 v[0:3], v[168:171], a[176:179], v[0:3]\n"
    "v_mfma_f32_16x16x32_bf16 v[0:3], v[164:167], a[168:171], v[0:3]\n"
    "v_mfma_f32_16x16x32_bf16 v[0:3], v[160:163], a[160:163], v[0:3]\n"
    "s_waitcnt lgkmcnt(4)\n"
    "v_mfma_f32_16x16x32_bf16 v[0:3], v[128:131], a[192:195], v[0:3]\n"
    "ds_read_b128 v[160:163], v234 offset:4096\n"
    "ds_read_b128 v[164:167], v234 offset:5120\n"
    "v_mfma_f32_16x16x32_bf16 v[0:3], v[132:135], a[200:203], v[0:3]\n"
    "v_mfma_f32_16x16x32_bf16 v[0:3], v[136:139], a[208:211], v[0:3]\n"
    "ds_read_b128 v[168:171], v234 offset:6144\n"
    "ds_read_b128 v[172:175], v234 offset:7168\n"
    "v_mfma_f32_16x16x32_bf16 v[0:3], v[140:143], a[216:219], v[0:3]\n"
    "v_mfma_f32_16x16x32_bf16 v[4:7], v[140:143], a[220:223], v[4:7]\n"
    "v_mfma_f32_16x16x32_bf16 v[4:7], v[136:139], a[212:215], v[4:7]\n"
    "v_mfma_f32_16x16x32_bf16 v[4:7], v[132:135], a[204:207], v[4:7]\n"
    "v_mfma_f32_16x16x32_bf16 v[4:7], v[128:131], a[196:199], v[4:7]\n"
    "s_waitcnt lgkmcnt(0)\n"
    "v_mfma_f32_16x16x32_bf16 v[4:7], v[144:147], a[228:231], v[4:7]\n"
    "s_barrier\n"
    "v_mfma_f32_16x16x32_bf16 v[4:7], v[148:151], a[236:239], v[4:7]\n"
    "L_chain4x1_mk:\n"
    "s_cbranch_execz L_pad4_5_mk\n"
    "v_mfma_f32_16x16x32_bf16 v[4:7], v[152:155], a[244:247], v[4:7]\n"
    "v_mfma_f32_16x16x32_bf16 v[4:7], v[156:159], a[252:255], v[4:7]\n"
    "v_mfma_f32_16x16x32_bf16 v[0:3], v[156:159], a[248:251], v[0:3]\n"
    "v_mfma_f32_16x16x32_bf16 v[0:3], v[152:155], a[240:243], v[0:3]\n"
    "v_mfma_f32_16x16x32_bf16 v[0:3], v[148:151], a[232:235], v[0:3]\n"
    "v_mfma_f32_16x16x32_bf16 v[0:3], v[144:147], a[224:227], v[0:3]\n"
    "v_mfma_f32_16x16x32_bf16 v[0:3], v[160:163], v[200:203], v[0:3]\n"
    "v_mfma_f32_16x16x32_bf16 v[0:3], v[164:167], v[208:211], v[0:3]\n"
    "v_mfma_f32_16x16x32_bf16 v[0:3], v[168:171], v[216:219], v[0:3]\n"
    "v_mfma_f32_16x16x32_bf16 v[0:3], v[172:175], v[224:227], v[0:3]\n"
    "v_mfma_f32_16x16x32_bf16 v[4:7], v[172:175], v[228:231], v[4:7]\n"
    "v_mfma_f32_16x16x32_bf16 v[4:7], v[168:171], v[220:223], v[4:7]\n"
    "v_mfma_f32_16x16x32_bf16 v[4:7], v[164:167], v[212:215], v[4:7]\n"
    "v_mfma_f32_16x16x32_bf16 v[4:7], v[160:163], v[204:207], v[4:7]\n"
    "s_nop 7\n"
    "v_cvt_pk_bf16_f32 v0, v0, v1\n"
    "v_cvt_pk_bf16_f32 v1, v2, v3\n"
    "v_cvt_pk_bf16_f32 v2, v4, v5\n"
    "v_cvt_pk_bf16_f32 v3, v6, v7\n"
    "buffer_store_dwordx4 v[0:3], v236, s[72:75], 0 offen offset:512 sc1\n"
    "s_nop 1\n"
    "s_lshl_b32 s42, s89, 2\n"
    "s_add_u32 s42, s42, s88\n"
    "s_and_b32 s40, s42, 15\n"
    "s_lshr_b32 s41, s42, 4\n"
    "s_lshl_b32 s41, s41, 5\n"
    "s_add_u32 s40, s40, s41\n"
    "s_mul_i32 s40, s40, 1152\n"
    "s_add_u32 s28, s86, s40\n"
    "s_addc_u32 s29, s87, 0\n"
    "s_and_b32 s29, s29, 0xffff\n"
    "s_mov_b32 s30, 19584\n"
    "s_mov_b32 s31, 0x20000\n"
    "s_mov_b32 s37, 18432\n"
    "s_mul_i32 s40, s42, 2304\n"
    "s_add_u32 s40, s40, 22020096\n"
    "s_add_u32 s20, s64, s40\n"
    "s_addc_u32 s21, s65, 0\n"
    "s_and_b32 s21, s21, 0xffff\n"
    "s_mov_b32 s22, 7080192\n"
    "s_mov_b32 s23, 0x20000\n"
    "v_mov_b32_e32 v136, v232\n"
    "v_add_u32_e32 v137, 1024, v232\n"
    "v_add_u32_e32 v138, 2048, v232\n"
    "v_mov_b32_e32 v120, 0x7f000000\n"
    "v_cmp_gt_u32_e32 vcc, 256, v232\n"
    "v_lshrrev_b32_e32 v139, 1, v136\n"
    "v_lshrrev_b32_e32 v140, 1, v137\n"
    "v_cndmask_b32_e32 v138, v120, v138, vcc\n"
    "v_lshrrev_b32_e32 v141, 1, v138\n"
    "s_add_u32 s40, s68, 11520\n"
    "buffer_load_dwordx2 v[148:149], v136, s[96:99], s40 offen sc1\n"
    "buffer_load_dwordx2 v[152:153], v137, s[96:99], s40 offen sc1\n"
    "buffer_load_dwordx2 v[156:157], v138, s[96:99], s40 offen sc1\n"
    "v_lshrrev_b32_e32 v194, 4, v232\n"
    "s_and_b32 s43, s89, 0xe0\n"
    "s_and_b32 s41, s89, 1\n"
    "s_lshl_b32 s41, s41, 4\n"
    "s_add_u32 s43, s43, s41\n"
    "s_bfe_u32 s40, s89, 0x40001\n"
    "v_mul_u32_u24_e32 v194, 2048, v194\n"
    "s_mul_i32 s43, s43, 2048\n"
    "s_mul_i32 s40, s40, 64\n"
    "s_add_u32 s43, s43, s40\n"
    "s_add_u32 s43, s43, 524292\n"
    "v_mov_b32_e32 v192, 1\n"
    "s_waitcnt vmcnt(3)\n"
    "s_barrier\n"
    "s_cmp_lg_u32 s88, 1\n"
    "s_cbranch_scc1 L_psh55_mk\n"
    "s_mov_b64 exec, 0xffff\n"
    "buffer_store_dword v192, v194, s[96:99], s43 offen sc1\n"
    "s_mov_b64 exec, -1\n"
    "L_psh55_mk:\n"
    "s_cmp_lg_u32 s88, 0\n"
    "s_cbranch_scc1 L_polled57_mk\n"
    "s_mul_i32 s41, s89, 2048\n"
    "s_add_u32 s41, s41, 524292\n"
    "v_and_b32_e32 v198, 240, v232\n"
    "v_lshlrev_b32_e32 v198, 2, v198\n"
    "s_mov_b32 s92, 0x40000\n"
    "L_poll56_mk:\n"
    "L_pad4_3_mk:\n"
    "s_cbranch_execz L_pad4_6_mk\n"
    "buffer_load_dword v196, v198, s[96:99], s41 offen sc1\n"
    "s_sub_u32 s92, s92, 1\n"
    "s_waitcnt vmcnt(0)\n"
    "v_cmp_gt_u32_e32 vcc, 1, v196\n"
    "s_cbranch_scc1 L_polled57_mk\n"
    "s_cbranch_vccnz L_poll56_mk\n"
    "L_polled57_mk:\n"
    "s_barrier\n"
    "s_mov_b32 s40, 0\n"
    "buffer_load_dwordx4 v[0:3], v136, s[20:23], s40 offen sc1\n"
    "s_mov_b32 s40, 2359296\n"
    "buffer_load_dwordx4 v[4:7], v136, s[20:23], s40 offen sc1\n"
    "s_mov_b32 s40, 4718592\n"
    "buffer_load_dwordx4 v[8:11], v136, s[20:23], s40 offen sc1\n"
    "s_mov_b32 s40, 7077888\n"
    "buffer_load_dwordx4 v[12:15], v136, s[20:23], s40 offen sc1\n"
    "s_mov_b32 s40, 0\n"
    "buffer_load_dwordx4 v[16:19], v137, s[20:23], s40 offen sc1\n"
    "s_mov_b32 s40, 2359296\n"
    "buffer_load_dwordx4 v[20:23], v137, s[20:23], s40 offen sc1\n"
    "s_mov_b32 s40, 4718592\n"
    "buffer_load_dwordx4 v[24:27], v137, s[20:23], s40 offen sc1\n"
    "s_mov_b32 s40, 7077888\n"
    "buffer_load_dwordx4 v[28:31], v137, s[20:23], s40 offen sc1\n"
    "s_mov_b32 s40, 0\n"
    "buffer_load_dwordx4 v[32:35], v138, s[20:23], s40 offen sc1\n"
    "s_mov_b32 s40, 2359296\n"
    "buffer_load_dwordx4 v[36:39], v138, s[20:23], s40 offen sc1\n"
    "s_mov_b32 s40, 4718592\n"
    "buffer_load_dwordx4 v[40:43], v138, s[20:23], s40 offen sc1\n"
    "s_mov_b32 s40, 7077888\n"
    "buffer_load_dwordx4 v[44:47], v138, s[20:23], s40 offen sc1\n"
    "s_waitcnt vmcnt(12)\n"
    "v_lshlrev_b32_e32 v48, 16, v148\n"
    "v_and_b32_e32 v49, 0xffff0000, v148\n"
    "v_lshlrev_b32_e32 v50, 16, v149\n"
    "v_and_b32_e32 v51, 0xffff0000, v149\n"
    "v_lshlrev_b32_e32 v52, 16, v152\n"
    "v_and_b32_e32 v53, 0xffff0000, v152\n"
    "v_lshlrev_b32_e32 v54, 16, v153\n"
    "v_and_b32_e32 v55, 0xffff0000, v153\n"
    "v_lshlrev_b32_e32 v56, 16, v156\n"
    "v_and_b32_e32 v57, 0xffff0000, v156\n"
    "v_lshlrev_b32_e32 v58, 16, v157\n"
    "v_and_b32_e32 v59, 0xffff0000, v157\n"
    "v_mov_b32_e32 v120, 0x3ef5fdf0\n"
    "v_pk_mul_f32 v[48:49], v[48:49], v[120:121] op_sel_hi:[1,0]\n"
    "v_pk_mul_f32 v[50:51], v[50:51], v[120:121] op_sel_hi:[1,0]\n"
    "v_pk_mul_f32 v[52:53], v[52:53], v[120:121] op_sel_hi:[1,0]\n"
    "v_pk_mul_f32 v[54:55], v[54:55], v[120:121] op_sel_hi:[1,0]\n"
    "v_pk_mul_f32 v[56:57], v[56:57], v[120:121] op_sel_hi:[1,0]\n"
    "v_pk_mul_f32 v[58:59], v[58:59], v[120:121] op_sel_hi:[1,0]\n"
    "v_lshlrev_b32_e32 v96, 16, v240\n"
    "v_and_b32_e32 v97, 0xffff0000, v240\n"
    "v_lshlrev_b32_e32 v98, 16, v241\n"
    "v_and_b32_e32 v99, 0xffff0000, v241\n"
    "v_lshlrev_b32_e32 v100, 16, v242\n"
    "v_and_b32_e32 v101, 0xffff0000, v242\n"
    "v_lshlrev_b32_e32 v102, 16, v243\n"
    "v_and_b32_e32 v103, 0xffff0000, v243\n"
    "v_lshlrev_b32_e32 v104, 16, v244\n"
    "v_and_b32_e32 v105, 0xffff0000, v244\n"
    "v_lshlrev_b32_e32 v106, 16, v245\n"
    "v_and_b32_e32 v107, 0xffff0000, v245\n"
    "v_lshlrev_b32_e32 v108, 16, v246\n"
    "v_and_b32_e32 v109, 0xffff0000, v246\n"
    "v_lshlrev_b32_e32 v110, 16, v247\n"
    "v_and_b32_e32 v111, 0xffff0000, v247\n"
    "v_lshlrev_b32_e32 v112, 16, v248\n"
    "v_and_b32_e32 v113, 0xffff0000, v248\n"
    "v_lshlrev_b32_e32 v114, 16, v249\n"
    "v_and_b32_e32 v115, 0xffff0000, v249\n"
    "v_lshlrev_b32_e32 v116, 16, v250\n"
    "v_and_b32_e32 v117, 0xffff0000, v250\n"
    "v_lshlrev_b32_e32 v118, 16, v251\n"
    "v_and_b32_e32 v119, 0xffff0000, v251\n"
    "s_waitcnt vmcnt(8)\n"
    "v_lshlrev_b32_e32 v120, 16, v0\n"
    "v_and_b32_e32 v121, 0xffff0000, v0\n"
    "v_lshlrev_b32_e32 v122, 16, v4\n"
    "L_pad4_4_mk:\n"
    "s_cbranch_execz L_ret4_mk\n"
    "v_and_b32_e32 v123, 0xffff0000, v4\n"
    "v_lshlrev_b32_e32 v124, 16, v8\n"
    "v_and_b32_e32 v125, 0xffff0000, v8\n"
    "v_lshlrev_b32_e32 v126, 16, v12\n"
    "v_and_b32_e32 v127, 0xffff0000, v12\n"
    "v_pk_add_f32 v[120:121], v[120:121], v[122:123]\n"
    "v_pk_add_f32 v[124:125], v[124:125], v[126:127]\n"
    "v_pk_add_f32 v[120:121], v[120:121], v[124:125]\n"
    "v_pk_fma_f32 v[96:97], v[120:121], v[48:49], v[96:97]\n"
    "v_cvt_pk_bf16_f32 v84, v96, v97\n"
    "v_lshlrev_b32_e32 v120, 16, v1\n"
    "v_and_b32_e32 v121, 0xffff0000, v1\n"
    "v_lshlrev_b32_e32 v122, 16, v5\n"
    "v_and_b32_e32 v123, 0xffff0000, v5\n"
    "v_lshlrev_b32_e32 v124, 16, v9\n"
    "v_and_b32_e32 v125, 0xffff0000, v9\n"
    "v_lshlrev_b32_e32 v126, 16, v13\n"
    "v_and_b32_e32 v127, 0xffff0000, v13\n"
    "v_pk_add_f32 v[120:121], v[120:121], v[122:123]\n"
    "v_pk_add_f32 v[124:125], v[124:125], v[126:127]\n"
    "v_pk_add_f32 v[120:121], v[120:121], v[124:125]\n"
    "v_pk_fma_f32 v[98:99], v[120:121], v[50:51], v[98:99]\n"
    "v_cvt_pk_bf16_f32 v85, v98, v99\n"
    "v_lshlrev_b32_e32 v120, 16, v2\n"
    "v_and_b32_e32 v121, 0xffff0000, v2\n"
    "v_lshlrev_b32_e32 v122, 16, v6\n"
    "v_and_b32_e32 v123, 0xffff0000, v6\n"
    "v_lshlrev_b32_e32 v124, 16, v10\n"
    "v_and_b32_e32 v125, 0xffff0000, v10\n"
    "v_lshlrev_b32_e32 v126, 16, v14\n"
    "v_and_b32_e32 v127, 0xffff0000, v14\n"
    "v_pk_add_f32 v[120:121], v[120:121], v[122:123]\n"
    "v_pk_add_f32 v[124:125], v[124:125], v[126:127]\n"
    "v_pk_add_f32 v[120:121], v[120:121], v[124:125]\n"
    "v_pk_fma_f32 v[100:101], v[120:121], v[48:49], v[100:101]\n"
    "v_cvt_pk_bf16_f32 v86, v100, v101\n"
    "v_lshlrev_b32_e32 v120, 16, v3\n"
    "v_and_b32_e32 v121, 0xffff0000, v3\n"
    "v_lshlrev_b32_e32 v122, 16, v7\n"
    "v_and_b32_e32 v123, 0xffff0000, v7\n"
    "v_lshlrev_b32_e32 v124, 16, v11\n"
    "v_and_b32_e32 v125, 0xffff0000, v11\n"
    "v_lshlrev_b32_e32 v126, 16, v15\n"
    "v_and_b32_e32 v127, 0xffff0000, v15\n"
    "v_pk_add_f32 v[120:121], v[120:121], v[122:123]\n"
    "v_pk_add_f32 v[124:125], v[124:125], v[126:127]\n"
    "v_pk_add_f32 v[120:121], v[120:121], v[124:125]\n"
    "v_pk_fma_f32 v[102:103], v[120:121], v[50:51], v[102:103]\n"
    "v_cvt_pk_bf16_f32 v87, v102, v103\n"
    "buffer_store_dwordx2 v[84:85], v139, s[28:31], 0 offen\n"
    "buffer_store_dwordx2 v[86:87], v139, s[28:31], s37 offen\n"
    "s_waitcnt vmcnt(6)\n"
    "v_lshlrev_b32_e32 v120, 16, v16\n"
    "v_and_b32_e32 v121, 0xffff0000, v16\n"
    "v_lshlrev_b32_e32 v122, 16, v20\n"
    "v_and_b32_e32 v123, 0xffff0000, v20\n"
    "v_lshlrev_b32_e32 v124, 16, v24\n"
    "v_and_b32_e32 v125, 0xffff0000, v24\n"
    "v_lshlrev_b32_e32 v126, 16, v28\n"
    "v_and_b32_e32 v127, 0xffff0000, v28\n"
    "v_pk_add_f32 v[120:121], v[120:121], v[122:123]\n"
    "v_pk_add_f32 v[124:125], v[124:125], v[126:127]\n"
    "v_pk_add_f32 v[120:121], v[120:121], v[124:125]\n"
    "v_pk_fma_f32 v[104:105], v[120:121], v[52:53], v[104:105]\n"
    "v_cvt_pk_bf16_f32 v88, v104, v105\n"
    "v_lshlrev_b32_e32 v120, 16, v17\n"
    "v_and_b32_e32 v121, 0xffff0000, v17\n"
    "v_lshlrev_b32_e32 v122, 16, v21\n"
    "v_and_b32_e32 v123, 0xffff0000, v21\n"
    "v_lshlrev_b32_e32 v124, 16, v25\n"
    "v_and_b32_e32 v125, 0xffff0000, v25\n"
    "v_lshlrev_b32_e32 v126, 16, v29\n"
    "v_and_b32_e32 v127, 0xffff0000, v29\n"
    "v_pk_add_f32 v[120:121], v[120:121], v[122:123]\n"
    "v_pk_add_f32 v[124:125], v[124:125], v[126:127]\n"
    "L_pad4_5_mk:\n"
    "s_cbranch_execz L_ret4_mk\n"
    "v_pk_add_f32 v[120:121], v[120:121], v[124:125]\n"
    "v_pk_fma_f32 v[106:107], v[120:121], v[54:55], v[106:107]\n"
    "v_cvt_pk_bf16_f32 v89, v106, v107\n"
    "v_lshlrev_b32_e32 v120, 16, v18\n"
    "v_and_b32_e32 v121, 0xffff0000, v18\n"
    "v_lshlrev_b32_e32 v122, 16, v22\n"
    "v_and_b32_e32 v123, 0xffff0000, v22\n"
    "v_lshlrev_b32_e32 v124, 16, v26\n"
    "v_and_b32_e32 v125, 0xffff0000, v26\n"
    "v_lshlrev_b32_e32 v126, 16, v30\n"
    "v_and_b32_e32 v127, 0xffff0000, v30\n"
    "v_pk_add_f32 v[120:121], v[120:121], v[122:123]\n"
    "v_pk_add_f32 v[124:125], v[124:125], v[126:127]\n"
    "v_pk_add_f32 v[120:121], v[120:121], v[124:125]\n"
    "v_pk_fma_f32 v[108:109], v[120:121], v[52:53], v[108:109]\n"
    "v_cvt_pk_bf16_f32 v90, v108, v109\n"
    "v_lshlrev_b32_e32 v120, 16, v19\n"
    "v_and_b32_e32 v121, 0xffff0000, v19\n"
    "v_lshlrev_b32_e32 v122, 16, v23\n"
    "v_and_b32_e32 v123, 0xffff0000, v23\n"
    "v_lshlrev_b32_e32 v124, 16, v27\n"
    "v_and_b32_e32 v125, 0xffff0000, v27\n"
    "v_lshlrev_b32_e32 v126, 16, v31\n"
    "v_and_b32_e32 v127, 0xffff0000, v31\n"
    "v_pk_add_f32 v[120:121], v[120:121], v[122:123]\n"
    "v_pk_add_f32 v[124:125], v[124:125], v[126:127]\n"
    "v_pk_add_f32 v[120:121], v[120:121], v[124:125]\n"
    "v_pk_fma_f32 v[110:111], v[120:121], v[54:55], v[110:111]\n"
    "v_cvt_pk_bf16_f32 v91, v110, v111\n"
    "buffer_store_dwordx2 v[88:89], v140, s[28:31], 0 offen\n"
    "buffer_store_dwordx2 v[90:91], v140, s[28:31], s37 offen\n"
    "s_waitcnt vmcnt(4)\n"
    "v_lshlrev_b32_e32 v120, 16, v32\n"
    "v_and_b32_e32 v121, 0xffff0000, v32\n"
    "v_lshlrev_b32_e32 v122, 16, v36\n"
    "v_and_b32_e32 v123, 0xffff0000, v36\n"
    "v_lshlrev_b32_e32 v124, 16, v40\n"
    "v_and_b32_e32 v125, 0xffff0000, v40\n"
    "v_lshlrev_b32_e32 v126, 16, v44\n"
    "v_and_b32_e32 v127, 0xffff0000, v44\n"
    "v_pk_add_f32 v[120:121], v[120:121], v[122:123]\n"
    "v_pk_add_f32 v[124:125], v[124:125], v[126:127]\n"
    "v_pk_add_f32 v[120:121], v[120:121], v[124:125]\n"
    "v_pk_fma_f32 v[112:113], v[120:121], v[56:57], v[112:113]\n"
    "v_cvt_pk_bf16_f32 v92, v112, v113\n"
    "v_lshlrev_b32_e32 v120, 16, v33\n"
    "v_and_b32_e32 v121, 0xffff0000, v33\n"
    "v_lshlrev_b32_e32 v122, 16, v37\n"
    "v_and_b32_e32 v123, 0xffff0000, v37\n"
    "v_lshlrev_b32_e32 v124, 16, v41\n"
    "v_and_b32_e32 v125, 0xffff0000, v41\n"
    "v_lshlrev_b32_e32 v126, 16, v45\n"
    "v_and_b32_e32 v127, 0xffff0000, v45\n"
    "v_pk_add_f32 v[120:121], v[120:121], v[122:123]\n"
    "v_pk_add_f32 v[124:125], v[124:125], v[126:127]\n"
    "v_pk_add_f32 v[120:121], v[120:121], v[124:125]\n"
    "v_pk_fma_f32 v[114:115], v[120:121], v[58:59], v[114:115]\n"
    "v_cvt_pk_bf16_f32 v93, v114, v115\n"
    "v_lshlrev_b32_e32 v120, 16, v34\n"
    "v_and_b32_e32 v121, 0xffff0000, v34\n"
    "v_lshlrev_b32_e32 v122, 16, v38\n"
    "v_and_b32_e32 v123, 0xffff0000, v38\n"
    "v_lshlrev_b32_e32 v124, 16, v42\n"
    "v_and_b32_e32 v125, 0xffff0000, v42\n"
    "v_lshlrev_b32_e32 v126, 16, v46\n"
    "v_and_b32_e32 v127, 0xffff0000, v46\n"
    "v_pk_add_f32 v[120:121], v[120:121], v[122:123]\n"
    "v_pk_add_f32 v[124:125], v[124:125], v[126:127]\n"
    "v_pk_add_f32 v[120:121], v[120:121], v[124:125]\n"
    "v_pk_fma_f32 v[116:117], v[120:121], v[56:57], v[116:117]\n"
    "v_cvt_pk_bf16_f32 v94, v116, v117\n"
    "v_lshlrev_b32_e32 v120, 16, v35\n"
    "v_and_b32_e32 v121, 0xffff0000, v35\n"
    "v_lshlrev_b32_e32 v122, 16, v39\n"
    "v_and_b32_e32 v123, 0xffff0000, v39\n"
    "L_pad4_6_mk:\n"
    "s_cbranch_execz L_ret4_mk\n"
    "v_lshlrev_b32_e32 v124, 16, v43\n"
    "v_and_b32_e32 v125, 0xffff0000, v43\n"
    "v_lshlrev_b32_e32 v126, 16, v47\n"
    "v_and_b32_e32 v127, 0xffff0000, v47\n"
    "v_pk_add_f32 v[120:121], v[120:121], v[122:123]\n"
    "v_pk_add_f32 v[124:125], v[124:125], v[126:127]\n"
    "v_pk_add_f32 v[120:121], v[120:121], v[124:125]\n"
    "v_pk_fma_f32 v[118:119], v[120:121], v[58:59], v[118:119]\n"
    "v_cvt_pk_bf16_f32 v95, v118, v119\n"
    "buffer_store_dwordx2 v[92:93], v141, s[28:31], 0 offen\n"
    "buffer_store_dwordx2 v[94:95], v141, s[28:31], s37 offen\n"
    "s_cmp_lg_u32 s89, 0\n"
    "s_cbranch_scc1 L_sig58_mk\n"
    "s_mov_b32 s43, 233472\n"
    "s_cmp_lg_u32 s88, 0\n"
    "s_cbranch_scc1 L_sig59_mk\n"
    "v_mov_b32_e32 v238, s90\n"
    "v_mov_b32_e32 v239, s91\n"
    "s_mov_b64 exec, 1\n"
    "buffer_store_dwordx2 v[238:239], off, s[96:99], s43 sc1\n"
    "s_mov_b64 exec, -1\n"
    "L_sig59_mk:\n"
    "L_sig58_mk:\n"
    "s_branch L_fin_mk\n"
    ".p2align 12\n"
    "L_pad1_15_mk:\n"
    "s_cbranch_execz L_ret1_mk\n"
    "s_nop 0\n"
    ".p2align 6\n"
    "s_nop 0\n"
    "s_nop 0\n"
    "L_loop_mk:\n"
    "v_mfma_f32_16x16x32_bf16 v[128:131], a[64:67], a[0:3], v[160:163]\n"
    "v_exp_f32_e32 v223, v155\n"
    "v_mfma_f32_16x16x32_bf16 v[128:131], a[68:71], a[4:7], v[128:131]\n"
    "v_add_u32_e32 v226, s36, v229\n"
    "v_cvt_pk_bf16_f32 v205, v222, v223\n"
    "v_mfma_f32_16x16x32_bf16 v[128:131], a[72:75], a[8:11], v[128:131]\n"
    "v_exp_f32_e32 v220, v156\n"
    "v_mfma_f32_16x16x32_bf16 v[128:131], a[76:79], a[12:15], v[128:131]\n"
    "ds_add_u32 v244, v239\n"
    "v_mfma_f32_16x16x32_bf16 v[132:135], a[92:95], a[12:15], v[160:163]\n"
    "v_exp_f32_e32 v221, v157\n"
    "v_mfma_f32_16x16x32_bf16 v[132:135], a[88:91], a[8:11], v[132:135]\n"
    "v_add_u32_e32 v227, s36, v230\n"
    "v_cvt_pk_bf16_f32 v206, v220, v221\n"
    "v_mfma_f32_16x16x32_bf16 v[132:135], a[84:87], a[4:7], v[132:135]\n"
    "s_mov_b32 m0, s37\n"
    "v_exp_f32_e32 v222, v158\n"
    "v_mfma_f32_16x16x32_bf16 v[132:135], a[80:83], a[0:3], v[132:135]\n"
    "ds_read_b128 a[96:99], v225 offset:0\n"
    "ds_read_b128 a[100:103], v225 offset:256\n"
    "v_mfma_f32_16x16x32_bf16 v[140:143], a[80:83], a[16:19], v[164:167]\n"
    "v_exp_f32_e32 v223, v159\n"
    "v_mfma_f32_16x16x32_bf16 v[140:143], a[84:87], a[20:23], v[140:143]\n"
    "ds_read_b128 a[104:107], v225 offset:512\n"
    "v_cvt_pk_bf16_f32 v207, v222, v223\n"
    "v_mfma_f32_16x16x32_bf16 v[140:143], a[88:91], a[24:27], v[140:143]\n"
    "v_exp_f32_e32 v220, v128\n"
    "v_mfma_f32_16x16x32_bf16 v[140:143], a[92:95], a[28:31], v[140:143]\n"
    "ds_read_b128 a[108:111], v225 offset:768\n"
    "ds_read_b128 a[112:115], v225 offset:4096\n"
    "v_mfma_f32_16x16x32_bf16 v[136:139], a[76:79], a[28:31], v[164:167]\n"
    "v_exp_f32_e32 v221, v129\n"
    "v_mfma_f32_16x16x32_bf16 v[136:139], a[72:75], a[24:27], v[136:139]\n"
    "ds_read_b128 a[116:119], v225 offset:4352\n"
    "v_cvt_pk_bf16_f32 v176, v220, v221\n"
    "v_mfma_f32_16x16x32_bf16 v[136:139], a[68:71], a[20:23], v[136:139]\n"
    "v_exp_f32_e32 v222, v130\n"
    "v_mfma_f32_16x16x32_bf16 v[136:139], a[64:67], a[16:19], v[136:139]\n"
    "ds_read_b128 a[120:123], v225 offset:4608\n"
    "ds_read_b128 a[124:127], v225 offset:4864\n"
    "v_smfmac_f32_16x16x64_bf16 v[212:215], a[192:195], v[200:207], v245\n"
    "ds_read_b64_tr_b16 a[128:129], v226 offset:16384\n"
    "ds_read_b64_tr_b16 a[130:131], v226 offset:20480\n"
    "v_mfma_f32_16x16x32_bf16 v[0:3], a[160:163], v[192:195], v[0:3]\n"
    "v_exp_f32_e32 v223, v131\n"
    "v_mfma_f32_16x16x32_bf16 v[32:35], a[160:163], v[196:199], v[32:35]\n"
    "ds_read_b64_tr_b16 a[132:133], v227 offset:16384\n"
    "v_cvt_pk_bf16_f32 v177, v222, v223\n"
    "v_mfma_f32_16x16x32_bf16 v[36:39], a[164:167], v[196:199], v[36:39]\n"
    "v_exp_f32_e32 v220, v132\n"
    "v_mfma_f32_16x16x32_bf16 v[4:7], a[164:167], v[192:195], v[4:7]\n"
    "ds_read_b64_tr_b16 a[134:135], v227 offset:20480\n"
    "v_mfma_f32_16x16x32_bf16 v[8:11], a[168:171], v[192:195], v[8:11]\n"
    "v_exp_f32_e32 v221, v133\n"
    "v_mfma_f32_16x16x32_bf16 v[40:43], a[168:171], v[196:199], v[40:43]\n"
    "v_add_f32_e32 v242, v212, v214\n"
    "v_cvt_pk_bf16_f32 v178, v220, v221\n"
    "v_mfma_f32_16x16x32_bf16 v[44:47], a[172:175], v[196:199], v[44:47]\n"
    "v_exp_f32_e32 v222, v134\n"
    "v_mfma_f32_16x16x32_bf16 v[12:15], a[172:175], v[192:195], v[12:15]\n"
    "ds_read_b64_tr_b16 a[136:137], v226 offset:16640\n"
    "v_add_f32_e32 v243, v213, v215\n"
    "v_mfma_f32_16x16x32_bf16 v[16:19], a[176:179], v[192:195], v[16:19]\n"
    "v_exp_f32_e32 v223, v135\n"
    "v_mfma_f32_16x16x32_bf16 v[48:51], a[176:179], v[196:199], v[48:51]\n"
    "v_max_f32_e32 v224, v242, v243\n"
    "v_cvt_pk_bf16_f32 v179, v222, v223\n"
    "v_mfma_f32_16x16x32_bf16 v[52:55], a[180:183], v[196:199], v[52:55]\n"
    "v_exp_f32_e32 v220, v136\n"
    "v_mfma_f32_16x16x32_bf16 v[20:23], a[180:183], v[192:195], v[20:23]\n"
    "ds_read_b32 v238, v244\n"
    "v_cmp_nge_f32_e32 vcc, 0x67800000, v224\n"
    "v_mfma_f32_16x16x32_bf16 v[24:27], a[184:187], v[192:195], v[24:27]\n"
    "v_exp_f32_e32 v221, v137\n"
    "v_mfma_f32_16x16x32_bf16 v[56:59], a[184:187], v[196:199], v[56:59]\n"
    "s_cbranch_vccnz L_rare3_mk\n"
    "L_back3_mk:\n"
    "v_cvt_pk_bf16_f32 v180, v220, v221\n"
    "v_mfma_f32_16x16x32_bf16 v[60:63], a[188:191], v[196:199], v[60:63]\n"
    "v_exp_f32_e32 v222, v138\n"
    "v_mfma_f32_16x16x32_bf16 v[28:31], a[188:191], v[192:195], v[28:31]\n"
    "ds_read_b64_tr_b16 a[138:139], v226 offset:20736\n"
    "ds_read_b64_tr_b16 a[140:141], v227 offset:16640\n"
    "v_mfma_f32_16x16x32_bf16 v[144:147], a[64:67], a[32:35], v[168:171]\n"
    "s_waitcnt lgkmcnt(2)\n"
    "v_exp_f32_e32 v223, v139\n"
    "v_mfma_f32_16x16x32_bf16 v[144:147], a[68:71], a[36:39], v[144:147]\n"
    "v_cmp_gt_u32_e32 vcc, s38, v238\n"
    "v_cvt_pk_bf16_f32 v181, v222, v223\n"
    "v_mfma_f32_16x16x32_bf16 v[144:147], a[72:75], a[40:43], v[144:147]\n"
    "v_exp_f32_e32 v220, v140\n"
    "v_mfma_f32_16x16x32_bf16 v[144:147], a[76:79], a[44:47], v[144:147]\n"
    "s_cbranch_vccnz L_spin_mk\n"
    "L_spinback_mk:\n"
    "v_mfma_f32_16x16x32_bf16 v[148:151], a[92:95], a[44:47], v[168:171]\n"
    "v_exp_f32_e32 v221, v141\n"
    "v_mfma_f32_16x16x32_bf16 v[148:151], a[88:91], a[40:43], v[148:151]\n"
    "ds_read_b64_tr_b16 a[142:143], v227 offset:20736\n"
    "v_cvt_pk_bf16_f32 v182, v220, v221\n"
    "v_mfma_f32_16x16x32_bf16 v[148:151], a[84:87], a[36:39], v[148:151]\n"
    "v_exp_f32_e32 v222, v142\n"
    "v_mfma_f32_16x16x32_bf16 v[148:151], a[80:83], a[32:35], v[148:151]\n"
    "ds_read_b64_tr_b16 a[144:145], v226 offset:16896\n"
    "ds_read_b64_tr_b16 a[146:147], v226 offset:20992\n"
    "v_mfma_f32_16x16x32_bf16 v[156:159], a[80:83], a[48:51], v[172:175]\n"
    "v_exp_f32_e32 v223, v143\n"
    "v_mfma_f32_16x16x32_bf16 v[156:159], a[84:87], a[52:55], v[156:159]\n"
    "ds_read_b64_tr_b16 a[148:149], v227 offset:16896\n"
    "v_cvt_pk_bf16_f32 v183, v222, v223\n"
    "v_mfma_f32_16x16x32_bf16 v[156:159], a[88:91], a[56:59], v[156:159]\n"
    "v_exp_f32_e32 v220, v144\n"
    "v_mfma_f32_16x16x32_bf16 v[156:159], a[92:95], a[60:63], v[156:159]\n"
    "buffer_load_dwordx4 v231, s[20:23], s38 offen offset:0 sc1 lds\n"
    "v_mfma_f32_16x16x32_bf16 v[152:155], a[76:79], a[60:63], v[172:175]\n"
    "v_exp_f32_e32 v221, v145\n"
    "v_mfma_f32_16x16x32_bf16 v[152:155], a[72:75], a[56:59], v[152:155]\n"
    "ds_read_b64_tr_b16 a[150:151], v227 offset:20992\n"
    "v_cvt_pk_bf16_f32 v184, v220, v221\n"
    "v_mfma_f32_16x16x32_bf16 v[152:155], a[68:71], a[52:55], v[152:155]\n"
    "v_exp_f32_e32 v222, v146\n"
    "v_mfma_f32_16x16x32_bf16 v[152:155], a[64:67], a[48:51], v[152:155]\n"
    "ds_read_b64_tr_b16 a[152:153], v226 offset:17152\n"
    "ds_read_b64_tr_b16 a[154:155], v226 offset:21248\n"
    "v_smfmac_f32_16x16x64_bf16 v[208:211], a[192:195], v[176:183], v245\n"
    "ds_read_b64_tr_b16 a[156:157], v227 offset:17152\n"
    "v_mfma_f32_16x16x32_bf16 v[64:67], a[160:163], v[200:203], v[64:67]\n"
    "v_exp_f32_e32 v223, v147\n"
    "v_mfma_f32_16x16x32_bf16 v[96:99], a[160:163], v[204:207], v[96:99]\n"
    "ds_read_b64_tr_b16 a[158:159], v227 offset:21248\n"
    "v_cvt_pk_bf16_f32 v185, v222, v223\n"
    "v_mfma_f32_16x16x32_bf16 v[100:103], a[164:167], v[204:207], v[100:103]\n"
    "v_exp_f32_e32 v220, v148\n"
    "v_mfma_f32_16x16x32_bf16 v[68:71], a[164:167], v[200:203], v[68:71]\n"
    "buffer_load_dwordx4 v231, s[20:23], s38 offen offset:1024 sc1 lds\n"
    "v_mfma_f32_16x16x32_bf16 v[72:75], a[168:171], v[200:203], v[72:75]\n"
    "v_exp_f32_e32 v221, v149\n"
    "v_mfma_f32_16x16x32_bf16 v[104:107], a[168:171], v[204:207], v[104:107]\n"
    "v_add_f32_e32 v216, v208, v210\n"
    "v_cvt_pk_bf16_f32 v186, v220, v221\n"
    "v_mfma_f32_16x16x32_bf16 v[108:111], a[172:175], v[204:207], v[108:111]\n"
    "v_exp_f32_e32 v222, v150\n"
    "v_mfma_f32_16x16x32_bf16 v[76:79], a[172:175], v[200:203], v[76:79]\n"
    "v_add_f32_e32 v217, v209, v211\n"
    "v_mfma_f32_16x16x32_bf16 v[80:83], a[176:179], v[200:203], v[80:83]\n"
    "v_exp_f32_e32 v223, v151\n"
    "v_mfma_f32_16x16x32_bf16 v[112:115], a[176:179], v[204:207], v[112:115]\n"
    "v_max_f32_e32 v224, v216, v217\n"
    "v_cvt_pk_bf16_f32 v187, v222, v223\n"
    "v_mfma_f32_16x16x32_bf16 v[116:119], a[180:183], v[204:207], v[116:119]\n"
    "v_exp_f32_e32 v220, v152\n"
    "v_mfma_f32_16x16x32_bf16 v[84:87], a[180:183], v[200:203], v[84:87]\n"
    "v_cmp_nge_f32_e32 vcc, 0x67800000, v224\n"
    "v_mfma_f32_16x16x32_bf16 v[88:91], a[184:187], v[200:203], v[88:91]\n"
    "v_exp_f32_e32 v221, v153\n"
    "v_mfma_f32_16x16x32_bf16 v[120:123], a[184:187], v[204:207], v[120:123]\n"
    "s_cbranch_vccnz L_rare0_mk\n"
    "L_back0_mk:\n"
    "v_cvt_pk_bf16_f32 v188, v220, v221\n"
    "v_mfma_f32_16x16x32_bf16 v[124:127], a[188:191], v[204:207], v[124:127]\n"
    "s_waitcnt lgkmcnt(0)\n"
    "v_exp_f32_e32 v222, v154\n"
    "v_mfma_f32_16x16x32_bf16 v[92:95], a[188:191], v[200:203], v[92:95]\n"
    "buffer_load_dwordx4 v233, s[20:23], s38 offen offset:2048 sc1 lds\n"
    "L_mid_mk:\n"
    "v_mfma_f32_16x16x32_bf16 v[128:131], a[96:99], a[0:3], v[160:163]\n"
    "v_exp_f32_e32 v223, v155\n"
    "v_mfma_f32_16x16x32_bf16 v[128:131], a[100:103], a[4:7], v[128:131]\n"
    "ds_read_b128 a[64:67], v225 offset:8192\n"
    "v_cvt_pk_bf16_f32 v189, v222, v223\n"
    "v_mfma_f32_16x16x32_bf16 v[128:131], a[104:107], a[8:11], v[128:131]\n"
    "v_exp_f32_e32 v220, v156\n"
    "v_mfma_f32_16x16x32_bf16 v[128:131], a[108:111], a[12:15], v[128:131]\n"
    "ds_read_b128 a[68:71], v225 offset:8448\n"
    "ds_read_b128 a[72:75], v225 offset:8704\n"
    "v_mfma_f32_16x16x32_bf16 v[132:135], a[124:127], a[12:15], v[160:163]\n"
    "v_exp_f32_e32 v221, v157\n"
    "v_mfma_f32_16x16x32_bf16 v[132:135], a[120:123], a[8:11], v[132:135]\n"
    "ds_read_b128 a[76:79], v225 offset:8960\n"
    "v_cvt_pk_bf16_f32 v190, v220, v221\n"
    "v_mfma_f32_16x16x32_bf16 v[132:135], a[116:119], a[4:7], v[132:135]\n"
    "v_exp_f32_e32 v222, v158\n"
    "v_mfma_f32_16x16x32_bf16 v[132:135], a[112:115], a[0:3], v[132:135]\n"
    "ds_read_b128 a[80:83], v225 offset:12288\n"
    "v_mfma_f32_16x16x32_bf16 v[140:143], a[112:115], a[16:19], v[164:167]\n"
    "v_exp_f32_e32 v223, v159\n"
    "v_mfma_f32_16x16x32_bf16 v[140:143], a[116:119], a[20:23], v[140:143]\n"
    "ds_read_b128 a[84:87], v225 offset:12544\n"
    "v_cvt_pk_bf16_f32 v191, v222, v223\n"
    "v_mfma_f32_16x16x32_bf16 v[140:143], a[120:123], a[24:27], v[140:143]\n"
    "v_exp_f32_e32 v220, v128\n"
    "v_mfma_f32_16x16x32_bf16 v[140:143], a[124:127], a[28:31], v[140:143]\n"
    "buffer_load_dwordx4 v233, s[20:23], s38 offen offset:3072 sc1 lds\n"
    "v_mfma_f32_16x16x32_bf16 v[136:139], a[108:111], a[28:31], v[164:167]\n"
    "s_add_u32 m0, s37, 0x4000\n"
    "v_exp_f32_e32 v221, v129\n"
    "v_mfma_f32_16x16x32_bf16 v[136:139], a[104:107], a[24:27], v[136:139]\n"
    "ds_read_b128 a[88:91], v225 offset:12800\n"
    "v_cvt_pk_bf16_f32 v192, v220, v221\n"
    "v_mfma_f32_16x16x32_bf16 v[136:139], a[100:103], a[20:23], v[136:139]\n"
    "v_exp_f32_e32 v222, v130\n"
    "v_mfma_f32_16x16x32_bf16 v[136:139], a[96:99], a[16:19], v[136:139]\n"
    "ds_read_b128 a[92:95], v225 offset:13056\n"
    "ds_read_b64_tr_b16 a[160:161], v226 offset:24576\n"
    "v_smfmac_f32_16x16x64_bf16 v[212:215], a[192:195], v[184:191], v245\n"
    "ds_read_b64_tr_b16 a[162:163], v226 offset:28672\n"
    "v_mfma_f32_16x16x32_bf16 v[0:3], a[128:131], v[176:179], v[0:3]\n"
    "v_exp_f32_e32 v223, v131\n"
    "v_mfma_f32_16x16x32_bf16 v[32:35], a[128:131], v[180:183], v[32:35]\n"
    "ds_read_b64_tr_b16 a[164:165], v227 offset:24576\n"
    "v_cvt_pk_bf16_f32 v193, v222, v223\n"
    "v_mfma_f32_16x16x32_bf16 v[36:39], a[132:135], v[180:183], v[36:39]\n"
    "v_exp_f32_e32 v220, v132\n"
    "v_mfma_f32_16x16x32_bf16 v[4:7], a[132:135], v[176:179], v[4:7]\n"
    "buffer_load_dwordx4 v231, s[24:27], s38 offen offset:0 sc1 lds\n"
    "v_mfma_f32_16x16x32_bf16 v[8:11], a[136:139], v[176:179], v[8:11]\n"
    "s_add_u32 s40, s36, 0x8000\n"
    "v_exp_f32_e32 v221, v133\n"
    "v_mfma_f32_16x16x32_bf16 v[40:43], a[136:139], v[180:183], v[40:43]\n"
    "v_add_f32_e32 v218, v212, v214\n"
    "v_cvt_pk_bf16_f32 v194, v220, v221\n"
    "v_mfma_f32_16x16x32_bf16 v[44:47], a[140:143], v[180:183], v[44:47]\n"
    "s_add_u32 s37, s36, s39\n"
    "v_exp_f32_e32 v222, v134\n"
    "v_mfma_f32_16x16x32_bf16 v[12:15], a[140:143], v[176:179], v[12:15]\n"
    "ds_read_b64_tr_b16 a[166:167], v227 offset:28672\n"
    "v_add_f32_e32 v219, v213, v215\n"
    "v_mfma_f32_16x16x32_bf16 v[16:19], a[144:147], v[176:179], v[16:19]\n"
    "s_and_b32 s36, s40, 0x1ffff\n"
    "v_exp_f32_e32 v223, v135\n"
    "v_mfma_f32_16x16x32_bf16 v[48:51], a[144:147], v[180:183], v[48:51]\n"
    "v_max_f32_e32 v224, v218, v219\n"
    "v_cvt_pk_bf16_f32 v195, v222, v223\n"
    "v_mfma_f32_16x16x32_bf16 v[52:55], a[148:151], v[180:183], v[52:55]\n"
    "v_exp_f32_e32 v220, v136\n"
    "v_mfma_f32_16x16x32_bf16 v[20:23], a[148:151], v[176:179], v[20:23]\n"
    "ds_read_b64_tr_b16 a[168:169], v226 offset:24832\n"
    "v_cmp_nge_f32_e32 vcc, 0x67800000, v224\n"
    "v_mfma_f32_16x16x32_bf16 v[24:27], a[152:155], v[176:179], v[24:27]\n"
    "v_exp_f32_e32 v221, v137\n"
    "v_mfma_f32_16x16x32_bf16 v[56:59], a[152:155], v[180:183], v[56:59]\n"
    "s_cbranch_vccnz L_rare1_mk\n"
    "L_back1_mk:\n"
    "v_cvt_pk_bf16_f32 v196, v220, v221\n"
    "v_mfma_f32_16x16x32_bf16 v[60:63], a[156:159], v[180:183], v[60:63]\n"
    "v_exp_f32_e32 v222, v138\n"
    "v_mfma_f32_16x16x32_bf16 v[28:31], a[156:159], v[176:179], v[28:31]\n"
    "buffer_load_dwordx4 v233, s[24:27], s38 offen offset:1024 sc1 lds\n"
    "v_mfma_f32_16x16x32_bf16 v[144:147], a[96:99], a[32:35], v[168:171]\n"
    "v_exp_f32_e32 v223, v139\n"
    "v_mfma_f32_16x16x32_bf16 v[144:147], a[100:103], a[36:39], v[144:147]\n"
    "ds_read_b64_tr_b16 a[170:171], v226 offset:28928\n"
    "v_cvt_pk_bf16_f32 v197, v222, v223\n"
    "v_mfma_f32_16x16x32_bf16 v[144:147], a[104:107], a[40:43], v[144:147]\n"
    "v_exp_f32_e32 v220, v140\n"
    "v_mfma_f32_16x16x32_bf16 v[144:147], a[108:111], a[44:47], v[144:147]\n"
    "ds_read_b64_tr_b16 a[172:173], v227 offset:24832\n"
    "ds_read_b64_tr_b16 a[174:175], v227 offset:28928\n"
    "v_mfma_f32_16x16x32_bf16 v[148:151], a[124:127], a[44:47], v[168:171]\n"
    "v_exp_f32_e32 v221, v141\n"
    "v_mfma_f32_16x16x32_bf16 v[148:151], a[120:123], a[40:43], v[148:151]\n"
    "ds_read_b64_tr_b16 a[176:177], v226 offset:25088\n"
    "v_cvt_pk_bf16_f32 v198, v220, v221\n"
    "v_mfma_f32_16x16x32_bf16 v[148:151], a[116:119], a[36:39], v[148:151]\n"
    "v_exp_f32_e32 v222, v142\n"
    "v_mfma_f32_16x16x32_bf16 v[148:151], a[112:115], a[32:35], v[148:151]\n"
    "buffer_load_dwordx4 v231, s[24:27], s38 offen offset:2048 sc1 lds\n"
    "v_mfma_f32_16x16x32_bf16 v[156:159], a[112:115], a[48:51], v[172:175]\n"
    "v_exp_f32_e32 v223, v143\n"
    "v_mfma_f32_16x16x32_bf16 v[156:159], a[116:119], a[52:55], v[156:159]\n"
    "ds_read_b64_tr_b16 a[178:179], v226 offset:29184\n"
    "v_cvt_pk_bf16_f32 v199, v222, v223\n"
    "v_mfma_f32_16x16x32_bf16 v[156:159], a[120:123], a[56:59], v[156:159]\n"
    "v_exp_f32_e32 v220, v144\n"
    "v_mfma_f32_16x16x32_bf16 v[156:159], a[124:127], a[60:63], v[156:159]\n"
    "ds_read_b64_tr_b16 a[180:181], v227 offset:25088\n"
    "v_mfma_f32_16x16x32_bf16 v[152:155], a[108:111], a[60:63], v[172:175]\n"
    "v_exp_f32_e32 v221, v145\n"
    "v_mfma_f32_16x16x32_bf16 v[152:155], a[104:107], a[56:59], v[152:155]\n"
    "ds_read_b64_tr_b16 a[182:183], v227 offset:29184\n"
    "v_cvt_pk_bf16_f32 v200, v220, v221\n"
    "v_mfma_f32_16x16x32_bf16 v[152:155], a[100:103], a[52:55], v[152:155]\n"
    "v_exp_f32_e32 v222, v146\n"
    "v_mfma_f32_16x16x32_bf16 v[152:155], a[96:99], a[48:51], v[152:155]\n"
    "buffer_load_dwordx4 v233, s[24:27], s38 offen offset:3072 sc1 lds\n"
    "v_smfmac_f32_16x16x64_bf16 v[208:211], a[192:195], v[192:199], v245\n"
    "ds_read_b64_tr_b16 a[184:185], v226 offset:25344\n"
    "ds_read_b64_tr_b16 a[186:187], v226 offset:29440\n"
    "v_mfma_f32_16x16x32_bf16 v[64:67], a[128:131], v[184:187], v[64:67]\n"
    "s_add_u32 s38, s38, 0x4000\n"
    "v_exp_f32_e32 v223, v147\n"
    "v_mfma_f32_16x16x32_bf16 v[96:99], a[128:131], v[188:191], v[96:99]\n"
    "ds_read_b64_tr_b16 a[188:189], v227 offset:25344\n"
    "v_cvt_pk_bf16_f32 v201, v222, v223\n"
    "v_mfma_f32_16x16x32_bf16 v[100:103], a[132:135], v[188:191], v[100:103]\n"
    "s_cmp_lg_u32 s38, s59\n"
    "v_exp_f32_e32 v220, v148\n"
    "v_mfma_f32_16x16x32_bf16 v[68:71], a[132:135], v[184:187], v[68:71]\n"
    "ds_read_b64_tr_b16 a[190:191], v227 offset:29440\n"
    "v_mfma_f32_16x16x32_bf16 v[72:75], a[136:139], v[184:187], v[72:75]\n"
    "v_exp_f32_e32 v221, v149\n"
    "v_mfma_f32_16x16x32_bf16 v[104:107], a[136:139], v[188:191], v[104:107]\n"
    "v_add_f32_e32 v240, v208, v210\n"
    "v_cvt_pk_bf16_f32 v202, v220, v221\n"
    "v_mfma_f32_16x16x32_bf16 v[108:111], a[140:143], v[188:191], v[108:111]\n"
    "v_exp_f32_e32 v222, v150\n"
    "v_mfma_f32_16x16x32_bf16 v[76:79], a[140:143], v[184:187], v[76:79]\n"
    "v_add_f32_e32 v241, v209, v211\n"
    "v_mfma_f32_16x16x32_bf16 v[80:83], a[144:147], v[184:187], v[80:83]\n"
    "v_exp_f32_e32 v223, v151\n"
    "v_mfma_f32_16x16x32_bf16 v[112:115], a[144:147], v[188:191], v[112:115]\n"
    "v_max_f32_e32 v224, v240, v241\n"
    "v_cvt_pk_bf16_f32 v203, v222, v223\n"
    "v_mfma_f32_16x16x32_bf16 v[116:119], a[148:151], v[188:191], v[116:119]\n"
    "v_exp_f32_e32 v220, v152\n"
    "v_mfma_f32_16x16x32_bf16 v[84:87], a[148:151], v[184:187], v[84:87]\n"
    "v_add_u32_e32 v225, s36, v228\n"
    "v_cmp_nge_f32_e32 vcc, 0x67800000, v224\n"
    "v_mfma_f32_16x16x32_bf16 v[88:91], a[152:155], v[184:187], v[88:91]\n"
    "v_exp_f32_e32 v221, v153\n"
    "v_mfma_f32_16x16x32_bf16 v[120:123], a[152:155], v[188:191], v[120:123]\n"
    "s_cbranch_vccnz L_rare2_mk\n"
    "L_back2_mk:\n"
    "v_cvt_pk_bf16_f32 v204, v220, v221\n"
    "v_mfma_f32_16x16x32_bf16 v[124:127], a[156:159], v[188:191], v[124:127]\n"
    "s_waitcnt vmcnt(8) lgkmcnt(0)\n"
    "v_exp_f32_e32 v222, v154\n"
    "v_mfma_f32_16x16x32_bf16 v[92:95], a[156:159], v[184:187], v[92:95]\n"
    "s_cbranch_scc1 L_loop_mk\n"
    "s_setpc_b64 s[62:63]\n"
    "L_end_mk:\n"
    "L_frl25_mk:\n"
    "s_mov_b32 s43, 229376\n"
    "buffer_load_dwordx4 v[246:249], v232, s[96:99], s43 offen sc1\n"
    "buffer_load_dwordx4 v[250:253], v232, s[96:99], s43 offen offset:1024 sc1\n"
    "s_waitcnt vmcnt(0)\n"
    "s_sub_u32 s92, s92, 1\n"
    "s_cbranch_scc0 L_frc23_mk\n"
    "s_branch L_frd24_mk\n"
    "L_spn45_mk:\n"
    "s_mov_b32 s92, 0x40000\n"
    "L_spn45_mkx:\n"
    "buffer_load_dword v238, v239, s[96:99], s35 offen sc1\n"
    "s_sub_u32 s92, s92, 1\n"
    "s_waitcnt vmcnt(0)\n"
    "v_cmp_gt_u32_e32 vcc, 1, v238\n"
    "s_cbranch_scc1 L_spb46_mk\n"
    "s_cbranch_vccnz L_spn45_mkx\n"
    "s_branch L_spb46_mk\n"
    "L_spn47_mk:\n"
    "s_mov_b32 s92, 0x40000\n"
    "L_spn47_mkx:\n"
    "buffer_load_dword v238, v239, s[96:99], s35 offen sc1\n"
    "s_sub_u32 s92, s92, 1\n"
    "s_waitcnt vmcnt(0)\n"
    "v_cmp_gt_u32_e32 vcc, 2, v238\n"
    "s_cbranch_scc1 L_spb48_mk\n"
    "s_cbranch_vccnz L_spn47_mkx\n"
    "s_branch L_spb48_mk\n"
    "L_spn50_mk:\n"
    "s_mov_b32 s92, 0x40000\n"
    "L_spn50_mkx:\n"
    "buffer_load_dword v238, v239, s[96:99], s35 offen sc1\n"
    "s_sub_u32 s92, s92, 1\n"
    "s_waitcnt vmcnt(0)\n"
    "v_cmp_gt_u32_e32 vcc, 3, v238\n"
    "s_cbranch_scc1 L_spb51_mk\n"
    "s_cbranch_vccnz L_spn50_mkx\n"
    "s_branch L_spb51_mk\n"
    "L_spn52_mk:\n"
    "s_mov_b32 s92, 0x40000\n"
    "L_spn52_mkx:\n"
    "buffer_load_dword v238, v239, s[96:99], s35 offen sc1\n"
    "s_sub_u32 s92, s92, 1\n"
    "s_waitcnt vmcnt(0)\n"
    "v_cmp_gt_u32_e32 vcc, 4, v238\n"
    "s_cbranch_scc1 L_spb53_mk\n"
    "s_cbranch_vccnz L_spn52_mkx\n"
    "s_branch L_spb53_mk\n"
    "L_rare3_mk:\n"
    "v_max3_f32 v234, v144, v145, v146\n"
    "v_max3_f32 v235, v147, v148, v149\n"
    "v_max3_f32 v234, v234, v150, v151\n"
    "v_max_f32_e32 v234, v234, v235\n"
    "v_mov_b32_e32 v235, v234\n"
    "s_nop 1\n"
    "v_permlane32_swap_b32_e32 v234, v235\n"
    "v_max_f32_e32 v234, v234, v235\n"
    "v_mov_b32_e32 v235, v234\n"
    "s_nop 1\n"
    "v_permlane16_swap_b32_e32 v234, v235\n"
    "v_max_f32_e32 v234, v234, v235\n"
    "v_add_f32_e32 v234, 0x42c00000, v234\n"
    "v_max_f32_e32 v234, 0, v234\n"
    "v_ceil_f32_e32 v234, v234\n"
    "v_cvt_i32_f32_e32 v235, v234\n"
    "v_sub_u32_e32 v235, 0, v235\n"
    "v_ldexp_f32 v64, v64, v235\n"
    "v_ldexp_f32 v65, v65, v235\n"
    "v_ldexp_f32 v66, v66, v235\n"
    "v_ldexp_f32 v67, v67, v235\n"
    "v_ldexp_f32 v68, v68, v235\n"
    "v_ldexp_f32 v69, v69, v235\n"
    "v_ldexp_f32 v70, v70, v235\n"
    "v_ldexp_f32 v71, v71, v235\n"
    "v_ldexp_f32 v72, v72, v235\n"
    "v_ldexp_f32 v73, v73, v235\n"
    "v_ldexp_f32 v74, v74, v235\n"
    "v_ldexp_f32 v75, v75, v235\n"
    "v_ldexp_f32 v76, v76, v235\n"
    "v_ldexp_f32 v77, v77, v235\n"
    "v_ldexp_f32 v78, v78, v235\n"
    "v_ldexp_f32 v79, v79, v235\n"
    "v_ldexp_f32 v80, v80, v235\n"
    "v_ldexp_f32 v81, v81, v235\n"
    "v_ldexp_f32 v82, v82, v235\n"
    "v_ldexp_f32 v83, v83, v235\n"
    "v_ldexp_f32 v84, v84, v235\n"
    "v_ldexp_f32 v85, v85, v235\n"
    "v_ldexp_f32 v86, v86, v235\n"
    "v_ldexp_f32 v87, v87, v235\n"
    "v_ldexp_f32 v88, v88, v235\n"
    "v_ldexp_f32 v89, v89, v235\n"
    "v_ldexp_f32 v90, v90, v235\n"
    "v_ldexp_f32 v91, v91, v235\n"
    "v_ldexp_f32 v92, v92, v235\n"
    "v_ldexp_f32 v93, v93, v235\n"
    "v_ldexp_f32 v94, v94, v235\n"
    "v_ldexp_f32 v95, v95, v235\n"
    "v_ldexp_f32 v212, v218, v235\n"
    "v_mov_b32_e32 v214, 0\n"
    "v_sub_f32_e32 v168, v168, v234\n"
    "v_sub_f32_e32 v169, v169, v234\n"
    "v_sub_f32_e32 v170, v170, v234\n"
    "v_sub_f32_e32 v171, v171, v234\n"
    "v_sub_f32_e32 v144, v144, v234\n"
    "v_sub_f32_e32 v145, v145, v234\n"
    "v_sub_f32_e32 v146, v146, v234\n"
    "v_sub_f32_e32 v147, v147, v234\n"
    "v_sub_f32_e32 v148, v148, v234\n"
    "v_sub_f32_e32 v149, v149, v234\n"
    "v_sub_f32_e32 v150, v150, v234\n"
    "v_sub_f32_e32 v151, v151, v234\n"
    "v_exp_f32_e32 v144, v144\n"
    "v_exp_f32_e32 v145, v145\n"
    "v_exp_f32_e32 v146, v146\n"
    "v_exp_f32_e32 v147, v147\n"
    "v_exp_f32_e32 v148, v148\n"
    "v_exp_f32_e32 v149, v149\n"
    "v_exp_f32_e32 v150, v150\n"
    "v_exp_f32_e32 v151, v151\n"
    "s_nop 0\n"
    "v_cvt_pk_bf16_f32 v200, v144, v145\n"
    "v_cvt_pk_bf16_f32 v201, v146, v147\n"
    "v_cvt_pk_bf16_f32 v202, v148, v149\n"
    "v_cvt_pk_bf16_f32 v203, v150, v151\n"
    "v_max3_f32 v234, v152, v153, v154\n"
    "v_max3_f32 v235, v155, v156, v157\n"
    "v_max3_f32 v234, v234, v158, v159\n"
    "v_max_f32_e32 v234, v234, v235\n"
    "v_mov_b32_e32 v235, v234\n"
    "s_nop 1\n"
    "v_permlane32_swap_b32_e32 v234, v235\n"
    "v_max_f32_e32 v234, v234, v235\n"
    "v_mov_b32_e32 v235, v234\n"
    "s_nop 1\n"
    "v_permlane16_swap_b32_e32 v234, v235\n"
    "v_max_f32_e32 v234, v234, v235\n"
    "v_add_f32_e32 v234, 0x42c00000, v234\n"
    "v_max_f32_e32 v234, 0, v234\n"
    "v_ceil_f32_e32 v234, v234\n"
    "v_cvt_i32_f32_e32 v235, v234\n"
    "v_sub_u32_e32 v235, 0, v235\n"
    "v_ldexp_f32 v96, v96, v235\n"
    "v_ldexp_f32 v97, v97, v235\n"
    "v_ldexp_f32 v98, v98, v235\n"
    "v_ldexp_f32 v99, v99, v235\n"
    "v_ldexp_f32 v100, v100, v235\n"
    "v_ldexp_f32 v101, v101, v235\n"
    "v_ldexp_f32 v102, v102, v235\n"
    "v_ldexp_f32 v103, v103, v235\n"
    "v_ldexp_f32 v104, v104, v235\n"
    "v_ldexp_f32 v105, v105, v235\n"
    "v_ldexp_f32 v106, v106, v235\n"
    "v_ldexp_f32 v107, v107, v235\n"
    "v_ldexp_f32 v108, v108, v235\n"
    "v_ldexp_f32 v109, v109, v235\n"
    "v_ldexp_f32 v110, v110, v235\n"
    "v_ldexp_f32 v111, v111, v235\n"
    "v_ldexp_f32 v112, v112, v235\n"
    "v_ldexp_f32 v113, v113, v235\n"
    "v_ldexp_f32 v114, v114, v235\n"
    "v_ldexp_f32 v115, v115, v235\n"
    "v_ldexp_f32 v116, v116, v235\n"
    "v_ldexp_f32 v117, v117, v235\n"
    "v_ldexp_f32 v118, v118, v235\n"
    "v_ldexp_f32 v119, v119, v235\n"
    "v_ldexp_f32 v120, v120, v235\n"
    "v_ldexp_f32 v121, v121, v235\n"
    "v_ldexp_f32 v122, v122, v235\n"
    "v_ldexp_f32 v123, v123, v235\n"
    "v_ldexp_f32 v124, v124, v235\n"
    "v_ldexp_f32 v125, v125, v235\n"
    "v_ldexp_f32 v126, v126, v235\n"
    "v_ldexp_f32 v127, v127, v235\n"
    "v_ldexp_f32 v213, v219, v235\n"
    "v_mov_b32_e32 v215, 0\n"
    "v_sub_f32_e32 v172, v172, v234\n"
    "v_sub_f32_e32 v173, v173, v234\n"
    "v_sub_f32_e32 v174, v174, v234\n"
    "v_sub_f32_e32 v175, v175, v234\n"
    "v_sub_f32_e32 v152, v152, v234\n"
    "v_sub_f32_e32 v153, v153, v234\n"
    "v_sub_f32_e32 v154, v154, v234\n"
    "v_sub_f32_e32 v155, v155, v234\n"
    "v_sub_f32_e32 v156, v156, v234\n"
    "v_sub_f32_e32 v157, v157, v234\n"
    "v_sub_f32_e32 v158, v158, v234\n"
    "v_sub_f32_e32 v159, v159, v234\n"
    "v_exp_f32_e32 v152, v152\n"
    "v_exp_f32_e32 v153, v153\n"
    "v_exp_f32_e32 v154, v154\n"
    "v_exp_f32_e32 v155, v155\n"
    "v_exp_f32_e32 v156, v156\n"
    "v_exp_f32_e32 v157, v157\n"
    "v_exp_f32_e32 v158, v158\n"
    "v_exp_f32_e32 v159, v159\n"
    "s_nop 0\n"
    "v_cvt_pk_bf16_f32 v204, v152, v153\n"
    "v_cvt_pk_bf16_f32 v205, v154, v155\n"
    "v_cvt_pk_bf16_f32 v206, v156, v157\n"
    "v_cvt_pk_bf16_f32 v207, v158, v159\n"
    "s_nop 1\n"
    "v_smfmac_f32_16x16x64_bf16 v[212:215], a[192:195], v[200:207], v245\n"
    "s_nop 7\n"
    "v_add_f32_e32 v242, v212, v214\n"
    "v_add_f32_e32 v243, v213, v215\n"
    "s_branch L_back3_mk\n"
    "L_spin_mk:\n"
    "ds_read_b32 v238, v244\n"
    "s_waitcnt lgkmcnt(0)\n"
    "v_cmp_gt_u32_e32 vcc, s38, v238\n"
    "s_nop 1\n"
    "s_cbranch_vccnz L_spin_mk\n"
    "s_branch L_spinback_mk\n"
    "L_rare0_mk:\n"
    "v_max3_f32 v234, v128, v129, v130\n"
    "v_max3_f32 v235, v131, v132, v133\n"
    "v_max3_f32 v234, v234, v134, v135\n"
    "v_max_f32_e32 v234, v234, v235\n"
    "v_mov_b32_e32 v235, v234\n"
    "s_nop 1\n"
    "v_permlane32_swap_b32_e32 v234, v235\n"
    "v_max_f32_e32 v234, v234, v235\n"
    "v_mov_b32_e32 v235, v234\n"
    "s_nop 1\n"
    "v_permlane16_swap_b32_e32 v234, v235\n"
    "v_max_f32_e32 v234, v234, v235\n"
    "v_add_f32_e32 v234, 0x42c00000, v234\n"
    "v_max_f32_e32 v234, 0, v234\n"
    "v_ceil_f32_e32 v234, v234\n"
    "v_cvt_i32_f32_e32 v235, v234\n"
    "v_sub_u32_e32 v235, 0, v235\n"
    "v_ldexp_f32 v0, v0, v235\n"
    "v_ldexp_f32 v1, v1, v235\n"
    "v_ldexp_f32 v2, v2, v235\n"
    "v_ldexp_f32 v3, v3, v235\n"
    "v_ldexp_f32 v4, v4, v235\n"
    "v_ldexp_f32 v5, v5, v235\n"
    "v_ldexp_f32 v6, v6, v235\n"
    "v_ldexp_f32 v7, v7, v235\n"
    "v_ldexp_f32 v8, v8, v235\n"
    "v_ldexp_f32 v9, v9, v235\n"
    "v_ldexp_f32 v10, v10, v235\n"
    "v_ldexp_f32 v11, v11, v235\n"
    "v_ldexp_f32 v12, v12, v235\n"
    "v_ldexp_f32 v13, v13, v235\n"
    "v_ldexp_f32 v14, v14, v235\n"
    "v_ldexp_f32 v15, v15, v235\n"
    "v_ldexp_f32 v16, v16, v235\n"
    "v_ldexp_f32 v17, v17, v235\n"
    "v_ldexp_f32 v18, v18, v235\n"
    "v_ldexp_f32 v19, v19, v235\n"
    "v_ldexp_f32 v20, v20, v235\n"
    "v_ldexp_f32 v21, v21, v235\n"
    "v_ldexp_f32 v22, v22, v235\n"
    "v_ldexp_f32 v23, v23, v235\n"
    "v_ldexp_f32 v24, v24, v235\n"
    "v_ldexp_f32 v25, v25, v235\n"
    "v_ldexp_f32 v26, v26, v235\n"
    "v_ldexp_f32 v27, v27, v235\n"
    "v_ldexp_f32 v28, v28, v235\n"
    "v_ldexp_f32 v29, v29, v235\n"
    "v_ldexp_f32 v30, v30, v235\n"
    "v_ldexp_f32 v31, v31, v235\n"
    "v_ldexp_f32 v208, v240, v235\n"
    "v_mov_b32_e32 v210, 0\n"
    "v_sub_f32_e32 v160, v160, v234\n"
    "v_sub_f32_e32 v161, v161, v234\n"
    "v_sub_f32_e32 v162, v162, v234\n"
    "v_sub_f32_e32 v163, v163, v234\n"
    "v_sub_f32_e32 v128, v128, v234\n"
    "v_sub_f32_e32 v129, v129, v234\n"
    "v_sub_f32_e32 v130, v130, v234\n"
    "v_sub_f32_e32 v131, v131, v234\n"
    "v_sub_f32_e32 v132, v132, v234\n"
    "v_sub_f32_e32 v133, v133, v234\n"
    "v_sub_f32_e32 v134, v134, v234\n"
    "v_sub_f32_e32 v135, v135, v234\n"
    "v_exp_f32_e32 v128, v128\n"
    "v_exp_f32_e32 v129, v129\n"
    "v_exp_f32_e32 v130, v130\n"
    "v_exp_f32_e32 v131, v131\n"
    "v_exp_f32_e32 v132, v132\n"
    "v_exp_f32_e32 v133, v133\n"
    "v_exp_f32_e32 v134, v134\n"
    "v_exp_f32_e32 v135, v135\n"
    "s_nop 0\n"
    "v_cvt_pk_bf16_f32 v176, v128, v129\n"
    "v_cvt_pk_bf16_f32 v177, v130, v131\n"
    "v_cvt_pk_bf16_f32 v178, v132, v133\n"
    "v_cvt_pk_bf16_f32 v179, v134, v135\n"
    "v_max3_f32 v234, v136, v137, v138\n"
    "v_max3_f32 v235, v139, v140, v141\n"
    "v_max3_f32 v234, v234, v142, v143\n"
    "v_max_f32_e32 v234, v234, v235\n"
    "v_mov_b32_e32 v235, v234\n"
    "s_nop 1\n"
    "v_permlane32_swap_b32_e32 v234, v235\n"
    "v_max_f32_e32 v234, v234, v235\n"
    "v_mov_b32_e32 v235, v234\n"
    "s_nop 1\n"
    "v_permlane16_swap_b32_e32 v234, v235\n"
    "v_max_f32_e32 v234, v234, v235\n"
    "v_add_f32_e32 v234, 0x42c00000, v234\n"
    "v_max_f32_e32 v234, 0, v234\n"
    "v_ceil_f32_e32 v234, v234\n"
    "v_cvt_i32_f32_e32 v235, v234\n"
    "v_sub_u32_e32 v235, 0, v235\n"
    "v_ldexp_f32 v32, v32, v235\n"
    "v_ldexp_f32 v33, v33, v235\n"
    "v_ldexp_f32 v34, v34, v235\n"
    "v_ldexp_f32 v35, v35, v235\n"
    "v_ldexp_f32 v36, v36, v235\n"
    "v_ldexp_f32 v37, v37, v235\n"
    "v_ldexp_f32 v38, v38, v235\n"
    "v_ldexp_f32 v39, v39, v235\n"
    "v_ldexp_f32 v40, v40, v235\n"
    "v_ldexp_f32 v41, v41, v235\n"
    "v_ldexp_f32 v42, v42, v235\n"
    "v_ldexp_f32 v43, v43, v235\n"
    "v_ldexp_f32 v44, v44, v235\n"
    "v_ldexp_f32 v45, v45, v235\n"
    "v_ldexp_f32 v46, v46, v235\n"
    "v_ldexp_f32 v47, v47, v235\n"
    "v_ldexp_f32 v48, v48, v235\n"
    "v_ldexp_f32 v49, v49, v235\n"
    "v_ldexp_f32 v50, v50, v235\n"
    "v_ldexp_f32 v51, v51, v235\n"
    "v_ldexp_f32 v52, v52, v235\n"
    "v_ldexp_f32 v53, v53, v235\n"
    "v_ldexp_f32 v54, v54, v235\n"
    "v_ldexp_f32 v55, v55, v235\n"
    "v_ldexp_f32 v56, v56, v235\n"
    "v_ldexp_f32 v57, v57, v235\n"
    "v_ldexp_f32 v58, v58, v235\n"
    "v_ldexp_f32 v59, v59, v235\n"
    "v_ldexp_f32 v60, v60, v235\n"
    "v_ldexp_f32 v61, v61, v235\n"
    "v_ldexp_f32 v62, v62, v235\n"
    "v_ldexp_f32 v63, v63, v235\n"
    "v_ldexp_f32 v209, v241, v235\n"
    "v_mov_b32_e32 v211, 0\n"
    "v_sub_f32_e32 v164, v164, v234\n"
    "v_sub_f32_e32 v165, v165, v234\n"
    "v_sub_f32_e32 v166, v166, v234\n"
    "v_sub_f32_e32 v167, v167, v234\n"
    "v_sub_f32_e32 v136, v136, v234\n"
    "v_sub_f32_e32 v137, v137, v234\n"
    "v_sub_f32_e32 v138, v138, v234\n"
    "v_sub_f32_e32 v139, v139, v234\n"
    "v_sub_f32_e32 v140, v140, v234\n"
    "v_sub_f32_e32 v141, v141, v234\n"
    "v_sub_f32_e32 v142, v142, v234\n"
    "v_sub_f32_e32 v143, v143, v234\n"
    "v_exp_f32_e32 v136, v136\n"
    "v_exp_f32_e32 v137, v137\n"
    "v_exp_f32_e32 v138, v138\n"
    "v_exp_f32_e32 v139, v139\n"
    "v_exp_f32_e32 v140, v140\n"
    "v_exp_f32_e32 v141, v141\n"
    "v_exp_f32_e32 v142, v142\n"
    "v_exp_f32_e32 v143, v143\n"
    "s_nop 0\n"
    "v_cvt_pk_bf16_f32 v180, v136, v137\n"
    "v_cvt_pk_bf16_f32 v181, v138, v139\n"
    "v_cvt_pk_bf16_f32 v182, v140, v141\n"
    "v_cvt_pk_bf16_f32 v183, v142, v143\n"
    "s_nop 1\n"
    "v_smfmac_f32_16x16x64_bf16 v[208:211], a[192:195], v[176:183], v245\n"
    "s_nop 7\n"
    "v_add_f32_e32 v216, v208, v210\n"
    "v_add_f32_e32 v217, v209, v211\n"
    "s_branch L_back0_mk\n"
    "L_rare1_mk:\n"
    "v_max3_f32 v234, v144, v145, v146\n"
    "v_max3_f32 v235, v147, v148, v149\n"
    "v_max3_f32 v234, v234, v150, v151\n"
    "v_max_f32_e32 v234, v234, v235\n"
    "v_mov_b32_e32 v235, v234\n"
    "s_nop 1\n"
    "v_permlane32_swap_b32_e32 v234, v235\n"
    "v_max_f32_e32 v234, v234, v235\n"
    "v_mov_b32_e32 v235, v234\n"
    "s_nop 1\n"
    "v_permlane16_swap_b32_e32 v234, v235\n"
    "v_max_f32_e32 v234, v234, v235\n"
    "v_add_f32_e32 v234, 0x42c00000, v234\n"
    "v_max_f32_e32 v234, 0, v234\n"
    "v_ceil_f32_e32 v234, v234\n"
    "v_cvt_i32_f32_e32 v235, v234\n"
    "v_sub_u32_e32 v235, 0, v235\n"
    "v_ldexp_f32 v64, v64, v235\n"
    "v_ldexp_f32 v65, v65, v235\n"
    "v_ldexp_f32 v66, v66, v235\n"
    "v_ldexp_f32 v67, v67, v235\n"
    "v_ldexp_f32 v68, v68, v235\n"
    "v_ldexp_f32 v69, v69, v235\n"
    "v_ldexp_f32 v70, v70, v235\n"
    "v_ldexp_f32 v71, v71, v235\n"
    "v_ldexp_f32 v72, v72, v235\n"
    "v_ldexp_f32 v73, v73, v235\n"
    "v_ldexp_f32 v74, v74, v235\n"
    "v_ldexp_f32 v75, v75, v235\n"
    "v_ldexp_f32 v76, v76, v235\n"
    "v_ldexp_f32 v77, v77, v235\n"
    "v_ldexp_f32 v78, v78, v235\n"
    "v_ldexp_f32 v79, v79, v235\n"
    "v_ldexp_f32 v80, v80, v235\n"
    "v_ldexp_f32 v81, v81, v235\n"
    "v_ldexp_f32 v82, v82, v235\n"
    "v_ldexp_f32 v83, v83, v235\n"
    "v_ldexp_f32 v84, v84, v235\n"
    "v_ldexp_f32 v85, v85, v235\n"
    "v_ldexp_f32 v86, v86, v235\n"
    "v_ldexp_f32 v87, v87, v235\n"
    "v_ldexp_f32 v88, v88, v235\n"
    "v_ldexp_f32 v89, v89, v235\n"
    "v_ldexp_f32 v90, v90, v235\n"
    "v_ldexp_f32 v91, v91, v235\n"
    "v_ldexp_f32 v92, v92, v235\n"
    "v_ldexp_f32 v93, v93, v235\n"
    "v_ldexp_f32 v94, v94, v235\n"
    "v_ldexp_f32 v95, v95, v235\n"
    "v_ldexp_f32 v212, v242, v235\n"
    "v_mov_b32_e32 v214, 0\n"
    "v_sub_f32_e32 v168, v168, v234\n"
    "v_sub_f32_e32 v169, v169, v234\n"
    "v_sub_f32_e32 v170, v170, v234\n"
    "v_sub_f32_e32 v171, v171, v234\n"
    "v_sub_f32_e32 v144, v144, v234\n"
    "v_sub_f32_e32 v145, v145, v234\n"
    "v_sub_f32_e32 v146, v146, v234\n"
    "v_sub_f32_e32 v147, v147, v234\n"
    "v_sub_f32_e32 v148, v148, v234\n"
    "v_sub_f32_e32 v149, v149, v234\n"
    "v_sub_f32_e32 v150, v150, v234\n"
    "v_sub_f32_e32 v151, v151, v234\n"
    "v_exp_f32_e32 v144, v144\n"
    "v_exp_f32_e32 v145, v145\n"
    "v_exp_f32_e32 v146, v146\n"
    "v_exp_f32_e32 v147, v147\n"
    "v_exp_f32_e32 v148, v148\n"
    "v_exp_f32_e32 v149, v149\n"
    "v_exp_f32_e32 v150, v150\n"
    "v_exp_f32_e32 v151, v151\n"
    "s_nop 0\n"
    "v_cvt_pk_bf16_f32 v184, v144, v145\n"
    "v_cvt_pk_bf16_f32 v185, v146, v147\n"
    "v_cvt_pk_bf16_f32 v186, v148, v149\n"
    "v_cvt_pk_bf16_f32 v187, v150, v151\n"
    "v_max3_f32 v234, v152, v153, v154\n"
    "v_max3_f32 v235, v155, v156, v157\n"
    "v_max3_f32 v234, v234, v158, v159\n"
    "v_max_f32_e32 v234, v234, v235\n"
    "v_mov_b32_e32 v235, v234\n"
    "s_nop 1\n"
    "v_permlane32_swap_b32_e32 v234, v235\n"
    "v_max_f32_e32 v234, v234, v235\n"
    "v_mov_b32_e32 v235, v234\n"
    "s_nop 1\n"
    "v_permlane16_swap_b32_e32 v234, v235\n"
    "v_max_f32_e32 v234, v234, v235\n"
    "v_add_f32_e32 v234, 0x42c00000, v234\n"
    "v_max_f32_e32 v234, 0, v234\n"
    "v_ceil_f32_e32 v234, v234\n"
    "v_cvt_i32_f32_e32 v235, v234\n"
    "v_sub_u32_e32 v235, 0, v235\n"
    "v_ldexp_f32 v96, v96, v235\n"
    "v_ldexp_f32 v97, v97, v235\n"
    "v_ldexp_f32 v98, v98, v235\n"
    "v_ldexp_f32 v99, v99, v235\n"
    "v_ldexp_f32 v100, v100, v235\n"
    "v_ldexp_f32 v101, v101, v235\n"
    "v_ldexp_f32 v102, v102, v235\n"
    "v_ldexp_f32 v103, v103, v235\n"
    "v_ldexp_f32 v104, v104, v235\n"
    "v_ldexp_f32 v105, v105, v235\n"
    "v_ldexp_f32 v106, v106, v235\n"
    "v_ldexp_f32 v107, v107, v235\n"
    "v_ldexp_f32 v108, v108, v235\n"
    "v_ldexp_f32 v109, v109, v235\n"
    "v_ldexp_f32 v110, v110, v235\n"
    "v_ldexp_f32 v111, v111, v235\n"
    "v_ldexp_f32 v112, v112, v235\n"
    "v_ldexp_f32 v113, v113, v235\n"
    "v_ldexp_f32 v114, v114, v235\n"
    "v_ldexp_f32 v115, v115, v235\n"
    "v_ldexp_f32 v116, v116, v235\n"
    "v_ldexp_f32 v117, v117, v235\n"
    "v_ldexp_f32 v118, v118, v235\n"
    "v_ldexp_f32 v119, v119, v235\n"
    "v_ldexp_f32 v120, v120, v235\n"
    "v_ldexp_f32 v121, v121, v235\n"
    "v_ldexp_f32 v122, v122, v235\n"
    "v_ldexp_f32 v123, v123, v235\n"
    "v_ldexp_f32 v124, v124, v235\n"
    "v_ldexp_f32 v125, v125, v235\n"
    "v_ldexp_f32 v126, v126, v235\n"
    "v_ldexp_f32 v127, v127, v235\n"
    "v_ldexp_f32 v213, v243, v235\n"
    "v_mov_b32_e32 v215, 0\n"
    "v_sub_f32_e32 v172, v172, v234\n"
    "v_sub_f32_e32 v173, v173, v234\n"
    "v_sub_f32_e32 v174, v174, v234\n"
    "v_sub_f32_e32 v175, v175, v234\n"
    "v_sub_f32_e32 v152, v152, v234\n"
    "v_sub_f32_e32 v153, v153, v234\n"
    "v_sub_f32_e32 v154, v154, v234\n"
    "v_sub_f32_e32 v155, v155, v234\n"
    "v_sub_f32_e32 v156, v156, v234\n"
    "v_sub_f32_e32 v157, v157, v234\n"
    "v_sub_f32_e32 v158, v158, v234\n"
    "v_sub_f32_e32 v159, v159, v234\n"
    "v_exp_f32_e32 v152, v152\n"
    "v_exp_f32_e32 v153, v153\n"
    "v_exp_f32_e32 v154, v154\n"
    "v_exp_f32_e32 v155, v155\n"
    "v_exp_f32_e32 v156, v156\n"
    "v_exp_f32_e32 v157, v157\n"
    "v_exp_f32_e32 v158, v158\n"
    "v_exp_f32_e32 v159, v159\n"
    "s_nop 0\n"
    "v_cvt_pk_bf16_f32 v188, v152, v153\n"
    "v_cvt_pk_bf16_f32 v189, v154, v155\n"
    "v_cvt_pk_bf16_f32 v190, v156, v157\n"
    "v_cvt_pk_bf16_f32 v191, v158, v159\n"
    "s_nop 1\n"
    "v_smfmac_f32_16x16x64_bf16 v[212:215], a[192:195], v[184:191], v245\n"
    "s_nop 7\n"
    "v_add_f32_e32 v218, v212, v214\n"
    "v_add_f32_e32 v219, v213, v215\n"
    "s_branch L_back1_mk\n"
    "L_rare2_mk:\n"
    "v_max3_f32 v234, v128, v129, v130\n"
    "v_max3_f32 v235, v131, v132, v133\n"
    "v_max3_f32 v234, v234, v134, v135\n"
    "v_max_f32_e32 v234, v234, v235\n"
    "v_mov_b32_e32 v235, v234\n"
    "s_nop 1\n"
    "v_permlane32_swap_b32_e32 v234, v235\n"
    "v_max_f32_e32 v234, v234, v235\n"
    "v_mov_b32_e32 v235, v234\n"
    "s_nop 1\n"
    "v_permlane16_swap_b32_e32 v234, v235\n"
    "v_max_f32_e32 v234, v234, v235\n"
    "v_add_f32_e32 v234, 0x42c00000, v234\n"
    "v_max_f32_e32 v234, 0, v234\n"
    "v_ceil_f32_e32 v234, v234\n"
    "v_cvt_i32_f32_e32 v235, v234\n"
    "v_sub_u32_e32 v235, 0, v235\n"
    "v_ldexp_f32 v0, v0, v235\n"
    "v_ldexp_f32 v1, v1, v235\n"
    "v_ldexp_f32 v2, v2, v235\n"
    "v_ldexp_f32 v3, v3, v235\n"
    "v_ldexp_f32 v4, v4, v235\n"
    "v_ldexp_f32 v5, v5, v235\n"
    "v_ldexp_f32 v6, v6, v235\n"
    "v_ldexp_f32 v7, v7, v235\n"
    "v_ldexp_f32 v8, v8, v235\n"
    "v_ldexp_f32 v9, v9, v235\n"
    "v_ldexp_f32 v10, v10, v235\n"
    "v_ldexp_f32 v11, v11, v235\n"
    "v_ldexp_f32 v12, v12, v235\n"
    "v_ldexp_f32 v13, v13, v235\n"
    "v_ldexp_f32 v14, v14, v235\n"
    "v_ldexp_f32 v15, v15, v235\n"
    "v_ldexp_f32 v16, v16, v235\n"
    "v_ldexp_f32 v17, v17, v235\n"
    "v_ldexp_f32 v18, v18, v235\n"
    "v_ldexp_f32 v19, v19, v235\n"
    "v_ldexp_f32 v20, v20, v235\n"
    "v_ldexp_f32 v21, v21, v235\n"
    "v_ldexp_f32 v22, v22, v235\n"
    "v_ldexp_f32 v23, v23, v235\n"
    "v_ldexp_f32 v24, v24, v235\n"
    "v_ldexp_f32 v25, v25, v235\n"
    "v_ldexp_f32 v26, v26, v235\n"
    "v_ldexp_f32 v27, v27, v235\n"
    "v_ldexp_f32 v28, v28, v235\n"
    "v_ldexp_f32 v29, v29, v235\n"
    "v_ldexp_f32 v30, v30, v235\n"
    "v_ldexp_f32 v31, v31, v235\n"
    "v_ldexp_f32 v208, v216, v235\n"
    "v_mov_b32_e32 v210, 0\n"
    "v_sub_f32_e32 v160, v160, v234\n"
    "v_sub_f32_e32 v161, v161, v234\n"
    "v_sub_f32_e32 v162, v162, v234\n"
    "v_sub_f32_e32 v163, v163, v234\n"
    "v_sub_f32_e32 v128, v128, v234\n"
    "v_sub_f32_e32 v129, v129, v234\n"
    "v_sub_f32_e32 v130, v130, v234\n"
    "v_sub_f32_e32 v131, v131, v234\n"
    "v_sub_f32_e32 v132, v132, v234\n"
    "v_sub_f32_e32 v133, v133, v234\n"
    "v_sub_f32_e32 v134, v134, v234\n"
    "v_sub_f32_e32 v135, v135, v234\n"
    "v_exp_f32_e32 v128, v128\n"
    "v_exp_f32_e32 v129, v129\n"
    "v_exp_f32_e32 v130, v130\n"
    "v_exp_f32_e32 v131, v131\n"
    "v_exp_f32_e32 v132, v132\n"
    "v_exp_f32_e32 v133, v133\n"
    "v_exp_f32_e32 v134, v134\n"
    "v_exp_f32_e32 v135, v135\n"
    "s_nop 0\n"
    "v_cvt_pk_bf16_f32 v192, v128, v129\n"
    "v_cvt_pk_bf16_f32 v193, v130, v131\n"
    "v_cvt_pk_bf16_f32 v194, v132, v133\n"
    "v_cvt_pk_bf16_f32 v195, v134, v135\n"
    "v_max3_f32 v234, v136, v137, v138\n"
    "v_max3_f32 v235, v139, v140, v141\n"
    "v_max3_f32 v234, v234, v142, v143\n"
    "v_max_f32_e32 v234, v234, v235\n"
    "v_mov_b32_e32 v235, v234\n"
    "s_nop 1\n"
    "v_permlane32_swap_b32_e32 v234, v235\n"
    "v_max_f32_e32 v234, v234, v235\n"
    "v_mov_b32_e32 v235, v234\n"
    "s_nop 1\n"
    "v_permlane16_swap_b32_e32 v234, v235\n"
    "v_max_f32_e32 v234, v234, v235\n"
    "v_add_f32_e32 v234, 0x42c00000, v234\n"
    "v_max_f32_e32 v234, 0, v234\n"
    "v_ceil_f32_e32 v234, v234\n"
    "v_cvt_i32_f32_e32 v235, v234\n"
    "v_sub_u32_e32 v235, 0, v235\n"
    "v_ldexp_f32 v32, v32, v235\n"
    "v_ldexp_f32 v33, v33, v235\n"
    "v_ldexp_f32 v34, v34, v235\n"
    "v_ldexp_f32 v35, v35, v235\n"
    "v_ldexp_f32 v36, v36, v235\n"
    "v_ldexp_f32 v37, v37, v235\n"
    "v_ldexp_f32 v38, v38, v235\n"
    "v_ldexp_f32 v39, v39, v235\n"
    "v_ldexp_f32 v40, v40, v235\n"
    "v_ldexp_f32 v41, v41, v235\n"
    "v_ldexp_f32 v42, v42, v235\n"
    "v_ldexp_f32 v43, v43, v235\n"
    "v_ldexp_f32 v44, v44, v235\n"
    "v_ldexp_f32 v45, v45, v235\n"
    "v_ldexp_f32 v46, v46, v235\n"
    "v_ldexp_f32 v47, v47, v235\n"
    "v_ldexp_f32 v48, v48, v235\n"
    "v_ldexp_f32 v49, v49, v235\n"
    "v_ldexp_f32 v50, v50, v235\n"
    "v_ldexp_f32 v51, v51, v235\n"
    "v_ldexp_f32 v52, v52, v235\n"
    "v_ldexp_f32 v53, v53, v235\n"
    "v_ldexp_f32 v54, v54, v235\n"
    "v_ldexp_f32 v55, v55, v235\n"
    "v_ldexp_f32 v56, v56, v235\n"
    "v_ldexp_f32 v57, v57, v235\n"
    "v_ldexp_f32 v58, v58, v235\n"
    "v_ldexp_f32 v59, v59, v235\n"
    "v_ldexp_f32 v60, v60, v235\n"
    "v_ldexp_f32 v61, v61, v235\n"
    "v_ldexp_f32 v62, v62, v235\n"
    "v_ldexp_f32 v63, v63, v235\n"
    "v_ldexp_f32 v209, v217, v235\n"
    "v_mov_b32_e32 v211, 0\n"
    "v_sub_f32_e32 v164, v164, v234\n"
    "v_sub_f32_e32 v165, v165, v234\n"
    "v_sub_f32_e32 v166, v166, v234\n"
    "v_sub_f32_e32 v167, v167, v234\n"
    "v_sub_f32_e32 v136, v136, v234\n"
    "v_sub_f32_e32 v137, v137, v234\n"
    "v_sub_f32_e32 v138, v138, v234\n"
    "v_sub_f32_e32 v139, v139, v234\n"
    "v_sub_f32_e32 v140, v140, v234\n"
    "v_sub_f32_e32 v141, v141, v234\n"
    "v_sub_f32_e32 v142, v142, v234\n"
    "v_sub_f32_e32 v143, v143, v234\n"
    "v_exp_f32_e32 v136, v136\n"
    "v_exp_f32_e32 v137, v137\n"
    "v_exp_f32_e32 v138, v138\n"
    "v_exp_f32_e32 v139, v139\n"
    "v_exp_f32_e32 v140, v140\n"
    "v_exp_f32_e32 v141, v141\n"
    "v_exp_f32_e32 v142, v142\n"
    "v_exp_f32_e32 v143, v143\n"
    "s_nop 0\n"
    "v_cvt_pk_bf16_f32 v196, v136, v137\n"
    "v_cvt_pk_bf16_f32 v197, v138, v139\n"
    "v_cvt_pk_bf16_f32 v198, v140, v141\n"
    "v_cvt_pk_bf16_f32 v199, v142, v143\n"
    "s_nop 1\n"
    "v_smfmac_f32_16x16x64_bf16 v[208:211], a[192:195], v[192:199], v245\n"
    "s_nop 7\n"
    "v_add_f32_e32 v240, v208, v210\n"
    "v_add_f32_e32 v241, v209, v211\n"
    "s_branch L_back2_mk\n"
    "L_rareT_mk:\n"
    "v_max3_f32 v234, v144, v145, v146\n"
    "v_max3_f32 v235, v147, v148, v149\n"
    "v_max3_f32 v234, v234, v150, v151\n"
    "v_max_f32_e32 v234, v234, v235\n"
    "v_mov_b32_e32 v235, v234\n"
    "s_nop 1\n"
    "v_permlane32_swap_b32_e32 v234, v235\n"
    "v_max_f32_e32 v234, v234, v235\n"
    "v_mov_b32_e32 v235, v234\n"
    "s_nop 1\n"
    "v_permlane16_swap_b32_e32 v234, v235\n"
    "v_max_f32_e32 v234, v234, v235\n"
    "v_add_f32_e32 v234, 0x42c00000, v234\n"
    "v_max_f32_e32 v234, 0, v234\n"
    "v_ceil_f32_e32 v234, v234\n"
    "v_cvt_i32_f32_e32 v235, v234\n"
    "v_sub_u32_e32 v235, 0, v235\n"
    "v_ldexp_f32 v64, v64, v235\n"
    "v_ldexp_f32 v65, v65, v235\n"
    "v_ldexp_f32 v66, v66, v235\n"
    "v_ldexp_f32 v67, v67, v235\n"
    "v_ldexp_f32 v68, v68, v235\n"
    "v_ldexp_f32 v69, v69, v235\n"
    "v_ldexp_f32 v70, v70, v235\n"
    "v_ldexp_f32 v71, v71, v235\n"
    "v_ldexp_f32 v72, v72, v235\n"
    "v_ldexp_f32 v73, v73, v235\n"
    "v_ldexp_f32 v74, v74, v235\n"
    "v_ldexp_f32 v75, v75, v235\n"
    "v_ldexp_f32 v76, v76, v235\n"
    "v_ldexp_f32 v77, v77, v235\n"
    "v_ldexp_f32 v78, v78, v235\n"
    "v_ldexp_f32 v79, v79, v235\n"
    "v_ldexp_f32 v80, v80, v235\n"
    "v_ldexp_f32 v81, v81, v235\n"
    "v_ldexp_f32 v82, v82, v235\n"
    "v_ldexp_f32 v83, v83, v235\n"
    "v_ldexp_f32 v84, v84, v235\n"
    "v_ldexp_f32 v85, v85, v235\n"
    "v_ldexp_f32 v86, v86, v235\n"
    "v_ldexp_f32 v87, v87, v235\n"
    "v_ldexp_f32 v88, v88, v235\n"
    "v_ldexp_f32 v89, v89, v235\n"
    "v_ldexp_f32 v90, v90, v235\n"
    "v_ldexp_f32 v91, v91, v235\n"
    "v_ldexp_f32 v92, v92, v235\n"
    "v_ldexp_f32 v93, v93, v235\n"
    "v_ldexp_f32 v94, v94, v235\n"
    "v_ldexp_f32 v95, v95, v235\n"
    "v_ldexp_f32 v212, v218, v235\n"
    "v_mov_b32_e32 v214, 0\n"
    "v_sub_f32_e32 v168, v168, v234\n"
    "v_sub_f32_e32 v169, v169, v234\n"
    "v_sub_f32_e32 v170, v170, v234\n"
    "v_sub_f32_e32 v171, v171, v234\n"
    "v_sub_f32_e32 v144, v144, v234\n"
    "v_sub_f32_e32 v145, v145, v234\n"
    "v_sub_f32_e32 v146, v146, v234\n"
    "v_sub_f32_e32 v147, v147, v234\n"
    "v_sub_f32_e32 v148, v148, v234\n"
    "v_sub_f32_e32 v149, v149, v234\n"
    "v_sub_f32_e32 v150, v150, v234\n"
    "v_sub_f32_e32 v151, v151, v234\n"
    "v_exp_f32_e32 v144, v144\n"
    "v_exp_f32_e32 v145, v145\n"
    "v_exp_f32_e32 v146, v146\n"
    "v_exp_f32_e32 v147, v147\n"
    "v_exp_f32_e32 v148, v148\n"
    "v_exp_f32_e32 v149, v149\n"
    "v_exp_f32_e32 v150, v150\n"
    "v_exp_f32_e32 v151, v151\n"
    "s_nop 0\n"
    "v_cvt_pk_bf16_f32 v200, v144, v145\n"
    "v_cvt_pk_bf16_f32 v201, v146, v147\n"
    "v_cvt_pk_bf16_f32 v202, v148, v149\n"
    "v_cvt_pk_bf16_f32 v203, v150, v151\n"
    "v_max3_f32 v234, v152, v153, v154\n"
    "v_max3_f32 v235, v155, v156, v157\n"
    "v_max3_f32 v234, v234, v158, v159\n"
    "v_max_f32_e32 v234, v234, v235\n"
    "v_mov_b32_e32 v235, v234\n"
    "s_nop 1\n"
    "v_permlane32_swap_b32_e32 v234, v235\n"
    "v_max_f32_e32 v234, v234, v235\n"
    "v_mov_b32_e32 v235, v234\n"
    "s_nop 1\n"
    "v_permlane16_swap_b32_e32 v234, v235\n"
    "v_max_f32_e32 v234, v234, v235\n"
    "v_add_f32_e32 v234, 0x42c00000, v234\n"
    "v_max_f32_e32 v234, 0, v234\n"
    "v_ceil_f32_e32 v234, v234\n"
    "v_cvt_i32_f32_e32 v235, v234\n"
    "v_sub_u32_e32 v235, 0, v235\n"
    "v_ldexp_f32 v96, v96, v235\n"
    "v_ldexp_f32 v97, v97, v235\n"
    "v_ldexp_f32 v98, v98, v235\n"
    "v_ldexp_f32 v99, v99, v235\n"
    "v_ldexp_f32 v100, v100, v235\n"
    "v_ldexp_f32 v101, v101, v235\n"
    "v_ldexp_f32 v102, v102, v235\n"
    "v_ldexp_f32 v103, v103, v235\n"
    "v_ldexp_f32 v104, v104, v235\n"
    "v_ldexp_f32 v105, v105, v235\n"
    "v_ldexp_f32 v106, v106, v235\n"
    "v_ldexp_f32 v107, v107, v235\n"
    "v_ldexp_f32 v108, v108, v235\n"
    "v_ldexp_f32 v109, v109, v235\n"
    "v_ldexp_f32 v110, v110, v235\n"
    "v_ldexp_f32 v111, v111, v235\n"
    "v_ldexp_f32 v112, v112, v235\n"
    "v_ldexp_f32 v113, v113, v235\n"
    "v_ldexp_f32 v114, v114, v235\n"
    "v_ldexp_f32 v115, v115, v235\n"
    "v_ldexp_f32 v116, v116, v235\n"
    "v_ldexp_f32 v117, v117, v235\n"
    "v_ldexp_f32 v118, v118, v235\n"
    "v_ldexp_f32 v119, v119, v235\n"
    "v_ldexp_f32 v120, v120, v235\n"
    "v_ldexp_f32 v121, v121, v235\n"
    "v_ldexp_f32 v122, v122, v235\n"
    "v_ldexp_f32 v123, v123, v235\n"
    "v_ldexp_f32 v124, v124, v235\n"
    "v_ldexp_f32 v125, v125, v235\n"
    "v_ldexp_f32 v126, v126, v235\n"
    "v_ldexp_f32 v127, v127, v235\n"
    "v_ldexp_f32 v213, v219, v235\n"
    "v_mov_b32_e32 v215, 0\n"
    "v_sub_f32_e32 v172, v172, v234\n"
    "v_sub_f32_e32 v173, v173, v234\n"
    "v_sub_f32_e32 v174, v174, v234\n"
    "v_sub_f32_e32 v175, v175, v234\n"
    "v_sub_f32_e32 v152, v152, v234\n"
    "v_sub_f32_e32 v153, v153, v234\n"
    "v_sub_f32_e32 v154, v154, v234\n"
    "v_sub_f32_e32 v155, v155, v234\n"
    "v_sub_f32_e32 v156, v156, v234\n"
    "v_sub_f32_e32 v157, v157, v234\n"
    "v_sub_f32_e32 v158, v158, v234\n"
    "v_sub_f32_e32 v159, v159, v234\n"
    "v_exp_f32_e32 v152, v152\n"
    "v_exp_f32_e32 v153, v153\n"
    "v_exp_f32_e32 v154, v154\n"
    "v_exp_f32_e32 v155, v155\n"
    "v_exp_f32_e32 v156, v156\n"
    "v_exp_f32_e32 v157, v157\n"
    "v_exp_f32_e32 v158, v158\n"
    "v_exp_f32_e32 v159, v159\n"
    "s_nop 0\n"
    "v_cvt_pk_bf16_f32 v204, v152, v153\n"
    "v_cvt_pk_bf16_f32 v205, v154, v155\n"
    "v_cvt_pk_bf16_f32 v206, v156, v157\n"
    "v_cvt_pk_bf16_f32 v207, v158, v159\n"
    "s_nop 1\n"
    "v_smfmac_f32_16x16x64_bf16 v[212:215], a[192:195], v[200:207], v245\n"
    "s_nop 7\n"
    "v_add_f32_e32 v242, v212, v214\n"
    "v_add_f32_e32 v243, v213, v215\n"
    "s_branch L_backT_mk\n"
    "L_fin_mk:\n"
    :: "s"(__builtin_amdgcn_kernarg_segment_ptr()), "s"(wid), "s"(blockIdx.x)
    : "v0", "v1", "v2", "v3", "v4", "v5", "v6", "v7", "v8", "v9", "v10", "v11", "v12", "v13", "v14", "v15", "v16", "v17", "v18", "v19", "v20", "v21", "v22", "v23", "v24", "v25", "v26", "v27", "v28", "v29", "v30", "v31", "v32", "v33", "v34", "v35", "v36", "v37", "v38", "v39", "v40", "v41", "v42", "v43", "v44", "v45", "v46", "v47", "v48", "v49", "v50", "v51", "v52", "v53", "v54", "v55", "v56", "v57", "v58", "v59", "v60", "v61", "v62", "v63", "v64", "v65", "v66", "v67", "v68", "v69", "v70", "v71", "v72", "v73", "v74", "v75", "v76", "v77", "v78", "v79", "v80", "v81", "v82", "v83", "v84", "v85", "v86", "v87", "v88", "v89", "v90", "v91", "v92", "v93", "v94", "v95", "v96", "v97", "v98", "v99", "v100", "v101", "v102", "v103", "v104", "v105", "v106", "v107", "v108", "v109", "v110", "v111", "v112", "v113", "v114", "v115", "v116", "v117", "v118", "v119", "v120", "v121", "v122", "v123", "v124", "v125", "v126", "v127", "v128", "v129", "v130", "v131", "v132", "v133", "v134", "v135", "v136", "v137", "v138", "v139", "v140", "v141", "v142", "v143", "v144", "v145", "v146", "v147", "v148", "v149", "v150", "v151", "v152", "v153", "v154", "v155", "v156", "v157", "v158", "v159", "v160", "v161", "v162", "v163", "v164", "v165", "v166", "v167", "v168", "v169", "v170", "v171", "v172", "v173", "v174", "v175", "v176", "v177", "v178", "v179", "v180", "v181", "v182", "v183", "v184", "v185", "v186", "v187", "v188", "v189", "v190", "v191", "v192", "v193", "v194", "v195", "v196", "v197", "v198", "v199", "v200", "v201", "v202", "v203", "v204", "v205", "v206", "v207", "v208", "v209", "v210", "v211", "v212", "v213", "v214", "v215", "v216", "v217", "v218", "v219", "v220", "v221", "v222", "v223", "v224", "v225", "v226", "v227", "v228", "v229", "v230", "v231", "v232", "v233", "v234", "v235", "v236", "v237", "v238", "v239", "v240", "v241", "v242", "v243", "v244", "v245", "v246", "v247", "v248", "v249", "v250", "v251", "v252", "v253", "v254", "v255", "a0", "a1", "a2", "a3", "a4", "a5", "a6", "a7", "a8", "a9", "a10", "a11", "a12", "a13", "a14", "a15", "a16", "a17", "a18", "a19", "a20", "a21", "a22", "a23", "a24", "a25", "a26", "a27", "a28", "a29", "a30", "a31", "a32", "a33", "a34", "a35", "a36", "a37", "a38", "a39", "a40", "a41", "a42", "a43", "a44", "a45", "a46", "a47", "a48", "a49", "a50", "a51", "a52", "a53", "a54", "a55", "a56", "a57", "a58", "a59", "a60", "a61", "a62", "a63", "a64", "a65", "a66", "a67", "a68", "a69", "a70", "a71", "a72", "a73", "a74", "a75", "a76", "a77", "a78", "a79", "a80", "a81", "a82", "a83", "a84", "a85", "a86", "a87", "a88", "a89", "a90", "a91", "a92", "a93", "a94", "a95", "a96", "a97", "a98", "a99", "a100", "a101", "a102", "a103", "a104", "a105", "a106", "a107", "a108", "a109", "a110", "a111", "a112", "a113", "a114", "a115", "a116", "a117", "a118", "a119", "a120", "a121", "a122", "a123", "a124", "a125", "a126", "a127", "a128", "a129", "a130", "a131", "a132", "a133", "a134", "a135", "a136", "a137", "a138", "a139", "a140", "a141", "a142", "a143", "a144", "a145", "a146", "a147", "a148", "a149", "a150", "a151", "a152", "a153", "a154", "a155", "a156", "a157", "a158", "a159", "a160", "a161", "a162", "a163", "a164", "a165", "a166", "a167", "a168", "a169", "a170", "a171", "a172", "a173", "a174", "a175", "a176", "a177", "a178", "a179", "a180", "a181", "a182", "a183", "a184", "a185", "a186", "a187", "a188", "a189", "a190", "a191", "a192", "a193", "a194", "a195", "a196", "a197", "a198", "a199", "a200", "a201", "a202", "a203", "a204", "a205", "a206", "a207", "a208", "a209", "a210", "a211", "a212", "a213", "a214", "a215", "a216", "a217", "a218", "a219", "a220", "a221", "a222", "a223", "a224", "a225", "a226", "a227", "a228", "a229", "a230", "a231", "a232", "a233", "a234", "a235", "a236", "a237", "a238", "a239", "a240", "a241", "a242", "a243", "a244", "a245", "a246", "a247", "a248", "a249", "a250", "a251", "a252", "a253", "a254", "a255", "s20", "s21", "s22", "s23", "s24", "s25", "s26", "s27", "s28", "s29", "s30", "s31", "s32", "s33", "s34", "s35", "s36", "s37", "s38", "s39", "s40", "s41", "s42", "s43", "s44", "s45", "s46", "s47", "s48", "s49", "s50", "s51", "s52", "s53", "s54", "s55", "s56", "s57", "s58", "s59", "s60", "s61", "s62", "s63", "s64", "s65", "s66", "s67", "s68", "s69", "s70", "s71", "s72", "s73", "s74", "s75", "s76", "s77", "s78", "s79", "s80", "s81", "s82", "s83", "s84", "s85", "s86", "s87", "s88", "s89", "s90", "s91", "s92", "s93", "s94", "s95", "s96", "s97", "s98", "s99", "vcc", "scc", "memory");
}
extern "C" void kernel_launch(void* const* d_in, const int* in_sizes, int n_in, void* d_out, int out_size, void* d_ws, size_t ws_size, hipStream_t stream) {
  const int LDS = 163840;
  (void)hipFuncSetAttribute((const void*)k_block, hipFuncAttributeMaxDynamicSharedMemorySize, LDS);
  k_block<<<256, 256, LDS, stream>>>((char*)d_ws, (const u16*)d_in[0], (const u16*)d_in[1], (const u16*)d_in[2], (const u16*)d_in[3], (const u16*)d_in[4],
                                     (const u16*)d_in[6], (const u16*)d_in[7], (const u16*)d_in[8], (const u16*)d_in[9], (const u16*)d_in[10], (u16*)d_out);
}
```
